```python
import torch

def _softmax(x, axis=-1):
    return torch.softmax(x, dim=axis)
def _gelu(x):
    return torch.nn.functional.gelu(x, approximate="tanh")
def _silu(x):
    return torch.nn.functional.silu(x)
def _sigmoid(x):
    return torch.sigmoid(x)
def _layernorm(x, axis=-1, eps=1e-5):
    m = x.mean(dim=axis, keepdim=True)
    v = x.var(dim=axis, keepdim=True, unbiased=False)
    return (x - m) / torch.sqrt(v + eps)
def _scatter_add(tgt, idx, src, axis=0):
    return tgt.clone().index_add_(axis, idx, src)
def _segment_sum(src, seg, n):
    out = torch.zeros((n,) + src.shape[1:], dtype=src.dtype)
    return out.index_add_(0, seg, src)
def _pad(x, pw):
    flat = tuple(v for pair in reversed(pw) for v in pair)
    return torch.nn.functional.pad(x, flat)

kernel_name = 'concat_heads__gqa_block_cached__mha_block_cached__near_ridge'

def setup_inputs():

    _w = (lambda a: (lambda t: t.to(torch.bfloat16) if t.is_floating_point() else t)(torch.as_tensor(a)))
    def _A_setup():
        B, Sq, D = 16, 80, 576
        Hq, Hkv, Sk, Dh = 32, 16, 512, 128
        assert Hq % Hkv == 0, (Hq, Hkv)
        return {
            "x": _w((lambda *s: torch.randn(*s, dtype=torch.bfloat16) * 0.8)(B, Sq, D)),
            "k_cache": _w((lambda *s: torch.randn(*s, dtype=torch.bfloat16) * 0.8)(B, Hkv, Sk, Dh)),
            "v_cache": _w((lambda *s: torch.randn(*s, dtype=torch.bfloat16) * 0.05)(B, Hkv, Sk, Dh)),
            "wq": _w((lambda *s: torch.randn(*s, dtype=torch.bfloat16) * 0.8)(D, Hq * Dh) * (D ** -0.5)),
            "wo": _w((lambda *s: torch.randn(*s, dtype=torch.bfloat16) * 0.05)(Hq * Dh, D) * ((Hq * Dh) ** -0.5)),
        }
    _A_in = _A_setup()

    def _B_setup():
        B, Sq, D = 16, 80, 576
        H, Sk, Dh = 32, 512, 128
        return {
            "x": _w((lambda *s: torch.randn(*s, dtype=torch.bfloat16) * 0.8)(B, Sq, D)),
            "k_cache": _w((lambda *s: torch.randn(*s, dtype=torch.bfloat16) * 0.8)(B, H, Sk, Dh)),
            "v_cache": _w((lambda *s: torch.randn(*s, dtype=torch.bfloat16) * 0.05)(B, H, Sk, Dh)),
            "wq": _w((lambda *s: torch.randn(*s, dtype=torch.bfloat16) * 0.8)(D, H * Dh) * (D ** -0.5)),
            "wo": _w((lambda *s: torch.randn(*s, dtype=torch.bfloat16) * 0.05)(H * Dh, D) * ((H * Dh) ** -0.5)),
        }
    _B_in = _B_setup()

    return {
        "A_x": _A_in["x"],
        "A_k_cache": _A_in["k_cache"],
        "A_v_cache": _A_in["v_cache"],
        "A_wq": _A_in["wq"],
        "A_wo": _A_in["wo"],
        "B_x": _B_in["x"],
        "B_k_cache": _B_in["k_cache"],
        "B_v_cache": _B_in["v_cache"],
        "B_wq": _B_in["wq"],
        "B_wo": _B_in["wo"],
    }

def reference(A_x, A_k_cache, A_v_cache, A_wq, A_wo, B_x, B_k_cache, B_v_cache, B_wq, B_wo):
    def _A_ref(x, k_cache, v_cache, wq, wo):
        B, Sq, D = x.shape
        Hq, Dh = 32, 128
        Hkv = k_cache.shape[1]
        rep = Hq // Hkv
        q = (lambda x, *d: x.permute(*d))((x @ wq).reshape(B, Sq, Hq, Dh), 0, 2, 1, 3)
        k = (lambda x, n, axis: torch.repeat_interleave(x, n, dim=axis))(k_cache, rep, axis=1)
        v = (lambda x, n, axis: torch.repeat_interleave(x, n, dim=axis))(v_cache, rep, axis=1)
        scores = torch.einsum("bhqd,bhkd->bhqk", q, k) * (Dh ** -0.5)
        p = _softmax(scores, axis=-1)
        o = torch.einsum("bhqk,bhkd->bhqd", p, v)
        return (lambda x, *d: x.permute(*d))(o, 0, 2, 1, 3).reshape(B, Sq, Hq * Dh) @ wo

    def _B_ref(x, k_cache, v_cache, wq, wo):
        B, Sq, D = x.shape
        H, Dh = 32, 128
        q = (lambda x, *d: x.permute(*d))((x @ wq).reshape(B, Sq, H, Dh), 0, 2, 1, 3)
        scores = torch.einsum("bhqd,bhkd->bhqk", q, k_cache) * (Dh ** -0.5)
        p = _softmax(scores, axis=-1)
        o = torch.einsum("bhqk,bhkd->bhqd", p, v_cache)
        return (lambda x, *d: x.permute(*d))(o, 0, 2, 1, 3).reshape(B, Sq, H * Dh) @ wo

    a = _A_ref(A_x, A_k_cache, A_v_cache, A_wq, A_wo)
    b = _B_ref(B_x, B_k_cache, B_v_cache, B_wq, B_wo)
    return torch.concatenate([a, b], axis=-1)
```

```cpp
#include <hip/hip_runtime.h>
#include <hip/hip_bf16.h>
#include <stdint.h>
typedef unsigned short u16;
__device__ __forceinline__ float bf2f(u16 v){ return __uint_as_float(((unsigned)v)<<16); }
__device__ __forceinline__ u16 f2bf(float f){ unsigned u=__float_as_uint(f); u += 0x7fffu + ((u>>16)&1u); return (u16)(u>>16); }

__global__ void n_gemm(const u16* A, const u16* W, u16* C, int M, int N, int K, int ldc, int coff){
  int n = blockIdx.x*64 + threadIdx.x; int m = blockIdx.y*4 + threadIdx.y;
  if (n>=N || m>=M) return;
  float acc=0.f;
  for (int k=0;k<K;++k) acc += bf2f(A[(size_t)m*K+k]) * bf2f(W[(size_t)k*N+n]);
  C[(size_t)m*ldc + coff + n] = f2bf(acc);
}
__global__ void n_attn(const u16* Q, const u16* Kc, const u16* Vc, u16* O, int Hkv){
  int q = blockIdx.x, h = blockIdx.y, b = blockIdx.z; int t = threadIdx.x;
  int kvh = h / (32/Hkv);
  __shared__ float qs[128]; __shared__ float s[512]; __shared__ float red[128];
  qs[t] = bf2f(Q[((size_t)(b*80+q))*4096 + h*128 + t]);
  __syncthreads();
  const u16* Kp = Kc + ((size_t)(b*Hkv+kvh))*512*128; const u16* Vp = Vc + ((size_t)(b*Hkv+kvh))*512*128;
  float mx=-1e30f;
  for (int j=t;j<512;j+=128){ float a=0; for(int d=0;d<128;++d) a+=qs[d]*bf2f(Kp[j*128+d]); a*=0.08838834764831845f; s[j]=a; mx=fmaxf(mx,a);}
  red[t]=mx; __syncthreads();
  for(int o=64;o>0;o>>=1){ if(t<o) red[t]=fmaxf(red[t],red[t+o]); __syncthreads(); }
  mx=red[0]; __syncthreads();
  float sm=0; for (int j=t;j<512;j+=128){ float e=expf(s[j]-mx); s[j]=e; sm+=e; }
  red[t]=sm; __syncthreads();
  for(int o=64;o>0;o>>=1){ if(t<o) red[t]+=red[t+o]; __syncthreads(); }
  sm=red[0];
  float a=0; for(int j=0;j<512;++j) a+=s[j]*bf2f(Vp[j*128+t]);
  O[((size_t)(b*80+q))*4096 + h*128 + t] = f2bf(a/sm);
}
extern "C" void kernel_launch(void* const* d_in, const int* in_sizes, int n_in, void* d_out, int out_size, void* d_ws, size_t ws_size, hipStream_t stream){
  u16* ws=(u16*)d_ws;
  for (int br=0;br<2;++br){
    const u16* x=(const u16*)d_in[5*br+0]; const u16* k=(const u16*)d_in[5*br+1]; const u16* v=(const u16*)d_in[5*br+2];
    const u16* wq=(const u16*)d_in[5*br+3]; const u16* wo=(const u16*)d_in[5*br+4];
    u16* Q=ws+(size_t)br*2*1280*4096; u16* O=Q+(size_t)1280*4096;
    n_gemm<<<dim3(4096/64,1280/4),dim3(64,4),0,stream>>>(x,wq,Q,1280,4096,576,4096,0);
    n_attn<<<dim3(80,32,16),128,0,stream>>>(Q,k,v,O,br==0?16:32);
    n_gemm<<<dim3(576/64,1280/4),dim3(64,4),0,stream>>>(O,wo,(u16*)d_out,1280,576,4096,1152,br*576);
  }
}
```

```cpp
#include <hip/hip_runtime.h>
#include <hip/hip_bf16.h>
#include <stdint.h>

typedef unsigned short u16;

#define K1_MAP 5
#define K1_DBG 0

__global__ __launch_bounds__(256, 1) void k1_attn(const void* xA, const void* kA, const void* vA, const void* wqA, const void* woA,
                                                  const void* xB, const void* kB, const void* vB, const void* wqB, const void* woB,
                                                  void* ws, void* out) {
  __shared__ __attribute__((aligned(1024))) char lds[163840];
  const unsigned w = __builtin_amdgcn_readfirstlane(threadIdx.x >> 6);
  asm volatile("" ::"s"((unsigned)(size_t)lds));
  asm volatile(
    "s_load_dwordx16 s[4:19], %0, 0x0\n"
    "s_load_dwordx8 s[20:27], %0, 0x40\n"
    "s_and_b32 s35, %1, 7\n"
    "s_bfe_u32 s34, %1, 0x20003\n"
    "s_lshr_b32 s76, %1, 6\n"
    "s_lshl_b32 s76, s76, 2\n"
    "s_or_b32 s34, s34, s76\n"
    "s_bitcmp0_b32 %1, 5\n"
    "s_cbranch_scc0 A_START\n"
    "B_START:\n"
    "v_mbcnt_lo_u32_b32 v239, -1, 0\n"
    "v_mbcnt_hi_u32_b32 v239, -1, v239\n"
    "s_mov_b32 s72, %2\n"
    "s_mul_i32 s71, s72, 0xa000\n"
    "s_sub_u32 s79, s34, 9\n"
    "s_cselect_b32 s79, s34, s79\n"
    "s_lshl_b32 s76, s72, 2\n"
    "s_add_u32 s76, s76, 0\n"
    "s_min_u32 s76, s76, 14\n"
    "s_mul_i32 s77, s76, 52\n"
    "s_lshr_b32 s77, s77, 8\n"
    "s_mul_i32 s28, s77, 5\n"
    "s_sub_u32 s76, s76, s28\n"
    "s_mul_i32 s76, s76, 0x4800\n"
    "s_add_u32 s84, s76, 0x480\n"
    "s_add_u32 s77, s77, s79\n"
    "s_lshl_b32 s77, s77, 7\n"
    "s_add_u32 s28, s76, s77\n"
    "s_lshl_b32 s76, s72, 2\n"
    "s_add_u32 s76, s76, 1\n"
    "s_min_u32 s76, s76, 14\n"
    "s_mul_i32 s77, s76, 52\n"
    "s_lshr_b32 s77, s77, 8\n"
    "s_mul_i32 s29, s77, 5\n"
    "s_sub_u32 s76, s76, s29\n"
    "s_mul_i32 s76, s76, 0x4800\n"
    "s_add_u32 s85, s76, 0x480\n"
    "s_add_u32 s77, s77, s79\n"
    "s_lshl_b32 s77, s77, 7\n"
    "s_add_u32 s29, s76, s77\n"
    "s_lshl_b32 s76, s72, 2\n"
    "s_add_u32 s76, s76, 2\n"
    "s_min_u32 s76, s76, 14\n"
    "s_mul_i32 s77, s76, 52\n"
    "s_lshr_b32 s77, s77, 8\n"
    "s_mul_i32 s30, s77, 5\n"
    "s_sub_u32 s76, s76, s30\n"
    "s_mul_i32 s76, s76, 0x4800\n"
    "s_add_u32 s87, s76, 0x480\n"
    "s_add_u32 s77, s77, s79\n"
    "s_lshl_b32 s77, s77, 7\n"
    "s_add_u32 s30, s76, s77\n"
    "s_lshl_b32 s76, s72, 2\n"
    "s_add_u32 s76, s76, 3\n"
    "s_min_u32 s76, s76, 14\n"
    "s_mul_i32 s77, s76, 52\n"
    "s_lshr_b32 s77, s77, 8\n"
    "s_mul_i32 s31, s77, 5\n"
    "s_sub_u32 s76, s76, s31\n"
    "s_mul_i32 s76, s76, 0x4800\n"
    "s_add_u32 s88, s76, 0x480\n"
    "s_add_u32 s77, s77, s79\n"
    "s_lshl_b32 s77, s77, 7\n"
    "s_add_u32 s31, s76, s77\n"
    "s_lshl_b32 s73, s79, 19\n"
    "s_lshl_b32 s78, s35, 2\n"
    "s_add_u32 s78, s78, s72\n"
    "s_lshl_b32 s90, s78, 8\n"
    "s_mul_i32 s89, s34, 0x16800\n"
    "s_lshl_b32 s76, s34, 5\n"
    "s_add_u32 s81, s78, s76\n"
    "s_waitcnt lgkmcnt(0)\n"
    "s_add_u32 s92, s14, s89\n"
    "s_addc_u32 s93, s15, 0\n"
    "s_and_b32 s93, s93, 0xffff\n"
    "s_mov_b32 s94, 0x16800\n"
    "s_mov_b32 s95, 0x20000\n"
    "v_lshrrev_b32 v209, 3, v239\n"
    "v_and_b32 v210, 7, v239\n"
    "v_lshrrev_b32 v211, 4, v239\n"
    "v_xor_b32 v210, v210, v211\n"
    "v_lshlrev_b32 v210, 4, v210\n"
    "v_mul_u32_u24 v209, 0x480, v209\n"
    "v_add_u32 v206, v209, v210\n"
    "v_xor_b32 v210, 64, v210\n"
    "v_add_u32 v209, 0x2000, v209\n"
    "v_add_u32 v207, v209, v210\n"
    "s_cmp_ge_u32 s28, s84\n"
    "s_cselect_b32 s76, 0x480, 0\n"
    "s_add_u32 m0, s71, 0x6000\n"
    "s_sub_u32 s28, s28, s76\n"
    "buffer_load_dwordx4 v206, s[92:95], s28 offen lds\n"
    "buffer_load_dwordx4 v207, s[92:95], s28 offen offset:1024 lds\n"
    "s_cmp_ge_u32 s29, s85\n"
    "s_cselect_b32 s76, 0x480, 0\n"
    "s_add_u32 m0, s71, 0x6800\n"
    "s_sub_u32 s29, s29, s76\n"
    "buffer_load_dwordx4 v206, s[92:95], s29 offen lds\n"
    "buffer_load_dwordx4 v207, s[92:95], s29 offen offset:1024 lds\n"
    "s_cmp_ge_u32 s30, s87\n"
    "s_cselect_b32 s76, 0x480, 0\n"
    "s_add_u32 m0, s71, 0x7000\n"
    "s_sub_u32 s30, s30, s76\n"
    "buffer_load_dwordx4 v206, s[92:95], s30 offen lds\n"
    "buffer_load_dwordx4 v207, s[92:95], s30 offen offset:1024 lds\n"
    "s_cmp_ge_u32 s31, s88\n"
    "s_cselect_b32 s76, 0x480, 0\n"
    "s_add_u32 m0, s71, 0x7800\n"
    "s_sub_u32 s31, s31, s76\n"
    "buffer_load_dwordx4 v206, s[92:95], s31 offen lds\n"
    "buffer_load_dwordx4 v207, s[92:95], s31 offen offset:1024 lds\n"
    "s_add_u32 s36, s20, s90\n"
    "s_addc_u32 s37, s21, 0\n"
    "s_and_b32 s37, s37, 0xffff\n"
    "s_mov_b32 s38, 0x47e100\n"
    "s_mov_b32 s39, 0x20000\n"
    "s_mov_b32 s60, 0x2000\n"
    "s_mov_b32 s61, 0x4000\n"
    "s_mov_b32 s62, 0x6000\n"
    "s_mov_b32 s63, 0x8000\n"
    "s_mov_b32 s64, 0xa000\n"
    "s_mov_b32 s65, 0xc000\n"
    "s_mov_b32 s66, 0xe000\n"
    "v_lshrrev_b32 v209, 4, v239\n"
    "v_and_b32 v210, 15, v239\n"
    "v_lshlrev_b32 v209, 16, v209\n"
    "v_lshl_add_u32 v200, v210, 4, v209\n"
    "s_getpc_b64 s[56:57]\n"
    "B_PC:\n"
    "s_and_b32 s57, s57, 0xffff\n"
    "s_mov_b32 s58, B_END-B_PC\n"
    "s_mov_b32 s59, 0x20000\n"
    "v_lshlrev_b32 v209, 13, v239\n"
    "s_lshl_b32 s76, s34, 2\n"
    "s_add_u32 s76, s76, s72\n"
    "s_lshl_b32 s76, s76, 7\n"
    "buffer_load_dword v252, v209, s[56:59], s76 offen\n"
    "v_add_u32 v201, s73, v200\n"
    "s_add_u32 s73, s73, 0x40000\n"
    "s_cmp_eq_u32 s73, 0x480000\n"
    "s_cselect_b32 s73, 0, s73\n"
    "buffer_load_dwordx4 v[128:131], v201, s[36:39], 0 offen\n"
    "buffer_load_dwordx4 v[132:135], v201, s[36:39], s60 offen\n"
    "buffer_load_dwordx4 v[136:139], v201, s[36:39], s61 offen\n"
    "buffer_load_dwordx4 v[140:143], v201, s[36:39], s62 offen\n"
    "buffer_load_dwordx4 v[144:147], v201, s[36:39], s63 offen\n"
    "s_lshl_b32 s76, s81, 17\n"
    "s_add_u32 s40, s16, s76\n"
    "s_addc_u32 s41, s17, 0\n"
    "s_and_b32 s41, s41, 0xffff\n"
    "s_mov_b32 s42, 0x20000\n"
    "s_mov_b32 s43, 0x20000\n"
    "s_add_u32 s44, s18, s76\n"
    "s_addc_u32 s45, s19, 0\n"
    "s_and_b32 s45, s45, 0xffff\n"
    "s_mov_b32 s46, 0x20000\n"
    "s_mov_b32 s47, 0x20000\n"
    "s_mul_i32 s82, s35, 0x90000\n"
    "s_add_u32 s52, s22, s82\n"
    "s_addc_u32 s53, s23, 0\n"
    "s_and_b32 s53, s53, 0xffff\n"
    "s_mov_b32 s54, 0x90000\n"
    "s_mov_b32 s55, 0x20000\n"
    "s_add_u32 s76, s34, 16\n"
    "s_mul_i32 s76, s76, 0xb4000\n"
    "s_add_u32 s48, s24, s76\n"
    "s_addc_u32 s49, s25, 0\n"
    "s_and_b32 s49, s49, 0xffff\n"
    "s_mov_b32 s50, 0xb4000\n"
    "s_mov_b32 s51, 0x20000\n"
    "s_add_u32 s74, s35, s34\n"
    "s_add_u32 s74, s74, 1\n"
    "s_sub_u32 s76, s74, 9\n"
    "s_cselect_b32 s74, s74, s76\n"
    "s_sub_u32 s76, s74, 9\n"
    "s_cselect_b32 s74, s74, s76\n"
    "s_lshl_b32 s74, s74, 7\n"
    "s_mul_i32 s75, s81, 5\n"
    "s_mul_i32 s76, s34, 1\n"
    "s_add_u32 s75, s75, s76\n"
    "s_mov_b32 s76, s75\n"
    "s_mov_b32 s75, 0\n"
    "s_bitcmp1_b32 s76, 0\n"
    "s_cselect_b32 s77, 0x2000, 0\n"
    "s_or_b32 s75, s75, s77\n"
    "s_bitcmp1_b32 s76, 1\n"
    "s_cselect_b32 s77, 0x4000, 0\n"
    "s_or_b32 s75, s75, s77\n"
    "s_bitcmp1_b32 s76, 2\n"
    "s_cselect_b32 s77, 0x8000, 0\n"
    "s_or_b32 s75, s75, s77\n"
    "s_bitcmp1_b32 s76, 3\n"
    "s_cselect_b32 s77, 0x10000, 0\n"
    "s_or_b32 s75, s75, s77\n"
    "s_mov_b32 s79, s71\n"
    "s_mov_b32 s67, 0x5040100\n"
    "s_mov_b32 s68, 0x7060302\n"
    "s_mov_b32 s69, 0x3e0293ee\n"
    "s_mov_b32 s70, 0xbe0293ee\n"
    "v_lshrrev_b32 v209, 4, v239\n"
    "v_and_b32 v210, 15, v239\n"
    "v_lshlrev_b32 v238, 4, v239\n"
    "v_and_b32 v248, 0x1ff, v238\n"
    "v_lshrrev_b32 v211, 5, v239\n"
    "v_lshl_or_b32 v248, v211, 3, v248\n"
    "v_lshrrev_b32 v211, 1, v210\n"
    "v_xor_b32 v211, v211, v209\n"
    "v_lshlrev_b32 v211, 4, v211\n"
    "v_lshl_add_u32 v211, v210, 7, v211\n"
    "v_add_u32 v202, 0x6000, v211\n"
    "v_xor_b32 v204, 64, v202\n"
    "v_add_u32 v203, 0x14000, v202\n"
    "v_add_u32 v205, 0x14000, v204\n"
    "v_lshlrev_b32 v211, 2, v209\n"
    "v_xor_b32 v211, v211, v210\n"
    "v_lshlrev_b32 v211, 4, v211\n"
    "v_lshl_add_u32 v226, v209, 8, v211\n"
    "v_xor_b32 v227, 32, v226\n"
    "v_xor_b32 v228, 16, v226\n"
    "v_xor_b32 v229, 48, v226\n"
    "v_mov_b32 v230, v226\n"
    "v_mov_b32 v231, v227\n"
    "v_mov_b32 v232, v228\n"
    "v_mov_b32 v233, v229\n"
    "v_and_b32 v211, 3, v210\n"
    "v_xor_b32 v211, v211, v209\n"
    "v_lshlrev_b32 v211, 2, v211\n"
    "v_bfe_u32 v212, v210, 3, 1\n"
    "v_or_b32 v211, v211, v212\n"
    "v_bfe_u32 v212, v210, 2, 1\n"
    "v_lshl_or_b32 v211, v212, 1, v211\n"
    "v_lshlrev_b32 v211, 4, v211\n"
    "v_lshl_add_u32 v211, v210, 8, v211\n"
    "v_add_u32 v214, s79, v211\n"
    "v_xor_b32 v215, 16, v214\n"
    "v_xor_b32 v216, 32, v214\n"
    "v_xor_b32 v217, 48, v214\n"
    "v_and_b32 v211, 12, v210\n"
    "v_bfe_u32 v212, v210, 1, 1\n"
    "v_or_b32 v211, v211, v212\n"
    "v_lshrrev_b32 v212, 1, v209\n"
    "v_xor_b32 v211, v211, v212\n"
    "v_and_b32 v212, 1, v209\n"
    "v_lshlrev_b32 v212, 1, v212\n"
    "v_xor_b32 v211, v211, v212\n"
    "v_lshlrev_b32 v211, 4, v211\n"
    "v_and_b32 v212, 1, v210\n"
    "v_lshl_or_b32 v211, v212, 3, v211\n"
    "v_lshrrev_b32 v212, 2, v239\n"
    "v_lshl_add_u32 v211, v212, 8, v211\n"
    "v_add_u32 v218, s79, v211\n"
    "v_xor_b32 v219, 32, v218\n"
    "v_xor_b32 v220, 64, v218\n"
    "v_xor_b32 v221, 0x60, v218\n"
    "v_xor_b32 v222, 0x80, v218\n"
    "v_xor_b32 v223, 0xa0, v218\n"
    "v_xor_b32 v224, 0xc0, v218\n"
    "v_xor_b32 v225, 0xe0, v218\n"
    "v_and_b32 v211, 7, v239\n"
    "v_lshlrev_b32 v212, 1, v209\n"
    "v_xor_b32 v211, v211, v212\n"
    "v_lshlrev_b32 v211, 4, v211\n"
    "v_lshrrev_b32 v212, 3, v239\n"
    "v_mul_u32_u24 v212, 0x480, v212\n"
    "v_add_u32 v234, v211, v212\n"
    "v_add_u32 v235, 0x2000, v234\n"
    "v_add_u32 v236, 0x4000, v234\n"
    "v_add_u32 v237, 0x6000, v234\n"
    "v_bfe_u32 v211, v239, 3, 2\n"
    "v_xor_b32 v211, s72, v211\n"
    "v_lshlrev_b32 v211, 5, v211\n"
    "v_and_b32 v212, 3, v239\n"
    "v_lshl_or_b32 v211, v212, 3, v211\n"
    "v_lshrrev_b32 v212, 2, v239\n"
    "v_lshl_add_u32 v211, v212, 7, v211\n"
    "s_add_u32 s76, s72, 0\n"
    "s_and_b32 s76, s76, 3\n"
    "s_mul_i32 s76, s76, 0xa000\n"
    "v_add_u32 v244, s76, v211\n"
    "v_add_u32 v240, s76, v238\n"
    "s_add_u32 s76, s72, 1\n"
    "s_and_b32 s76, s76, 3\n"
    "s_mul_i32 s76, s76, 0xa000\n"
    "v_add_u32 v245, s76, v211\n"
    "v_add_u32 v241, s76, v238\n"
    "s_add_u32 s76, s72, 2\n"
    "s_and_b32 s76, s76, 3\n"
    "s_mul_i32 s76, s76, 0xa000\n"
    "v_add_u32 v246, s76, v211\n"
    "v_add_u32 v242, s76, v238\n"
    "s_add_u32 s76, s72, 3\n"
    "s_and_b32 s76, s76, 3\n"
    "s_mul_i32 s76, s76, 0xa000\n"
    "v_add_u32 v247, s76, v211\n"
    "v_add_u32 v243, s76, v238\n"
    "s_lshl_b32 s76, s35, 9\n"
    "s_lshl_b32 s77, s72, 7\n"
    "s_add_u32 s76, s76, s77\n"
    "v_lshl_add_u32 v249, v239, 2, s76\n"
    "v_mov_b32 v212, 0x40000000\n"
    "v_cmp_gt_u32 vcc, 16, v239\n"
    "s_add_u32 s76, s35, 1\n"
    "s_nop 0\n"
    "v_cndmask_b32 v249, v212, v249, vcc\n"
    "v_add_u32 v211, s76, v239\n"
    "v_subrev_u32 v209, 9, v211\n"
    "v_min_u32 v211, v211, v209\n"
    "v_lshlrev_b32 v211, 9, v211\n"
    "v_lshl_add_u32 v211, v239, 2, v211\n"
    "s_sub_u32 s76, 15, s35\n"
    "s_lshl_b32 s76, s76, 2\n"
    "v_and_b32 v209, 7, v239\n"
    "v_lshl_add_u32 v209, v209, 9, s76\n"
    "v_cndmask_b32 v209, v212, v209, vcc\n"
    "v_cmp_gt_u32 vcc, 8, v239\n"
    "s_nop 1\n"
    "v_cndmask_b32 v211, v209, v211, vcc\n"
    "v_add_u32 v250, s77, v211\n"
    "s_mul_i32 s91, s34, 0x9000\n"
    "s_mul_i32 s76, s72, 0x2400\n"
    "s_add_u32 s91, s91, s76\n"
    "s_mul_i32 s86, s72, 0x24000\n"
    "s_mul_i32 s90, s72, 0xa00\n"
    "s_mov_b64 exec, 0\n"
    "s_cmp_eq_u32 s72, 0\n"
    "s_cbranch_scc1 B_TH_FIRST0\n"
    "s_cmp_eq_u32 s72, 1\n"
    "s_cbranch_scc1 B_TH_FIRST1\n"
    "s_cmp_eq_u32 s72, 2\n"
    "s_cbranch_scc1 B_TH_FIRST2\n"
    "s_cmp_eq_u32 s72, 3\n"
    "s_cbranch_scc1 B_TH_FIRST3\n"
    "B_TH_RET:\n"
    "s_mov_b64 exec, -1\n"
    "buffer_load_dwordx4 v[148:151], v201, s[36:39], s64 offen\n"
    "buffer_load_dwordx4 v[152:155], v201, s[36:39], s65 offen\n"
    "buffer_load_dwordx4 v[156:159], v201, s[36:39], s66 offen\n"
    "v_add_u32 v208, s73, v200\n"
    "s_add_u32 s73, s73, 0x40000\n"
    "s_cmp_eq_u32 s73, 0x480000\n"
    "s_cselect_b32 s73, 0, s73\n"
    "buffer_load_dwordx4 v[160:163], v208, s[36:39], 0 offen\n"
    "buffer_load_dwordx4 v[164:167], v208, s[36:39], s60 offen\n"
    "buffer_load_dwordx4 v[168:171], v208, s[36:39], s61 offen\n"
    "buffer_load_dwordx4 v[172:175], v208, s[36:39], s62 offen\n"
    "buffer_load_dwordx4 v[176:179], v208, s[36:39], s63 offen\n"
    "buffer_load_dwordx4 v[180:183], v208, s[36:39], s64 offen\n"
    "buffer_load_dwordx4 v[184:187], v208, s[36:39], s65 offen\n"
    "buffer_load_dwordx4 v[188:191], v208, s[36:39], s66 offen\n"
    "v_add_u32 v201, s73, v200\n"
    "s_add_u32 s73, s73, 0x40000\n"
    "s_cmp_eq_u32 s73, 0x480000\n"
    "s_cselect_b32 s73, 0, s73\n"
    "buffer_load_dwordx4 v[64:67], v201, s[36:39], 0 offen\n"
    "buffer_load_dwordx4 v[68:71], v201, s[36:39], s60 offen\n"
    "buffer_load_dwordx4 v[72:75], v201, s[36:39], s61 offen\n"
    "buffer_load_dwordx4 v[76:79], v201, s[36:39], s62 offen\n"
    "buffer_load_dwordx4 v[80:83], v201, s[36:39], s63 offen\n"
    "buffer_load_dwordx4 v[84:87], v201, s[36:39], s64 offen\n"
    "buffer_load_dwordx4 v[88:91], v201, s[36:39], s65 offen\n"
    "buffer_load_dwordx4 v[92:95], v201, s[36:39], s66 offen\n"
    "v_add_u32 v208, s73, v200\n"
    "s_add_u32 s73, s73, 0x40000\n"
    "s_cmp_eq_u32 s73, 0x480000\n"
    "s_cselect_b32 s73, 0, s73\n"
    "B_TH_BEGIN:\n"
    "s_waitcnt vmcnt(16)\n"
    "s_barrier\n"
    "ds_read_b128 a[160:163], v202 offset:0\n"
    "ds_read_b128 a[164:167], v202 offset:2048\n"
    "ds_read_b128 a[168:171], v202 offset:4096\n"
    "ds_read_b128 a[172:175], v202 offset:6144\n"
    "ds_read_b128 a[176:179], v202 offset:40960\n"
    "v_perm_b32 v192, v132, v128, s67\n"
    "v_perm_b32 v193, v140, v136, s67\n"
    "v_perm_b32 v194, v148, v144, s67\n"
    "v_perm_b32 v195, v156, v152, s67\n"
    "s_waitcnt lgkmcnt(0)\n"
    "s_nop 0\n"
    "v_mfma_f32_16x16x32_bf16 a[0:3], v[192:195], a[160:163], 0\n"
    "v_perm_b32 v196, v132, v128, s68\n"
    "v_perm_b32 v197, v140, v136, s68\n"
    "v_mfma_f32_16x16x32_bf16 a[32:35], v[192:195], a[164:167], 0\n"
    "v_perm_b32 v198, v148, v144, s68\n"
    "v_perm_b32 v199, v156, v152, s68\n"
    "v_mfma_f32_16x16x32_bf16 a[64:67], v[192:195], a[168:171], 0\n"
    "buffer_load_dwordx4 v[96:99], v208, s[36:39], 0 offen\n"
    "v_mfma_f32_16x16x32_bf16 a[96:99], v[192:195], a[172:175], 0\n"
    "v_mfma_f32_16x16x32_bf16 a[128:131], v[192:195], a[176:179], 0\n"
    "v_mfma_f32_16x16x32_bf16 a[4:7], v[196:199], a[160:163], 0\n"
    "v_perm_b32 v192, v133, v129, s67\n"
    "v_perm_b32 v193, v141, v137, s67\n"
    "v_mfma_f32_16x16x32_bf16 a[36:39], v[196:199], a[164:167], 0\n"
    "v_perm_b32 v194, v149, v145, s67\n"
    "v_perm_b32 v195, v157, v153, s67\n"
    "v_mfma_f32_16x16x32_bf16 a[68:71], v[196:199], a[168:171], 0\n"
    "buffer_load_dwordx4 v[100:103], v208, s[36:39], s60 offen\n"
    "v_mfma_f32_16x16x32_bf16 a[100:103], v[196:199], a[172:175], 0\n"
    "ds_read_b128 a[180:183], v204 offset:0\n"
    "v_mfma_f32_16x16x32_bf16 a[132:135], v[196:199], a[176:179], 0\n"
    "v_mfma_f32_16x16x32_bf16 a[8:11], v[192:195], a[160:163], 0\n"
    "v_perm_b32 v196, v133, v129, s68\n"
    "v_perm_b32 v197, v141, v137, s68\n"
    "v_mfma_f32_16x16x32_bf16 a[40:43], v[192:195], a[164:167], 0\n"
    "v_perm_b32 v198, v149, v145, s68\n"
    "v_perm_b32 v199, v157, v153, s68\n"
    "v_mfma_f32_16x16x32_bf16 a[72:75], v[192:195], a[168:171], 0\n"
    "buffer_load_dwordx4 v[104:107], v208, s[36:39], s61 offen\n"
    "v_mfma_f32_16x16x32_bf16 a[104:107], v[192:195], a[172:175], 0\n"
    "ds_read_b128 a[184:187], v204 offset:2048\n"
    "v_mfma_f32_16x16x32_bf16 a[136:139], v[192:195], a[176:179], 0\n"
    "v_mfma_f32_16x16x32_bf16 a[12:15], v[196:199], a[160:163], 0\n"
    "v_perm_b32 v192, v134, v130, s67\n"
    "v_perm_b32 v193, v142, v138, s67\n"
    "v_mfma_f32_16x16x32_bf16 a[44:47], v[196:199], a[164:167], 0\n"
    "v_perm_b32 v194, v150, v146, s67\n"
    "v_perm_b32 v195, v158, v154, s67\n"
    "v_mfma_f32_16x16x32_bf16 a[76:79], v[196:199], a[168:171], 0\n"
    "buffer_load_dwordx4 v[108:111], v208, s[36:39], s62 offen\n"
    "v_mfma_f32_16x16x32_bf16 a[108:111], v[196:199], a[172:175], 0\n"
    "ds_read_b128 a[188:191], v204 offset:4096\n"
    "v_mfma_f32_16x16x32_bf16 a[140:143], v[196:199], a[176:179], 0\n"
    "v_mfma_f32_16x16x32_bf16 a[16:19], v[192:195], a[160:163], 0\n"
    "v_perm_b32 v196, v134, v130, s68\n"
    "v_perm_b32 v197, v142, v138, s68\n"
    "v_mfma_f32_16x16x32_bf16 a[48:51], v[192:195], a[164:167], 0\n"
    "v_perm_b32 v198, v150, v146, s68\n"
    "v_perm_b32 v199, v158, v154, s68\n"
    "v_mfma_f32_16x16x32_bf16 a[80:83], v[192:195], a[168:171], 0\n"
    "buffer_load_dwordx4 v[112:115], v208, s[36:39], s63 offen\n"
    "v_mfma_f32_16x16x32_bf16 a[112:115], v[192:195], a[172:175], 0\n"
    "ds_read_b128 a[192:195], v204 offset:6144\n"
    "v_mfma_f32_16x16x32_bf16 a[144:147], v[192:195], a[176:179], 0\n"
    "v_mfma_f32_16x16x32_bf16 a[20:23], v[196:199], a[160:163], 0\n"
    "v_perm_b32 v192, v135, v131, s67\n"
    "v_perm_b32 v193, v143, v139, s67\n"
    "v_mfma_f32_16x16x32_bf16 a[52:55], v[196:199], a[164:167], 0\n"
    "v_perm_b32 v194, v151, v147, s67\n"
    "v_perm_b32 v195, v159, v155, s67\n"
    "v_mfma_f32_16x16x32_bf16 a[84:87], v[196:199], a[168:171], 0\n"
    "buffer_load_dwordx4 v[116:119], v208, s[36:39], s64 offen\n"
    "v_mfma_f32_16x16x32_bf16 a[116:119], v[196:199], a[172:175], 0\n"
    "ds_read_b128 a[196:199], v204 offset:40960\n"
    "v_mfma_f32_16x16x32_bf16 a[148:151], v[196:199], a[176:179], 0\n"
    "v_mfma_f32_16x16x32_bf16 a[24:27], v[192:195], a[160:163], 0\n"
    "v_perm_b32 v196, v135, v131, s68\n"
    "v_perm_b32 v197, v143, v139, s68\n"
    "v_mfma_f32_16x16x32_bf16 a[56:59], v[192:195], a[164:167], 0\n"
    "v_perm_b32 v198, v151, v147, s68\n"
    "v_perm_b32 v199, v159, v155, s68\n"
    "v_mfma_f32_16x16x32_bf16 a[88:91], v[192:195], a[168:171], 0\n"
    "buffer_load_dwordx4 v[120:123], v208, s[36:39], s65 offen\n"
    "v_mfma_f32_16x16x32_bf16 a[120:123], v[192:195], a[172:175], 0\n"
    "v_mfma_f32_16x16x32_bf16 a[152:155], v[192:195], a[176:179], 0\n"
    "s_waitcnt vmcnt(15)\n"
    "v_mfma_f32_16x16x32_bf16 a[28:31], v[196:199], a[160:163], 0\n"
    "v_perm_b32 v192, v164, v160, s67\n"
    "v_perm_b32 v193, v172, v168, s67\n"
    "v_mfma_f32_16x16x32_bf16 a[60:63], v[196:199], a[164:167], 0\n"
    "v_perm_b32 v194, v180, v176, s67\n"
    "v_perm_b32 v195, v188, v184, s67\n"
    "v_mfma_f32_16x16x32_bf16 a[92:95], v[196:199], a[168:171], 0\n"
    "buffer_load_dwordx4 v[124:127], v208, s[36:39], s66 offen\n"
    "v_mfma_f32_16x16x32_bf16 a[124:127], v[196:199], a[172:175], 0\n"
    "v_add_u32 v201, s73, v200\n"
    "v_mfma_f32_16x16x32_bf16 a[156:159], v[196:199], a[176:179], 0\n"
    "s_waitcnt lgkmcnt(0)\n"
    "s_add_u32 s73, s73, 0x40000\n"
    "s_cmp_eq_u32 s73, 0x480000\n"
    "s_cselect_b32 s73, 0, s73\n"
    "v_mfma_f32_16x16x32_bf16 a[0:3], v[192:195], a[180:183], a[0:3]\n"
    "v_perm_b32 v196, v164, v160, s68\n"
    "v_perm_b32 v197, v172, v168, s68\n"
    "v_mfma_f32_16x16x32_bf16 a[32:35], v[192:195], a[184:187], a[32:35]\n"
    "v_perm_b32 v198, v180, v176, s68\n"
    "v_perm_b32 v199, v188, v184, s68\n"
    "v_mfma_f32_16x16x32_bf16 a[64:67], v[192:195], a[188:191], a[64:67]\n"
    "buffer_load_dwordx4 v[128:131], v201, s[36:39], 0 offen\n"
    "v_mfma_f32_16x16x32_bf16 a[96:99], v[192:195], a[192:195], a[96:99]\n"
    "s_add_u32 s28, s28, 0x180\n"
    "s_cmp_ge_u32 s28, s84\n"
    "s_cselect_b32 s76, 0x480, 0\n"
    "v_mfma_f32_16x16x32_bf16 a[128:131], v[192:195], a[196:199], a[128:131]\n"
    "s_add_u32 m0, s71, 0x8000\n"
    "s_sub_u32 s28, s28, s76\n"
    "v_mfma_f32_16x16x32_bf16 a[4:7], v[196:199], a[180:183], a[4:7]\n"
    "v_perm_b32 v192, v165, v161, s67\n"
    "v_perm_b32 v193, v173, v169, s67\n"
    "v_mfma_f32_16x16x32_bf16 a[36:39], v[196:199], a[184:187], a[36:39]\n"
    "v_perm_b32 v194, v181, v177, s67\n"
    "v_perm_b32 v195, v189, v185, s67\n"
    "v_mfma_f32_16x16x32_bf16 a[68:71], v[196:199], a[188:191], a[68:71]\n"
    "buffer_load_dwordx4 v[132:135], v201, s[36:39], s60 offen\n"
    "v_mfma_f32_16x16x32_bf16 a[100:103], v[196:199], a[192:195], a[100:103]\n"
    "ds_read_b128 a[160:163], v202 offset:43008\n"
    "buffer_load_dwordx4 v206, s[92:95], s28 offen lds\n"
    "v_mfma_f32_16x16x32_bf16 a[132:135], v[196:199], a[196:199], a[132:135]\n"
    "buffer_load_dwordx4 v207, s[92:95], s28 offen offset:1024 lds\n"
    "v_mfma_f32_16x16x32_bf16 a[8:11], v[192:195], a[180:183], a[8:11]\n"
    "v_perm_b32 v196, v165, v161, s68\n"
    "v_perm_b32 v197, v173, v169, s68\n"
    "v_mfma_f32_16x16x32_bf16 a[40:43], v[192:195], a[184:187], a[40:43]\n"
    "v_perm_b32 v198, v181, v177, s68\n"
    "v_perm_b32 v199, v189, v185, s68\n"
    "v_mfma_f32_16x16x32_bf16 a[72:75], v[192:195], a[188:191], a[72:75]\n"
    "s_add_u32 s29, s29, 0x180\n"
    "s_cmp_ge_u32 s29, s85\n"
    "s_cselect_b32 s76, 0x480, 0\n"
    "buffer_load_dwordx4 v[136:139], v201, s[36:39], s61 offen\n"
    "v_mfma_f32_16x16x32_bf16 a[104:107], v[192:195], a[192:195], a[104:107]\n"
    "s_add_u32 m0, s71, 0x8800\n"
    "s_sub_u32 s29, s29, s76\n"
    "ds_read_b128 a[164:167], v202 offset:45056\n"
    "v_mfma_f32_16x16x32_bf16 a[136:139], v[192:195], a[196:199], a[136:139]\n"
    "buffer_load_dwordx4 v206, s[92:95], s29 offen lds\n"
    "v_mfma_f32_16x16x32_bf16 a[12:15], v[196:199], a[180:183], a[12:15]\n"
    "v_perm_b32 v192, v166, v162, s67\n"
    "v_perm_b32 v193, v174, v170, s67\n"
    "v_mfma_f32_16x16x32_bf16 a[44:47], v[196:199], a[184:187], a[44:47]\n"
    "v_perm_b32 v194, v182, v178, s67\n"
    "v_perm_b32 v195, v190, v186, s67\n"
    "v_mfma_f32_16x16x32_bf16 a[76:79], v[196:199], a[188:191], a[76:79]\n"
    "buffer_load_dwordx4 v[140:143], v201, s[36:39], s62 offen\n"
    "v_mfma_f32_16x16x32_bf16 a[108:111], v[196:199], a[192:195], a[108:111]\n"
    "ds_read_b128 a[168:171], v202 offset:47104\n"
    "buffer_load_dwordx4 v207, s[92:95], s29 offen offset:1024 lds\n"
    "v_mfma_f32_16x16x32_bf16 a[140:143], v[196:199], a[196:199], a[140:143]\n"
    "v_mfma_f32_16x16x32_bf16 a[16:19], v[192:195], a[180:183], a[16:19]\n"
    "v_perm_b32 v196, v166, v162, s68\n"
    "v_perm_b32 v197, v174, v170, s68\n"
    "v_mfma_f32_16x16x32_bf16 a[48:51], v[192:195], a[184:187], a[48:51]\n"
    "v_perm_b32 v198, v182, v178, s68\n"
    "v_perm_b32 v199, v190, v186, s68\n"
    "v_mfma_f32_16x16x32_bf16 a[80:83], v[192:195], a[188:191], a[80:83]\n"
    "buffer_load_dwordx4 v[144:147], v201, s[36:39], s63 offen\n"
    "v_mfma_f32_16x16x32_bf16 a[112:115], v[192:195], a[192:195], a[112:115]\n"
    "ds_read_b128 a[172:175], v203 offset:0\n"
    "v_mfma_f32_16x16x32_bf16 a[144:147], v[192:195], a[196:199], a[144:147]\n"
    "v_mfma_f32_16x16x32_bf16 a[20:23], v[196:199], a[180:183], a[20:23]\n"
    "v_perm_b32 v192, v167, v163, s67\n"
    "v_perm_b32 v193, v175, v171, s67\n"
    "v_mfma_f32_16x16x32_bf16 a[52:55], v[196:199], a[184:187], a[52:55]\n"
    "v_perm_b32 v194, v183, v179, s67\n"
    "v_perm_b32 v195, v191, v187, s67\n"
    "v_mfma_f32_16x16x32_bf16 a[84:87], v[196:199], a[188:191], a[84:87]\n"
    "buffer_load_dwordx4 v[148:151], v201, s[36:39], s64 offen\n"
    "v_mfma_f32_16x16x32_bf16 a[116:119], v[196:199], a[192:195], a[116:119]\n"
    "ds_read_b128 a[176:179], v203 offset:2048\n"
    "v_mfma_f32_16x16x32_bf16 a[148:151], v[196:199], a[196:199], a[148:151]\n"
    "v_mfma_f32_16x16x32_bf16 a[24:27], v[192:195], a[180:183], a[24:27]\n"
    "v_perm_b32 v196, v167, v163, s68\n"
    "v_perm_b32 v197, v175, v171, s68\n"
    "v_mfma_f32_16x16x32_bf16 a[56:59], v[192:195], a[184:187], a[56:59]\n"
    "v_perm_b32 v198, v183, v179, s68\n"
    "v_perm_b32 v199, v191, v187, s68\n"
    "v_mfma_f32_16x16x32_bf16 a[88:91], v[192:195], a[188:191], a[88:91]\n"
    "buffer_load_dwordx4 v[152:155], v201, s[36:39], s65 offen\n"
    "v_mfma_f32_16x16x32_bf16 a[120:123], v[192:195], a[192:195], a[120:123]\n"
    "v_mfma_f32_16x16x32_bf16 a[152:155], v[192:195], a[196:199], a[152:155]\n"
    "s_waitcnt vmcnt(19)\n"
    "v_mfma_f32_16x16x32_bf16 a[28:31], v[196:199], a[180:183], a[28:31]\n"
    "v_perm_b32 v192, v68, v64, s67\n"
    "v_perm_b32 v193, v76, v72, s67\n"
    "v_mfma_f32_16x16x32_bf16 a[60:63], v[196:199], a[184:187], a[60:63]\n"
    "v_perm_b32 v194, v84, v80, s67\n"
    "v_perm_b32 v195, v92, v88, s67\n"
    "v_mfma_f32_16x16x32_bf16 a[92:95], v[196:199], a[188:191], a[92:95]\n"
    "buffer_load_dwordx4 v[156:159], v201, s[36:39], s66 offen\n"
    "v_mfma_f32_16x16x32_bf16 a[124:127], v[196:199], a[192:195], a[124:127]\n"
    "v_add_u32 v208, s73, v200\n"
    "v_mfma_f32_16x16x32_bf16 a[156:159], v[196:199], a[196:199], a[156:159]\n"
    "s_waitcnt lgkmcnt(0)\n"
    "s_add_u32 s73, s73, 0x40000\n"
    "s_cmp_eq_u32 s73, 0x480000\n"
    "s_cselect_b32 s73, 0, s73\n"
    "v_mfma_f32_16x16x32_bf16 a[0:3], v[192:195], a[160:163], a[0:3]\n"
    "v_perm_b32 v196, v68, v64, s68\n"
    "v_perm_b32 v197, v76, v72, s68\n"
    "v_mfma_f32_16x16x32_bf16 a[32:35], v[192:195], a[164:167], a[32:35]\n"
    "v_perm_b32 v198, v84, v80, s68\n"
    "v_perm_b32 v199, v92, v88, s68\n"
    "v_mfma_f32_16x16x32_bf16 a[64:67], v[192:195], a[168:171], a[64:67]\n"
    "buffer_load_dwordx4 v[160:163], v208, s[36:39], 0 offen\n"
    "v_mfma_f32_16x16x32_bf16 a[96:99], v[192:195], a[172:175], a[96:99]\n"
    "s_add_u32 s30, s30, 0x180\n"
    "s_cmp_ge_u32 s30, s87\n"
    "s_cselect_b32 s76, 0x480, 0\n"
    "v_mfma_f32_16x16x32_bf16 a[128:131], v[192:195], a[176:179], a[128:131]\n"
    "s_add_u32 m0, s71, 0x9000\n"
    "s_sub_u32 s30, s30, s76\n"
    "v_mfma_f32_16x16x32_bf16 a[4:7], v[196:199], a[160:163], a[4:7]\n"
    "v_perm_b32 v192, v69, v65, s67\n"
    "v_perm_b32 v193, v77, v73, s67\n"
    "v_mfma_f32_16x16x32_bf16 a[36:39], v[196:199], a[164:167], a[36:39]\n"
    "v_perm_b32 v194, v85, v81, s67\n"
    "v_perm_b32 v195, v93, v89, s67\n"
    "v_mfma_f32_16x16x32_bf16 a[68:71], v[196:199], a[168:171], a[68:71]\n"
    "buffer_load_dwordx4 v[164:167], v208, s[36:39], s60 offen\n"
    "v_mfma_f32_16x16x32_bf16 a[100:103], v[196:199], a[172:175], a[100:103]\n"
    "ds_read_b128 a[180:183], v204 offset:43008\n"
    "buffer_load_dwordx4 v206, s[92:95], s30 offen lds\n"
    "v_mfma_f32_16x16x32_bf16 a[132:135], v[196:199], a[176:179], a[132:135]\n"
    "buffer_load_dwordx4 v207, s[92:95], s30 offen offset:1024 lds\n"
    "v_mfma_f32_16x16x32_bf16 a[8:11], v[192:195], a[160:163], a[8:11]\n"
    "v_perm_b32 v196, v69, v65, s68\n"
    "v_perm_b32 v197, v77, v73, s68\n"
    "v_mfma_f32_16x16x32_bf16 a[40:43], v[192:195], a[164:167], a[40:43]\n"
    "v_perm_b32 v198, v85, v81, s68\n"
    "v_perm_b32 v199, v93, v89, s68\n"
    "v_mfma_f32_16x16x32_bf16 a[72:75], v[192:195], a[168:171], a[72:75]\n"
    "s_add_u32 s31, s31, 0x180\n"
    "s_cmp_ge_u32 s31, s88\n"
    "s_cselect_b32 s76, 0x480, 0\n"
    "buffer_load_dwordx4 v[168:171], v208, s[36:39], s61 offen\n"
    "v_mfma_f32_16x16x32_bf16 a[104:107], v[192:195], a[172:175], a[104:107]\n"
    "s_add_u32 m0, s71, 0x9800\n"
    "s_sub_u32 s31, s31, s76\n"
    "ds_read_b128 a[184:187], v204 offset:45056\n"
    "v_mfma_f32_16x16x32_bf16 a[136:139], v[192:195], a[176:179], a[136:139]\n"
    "buffer_load_dwordx4 v206, s[92:95], s31 offen lds\n"
    "v_mfma_f32_16x16x32_bf16 a[12:15], v[196:199], a[160:163], a[12:15]\n"
    "v_perm_b32 v192, v70, v66, s67\n"
    "v_perm_b32 v193, v78, v74, s67\n"
    "v_mfma_f32_16x16x32_bf16 a[44:47], v[196:199], a[164:167], a[44:47]\n"
    "v_perm_b32 v194, v86, v82, s67\n"
    "v_perm_b32 v195, v94, v90, s67\n"
    "v_mfma_f32_16x16x32_bf16 a[76:79], v[196:199], a[168:171], a[76:79]\n"
    "buffer_load_dwordx4 v[172:175], v208, s[36:39], s62 offen\n"
    "v_mfma_f32_16x16x32_bf16 a[108:111], v[196:199], a[172:175], a[108:111]\n"
    "ds_read_b128 a[188:191], v204 offset:47104\n"
    "buffer_load_dwordx4 v207, s[92:95], s31 offen offset:1024 lds\n"
    "v_mfma_f32_16x16x32_bf16 a[140:143], v[196:199], a[176:179], a[140:143]\n"
    "v_mfma_f32_16x16x32_bf16 a[16:19], v[192:195], a[160:163], a[16:19]\n"
    "v_perm_b32 v196, v70, v66, s68\n"
    "v_perm_b32 v197, v78, v74, s68\n"
    "B_TH_FIRST0:\n"
    "B_TH_S0:\n"
    "s_cbranch_execz B_TH_RET\n"
    "v_mfma_f32_16x16x32_bf16 a[48:51], v[192:195], a[164:167], a[48:51]\n"
    "v_perm_b32 v198, v86, v82, s68\n"
    "v_perm_b32 v199, v94, v90, s68\n"
    "v_mfma_f32_16x16x32_bf16 a[80:83], v[192:195], a[168:171], a[80:83]\n"
    "buffer_load_dwordx4 v[176:179], v208, s[36:39], s63 offen\n"
    "v_mfma_f32_16x16x32_bf16 a[112:115], v[192:195], a[172:175], a[112:115]\n"
    "ds_read_b128 a[192:195], v205 offset:0\n"
    "v_mfma_f32_16x16x32_bf16 a[144:147], v[192:195], a[176:179], a[144:147]\n"
    "v_mfma_f32_16x16x32_bf16 a[20:23], v[196:199], a[160:163], a[20:23]\n"
    "v_perm_b32 v192, v71, v67, s67\n"
    "v_perm_b32 v193, v79, v75, s67\n"
    "v_mfma_f32_16x16x32_bf16 a[52:55], v[196:199], a[164:167], a[52:55]\n"
    "v_perm_b32 v194, v87, v83, s67\n"
    "v_perm_b32 v195, v95, v91, s67\n"
    "v_mfma_f32_16x16x32_bf16 a[84:87], v[196:199], a[168:171], a[84:87]\n"
    "buffer_load_dwordx4 v[180:183], v208, s[36:39], s64 offen\n"
    "v_mfma_f32_16x16x32_bf16 a[116:119], v[196:199], a[172:175], a[116:119]\n"
    "ds_read_b128 a[196:199], v205 offset:2048\n"
    "v_mfma_f32_16x16x32_bf16 a[148:151], v[196:199], a[176:179], a[148:151]\n"
    "v_mfma_f32_16x16x32_bf16 a[24:27], v[192:195], a[160:163], a[24:27]\n"
    "v_perm_b32 v196, v71, v67, s68\n"
    "v_perm_b32 v197, v79, v75, s68\n"
    "v_mfma_f32_16x16x32_bf16 a[56:59], v[192:195], a[164:167], a[56:59]\n"
    "v_perm_b32 v198, v87, v83, s68\n"
    "v_perm_b32 v199, v95, v91, s68\n"
    "v_mfma_f32_16x16x32_bf16 a[88:91], v[192:195], a[168:171], a[88:91]\n"
    "buffer_load_dwordx4 v[184:187], v208, s[36:39], s65 offen\n"
    "v_mfma_f32_16x16x32_bf16 a[120:123], v[192:195], a[172:175], a[120:123]\n"
    "v_mfma_f32_16x16x32_bf16 a[152:155], v[192:195], a[176:179], a[152:155]\n"
    "s_waitcnt vmcnt(23)\n"
    "v_mfma_f32_16x16x32_bf16 a[28:31], v[196:199], a[160:163], a[28:31]\n"
    "v_perm_b32 v192, v100, v96, s67\n"
    "v_perm_b32 v193, v108, v104, s67\n"
    "v_mfma_f32_16x16x32_bf16 a[60:63], v[196:199], a[164:167], a[60:63]\n"
    "v_perm_b32 v194, v116, v112, s67\n"
    "v_perm_b32 v195, v124, v120, s67\n"
    "v_mfma_f32_16x16x32_bf16 a[92:95], v[196:199], a[168:171], a[92:95]\n"
    "buffer_load_dwordx4 v[188:191], v208, s[36:39], s66 offen\n"
    "v_mfma_f32_16x16x32_bf16 a[124:127], v[196:199], a[172:175], a[124:127]\n"
    "v_add_u32 v201, s73, v200\n"
    "v_mfma_f32_16x16x32_bf16 a[156:159], v[196:199], a[176:179], a[156:159]\n"
    "s_waitcnt lgkmcnt(0)\n"
    "s_add_u32 s73, s73, 0x40000\n"
    "s_cmp_eq_u32 s73, 0x480000\n"
    "s_cselect_b32 s73, 0, s73\n"
    "v_mfma_f32_16x16x32_bf16 a[0:3], v[192:195], a[180:183], a[0:3]\n"
    "v_perm_b32 v196, v100, v96, s68\n"
    "v_perm_b32 v197, v108, v104, s68\n"
    "v_mfma_f32_16x16x32_bf16 a[32:35], v[192:195], a[184:187], a[32:35]\n"
    "v_perm_b32 v198, v116, v112, s68\n"
    "v_perm_b32 v199, v124, v120, s68\n"
    "v_mfma_f32_16x16x32_bf16 a[64:67], v[192:195], a[188:191], a[64:67]\n"
    "buffer_load_dwordx4 v[64:67], v201, s[36:39], 0 offen\n"
    "v_mfma_f32_16x16x32_bf16 a[96:99], v[192:195], a[192:195], a[96:99]\n"
    "s_add_u32 s28, s28, 0x180\n"
    "s_cmp_ge_u32 s28, s84\n"
    "s_cselect_b32 s76, 0x480, 0\n"
    "v_mfma_f32_16x16x32_bf16 a[128:131], v[192:195], a[196:199], a[128:131]\n"
    "s_add_u32 m0, s71, 0x4000\n"
    "s_sub_u32 s28, s28, s76\n"
    "v_mfma_f32_16x16x32_bf16 a[4:7], v[196:199], a[180:183], a[4:7]\n"
    "v_perm_b32 v192, v101, v97, s67\n"
    "v_perm_b32 v193, v109, v105, s67\n"
    "v_mfma_f32_16x16x32_bf16 a[36:39], v[196:199], a[184:187], a[36:39]\n"
    "v_perm_b32 v194, v117, v113, s67\n"
    "v_perm_b32 v195, v125, v121, s67\n"
    "v_mfma_f32_16x16x32_bf16 a[68:71], v[196:199], a[188:191], a[68:71]\n"
    "buffer_load_dwordx4 v[68:71], v201, s[36:39], s60 offen\n"
    "v_mfma_f32_16x16x32_bf16 a[100:103], v[196:199], a[192:195], a[100:103]\n"
    "ds_read_b128 a[160:163], v203 offset:4096\n"
    "buffer_load_dwordx4 v206, s[92:95], s28 offen lds\n"
    "v_mfma_f32_16x16x32_bf16 a[132:135], v[196:199], a[196:199], a[132:135]\n"
    "buffer_load_dwordx4 v207, s[92:95], s28 offen offset:1024 lds\n"
    "v_mfma_f32_16x16x32_bf16 a[8:11], v[192:195], a[180:183], a[8:11]\n"
    "v_perm_b32 v196, v101, v97, s68\n"
    "v_perm_b32 v197, v109, v105, s68\n"
    "v_mfma_f32_16x16x32_bf16 a[40:43], v[192:195], a[184:187], a[40:43]\n"
    "v_perm_b32 v198, v117, v113, s68\n"
    "v_perm_b32 v199, v125, v121, s68\n"
    "v_mfma_f32_16x16x32_bf16 a[72:75], v[192:195], a[188:191], a[72:75]\n"
    "s_add_u32 s29, s29, 0x180\n"
    "s_cmp_ge_u32 s29, s85\n"
    "s_cselect_b32 s76, 0x480, 0\n"
    "buffer_load_dwordx4 v[72:75], v201, s[36:39], s61 offen\n"
    "v_mfma_f32_16x16x32_bf16 a[104:107], v[192:195], a[192:195], a[104:107]\n"
    "s_add_u32 m0, s71, 0x4800\n"
    "s_sub_u32 s29, s29, s76\n"
    "ds_read_b128 a[164:167], v203 offset:6144\n"
    "v_mfma_f32_16x16x32_bf16 a[136:139], v[192:195], a[196:199], a[136:139]\n"
    "buffer_load_dwordx4 v206, s[92:95], s29 offen lds\n"
    "v_mfma_f32_16x16x32_bf16 a[12:15], v[196:199], a[180:183], a[12:15]\n"
    "v_perm_b32 v192, v102, v98, s67\n"
    "v_perm_b32 v193, v110, v106, s67\n"
    "v_mfma_f32_16x16x32_bf16 a[44:47], v[196:199], a[184:187], a[44:47]\n"
    "v_perm_b32 v194, v118, v114, s67\n"
    "v_perm_b32 v195, v126, v122, s67\n"
    "v_mfma_f32_16x16x32_bf16 a[76:79], v[196:199], a[188:191], a[76:79]\n"
    "buffer_load_dwordx4 v[76:79], v201, s[36:39], s62 offen\n"
    "v_mfma_f32_16x16x32_bf16 a[108:111], v[196:199], a[192:195], a[108:111]\n"
    "ds_read_b128 a[168:171], v203 offset:40960\n"
    "buffer_load_dwordx4 v207, s[92:95], s29 offen offset:1024 lds\n"
    "v_mfma_f32_16x16x32_bf16 a[140:143], v[196:199], a[196:199], a[140:143]\n"
    "v_mfma_f32_16x16x32_bf16 a[16:19], v[192:195], a[180:183], a[16:19]\n"
    "v_perm_b32 v196, v102, v98, s68\n"
    "v_perm_b32 v197, v110, v106, s68\n"
    "v_mfma_f32_16x16x32_bf16 a[48:51], v[192:195], a[184:187], a[48:51]\n"
    "v_perm_b32 v198, v118, v114, s68\n"
    "v_perm_b32 v199, v126, v122, s68\n"
    "v_mfma_f32_16x16x32_bf16 a[80:83], v[192:195], a[188:191], a[80:83]\n"
    "buffer_load_dwordx4 v[80:83], v201, s[36:39], s63 offen\n"
    "v_mfma_f32_16x16x32_bf16 a[112:115], v[192:195], a[192:195], a[112:115]\n"
    "ds_read_b128 a[172:175], v203 offset:43008\n"
    "v_mfma_f32_16x16x32_bf16 a[144:147], v[192:195], a[196:199], a[144:147]\n"
    "v_mfma_f32_16x16x32_bf16 a[20:23], v[196:199], a[180:183], a[20:23]\n"
    "v_perm_b32 v192, v103, v99, s67\n"
    "v_perm_b32 v193, v111, v107, s67\n"
    "v_mfma_f32_16x16x32_bf16 a[52:55], v[196:199], a[184:187], a[52:55]\n"
    "v_perm_b32 v194, v119, v115, s67\n"
    "v_perm_b32 v195, v127, v123, s67\n"
    "v_mfma_f32_16x16x32_bf16 a[84:87], v[196:199], a[188:191], a[84:87]\n"
    "buffer_load_dwordx4 v[84:87], v201, s[36:39], s64 offen\n"
    "v_mfma_f32_16x16x32_bf16 a[116:119], v[196:199], a[192:195], a[116:119]\n"
    "ds_read_b128 a[176:179], v203 offset:45056\n"
    "v_mfma_f32_16x16x32_bf16 a[148:151], v[196:199], a[196:199], a[148:151]\n"
    "v_mfma_f32_16x16x32_bf16 a[24:27], v[192:195], a[180:183], a[24:27]\n"
    "v_perm_b32 v196, v103, v99, s68\n"
    "v_perm_b32 v197, v111, v107, s68\n"
    "v_mfma_f32_16x16x32_bf16 a[56:59], v[192:195], a[184:187], a[56:59]\n"
    "v_perm_b32 v198, v119, v115, s68\n"
    "v_perm_b32 v199, v127, v123, s68\n"
    "v_mfma_f32_16x16x32_bf16 a[88:91], v[192:195], a[188:191], a[88:91]\n"
    "buffer_load_dwordx4 v[88:91], v201, s[36:39], s65 offen\n"
    "v_mfma_f32_16x16x32_bf16 a[120:123], v[192:195], a[192:195], a[120:123]\n"
    "v_mfma_f32_16x16x32_bf16 a[152:155], v[192:195], a[196:199], a[152:155]\n"
    "s_waitcnt vmcnt(23)\n"
    "v_mfma_f32_16x16x32_bf16 a[28:31], v[196:199], a[180:183], a[28:31]\n"
    "v_perm_b32 v192, v132, v128, s67\n"
    "v_perm_b32 v193, v140, v136, s67\n"
    "v_mfma_f32_16x16x32_bf16 a[60:63], v[196:199], a[184:187], a[60:63]\n"
    "v_perm_b32 v194, v148, v144, s67\n"
    "v_perm_b32 v195, v156, v152, s67\n"
    "v_mfma_f32_16x16x32_bf16 a[92:95], v[196:199], a[188:191], a[92:95]\n"
    "buffer_load_dwordx4 v[92:95], v201, s[36:39], s66 offen\n"
    "v_mfma_f32_16x16x32_bf16 a[124:127], v[196:199], a[192:195], a[124:127]\n"
    "v_add_u32 v208, s73, v200\n"
    "v_mfma_f32_16x16x32_bf16 a[156:159], v[196:199], a[196:199], a[156:159]\n"
    "s_waitcnt lgkmcnt(0)\n"
    "s_add_u32 s73, s73, 0x40000\n"
    "s_cmp_eq_u32 s73, 0x480000\n"
    "s_cselect_b32 s73, 0, s73\n"
    "v_mfma_f32_16x16x32_bf16 a[0:3], v[192:195], a[160:163], a[0:3]\n"
    "v_perm_b32 v196, v132, v128, s68\n"
    "v_perm_b32 v197, v140, v136, s68\n"
    "v_mfma_f32_16x16x32_bf16 a[32:35], v[192:195], a[164:167], a[32:35]\n"
    "v_perm_b32 v198, v148, v144, s68\n"
    "v_perm_b32 v199, v156, v152, s68\n"
    "v_mfma_f32_16x16x32_bf16 a[64:67], v[192:195], a[168:171], a[64:67]\n"
    "buffer_load_dwordx4 v[96:99], v208, s[36:39], 0 offen\n"
    "v_mfma_f32_16x16x32_bf16 a[96:99], v[192:195], a[172:175], a[96:99]\n"
    "s_add_u32 s30, s30, 0x180\n"
    "s_cmp_ge_u32 s30, s87\n"
    "s_cselect_b32 s76, 0x480, 0\n"
    "v_mfma_f32_16x16x32_bf16 a[128:131], v[192:195], a[176:179], a[128:131]\n"
    "s_add_u32 m0, s71, 0x5000\n"
    "s_sub_u32 s30, s30, s76\n"
    "v_mfma_f32_16x16x32_bf16 a[4:7], v[196:199], a[160:163], a[4:7]\n"
    "v_perm_b32 v192, v133, v129, s67\n"
    "v_perm_b32 v193, v141, v137, s67\n"
    "v_mfma_f32_16x16x32_bf16 a[36:39], v[196:199], a[164:167], a[36:39]\n"
    "v_perm_b32 v194, v149, v145, s67\n"
    "v_perm_b32 v195, v157, v153, s67\n"
    "v_mfma_f32_16x16x32_bf16 a[68:71], v[196:199], a[168:171], a[68:71]\n"
    "buffer_load_dwordx4 v[100:103], v208, s[36:39], s60 offen\n"
    "v_mfma_f32_16x16x32_bf16 a[100:103], v[196:199], a[172:175], a[100:103]\n"
    "ds_read_b128 a[180:183], v205 offset:4096\n"
    "buffer_load_dwordx4 v206, s[92:95], s30 offen lds\n"
    "v_mfma_f32_16x16x32_bf16 a[132:135], v[196:199], a[176:179], a[132:135]\n"
    "buffer_load_dwordx4 v207, s[92:95], s30 offen offset:1024 lds\n"
    "v_mfma_f32_16x16x32_bf16 a[8:11], v[192:195], a[160:163], a[8:11]\n"
    "v_perm_b32 v196, v133, v129, s68\n"
    "v_perm_b32 v197, v141, v137, s68\n"
    "v_mfma_f32_16x16x32_bf16 a[40:43], v[192:195], a[164:167], a[40:43]\n"
    "v_perm_b32 v198, v149, v145, s68\n"
    "v_perm_b32 v199, v157, v153, s68\n"
    "v_mfma_f32_16x16x32_bf16 a[72:75], v[192:195], a[168:171], a[72:75]\n"
    "s_add_u32 s31, s31, 0x180\n"
    "s_cmp_ge_u32 s31, s88\n"
    "s_cselect_b32 s76, 0x480, 0\n"
    "buffer_load_dwordx4 v[104:107], v208, s[36:39], s61 offen\n"
    "v_mfma_f32_16x16x32_bf16 a[104:107], v[192:195], a[172:175], a[104:107]\n"
    "s_add_u32 m0, s71, 0x5800\n"
    "s_sub_u32 s31, s31, s76\n"
    "ds_read_b128 a[184:187], v205 offset:6144\n"
    "v_mfma_f32_16x16x32_bf16 a[136:139], v[192:195], a[176:179], a[136:139]\n"
    "buffer_load_dwordx4 v206, s[92:95], s31 offen lds\n"
    "v_mfma_f32_16x16x32_bf16 a[12:15], v[196:199], a[160:163], a[12:15]\n"
    "v_perm_b32 v192, v134, v130, s67\n"
    "v_perm_b32 v193, v142, v138, s67\n"
    "v_mfma_f32_16x16x32_bf16 a[44:47], v[196:199], a[164:167], a[44:47]\n"
    "v_perm_b32 v194, v150, v146, s67\n"
    "v_perm_b32 v195, v158, v154, s67\n"
    "v_mfma_f32_16x16x32_bf16 a[76:79], v[196:199], a[168:171], a[76:79]\n"
    "buffer_load_dwordx4 v[108:111], v208, s[36:39], s62 offen\n"
    "v_mfma_f32_16x16x32_bf16 a[108:111], v[196:199], a[172:175], a[108:111]\n"
    "ds_read_b128 a[188:191], v205 offset:40960\n"
    "buffer_load_dwordx4 v207, s[92:95], s31 offen offset:1024 lds\n"
    "v_mfma_f32_16x16x32_bf16 a[140:143], v[196:199], a[176:179], a[140:143]\n"
    "v_mfma_f32_16x16x32_bf16 a[16:19], v[192:195], a[160:163], a[16:19]\n"
    "v_perm_b32 v196, v134, v130, s68\n"
    "v_perm_b32 v197, v142, v138, s68\n"
    "v_mfma_f32_16x16x32_bf16 a[48:51], v[192:195], a[164:167], a[48:51]\n"
    "v_perm_b32 v198, v150, v146, s68\n"
    "v_perm_b32 v199, v158, v154, s68\n"
    "v_mfma_f32_16x16x32_bf16 a[80:83], v[192:195], a[168:171], a[80:83]\n"
    "buffer_load_dwordx4 v[112:115], v208, s[36:39], s63 offen\n"
    "v_mfma_f32_16x16x32_bf16 a[112:115], v[192:195], a[172:175], a[112:115]\n"
    "ds_read_b128 a[192:195], v205 offset:43008\n"
    "v_mfma_f32_16x16x32_bf16 a[144:147], v[192:195], a[176:179], a[144:147]\n"
    "v_mfma_f32_16x16x32_bf16 a[20:23], v[196:199], a[160:163], a[20:23]\n"
    "v_perm_b32 v192, v135, v131, s67\n"
    "v_perm_b32 v193, v143, v139, s67\n"
    "v_mfma_f32_16x16x32_bf16 a[52:55], v[196:199], a[164:167], a[52:55]\n"
    "v_perm_b32 v194, v151, v147, s67\n"
    "v_perm_b32 v195, v159, v155, s67\n"
    "v_mfma_f32_16x16x32_bf16 a[84:87], v[196:199], a[168:171], a[84:87]\n"
    "buffer_load_dwordx4 v[116:119], v208, s[36:39], s64 offen\n"
    "v_mfma_f32_16x16x32_bf16 a[116:119], v[196:199], a[172:175], a[116:119]\n"
    "ds_read_b128 a[196:199], v205 offset:45056\n"
    "v_mfma_f32_16x16x32_bf16 a[148:151], v[196:199], a[176:179], a[148:151]\n"
    "v_mfma_f32_16x16x32_bf16 a[24:27], v[192:195], a[160:163], a[24:27]\n"
    "v_perm_b32 v196, v135, v131, s68\n"
    "v_perm_b32 v197, v143, v139, s68\n"
    "v_mfma_f32_16x16x32_bf16 a[56:59], v[192:195], a[164:167], a[56:59]\n"
    "v_perm_b32 v198, v151, v147, s68\n"
    "v_perm_b32 v199, v159, v155, s68\n"
    "v_mfma_f32_16x16x32_bf16 a[88:91], v[192:195], a[168:171], a[88:91]\n"
    "buffer_load_dwordx4 v[120:123], v208, s[36:39], s65 offen\n"
    "v_mfma_f32_16x16x32_bf16 a[120:123], v[192:195], a[172:175], a[120:123]\n"
    "v_mfma_f32_16x16x32_bf16 a[152:155], v[192:195], a[176:179], a[152:155]\n"
    "s_waitcnt vmcnt(23)\n"
    "v_mfma_f32_16x16x32_bf16 a[28:31], v[196:199], a[160:163], a[28:31]\n"
    "v_perm_b32 v192, v164, v160, s67\n"
    "v_perm_b32 v193, v172, v168, s67\n"
    "v_mfma_f32_16x16x32_bf16 a[60:63], v[196:199], a[164:167], a[60:63]\n"
    "v_perm_b32 v194, v180, v176, s67\n"
    "v_perm_b32 v195, v188, v184, s67\n"
    "v_mfma_f32_16x16x32_bf16 a[92:95], v[196:199], a[168:171], a[92:95]\n"
    "buffer_load_dwordx4 v[124:127], v208, s[36:39], s66 offen\n"
    "v_mfma_f32_16x16x32_bf16 a[124:127], v[196:199], a[172:175], a[124:127]\n"
    "v_add_u32 v201, s73, v200\n"
    "v_mfma_f32_16x16x32_bf16 a[156:159], v[196:199], a[176:179], a[156:159]\n"
    "s_waitcnt lgkmcnt(0)\n"
    "s_add_u32 s73, s73, 0x40000\n"
    "s_cmp_eq_u32 s73, 0x480000\n"
    "s_cselect_b32 s73, 0, s73\n"
    "v_mfma_f32_16x16x32_bf16 a[0:3], v[192:195], a[180:183], a[0:3]\n"
    "v_perm_b32 v196, v164, v160, s68\n"
    "v_perm_b32 v197, v172, v168, s68\n"
    "v_mfma_f32_16x16x32_bf16 a[32:35], v[192:195], a[184:187], a[32:35]\n"
    "v_perm_b32 v198, v180, v176, s68\n"
    "v_perm_b32 v199, v188, v184, s68\n"
    "v_mfma_f32_16x16x32_bf16 a[64:67], v[192:195], a[188:191], a[64:67]\n"
    "buffer_load_dwordx4 v[128:131], v201, s[36:39], 0 offen\n"
    "s_waitcnt vmcnt(29)\n"
    "s_barrier\n"
    "v_mfma_f32_16x16x32_bf16 a[96:99], v[192:195], a[192:195], a[96:99]\n"
    "v_add_u32 v202, 0x2000, v202\n"
    "v_add_u32 v203, 0x2000, v203\n"
    "v_mfma_f32_16x16x32_bf16 a[128:131], v[192:195], a[196:199], a[128:131]\n"
    "v_add_u32 v204, 0x2000, v204\n"
    "v_add_u32 v205, 0x2000, v205\n"
    "v_mfma_f32_16x16x32_bf16 a[4:7], v[196:199], a[180:183], a[4:7]\n"
    "v_perm_b32 v192, v165, v161, s67\n"
    "v_perm_b32 v193, v173, v169, s67\n"
    "v_mfma_f32_16x16x32_bf16 a[36:39], v[196:199], a[184:187], a[36:39]\n"
    "v_perm_b32 v194, v181, v177, s67\n"
    "v_perm_b32 v195, v189, v185, s67\n"
    "v_mfma_f32_16x16x32_bf16 a[68:71], v[196:199], a[188:191], a[68:71]\n"
    "buffer_load_dwordx4 v[132:135], v201, s[36:39], s60 offen\n"
    "v_mfma_f32_16x16x32_bf16 a[100:103], v[196:199], a[192:195], a[100:103]\n"
    "ds_read_b128 a[160:163], v202 offset:0\n"
    "v_mfma_f32_16x16x32_bf16 a[132:135], v[196:199], a[196:199], a[132:135]\n"
    "v_mfma_f32_16x16x32_bf16 a[8:11], v[192:195], a[180:183], a[8:11]\n"
    "v_perm_b32 v196, v165, v161, s68\n"
    "v_perm_b32 v197, v173, v169, s68\n"
    "v_mfma_f32_16x16x32_bf16 a[40:43], v[192:195], a[184:187], a[40:43]\n"
    "v_perm_b32 v198, v181, v177, s68\n"
    "v_perm_b32 v199, v189, v185, s68\n"
    "v_mfma_f32_16x16x32_bf16 a[72:75], v[192:195], a[188:191], a[72:75]\n"
    "buffer_load_dwordx4 v[136:139], v201, s[36:39], s61 offen\n"
    "v_mfma_f32_16x16x32_bf16 a[104:107], v[192:195], a[192:195], a[104:107]\n"
    "ds_read_b128 a[164:167], v202 offset:2048\n"
    "v_mfma_f32_16x16x32_bf16 a[136:139], v[192:195], a[196:199], a[136:139]\n"
    "v_mfma_f32_16x16x32_bf16 a[12:15], v[196:199], a[180:183], a[12:15]\n"
    "v_perm_b32 v192, v166, v162, s67\n"
    "v_perm_b32 v193, v174, v170, s67\n"
    "v_mfma_f32_16x16x32_bf16 a[44:47], v[196:199], a[184:187], a[44:47]\n"
    "v_perm_b32 v194, v182, v178, s67\n"
    "v_perm_b32 v195, v190, v186, s67\n"
    "v_mfma_f32_16x16x32_bf16 a[76:79], v[196:199], a[188:191], a[76:79]\n"
    "buffer_load_dwordx4 v[140:143], v201, s[36:39], s62 offen\n"
    "v_mfma_f32_16x16x32_bf16 a[108:111], v[196:199], a[192:195], a[108:111]\n"
    "ds_read_b128 a[168:171], v202 offset:4096\n"
    "v_mfma_f32_16x16x32_bf16 a[140:143], v[196:199], a[196:199], a[140:143]\n"
    "v_mfma_f32_16x16x32_bf16 a[16:19], v[192:195], a[180:183], a[16:19]\n"
    "v_perm_b32 v196, v166, v162, s68\n"
    "v_perm_b32 v197, v174, v170, s68\n"
    "v_mfma_f32_16x16x32_bf16 a[48:51], v[192:195], a[184:187], a[48:51]\n"
    "v_perm_b32 v198, v182, v178, s68\n"
    "v_perm_b32 v199, v190, v186, s68\n"
    "v_mfma_f32_16x16x32_bf16 a[80:83], v[192:195], a[188:191], a[80:83]\n"
    "buffer_load_dwordx4 v[144:147], v201, s[36:39], s63 offen\n"
    "v_mfma_f32_16x16x32_bf16 a[112:115], v[192:195], a[192:195], a[112:115]\n"
    "ds_read_b128 a[172:175], v202 offset:6144\n"
    "v_mfma_f32_16x16x32_bf16 a[144:147], v[192:195], a[196:199], a[144:147]\n"
    "v_mfma_f32_16x16x32_bf16 a[20:23], v[196:199], a[180:183], a[20:23]\n"
    "v_perm_b32 v192, v167, v163, s67\n"
    "v_perm_b32 v193, v175, v171, s67\n"
    "v_mfma_f32_16x16x32_bf16 a[52:55], v[196:199], a[184:187], a[52:55]\n"
    "v_perm_b32 v194, v183, v179, s67\n"
    "v_perm_b32 v195, v191, v187, s67\n"
    "v_mfma_f32_16x16x32_bf16 a[84:87], v[196:199], a[188:191], a[84:87]\n"
    "buffer_load_dwordx4 v[148:151], v201, s[36:39], s64 offen\n"
    "v_mfma_f32_16x16x32_bf16 a[116:119], v[196:199], a[192:195], a[116:119]\n"
    "ds_read_b128 a[176:179], v202 offset:40960\n"
    "v_mfma_f32_16x16x32_bf16 a[148:151], v[196:199], a[196:199], a[148:151]\n"
    "v_mfma_f32_16x16x32_bf16 a[24:27], v[192:195], a[180:183], a[24:27]\n"
    "v_perm_b32 v196, v167, v163, s68\n"
    "v_perm_b32 v197, v175, v171, s68\n"
    "v_mfma_f32_16x16x32_bf16 a[56:59], v[192:195], a[184:187], a[56:59]\n"
    "v_perm_b32 v198, v183, v179, s68\n"
    "v_perm_b32 v199, v191, v187, s68\n"
    "v_mfma_f32_16x16x32_bf16 a[88:91], v[192:195], a[188:191], a[88:91]\n"
    "buffer_load_dwordx4 v[152:155], v201, s[36:39], s65 offen\n"
    "v_mfma_f32_16x16x32_bf16 a[120:123], v[192:195], a[192:195], a[120:123]\n"
    "v_mfma_f32_16x16x32_bf16 a[152:155], v[192:195], a[196:199], a[152:155]\n"
    "s_waitcnt vmcnt(19)\n"
    "v_mfma_f32_16x16x32_bf16 a[28:31], v[196:199], a[180:183], a[28:31]\n"
    "v_perm_b32 v192, v68, v64, s67\n"
    "v_perm_b32 v193, v76, v72, s67\n"
    "v_mfma_f32_16x16x32_bf16 a[60:63], v[196:199], a[184:187], a[60:63]\n"
    "v_perm_b32 v194, v84, v80, s67\n"
    "v_perm_b32 v195, v92, v88, s67\n"
    "v_mfma_f32_16x16x32_bf16 a[92:95], v[196:199], a[188:191], a[92:95]\n"
    "buffer_load_dwordx4 v[156:159], v201, s[36:39], s66 offen\n"
    "v_mfma_f32_16x16x32_bf16 a[124:127], v[196:199], a[192:195], a[124:127]\n"
    "v_add_u32 v208, s73, v200\n"
    "v_mfma_f32_16x16x32_bf16 a[156:159], v[196:199], a[196:199], a[156:159]\n"
    "s_waitcnt lgkmcnt(0)\n"
    "s_add_u32 s73, s73, 0x40000\n"
    "s_cmp_eq_u32 s73, 0x480000\n"
    "s_cselect_b32 s73, 0, s73\n"
    "v_mfma_f32_16x16x32_bf16 a[0:3], v[192:195], a[160:163], a[0:3]\n"
    "v_perm_b32 v196, v68, v64, s68\n"
    "v_perm_b32 v197, v76, v72, s68\n"
    "v_mfma_f32_16x16x32_bf16 a[32:35], v[192:195], a[164:167], a[32:35]\n"
    "v_perm_b32 v198, v84, v80, s68\n"
    "v_perm_b32 v199, v92, v88, s68\n"
    "v_mfma_f32_16x16x32_bf16 a[64:67], v[192:195], a[168:171], a[64:67]\n"
    "buffer_load_dwordx4 v[160:163], v208, s[36:39], 0 offen\n"
    "v_mov_b32 v9, 0\n"
    "v_mfma_f32_16x16x32_bf16 a[96:99], v[192:195], a[172:175], a[96:99]\n"
    "v_mov_b32 v0, 0\n"
    "v_mov_b32 v10, 0\n"
    "v_mfma_f32_16x16x32_bf16 a[128:131], v[192:195], a[176:179], a[128:131]\n"
    "v_mov_b32 v1, 0\n"
    "v_mov_b32 v11, 0\n"
    "v_mfma_f32_16x16x32_bf16 a[4:7], v[196:199], a[160:163], a[4:7]\n"
    "v_perm_b32 v192, v69, v65, s67\n"
    "v_perm_b32 v193, v77, v73, s67\n"
    "v_mfma_f32_16x16x32_bf16 a[36:39], v[196:199], a[164:167], a[36:39]\n"
    "v_perm_b32 v194, v85, v81, s67\n"
    "v_perm_b32 v195, v93, v89, s67\n"
    "v_mfma_f32_16x16x32_bf16 a[68:71], v[196:199], a[168:171], a[68:71]\n"
    "buffer_load_dwordx4 v[164:167], v208, s[36:39], s60 offen\n"
    "v_mov_b32 v12, 0\n"
    "v_mfma_f32_16x16x32_bf16 a[100:103], v[196:199], a[172:175], a[100:103]\n"
    "ds_read_b128 a[180:183], v204 offset:0\n"
    "v_mov_b32 v13, 0\n"
    "v_mfma_f32_16x16x32_bf16 a[132:135], v[196:199], a[176:179], a[132:135]\n"
    "v_mov_b32 v2, 0\n"
    "v_mov_b32 v14, 0\n"
    "v_mfma_f32_16x16x32_bf16 a[8:11], v[192:195], a[160:163], a[8:11]\n"
    "v_perm_b32 v196, v69, v65, s68\n"
    "v_perm_b32 v197, v77, v73, s68\n"
    "v_mfma_f32_16x16x32_bf16 a[40:43], v[192:195], a[164:167], a[40:43]\n"
    "v_perm_b32 v198, v85, v81, s68\n"
    "v_perm_b32 v199, v93, v89, s68\n"
    "v_mfma_f32_16x16x32_bf16 a[72:75], v[192:195], a[168:171], a[72:75]\n"
    "buffer_load_dwordx4 v[168:171], v208, s[36:39], s61 offen\n"
    "v_mov_b32 v15, 0\n"
    "v_mfma_f32_16x16x32_bf16 a[104:107], v[192:195], a[172:175], a[104:107]\n"
    "ds_read_b128 a[184:187], v204 offset:2048\n"
    "v_mfma_f32_16x16x32_bf16 a[136:139], v[192:195], a[176:179], a[136:139]\n"
    "v_mov_b32 v3, 0\n"
    "v_mfma_f32_16x16x32_bf16 a[12:15], v[196:199], a[160:163], a[12:15]\n"
    "v_perm_b32 v192, v70, v66, s67\n"
    "v_perm_b32 v193, v78, v74, s67\n"
    "v_mfma_f32_16x16x32_bf16 a[44:47], v[196:199], a[164:167], a[44:47]\n"
    "v_perm_b32 v194, v86, v82, s67\n"
    "v_perm_b32 v195, v94, v90, s67\n"
    "v_mfma_f32_16x16x32_bf16 a[76:79], v[196:199], a[168:171], a[76:79]\n"
    "buffer_load_dwordx4 v[172:175], v208, s[36:39], s62 offen\n"
    "v_mfma_f32_16x16x32_bf16 a[108:111], v[196:199], a[172:175], a[108:111]\n"
    "ds_read_b128 a[188:191], v204 offset:4096\n"
    "v_mfma_f32_16x16x32_bf16 a[140:143], v[196:199], a[176:179], a[140:143]\n"
    "v_mov_b32 v4, 0\n"
    "v_mfma_f32_16x16x32_bf16 a[16:19], v[192:195], a[160:163], a[16:19]\n"
    "v_perm_b32 v196, v70, v66, s68\n"
    "v_perm_b32 v197, v78, v74, s68\n"
    "v_mfma_f32_16x16x32_bf16 a[48:51], v[192:195], a[164:167], a[48:51]\n"
    "v_perm_b32 v198, v86, v82, s68\n"
    "v_perm_b32 v199, v94, v90, s68\n"
    "v_mfma_f32_16x16x32_bf16 a[80:83], v[192:195], a[168:171], a[80:83]\n"
    "buffer_load_dwordx4 v[176:179], v208, s[36:39], s63 offen\n"
    "v_mfma_f32_16x16x32_bf16 a[112:115], v[192:195], a[172:175], a[112:115]\n"
    "ds_read_b128 a[192:195], v204 offset:6144\n"
    "v_mfma_f32_16x16x32_bf16 a[144:147], v[192:195], a[176:179], a[144:147]\n"
    "v_mov_b32 v5, 0\n"
    "v_mfma_f32_16x16x32_bf16 a[20:23], v[196:199], a[160:163], a[20:23]\n"
    "v_perm_b32 v192, v71, v67, s67\n"
    "v_perm_b32 v193, v79, v75, s67\n"
    "v_mfma_f32_16x16x32_bf16 a[52:55], v[196:199], a[164:167], a[52:55]\n"
    "v_perm_b32 v194, v87, v83, s67\n"
    "v_perm_b32 v195, v95, v91, s67\n"
    "v_mfma_f32_16x16x32_bf16 a[84:87], v[196:199], a[168:171], a[84:87]\n"
    "buffer_load_dwordx4 v[180:183], v208, s[36:39], s64 offen\n"
    "v_mfma_f32_16x16x32_bf16 a[116:119], v[196:199], a[172:175], a[116:119]\n"
    "ds_read_b128 a[196:199], v204 offset:40960\n"
    "v_mfma_f32_16x16x32_bf16 a[148:151], v[196:199], a[176:179], a[148:151]\n"
    "v_mov_b32 v6, 0\n"
    "v_mfma_f32_16x16x32_bf16 a[24:27], v[192:195], a[160:163], a[24:27]\n"
    "v_perm_b32 v196, v71, v67, s68\n"
    "v_perm_b32 v197, v79, v75, s68\n"
    "v_mfma_f32_16x16x32_bf16 a[56:59], v[192:195], a[164:167], a[56:59]\n"
    "v_perm_b32 v198, v87, v83, s68\n"
    "v_perm_b32 v199, v95, v91, s68\n"
    "v_mfma_f32_16x16x32_bf16 a[88:91], v[192:195], a[168:171], a[88:91]\n"
    "buffer_load_dwordx4 v[184:187], v208, s[36:39], s65 offen\n"
    "v_mfma_f32_16x16x32_bf16 a[120:123], v[192:195], a[172:175], a[120:123]\n"
    "v_mov_b32 v7, 0\n"
    "v_mfma_f32_16x16x32_bf16 a[152:155], v[192:195], a[176:179], a[152:155]\n"
    "s_waitcnt vmcnt(15)\n"
    "v_mov_b32 v8, 0\n"
    "v_mfma_f32_16x16x32_bf16 a[28:31], v[196:199], a[160:163], a[28:31]\n"
    "v_perm_b32 v192, v100, v96, s67\n"
    "v_perm_b32 v193, v108, v104, s67\n"
    "v_mfma_f32_16x16x32_bf16 a[60:63], v[196:199], a[164:167], a[60:63]\n"
    "v_perm_b32 v194, v116, v112, s67\n"
    "v_perm_b32 v195, v124, v120, s67\n"
    "v_mfma_f32_16x16x32_bf16 a[92:95], v[196:199], a[168:171], a[92:95]\n"
    "buffer_load_dwordx4 v[188:191], v208, s[36:39], s66 offen\n"
    "v_mfma_f32_16x16x32_bf16 a[124:127], v[196:199], a[172:175], a[124:127]\n"
    "v_add_u32 v201, s73, v200\n"
    "v_mfma_f32_16x16x32_bf16 a[156:159], v[196:199], a[176:179], a[156:159]\n"
    "s_waitcnt lgkmcnt(0)\n"
    "s_add_u32 s73, s73, 0x40000\n"
    "s_cmp_eq_u32 s73, 0x480000\n"
    "s_cselect_b32 s73, 0, s73\n"
    "v_mfma_f32_16x16x32_bf16 a[0:3], v[192:195], a[180:183], a[0:3]\n"
    "v_perm_b32 v196, v100, v96, s68\n"
    "v_perm_b32 v197, v108, v104, s68\n"
    "v_mfma_f32_16x16x32_bf16 a[32:35], v[192:195], a[184:187], a[32:35]\n"
    "v_perm_b32 v198, v116, v112, s68\n"
    "v_perm_b32 v199, v124, v120, s68\n"
    "v_mfma_f32_16x16x32_bf16 a[64:67], v[192:195], a[188:191], a[64:67]\n"
    "buffer_load_dwordx4 v[64:67], v201, s[36:39], 0 offen\n"
    "v_mov_b32 v25, 0\n"
    "v_mfma_f32_16x16x32_bf16 a[96:99], v[192:195], a[192:195], a[96:99]\n"
    "v_mov_b32 v16, 0\n"
    "v_mov_b32 v26, 0\n"
    "v_mfma_f32_16x16x32_bf16 a[128:131], v[192:195], a[196:199], a[128:131]\n"
    "v_mov_b32 v17, 0\n"
    "v_mov_b32 v27, 0\n"
    "v_mfma_f32_16x16x32_bf16 a[4:7], v[196:199], a[180:183], a[4:7]\n"
    "v_perm_b32 v192, v101, v97, s67\n"
    "v_perm_b32 v193, v109, v105, s67\n"
    "v_mfma_f32_16x16x32_bf16 a[36:39], v[196:199], a[184:187], a[36:39]\n"
    "v_perm_b32 v194, v117, v113, s67\n"
    "v_perm_b32 v195, v125, v121, s67\n"
    "v_mfma_f32_16x16x32_bf16 a[68:71], v[196:199], a[188:191], a[68:71]\n"
    "buffer_load_dwordx4 v[68:71], v201, s[36:39], s60 offen\n"
    "v_mov_b32 v28, 0\n"
    "v_mfma_f32_16x16x32_bf16 a[100:103], v[196:199], a[192:195], a[100:103]\n"
    "ds_read_b128 a[160:163], v202 offset:43008\n"
    "v_mov_b32 v29, 0\n"
    "v_mfma_f32_16x16x32_bf16 a[132:135], v[196:199], a[196:199], a[132:135]\n"
    "v_mov_b32 v18, 0\n"
    "v_mov_b32 v30, 0\n"
    "v_mfma_f32_16x16x32_bf16 a[8:11], v[192:195], a[180:183], a[8:11]\n"
    "v_perm_b32 v196, v101, v97, s68\n"
    "v_perm_b32 v197, v109, v105, s68\n"
    "v_mfma_f32_16x16x32_bf16 a[40:43], v[192:195], a[184:187], a[40:43]\n"
    "v_perm_b32 v198, v117, v113, s68\n"
    "v_perm_b32 v199, v125, v121, s68\n"
    "v_mfma_f32_16x16x32_bf16 a[72:75], v[192:195], a[188:191], a[72:75]\n"
    "buffer_load_dwordx4 v[72:75], v201, s[36:39], s61 offen\n"
    "v_mov_b32 v31, 0\n"
    "v_mfma_f32_16x16x32_bf16 a[104:107], v[192:195], a[192:195], a[104:107]\n"
    "ds_read_b128 a[164:167], v202 offset:45056\n"
    "v_mfma_f32_16x16x32_bf16 a[136:139], v[192:195], a[196:199], a[136:139]\n"
    "v_mov_b32 v19, 0\n"
    "v_mfma_f32_16x16x32_bf16 a[12:15], v[196:199], a[180:183], a[12:15]\n"
    "v_perm_b32 v192, v102, v98, s67\n"
    "v_perm_b32 v193, v110, v106, s67\n"
    "v_mfma_f32_16x16x32_bf16 a[44:47], v[196:199], a[184:187], a[44:47]\n"
    "v_perm_b32 v194, v118, v114, s67\n"
    "v_perm_b32 v195, v126, v122, s67\n"
    "v_mfma_f32_16x16x32_bf16 a[76:79], v[196:199], a[188:191], a[76:79]\n"
    "buffer_load_dwordx4 v[76:79], v201, s[36:39], s62 offen\n"
    "v_mfma_f32_16x16x32_bf16 a[108:111], v[196:199], a[192:195], a[108:111]\n"
    "ds_read_b128 a[168:171], v202 offset:47104\n"
    "v_mfma_f32_16x16x32_bf16 a[140:143], v[196:199], a[196:199], a[140:143]\n"
    "v_mov_b32 v20, 0\n"
    "v_mfma_f32_16x16x32_bf16 a[16:19], v[192:195], a[180:183], a[16:19]\n"
    "v_perm_b32 v196, v102, v98, s68\n"
    "v_perm_b32 v197, v110, v106, s68\n"
    "v_mfma_f32_16x16x32_bf16 a[48:51], v[192:195], a[184:187], a[48:51]\n"
    "v_perm_b32 v198, v118, v114, s68\n"
    "v_perm_b32 v199, v126, v122, s68\n"
    "v_mfma_f32_16x16x32_bf16 a[80:83], v[192:195], a[188:191], a[80:83]\n"
    "buffer_load_dwordx4 v[80:83], v201, s[36:39], s63 offen\n"
    "v_mfma_f32_16x16x32_bf16 a[112:115], v[192:195], a[192:195], a[112:115]\n"
    "ds_read_b128 a[172:175], v203 offset:0\n"
    "v_mfma_f32_16x16x32_bf16 a[144:147], v[192:195], a[196:199], a[144:147]\n"
    "v_mov_b32 v21, 0\n"
    "v_mfma_f32_16x16x32_bf16 a[20:23], v[196:199], a[180:183], a[20:23]\n"
    "v_perm_b32 v192, v103, v99, s67\n"
    "v_perm_b32 v193, v111, v107, s67\n"
    "v_mfma_f32_16x16x32_bf16 a[52:55], v[196:199], a[184:187], a[52:55]\n"
    "v_perm_b32 v194, v119, v115, s67\n"
    "v_perm_b32 v195, v127, v123, s67\n"
    "v_mfma_f32_16x16x32_bf16 a[84:87], v[196:199], a[188:191], a[84:87]\n"
    "buffer_load_dwordx4 v[84:87], v201, s[36:39], s64 offen\n"
    "v_mfma_f32_16x16x32_bf16 a[116:119], v[196:199], a[192:195], a[116:119]\n"
    "ds_read_b128 a[176:179], v203 offset:2048\n"
    "v_mfma_f32_16x16x32_bf16 a[148:151], v[196:199], a[196:199], a[148:151]\n"
    "v_mov_b32 v22, 0\n"
    "v_mfma_f32_16x16x32_bf16 a[24:27], v[192:195], a[180:183], a[24:27]\n"
    "v_perm_b32 v196, v103, v99, s68\n"
    "v_perm_b32 v197, v111, v107, s68\n"
    "B_TH_FIRST1:\n"
    "B_TH_S1:\n"
    "s_cbranch_execz B_TH_RET\n"
    "v_mfma_f32_16x16x32_bf16 a[56:59], v[192:195], a[184:187], a[56:59]\n"
    "v_perm_b32 v198, v119, v115, s68\n"
    "v_perm_b32 v199, v127, v123, s68\n"
    "v_mfma_f32_16x16x32_bf16 a[88:91], v[192:195], a[188:191], a[88:91]\n"
    "buffer_load_dwordx4 v[88:91], v201, s[36:39], s65 offen\n"
    "v_mfma_f32_16x16x32_bf16 a[120:123], v[192:195], a[192:195], a[120:123]\n"
    "v_mov_b32 v23, 0\n"
    "v_mfma_f32_16x16x32_bf16 a[152:155], v[192:195], a[196:199], a[152:155]\n"
    "s_waitcnt vmcnt(15)\n"
    "v_mov_b32 v24, 0\n"
    "v_mfma_f32_16x16x32_bf16 a[28:31], v[196:199], a[180:183], a[28:31]\n"
    "v_perm_b32 v192, v132, v128, s67\n"
    "v_perm_b32 v193, v140, v136, s67\n"
    "v_mfma_f32_16x16x32_bf16 a[60:63], v[196:199], a[184:187], a[60:63]\n"
    "v_perm_b32 v194, v148, v144, s67\n"
    "v_perm_b32 v195, v156, v152, s67\n"
    "v_mfma_f32_16x16x32_bf16 a[92:95], v[196:199], a[188:191], a[92:95]\n"
    "buffer_load_dwordx4 v[92:95], v201, s[36:39], s66 offen\n"
    "v_mfma_f32_16x16x32_bf16 a[124:127], v[196:199], a[192:195], a[124:127]\n"
    "v_add_u32 v208, s73, v200\n"
    "v_mfma_f32_16x16x32_bf16 a[156:159], v[196:199], a[196:199], a[156:159]\n"
    "s_waitcnt lgkmcnt(0)\n"
    "s_add_u32 s73, s73, 0x40000\n"
    "s_cmp_eq_u32 s73, 0x480000\n"
    "s_cselect_b32 s73, 0, s73\n"
    "v_mfma_f32_16x16x32_bf16 a[0:3], v[192:195], a[160:163], a[0:3]\n"
    "v_perm_b32 v196, v132, v128, s68\n"
    "v_perm_b32 v197, v140, v136, s68\n"
    "v_mfma_f32_16x16x32_bf16 a[32:35], v[192:195], a[164:167], a[32:35]\n"
    "v_perm_b32 v198, v148, v144, s68\n"
    "v_perm_b32 v199, v156, v152, s68\n"
    "v_mfma_f32_16x16x32_bf16 a[64:67], v[192:195], a[168:171], a[64:67]\n"
    "buffer_load_dwordx4 v[96:99], v208, s[36:39], 0 offen\n"
    "v_mov_b32 v41, 0\n"
    "v_mfma_f32_16x16x32_bf16 a[96:99], v[192:195], a[172:175], a[96:99]\n"
    "v_mov_b32 v32, 0\n"
    "v_mov_b32 v42, 0\n"
    "v_mfma_f32_16x16x32_bf16 a[128:131], v[192:195], a[176:179], a[128:131]\n"
    "v_mov_b32 v33, 0\n"
    "v_mov_b32 v43, 0\n"
    "v_mfma_f32_16x16x32_bf16 a[4:7], v[196:199], a[160:163], a[4:7]\n"
    "v_perm_b32 v192, v133, v129, s67\n"
    "v_perm_b32 v193, v141, v137, s67\n"
    "v_mfma_f32_16x16x32_bf16 a[36:39], v[196:199], a[164:167], a[36:39]\n"
    "v_perm_b32 v194, v149, v145, s67\n"
    "v_perm_b32 v195, v157, v153, s67\n"
    "v_mfma_f32_16x16x32_bf16 a[68:71], v[196:199], a[168:171], a[68:71]\n"
    "buffer_load_dwordx4 v[100:103], v208, s[36:39], s60 offen\n"
    "v_mov_b32 v44, 0\n"
    "v_mfma_f32_16x16x32_bf16 a[100:103], v[196:199], a[172:175], a[100:103]\n"
    "ds_read_b128 a[180:183], v204 offset:43008\n"
    "v_mov_b32 v45, 0\n"
    "v_mfma_f32_16x16x32_bf16 a[132:135], v[196:199], a[176:179], a[132:135]\n"
    "v_mov_b32 v34, 0\n"
    "v_mov_b32 v46, 0\n"
    "v_mfma_f32_16x16x32_bf16 a[8:11], v[192:195], a[160:163], a[8:11]\n"
    "v_perm_b32 v196, v133, v129, s68\n"
    "v_perm_b32 v197, v141, v137, s68\n"
    "v_mfma_f32_16x16x32_bf16 a[40:43], v[192:195], a[164:167], a[40:43]\n"
    "v_perm_b32 v198, v149, v145, s68\n"
    "v_perm_b32 v199, v157, v153, s68\n"
    "v_mfma_f32_16x16x32_bf16 a[72:75], v[192:195], a[168:171], a[72:75]\n"
    "buffer_load_dwordx4 v[104:107], v208, s[36:39], s61 offen\n"
    "v_mov_b32 v47, 0\n"
    "v_mfma_f32_16x16x32_bf16 a[104:107], v[192:195], a[172:175], a[104:107]\n"
    "ds_read_b128 a[184:187], v204 offset:45056\n"
    "v_mfma_f32_16x16x32_bf16 a[136:139], v[192:195], a[176:179], a[136:139]\n"
    "v_mov_b32 v35, 0\n"
    "v_mfma_f32_16x16x32_bf16 a[12:15], v[196:199], a[160:163], a[12:15]\n"
    "v_perm_b32 v192, v134, v130, s67\n"
    "v_perm_b32 v193, v142, v138, s67\n"
    "v_mfma_f32_16x16x32_bf16 a[44:47], v[196:199], a[164:167], a[44:47]\n"
    "v_perm_b32 v194, v150, v146, s67\n"
    "v_perm_b32 v195, v158, v154, s67\n"
    "v_mfma_f32_16x16x32_bf16 a[76:79], v[196:199], a[168:171], a[76:79]\n"
    "buffer_load_dwordx4 v[108:111], v208, s[36:39], s62 offen\n"
    "v_mfma_f32_16x16x32_bf16 a[108:111], v[196:199], a[172:175], a[108:111]\n"
    "ds_read_b128 a[188:191], v204 offset:47104\n"
    "v_mfma_f32_16x16x32_bf16 a[140:143], v[196:199], a[176:179], a[140:143]\n"
    "v_mov_b32 v36, 0\n"
    "v_mfma_f32_16x16x32_bf16 a[16:19], v[192:195], a[160:163], a[16:19]\n"
    "v_perm_b32 v196, v134, v130, s68\n"
    "v_perm_b32 v197, v142, v138, s68\n"
    "v_mfma_f32_16x16x32_bf16 a[48:51], v[192:195], a[164:167], a[48:51]\n"
    "v_perm_b32 v198, v150, v146, s68\n"
    "v_perm_b32 v199, v158, v154, s68\n"
    "v_mfma_f32_16x16x32_bf16 a[80:83], v[192:195], a[168:171], a[80:83]\n"
    "buffer_load_dwordx4 v[112:115], v208, s[36:39], s63 offen\n"
    "v_mfma_f32_16x16x32_bf16 a[112:115], v[192:195], a[172:175], a[112:115]\n"
    "ds_read_b128 a[192:195], v205 offset:0\n"
    "v_mfma_f32_16x16x32_bf16 a[144:147], v[192:195], a[176:179], a[144:147]\n"
    "v_mov_b32 v37, 0\n"
    "v_mfma_f32_16x16x32_bf16 a[20:23], v[196:199], a[160:163], a[20:23]\n"
    "v_perm_b32 v192, v135, v131, s67\n"
    "v_perm_b32 v193, v143, v139, s67\n"
    "v_mfma_f32_16x16x32_bf16 a[52:55], v[196:199], a[164:167], a[52:55]\n"
    "v_perm_b32 v194, v151, v147, s67\n"
    "v_perm_b32 v195, v159, v155, s67\n"
    "v_mfma_f32_16x16x32_bf16 a[84:87], v[196:199], a[168:171], a[84:87]\n"
    "buffer_load_dwordx4 v[116:119], v208, s[36:39], s64 offen\n"
    "v_mfma_f32_16x16x32_bf16 a[116:119], v[196:199], a[172:175], a[116:119]\n"
    "ds_read_b128 a[196:199], v205 offset:2048\n"
    "v_mfma_f32_16x16x32_bf16 a[148:151], v[196:199], a[176:179], a[148:151]\n"
    "v_mov_b32 v38, 0\n"
    "v_mfma_f32_16x16x32_bf16 a[24:27], v[192:195], a[160:163], a[24:27]\n"
    "v_perm_b32 v196, v135, v131, s68\n"
    "v_perm_b32 v197, v143, v139, s68\n"
    "v_mfma_f32_16x16x32_bf16 a[56:59], v[192:195], a[164:167], a[56:59]\n"
    "v_perm_b32 v198, v151, v147, s68\n"
    "v_perm_b32 v199, v159, v155, s68\n"
    "v_mfma_f32_16x16x32_bf16 a[88:91], v[192:195], a[168:171], a[88:91]\n"
    "buffer_load_dwordx4 v[120:123], v208, s[36:39], s65 offen\n"
    "v_mfma_f32_16x16x32_bf16 a[120:123], v[192:195], a[172:175], a[120:123]\n"
    "v_mov_b32 v39, 0\n"
    "v_mfma_f32_16x16x32_bf16 a[152:155], v[192:195], a[176:179], a[152:155]\n"
    "s_waitcnt vmcnt(15)\n"
    "v_mov_b32 v40, 0\n"
    "v_mfma_f32_16x16x32_bf16 a[28:31], v[196:199], a[160:163], a[28:31]\n"
    "v_perm_b32 v192, v164, v160, s67\n"
    "v_perm_b32 v193, v172, v168, s67\n"
    "v_mfma_f32_16x16x32_bf16 a[60:63], v[196:199], a[164:167], a[60:63]\n"
    "v_perm_b32 v194, v180, v176, s67\n"
    "v_perm_b32 v195, v188, v184, s67\n"
    "v_mfma_f32_16x16x32_bf16 a[92:95], v[196:199], a[168:171], a[92:95]\n"
    "buffer_load_dwordx4 v[124:127], v208, s[36:39], s66 offen\n"
    "v_mfma_f32_16x16x32_bf16 a[124:127], v[196:199], a[172:175], a[124:127]\n"
    "v_add_u32 v201, s73, v200\n"
    "v_mfma_f32_16x16x32_bf16 a[156:159], v[196:199], a[176:179], a[156:159]\n"
    "s_waitcnt lgkmcnt(0)\n"
    "s_add_u32 s73, s73, 0x40000\n"
    "s_cmp_eq_u32 s73, 0x480000\n"
    "s_cselect_b32 s73, 0, s73\n"
    "v_mfma_f32_16x16x32_bf16 a[0:3], v[192:195], a[180:183], a[0:3]\n"
    "v_perm_b32 v196, v164, v160, s68\n"
    "v_perm_b32 v197, v172, v168, s68\n"
    "v_mfma_f32_16x16x32_bf16 a[32:35], v[192:195], a[184:187], a[32:35]\n"
    "v_perm_b32 v198, v180, v176, s68\n"
    "v_perm_b32 v199, v188, v184, s68\n"
    "v_mfma_f32_16x16x32_bf16 a[64:67], v[192:195], a[188:191], a[64:67]\n"
    "buffer_load_dwordx4 v[128:131], v201, s[36:39], 0 offen\n"
    "v_mov_b32 v57, 0\n"
    "v_mfma_f32_16x16x32_bf16 a[96:99], v[192:195], a[192:195], a[96:99]\n"
    "v_mov_b32 v48, 0\n"
    "v_mov_b32 v58, 0\n"
    "v_mfma_f32_16x16x32_bf16 a[128:131], v[192:195], a[196:199], a[128:131]\n"
    "v_mov_b32 v49, 0\n"
    "v_mov_b32 v59, 0\n"
    "v_mfma_f32_16x16x32_bf16 a[4:7], v[196:199], a[180:183], a[4:7]\n"
    "v_perm_b32 v192, v165, v161, s67\n"
    "v_perm_b32 v193, v173, v169, s67\n"
    "v_mfma_f32_16x16x32_bf16 a[36:39], v[196:199], a[184:187], a[36:39]\n"
    "v_perm_b32 v194, v181, v177, s67\n"
    "v_perm_b32 v195, v189, v185, s67\n"
    "v_mfma_f32_16x16x32_bf16 a[68:71], v[196:199], a[188:191], a[68:71]\n"
    "buffer_load_dwordx4 v[132:135], v201, s[36:39], s60 offen\n"
    "v_mov_b32 v60, 0\n"
    "v_mfma_f32_16x16x32_bf16 a[100:103], v[196:199], a[192:195], a[100:103]\n"
    "ds_read_b128 a[160:163], v203 offset:4096\n"
    "v_mov_b32 v61, 0\n"
    "v_mfma_f32_16x16x32_bf16 a[132:135], v[196:199], a[196:199], a[132:135]\n"
    "v_mov_b32 v50, 0\n"
    "v_mov_b32 v62, 0\n"
    "v_mfma_f32_16x16x32_bf16 a[8:11], v[192:195], a[180:183], a[8:11]\n"
    "v_perm_b32 v196, v165, v161, s68\n"
    "v_perm_b32 v197, v173, v169, s68\n"
    "v_mfma_f32_16x16x32_bf16 a[40:43], v[192:195], a[184:187], a[40:43]\n"
    "v_perm_b32 v198, v181, v177, s68\n"
    "v_perm_b32 v199, v189, v185, s68\n"
    "v_mfma_f32_16x16x32_bf16 a[72:75], v[192:195], a[188:191], a[72:75]\n"
    "buffer_load_dwordx4 v[136:139], v201, s[36:39], s61 offen\n"
    "v_mov_b32 v63, 0\n"
    "v_mfma_f32_16x16x32_bf16 a[104:107], v[192:195], a[192:195], a[104:107]\n"
    "ds_read_b128 a[164:167], v203 offset:6144\n"
    "v_mfma_f32_16x16x32_bf16 a[136:139], v[192:195], a[196:199], a[136:139]\n"
    "v_mov_b32 v51, 0\n"
    "v_mfma_f32_16x16x32_bf16 a[12:15], v[196:199], a[180:183], a[12:15]\n"
    "v_perm_b32 v192, v166, v162, s67\n"
    "v_perm_b32 v193, v174, v170, s67\n"
    "v_mfma_f32_16x16x32_bf16 a[44:47], v[196:199], a[184:187], a[44:47]\n"
    "v_perm_b32 v194, v182, v178, s67\n"
    "v_perm_b32 v195, v190, v186, s67\n"
    "v_mfma_f32_16x16x32_bf16 a[76:79], v[196:199], a[188:191], a[76:79]\n"
    "buffer_load_dwordx4 v[140:143], v201, s[36:39], s62 offen\n"
    "v_mfma_f32_16x16x32_bf16 a[108:111], v[196:199], a[192:195], a[108:111]\n"
    "ds_read_b128 a[168:171], v203 offset:40960\n"
    "v_mfma_f32_16x16x32_bf16 a[140:143], v[196:199], a[196:199], a[140:143]\n"
    "v_mov_b32 v52, 0\n"
    "v_mfma_f32_16x16x32_bf16 a[16:19], v[192:195], a[180:183], a[16:19]\n"
    "v_perm_b32 v196, v166, v162, s68\n"
    "v_perm_b32 v197, v174, v170, s68\n"
    "v_mfma_f32_16x16x32_bf16 a[48:51], v[192:195], a[184:187], a[48:51]\n"
    "v_perm_b32 v198, v182, v178, s68\n"
    "v_perm_b32 v199, v190, v186, s68\n"
    "v_mfma_f32_16x16x32_bf16 a[80:83], v[192:195], a[188:191], a[80:83]\n"
    "buffer_load_dwordx4 v[144:147], v201, s[36:39], s63 offen\n"
    "v_mfma_f32_16x16x32_bf16 a[112:115], v[192:195], a[192:195], a[112:115]\n"
    "ds_read_b128 a[172:175], v203 offset:43008\n"
    "v_mfma_f32_16x16x32_bf16 a[144:147], v[192:195], a[196:199], a[144:147]\n"
    "v_mov_b32 v53, 0\n"
    "v_mfma_f32_16x16x32_bf16 a[20:23], v[196:199], a[180:183], a[20:23]\n"
    "v_perm_b32 v192, v167, v163, s67\n"
    "v_perm_b32 v193, v175, v171, s67\n"
    "v_mfma_f32_16x16x32_bf16 a[52:55], v[196:199], a[184:187], a[52:55]\n"
    "v_perm_b32 v194, v183, v179, s67\n"
    "v_perm_b32 v195, v191, v187, s67\n"
    "v_mfma_f32_16x16x32_bf16 a[84:87], v[196:199], a[188:191], a[84:87]\n"
    "buffer_load_dwordx4 v[148:151], v201, s[36:39], s64 offen\n"
    "v_mfma_f32_16x16x32_bf16 a[116:119], v[196:199], a[192:195], a[116:119]\n"
    "ds_read_b128 a[176:179], v203 offset:45056\n"
    "v_mfma_f32_16x16x32_bf16 a[148:151], v[196:199], a[196:199], a[148:151]\n"
    "v_mov_b32 v54, 0\n"
    "v_mfma_f32_16x16x32_bf16 a[24:27], v[192:195], a[180:183], a[24:27]\n"
    "v_perm_b32 v196, v167, v163, s68\n"
    "v_perm_b32 v197, v175, v171, s68\n"
    "v_mfma_f32_16x16x32_bf16 a[56:59], v[192:195], a[184:187], a[56:59]\n"
    "v_perm_b32 v198, v183, v179, s68\n"
    "v_perm_b32 v199, v191, v187, s68\n"
    "v_mfma_f32_16x16x32_bf16 a[88:91], v[192:195], a[188:191], a[88:91]\n"
    "buffer_load_dwordx4 v[152:155], v201, s[36:39], s65 offen\n"
    "v_mfma_f32_16x16x32_bf16 a[120:123], v[192:195], a[192:195], a[120:123]\n"
    "v_mov_b32 v55, 0\n"
    "v_mfma_f32_16x16x32_bf16 a[152:155], v[192:195], a[196:199], a[152:155]\n"
    "s_waitcnt vmcnt(15)\n"
    "v_mov_b32 v56, 0\n"
    "v_mfma_f32_16x16x32_bf16 a[28:31], v[196:199], a[180:183], a[28:31]\n"
    "v_perm_b32 v192, v68, v64, s67\n"
    "v_perm_b32 v193, v76, v72, s67\n"
    "v_mfma_f32_16x16x32_bf16 a[60:63], v[196:199], a[184:187], a[60:63]\n"
    "v_perm_b32 v194, v84, v80, s67\n"
    "v_perm_b32 v195, v92, v88, s67\n"
    "v_mfma_f32_16x16x32_bf16 a[92:95], v[196:199], a[188:191], a[92:95]\n"
    "buffer_load_dwordx4 v[156:159], v201, s[36:39], s66 offen\n"
    "v_mfma_f32_16x16x32_bf16 a[124:127], v[196:199], a[192:195], a[124:127]\n"
    "v_add_u32 v208, s73, v200\n"
    "v_mfma_f32_16x16x32_bf16 a[156:159], v[196:199], a[196:199], a[156:159]\n"
    "s_waitcnt lgkmcnt(0)\n"
    "s_add_u32 s73, s73, 0x40000\n"
    "s_cmp_eq_u32 s73, 0x480000\n"
    "s_cselect_b32 s73, 0, s73\n"
    "v_mfma_f32_16x16x32_bf16 a[0:3], v[192:195], a[160:163], a[0:3]\n"
    "v_perm_b32 v196, v68, v64, s68\n"
    "v_perm_b32 v197, v76, v72, s68\n"
    "v_mfma_f32_16x16x32_bf16 a[32:35], v[192:195], a[164:167], a[32:35]\n"
    "v_perm_b32 v198, v84, v80, s68\n"
    "v_perm_b32 v199, v92, v88, s68\n"
    "v_mfma_f32_16x16x32_bf16 a[64:67], v[192:195], a[168:171], a[64:67]\n"
    "buffer_load_dwordx4 v[160:163], v208, s[36:39], 0 offen\n"
    "v_mfma_f32_16x16x32_bf16 a[96:99], v[192:195], a[172:175], a[96:99]\n"
    "v_mfma_f32_16x16x32_bf16 a[128:131], v[192:195], a[176:179], a[128:131]\n"
    "v_mfma_f32_16x16x32_bf16 a[4:7], v[196:199], a[160:163], a[4:7]\n"
    "v_perm_b32 v192, v69, v65, s67\n"
    "v_perm_b32 v193, v77, v73, s67\n"
    "v_mfma_f32_16x16x32_bf16 a[36:39], v[196:199], a[164:167], a[36:39]\n"
    "v_perm_b32 v194, v85, v81, s67\n"
    "v_perm_b32 v195, v93, v89, s67\n"
    "v_mfma_f32_16x16x32_bf16 a[68:71], v[196:199], a[168:171], a[68:71]\n"
    "buffer_load_dwordx4 v[164:167], v208, s[36:39], s60 offen\n"
    "v_mfma_f32_16x16x32_bf16 a[100:103], v[196:199], a[172:175], a[100:103]\n"
    "ds_read_b128 a[180:183], v205 offset:4096\n"
    "v_mfma_f32_16x16x32_bf16 a[132:135], v[196:199], a[176:179], a[132:135]\n"
    "v_mfma_f32_16x16x32_bf16 a[8:11], v[192:195], a[160:163], a[8:11]\n"
    "v_perm_b32 v196, v69, v65, s68\n"
    "v_perm_b32 v197, v77, v73, s68\n"
    "v_mfma_f32_16x16x32_bf16 a[40:43], v[192:195], a[164:167], a[40:43]\n"
    "v_perm_b32 v198, v85, v81, s68\n"
    "v_perm_b32 v199, v93, v89, s68\n"
    "v_mfma_f32_16x16x32_bf16 a[72:75], v[192:195], a[168:171], a[72:75]\n"
    "buffer_load_dwordx4 v[168:171], v208, s[36:39], s61 offen\n"
    "v_mfma_f32_16x16x32_bf16 a[104:107], v[192:195], a[172:175], a[104:107]\n"
    "ds_read_b128 a[184:187], v205 offset:6144\n"
    "v_mfma_f32_16x16x32_bf16 a[136:139], v[192:195], a[176:179], a[136:139]\n"
    "v_mfma_f32_16x16x32_bf16 a[12:15], v[196:199], a[160:163], a[12:15]\n"
    "v_perm_b32 v192, v70, v66, s67\n"
    "v_perm_b32 v193, v78, v74, s67\n"
    "v_mfma_f32_16x16x32_bf16 a[44:47], v[196:199], a[164:167], a[44:47]\n"
    "v_perm_b32 v194, v86, v82, s67\n"
    "v_perm_b32 v195, v94, v90, s67\n"
    "v_mfma_f32_16x16x32_bf16 a[76:79], v[196:199], a[168:171], a[76:79]\n"
    "buffer_load_dwordx4 v[172:175], v208, s[36:39], s62 offen\n"
    "v_mfma_f32_16x16x32_bf16 a[108:111], v[196:199], a[172:175], a[108:111]\n"
    "ds_read_b128 a[188:191], v205 offset:40960\n"
    "v_mfma_f32_16x16x32_bf16 a[140:143], v[196:199], a[176:179], a[140:143]\n"
    "v_mfma_f32_16x16x32_bf16 a[16:19], v[192:195], a[160:163], a[16:19]\n"
    "v_perm_b32 v196, v70, v66, s68\n"
    "v_perm_b32 v197, v78, v74, s68\n"
    "v_mfma_f32_16x16x32_bf16 a[48:51], v[192:195], a[164:167], a[48:51]\n"
    "v_perm_b32 v198, v86, v82, s68\n"
    "v_perm_b32 v199, v94, v90, s68\n"
    "v_mfma_f32_16x16x32_bf16 a[80:83], v[192:195], a[168:171], a[80:83]\n"
    "buffer_load_dwordx4 v[176:179], v208, s[36:39], s63 offen\n"
    "v_mfma_f32_16x16x32_bf16 a[112:115], v[192:195], a[172:175], a[112:115]\n"
    "ds_read_b128 a[192:195], v205 offset:43008\n"
    "v_mfma_f32_16x16x32_bf16 a[144:147], v[192:195], a[176:179], a[144:147]\n"
    "v_mfma_f32_16x16x32_bf16 a[20:23], v[196:199], a[160:163], a[20:23]\n"
    "v_perm_b32 v192, v71, v67, s67\n"
    "v_perm_b32 v193, v79, v75, s67\n"
    "v_mfma_f32_16x16x32_bf16 a[52:55], v[196:199], a[164:167], a[52:55]\n"
    "v_perm_b32 v194, v87, v83, s67\n"
    "v_perm_b32 v195, v95, v91, s67\n"
    "v_mfma_f32_16x16x32_bf16 a[84:87], v[196:199], a[168:171], a[84:87]\n"
    "buffer_load_dwordx4 v[180:183], v208, s[36:39], s64 offen\n"
    "v_mfma_f32_16x16x32_bf16 a[116:119], v[196:199], a[172:175], a[116:119]\n"
    "ds_read_b128 a[196:199], v205 offset:45056\n"
    "v_mfma_f32_16x16x32_bf16 a[148:151], v[196:199], a[176:179], a[148:151]\n"
    "v_mfma_f32_16x16x32_bf16 a[24:27], v[192:195], a[160:163], a[24:27]\n"
    "v_perm_b32 v196, v71, v67, s68\n"
    "v_perm_b32 v197, v79, v75, s68\n"
    "v_mfma_f32_16x16x32_bf16 a[56:59], v[192:195], a[164:167], a[56:59]\n"
    "v_perm_b32 v198, v87, v83, s68\n"
    "v_perm_b32 v199, v95, v91, s68\n"
    "v_mfma_f32_16x16x32_bf16 a[88:91], v[192:195], a[168:171], a[88:91]\n"
    "buffer_load_dwordx4 v[184:187], v208, s[36:39], s65 offen\n"
    "v_mfma_f32_16x16x32_bf16 a[120:123], v[192:195], a[172:175], a[120:123]\n"
    "v_mfma_f32_16x16x32_bf16 a[152:155], v[192:195], a[176:179], a[152:155]\n"
    "s_waitcnt vmcnt(15)\n"
    "v_mfma_f32_16x16x32_bf16 a[28:31], v[196:199], a[160:163], a[28:31]\n"
    "v_perm_b32 v192, v100, v96, s67\n"
    "v_perm_b32 v193, v108, v104, s67\n"
    "v_mfma_f32_16x16x32_bf16 a[60:63], v[196:199], a[164:167], a[60:63]\n"
    "v_perm_b32 v194, v116, v112, s67\n"
    "v_perm_b32 v195, v124, v120, s67\n"
    "v_mfma_f32_16x16x32_bf16 a[92:95], v[196:199], a[168:171], a[92:95]\n"
    "buffer_load_dwordx4 v[188:191], v208, s[36:39], s66 offen\n"
    "v_mfma_f32_16x16x32_bf16 a[124:127], v[196:199], a[172:175], a[124:127]\n"
    "v_add_u32 v201, s73, v200\n"
    "v_mfma_f32_16x16x32_bf16 a[156:159], v[196:199], a[176:179], a[156:159]\n"
    "s_waitcnt lgkmcnt(0)\n"
    "s_add_u32 s73, s73, 0x40000\n"
    "s_cmp_eq_u32 s73, 0x480000\n"
    "s_cselect_b32 s73, 0, s73\n"
    "v_mfma_f32_16x16x32_bf16 a[0:3], v[192:195], a[180:183], a[0:3]\n"
    "v_perm_b32 v196, v100, v96, s68\n"
    "v_perm_b32 v197, v108, v104, s68\n"
    "v_mfma_f32_16x16x32_bf16 a[32:35], v[192:195], a[184:187], a[32:35]\n"
    "v_perm_b32 v198, v116, v112, s68\n"
    "v_perm_b32 v199, v124, v120, s68\n"
    "v_mfma_f32_16x16x32_bf16 a[64:67], v[192:195], a[188:191], a[64:67]\n"
    "buffer_load_dwordx4 v[64:67], v201, s[36:39], 0 offen\n"
    "s_waitcnt vmcnt(53)\n"
    "s_barrier\n"
    "v_mfma_f32_16x16x32_bf16 a[96:99], v[192:195], a[192:195], a[96:99]\n"
    "v_add_u32 v202, 0xffffc000, v202\n"
    "v_add_u32 v203, 0xffffc000, v203\n"
    "v_mfma_f32_16x16x32_bf16 a[128:131], v[192:195], a[196:199], a[128:131]\n"
    "v_add_u32 v204, 0xffffc000, v204\n"
    "v_add_u32 v205, 0xffffc000, v205\n"
    "v_mfma_f32_16x16x32_bf16 a[4:7], v[196:199], a[180:183], a[4:7]\n"
    "v_perm_b32 v192, v101, v97, s67\n"
    "v_perm_b32 v193, v109, v105, s67\n"
    "v_mfma_f32_16x16x32_bf16 a[36:39], v[196:199], a[184:187], a[36:39]\n"
    "v_perm_b32 v194, v117, v113, s67\n"
    "v_perm_b32 v195, v125, v121, s67\n"
    "v_mfma_f32_16x16x32_bf16 a[68:71], v[196:199], a[188:191], a[68:71]\n"
    "buffer_load_dwordx4 v[68:71], v201, s[36:39], s60 offen\n"
    "v_mfma_f32_16x16x32_bf16 a[100:103], v[196:199], a[192:195], a[100:103]\n"
    "ds_read_b128 a[160:163], v202 offset:0\n"
    "v_mfma_f32_16x16x32_bf16 a[132:135], v[196:199], a[196:199], a[132:135]\n"
    "v_mfma_f32_16x16x32_bf16 a[8:11], v[192:195], a[180:183], a[8:11]\n"
    "v_perm_b32 v196, v101, v97, s68\n"
    "v_perm_b32 v197, v109, v105, s68\n"
    "v_mfma_f32_16x16x32_bf16 a[40:43], v[192:195], a[184:187], a[40:43]\n"
    "v_perm_b32 v198, v117, v113, s68\n"
    "v_perm_b32 v199, v125, v121, s68\n"
    "v_mfma_f32_16x16x32_bf16 a[72:75], v[192:195], a[188:191], a[72:75]\n"
    "buffer_load_dwordx4 v[72:75], v201, s[36:39], s61 offen\n"
    "v_mfma_f32_16x16x32_bf16 a[104:107], v[192:195], a[192:195], a[104:107]\n"
    "ds_read_b128 a[164:167], v202 offset:2048\n"
    "v_mfma_f32_16x16x32_bf16 a[136:139], v[192:195], a[196:199], a[136:139]\n"
    "v_mfma_f32_16x16x32_bf16 a[12:15], v[196:199], a[180:183], a[12:15]\n"
    "v_perm_b32 v192, v102, v98, s67\n"
    "v_perm_b32 v193, v110, v106, s67\n"
    "v_mfma_f32_16x16x32_bf16 a[44:47], v[196:199], a[184:187], a[44:47]\n"
    "v_perm_b32 v194, v118, v114, s67\n"
    "v_perm_b32 v195, v126, v122, s67\n"
    "v_mfma_f32_16x16x32_bf16 a[76:79], v[196:199], a[188:191], a[76:79]\n"
    "buffer_load_dwordx4 v[76:79], v201, s[36:39], s62 offen\n"
    "v_mfma_f32_16x16x32_bf16 a[108:111], v[196:199], a[192:195], a[108:111]\n"
    "ds_read_b128 a[168:171], v202 offset:4096\n"
    "v_mfma_f32_16x16x32_bf16 a[140:143], v[196:199], a[196:199], a[140:143]\n"
    "v_mfma_f32_16x16x32_bf16 a[16:19], v[192:195], a[180:183], a[16:19]\n"
    "v_perm_b32 v196, v102, v98, s68\n"
    "v_perm_b32 v197, v110, v106, s68\n"
    "v_mfma_f32_16x16x32_bf16 a[48:51], v[192:195], a[184:187], a[48:51]\n"
    "v_perm_b32 v198, v118, v114, s68\n"
    "v_perm_b32 v199, v126, v122, s68\n"
    "v_mfma_f32_16x16x32_bf16 a[80:83], v[192:195], a[188:191], a[80:83]\n"
    "buffer_load_dwordx4 v[80:83], v201, s[36:39], s63 offen\n"
    "v_mfma_f32_16x16x32_bf16 a[112:115], v[192:195], a[192:195], a[112:115]\n"
    "ds_read_b128 a[172:175], v202 offset:6144\n"
    "v_mfma_f32_16x16x32_bf16 a[144:147], v[192:195], a[196:199], a[144:147]\n"
    "v_mfma_f32_16x16x32_bf16 a[20:23], v[196:199], a[180:183], a[20:23]\n"
    "v_perm_b32 v192, v103, v99, s67\n"
    "v_perm_b32 v193, v111, v107, s67\n"
    "v_mfma_f32_16x16x32_bf16 a[52:55], v[196:199], a[184:187], a[52:55]\n"
    "v_perm_b32 v194, v119, v115, s67\n"
    "v_perm_b32 v195, v127, v123, s67\n"
    "v_mfma_f32_16x16x32_bf16 a[84:87], v[196:199], a[188:191], a[84:87]\n"
    "buffer_load_dwordx4 v[84:87], v201, s[36:39], s64 offen\n"
    "v_mfma_f32_16x16x32_bf16 a[116:119], v[196:199], a[192:195], a[116:119]\n"
    "ds_read_b128 a[176:179], v202 offset:40960\n"
    "v_mfma_f32_16x16x32_bf16 a[148:151], v[196:199], a[196:199], a[148:151]\n"
    "v_mfma_f32_16x16x32_bf16 a[24:27], v[192:195], a[180:183], a[24:27]\n"
    "v_perm_b32 v196, v103, v99, s68\n"
    "v_perm_b32 v197, v111, v107, s68\n"
    "v_mfma_f32_16x16x32_bf16 a[56:59], v[192:195], a[184:187], a[56:59]\n"
    "v_perm_b32 v198, v119, v115, s68\n"
    "v_perm_b32 v199, v127, v123, s68\n"
    "v_mfma_f32_16x16x32_bf16 a[88:91], v[192:195], a[188:191], a[88:91]\n"
    "buffer_load_dwordx4 v[88:91], v201, s[36:39], s65 offen\n"
    "v_mfma_f32_16x16x32_bf16 a[120:123], v[192:195], a[192:195], a[120:123]\n"
    "v_mfma_f32_16x16x32_bf16 a[152:155], v[192:195], a[196:199], a[152:155]\n"
    "s_waitcnt vmcnt(15)\n"
    "v_mfma_f32_16x16x32_bf16 a[28:31], v[196:199], a[180:183], a[28:31]\n"
    "v_perm_b32 v192, v132, v128, s67\n"
    "v_perm_b32 v193, v140, v136, s67\n"
    "v_mfma_f32_16x16x32_bf16 a[60:63], v[196:199], a[184:187], a[60:63]\n"
    "v_perm_b32 v194, v148, v144, s67\n"
    "v_perm_b32 v195, v156, v152, s67\n"
    "v_mfma_f32_16x16x32_bf16 a[92:95], v[196:199], a[188:191], a[92:95]\n"
    "buffer_load_dwordx4 v[92:95], v201, s[36:39], s66 offen\n"
    "v_mfma_f32_16x16x32_bf16 a[124:127], v[196:199], a[192:195], a[124:127]\n"
    "v_add_u32 v208, s73, v200\n"
    "v_mfma_f32_16x16x32_bf16 a[156:159], v[196:199], a[196:199], a[156:159]\n"
    "s_waitcnt lgkmcnt(0)\n"
    "s_add_u32 s73, s73, 0x40000\n"
    "s_cmp_eq_u32 s73, 0x480000\n"
    "s_cselect_b32 s73, 0, s73\n"
    "v_mfma_f32_16x16x32_bf16 a[0:3], v[192:195], a[160:163], a[0:3]\n"
    "v_perm_b32 v196, v132, v128, s68\n"
    "v_perm_b32 v197, v140, v136, s68\n"
    "v_mfma_f32_16x16x32_bf16 a[32:35], v[192:195], a[164:167], a[32:35]\n"
    "v_perm_b32 v198, v148, v144, s68\n"
    "v_perm_b32 v199, v156, v152, s68\n"
    "v_mfma_f32_16x16x32_bf16 a[64:67], v[192:195], a[168:171], a[64:67]\n"
    "buffer_load_dwordx4 v[96:99], v208, s[36:39], 0 offen\n"
    "v_mfma_f32_16x16x32_bf16 a[96:99], v[192:195], a[172:175], a[96:99]\n"
    "v_mfma_f32_16x16x32_bf16 a[128:131], v[192:195], a[176:179], a[128:131]\n"
    "v_mfma_f32_16x16x32_bf16 a[4:7], v[196:199], a[160:163], a[4:7]\n"
    "v_perm_b32 v192, v133, v129, s67\n"
    "v_perm_b32 v193, v141, v137, s67\n"
    "v_mfma_f32_16x16x32_bf16 a[36:39], v[196:199], a[164:167], a[36:39]\n"
    "v_perm_b32 v194, v149, v145, s67\n"
    "v_perm_b32 v195, v157, v153, s67\n"
    "v_mfma_f32_16x16x32_bf16 a[68:71], v[196:199], a[168:171], a[68:71]\n"
    "buffer_load_dwordx4 v[100:103], v208, s[36:39], s60 offen\n"
    "v_mfma_f32_16x16x32_bf16 a[100:103], v[196:199], a[172:175], a[100:103]\n"
    "ds_read_b128 a[180:183], v204 offset:0\n"
    "v_mfma_f32_16x16x32_bf16 a[132:135], v[196:199], a[176:179], a[132:135]\n"
    "v_mfma_f32_16x16x32_bf16 a[8:11], v[192:195], a[160:163], a[8:11]\n"
    "v_perm_b32 v196, v133, v129, s68\n"
    "v_perm_b32 v197, v141, v137, s68\n"
    "v_mfma_f32_16x16x32_bf16 a[40:43], v[192:195], a[164:167], a[40:43]\n"
    "v_perm_b32 v198, v149, v145, s68\n"
    "v_perm_b32 v199, v157, v153, s68\n"
    "v_mfma_f32_16x16x32_bf16 a[72:75], v[192:195], a[168:171], a[72:75]\n"
    "buffer_load_dwordx4 v[104:107], v208, s[36:39], s61 offen\n"
    "v_mfma_f32_16x16x32_bf16 a[104:107], v[192:195], a[172:175], a[104:107]\n"
    "ds_read_b128 a[184:187], v204 offset:2048\n"
    "v_mfma_f32_16x16x32_bf16 a[136:139], v[192:195], a[176:179], a[136:139]\n"
    "v_mfma_f32_16x16x32_bf16 a[12:15], v[196:199], a[160:163], a[12:15]\n"
    "v_perm_b32 v192, v134, v130, s67\n"
    "v_perm_b32 v193, v142, v138, s67\n"
    "v_mfma_f32_16x16x32_bf16 a[44:47], v[196:199], a[164:167], a[44:47]\n"
    "v_perm_b32 v194, v150, v146, s67\n"
    "v_perm_b32 v195, v158, v154, s67\n"
    "v_mfma_f32_16x16x32_bf16 a[76:79], v[196:199], a[168:171], a[76:79]\n"
    "buffer_load_dwordx4 v[108:111], v208, s[36:39], s62 offen\n"
    "v_mfma_f32_16x16x32_bf16 a[108:111], v[196:199], a[172:175], a[108:111]\n"
    "ds_read_b128 a[188:191], v204 offset:4096\n"
    "v_mfma_f32_16x16x32_bf16 a[140:143], v[196:199], a[176:179], a[140:143]\n"
    "v_mfma_f32_16x16x32_bf16 a[16:19], v[192:195], a[160:163], a[16:19]\n"
    "v_perm_b32 v196, v134, v130, s68\n"
    "v_perm_b32 v197, v142, v138, s68\n"
    "v_mfma_f32_16x16x32_bf16 a[48:51], v[192:195], a[164:167], a[48:51]\n"
    "v_perm_b32 v198, v150, v146, s68\n"
    "v_perm_b32 v199, v158, v154, s68\n"
    "v_mfma_f32_16x16x32_bf16 a[80:83], v[192:195], a[168:171], a[80:83]\n"
    "buffer_load_dwordx4 v[112:115], v208, s[36:39], s63 offen\n"
    "v_mfma_f32_16x16x32_bf16 a[112:115], v[192:195], a[172:175], a[112:115]\n"
    "ds_read_b128 a[192:195], v204 offset:6144\n"
    "v_mfma_f32_16x16x32_bf16 a[144:147], v[192:195], a[176:179], a[144:147]\n"
    "v_mfma_f32_16x16x32_bf16 a[20:23], v[196:199], a[160:163], a[20:23]\n"
    "v_perm_b32 v192, v135, v131, s67\n"
    "v_perm_b32 v193, v143, v139, s67\n"
    "v_mfma_f32_16x16x32_bf16 a[52:55], v[196:199], a[164:167], a[52:55]\n"
    "v_perm_b32 v194, v151, v147, s67\n"
    "v_perm_b32 v195, v159, v155, s67\n"
    "v_mfma_f32_16x16x32_bf16 a[84:87], v[196:199], a[168:171], a[84:87]\n"
    "buffer_load_dwordx4 v[116:119], v208, s[36:39], s64 offen\n"
    "v_mfma_f32_16x16x32_bf16 a[116:119], v[196:199], a[172:175], a[116:119]\n"
    "ds_read_b128 a[196:199], v204 offset:40960\n"
    "v_mfma_f32_16x16x32_bf16 a[148:151], v[196:199], a[176:179], a[148:151]\n"
    "v_mfma_f32_16x16x32_bf16 a[24:27], v[192:195], a[160:163], a[24:27]\n"
    "v_perm_b32 v196, v135, v131, s68\n"
    "v_perm_b32 v197, v143, v139, s68\n"
    "v_mfma_f32_16x16x32_bf16 a[56:59], v[192:195], a[164:167], a[56:59]\n"
    "v_perm_b32 v198, v151, v147, s68\n"
    "v_perm_b32 v199, v159, v155, s68\n"
    "v_mfma_f32_16x16x32_bf16 a[88:91], v[192:195], a[168:171], a[88:91]\n"
    "buffer_load_dwordx4 v[120:123], v208, s[36:39], s65 offen\n"
    "v_mfma_f32_16x16x32_bf16 a[120:123], v[192:195], a[172:175], a[120:123]\n"
    "v_mfma_f32_16x16x32_bf16 a[152:155], v[192:195], a[176:179], a[152:155]\n"
    "s_waitcnt vmcnt(15)\n"
    "v_mfma_f32_16x16x32_bf16 a[28:31], v[196:199], a[160:163], a[28:31]\n"
    "v_perm_b32 v192, v164, v160, s67\n"
    "v_perm_b32 v193, v172, v168, s67\n"
    "v_mfma_f32_16x16x32_bf16 a[60:63], v[196:199], a[164:167], a[60:63]\n"
    "v_perm_b32 v194, v180, v176, s67\n"
    "v_perm_b32 v195, v188, v184, s67\n"
    "v_mfma_f32_16x16x32_bf16 a[92:95], v[196:199], a[168:171], a[92:95]\n"
    "buffer_load_dwordx4 v[124:127], v208, s[36:39], s66 offen\n"
    "v_mfma_f32_16x16x32_bf16 a[124:127], v[196:199], a[172:175], a[124:127]\n"
    "v_add_u32 v201, s73, v200\n"
    "v_mfma_f32_16x16x32_bf16 a[156:159], v[196:199], a[176:179], a[156:159]\n"
    "s_waitcnt lgkmcnt(0)\n"
    "s_add_u32 s73, s73, 0x40000\n"
    "s_cmp_eq_u32 s73, 0x480000\n"
    "s_cselect_b32 s73, 0, s73\n"
    "v_mfma_f32_16x16x32_bf16 a[0:3], v[192:195], a[180:183], a[0:3]\n"
    "v_perm_b32 v196, v164, v160, s68\n"
    "v_perm_b32 v197, v172, v168, s68\n"
    "v_mfma_f32_16x16x32_bf16 a[32:35], v[192:195], a[184:187], a[32:35]\n"
    "v_perm_b32 v198, v180, v176, s68\n"
    "v_perm_b32 v199, v188, v184, s68\n"
    "v_mfma_f32_16x16x32_bf16 a[64:67], v[192:195], a[188:191], a[64:67]\n"
    "buffer_load_dwordx4 v[128:131], v201, s[36:39], 0 offen\n"
    "v_mfma_f32_16x16x32_bf16 a[96:99], v[192:195], a[192:195], a[96:99]\n"
    "v_mfma_f32_16x16x32_bf16 a[128:131], v[192:195], a[196:199], a[128:131]\n"
    "v_mfma_f32_16x16x32_bf16 a[4:7], v[196:199], a[180:183], a[4:7]\n"
    "v_perm_b32 v192, v165, v161, s67\n"
    "v_perm_b32 v193, v173, v169, s67\n"
    "v_mfma_f32_16x16x32_bf16 a[36:39], v[196:199], a[184:187], a[36:39]\n"
    "v_perm_b32 v194, v181, v177, s67\n"
    "v_perm_b32 v195, v189, v185, s67\n"
    "v_mfma_f32_16x16x32_bf16 a[68:71], v[196:199], a[188:191], a[68:71]\n"
    "buffer_load_dwordx4 v[132:135], v201, s[36:39], s60 offen\n"
    "v_mfma_f32_16x16x32_bf16 a[100:103], v[196:199], a[192:195], a[100:103]\n"
    "ds_read_b128 a[160:163], v202 offset:43008\n"
    "v_mfma_f32_16x16x32_bf16 a[132:135], v[196:199], a[196:199], a[132:135]\n"
    "v_mfma_f32_16x16x32_bf16 a[8:11], v[192:195], a[180:183], a[8:11]\n"
    "v_perm_b32 v196, v165, v161, s68\n"
    "v_perm_b32 v197, v173, v169, s68\n"
    "B_TH_FIRST2:\n"
    "B_TH_S2:\n"
    "s_cbranch_execz B_TH_RET\n"
    "v_mfma_f32_16x16x32_bf16 a[40:43], v[192:195], a[184:187], a[40:43]\n"
    "v_perm_b32 v198, v181, v177, s68\n"
    "v_perm_b32 v199, v189, v185, s68\n"
    "v_mfma_f32_16x16x32_bf16 a[72:75], v[192:195], a[188:191], a[72:75]\n"
    "buffer_load_dwordx4 v[136:139], v201, s[36:39], s61 offen\n"
    "v_mfma_f32_16x16x32_bf16 a[104:107], v[192:195], a[192:195], a[104:107]\n"
    "ds_read_b128 a[164:167], v202 offset:45056\n"
    "v_mfma_f32_16x16x32_bf16 a[136:139], v[192:195], a[196:199], a[136:139]\n"
    "v_mfma_f32_16x16x32_bf16 a[12:15], v[196:199], a[180:183], a[12:15]\n"
    "v_perm_b32 v192, v166, v162, s67\n"
    "v_perm_b32 v193, v174, v170, s67\n"
    "v_mfma_f32_16x16x32_bf16 a[44:47], v[196:199], a[184:187], a[44:47]\n"
    "v_perm_b32 v194, v182, v178, s67\n"
    "v_perm_b32 v195, v190, v186, s67\n"
    "v_mfma_f32_16x16x32_bf16 a[76:79], v[196:199], a[188:191], a[76:79]\n"
    "buffer_load_dwordx4 v[140:143], v201, s[36:39], s62 offen\n"
    "v_mfma_f32_16x16x32_bf16 a[108:111], v[196:199], a[192:195], a[108:111]\n"
    "ds_read_b128 a[168:171], v202 offset:47104\n"
    "v_mfma_f32_16x16x32_bf16 a[140:143], v[196:199], a[196:199], a[140:143]\n"
    "v_mfma_f32_16x16x32_bf16 a[16:19], v[192:195], a[180:183], a[16:19]\n"
    "v_perm_b32 v196, v166, v162, s68\n"
    "v_perm_b32 v197, v174, v170, s68\n"
    "v_mfma_f32_16x16x32_bf16 a[48:51], v[192:195], a[184:187], a[48:51]\n"
    "v_perm_b32 v198, v182, v178, s68\n"
    "v_perm_b32 v199, v190, v186, s68\n"
    "v_mfma_f32_16x16x32_bf16 a[80:83], v[192:195], a[188:191], a[80:83]\n"
    "buffer_load_dwordx4 v[144:147], v201, s[36:39], s63 offen\n"
    "v_mfma_f32_16x16x32_bf16 a[112:115], v[192:195], a[192:195], a[112:115]\n"
    "ds_read_b128 a[172:175], v203 offset:0\n"
    "v_mfma_f32_16x16x32_bf16 a[144:147], v[192:195], a[196:199], a[144:147]\n"
    "v_mfma_f32_16x16x32_bf16 a[20:23], v[196:199], a[180:183], a[20:23]\n"
    "v_perm_b32 v192, v167, v163, s67\n"
    "v_perm_b32 v193, v175, v171, s67\n"
    "v_mfma_f32_16x16x32_bf16 a[52:55], v[196:199], a[184:187], a[52:55]\n"
    "v_perm_b32 v194, v183, v179, s67\n"
    "v_perm_b32 v195, v191, v187, s67\n"
    "v_mfma_f32_16x16x32_bf16 a[84:87], v[196:199], a[188:191], a[84:87]\n"
    "buffer_load_dwordx4 v[148:151], v201, s[36:39], s64 offen\n"
    "v_mfma_f32_16x16x32_bf16 a[116:119], v[196:199], a[192:195], a[116:119]\n"
    "ds_read_b128 a[176:179], v203 offset:2048\n"
    "v_mfma_f32_16x16x32_bf16 a[148:151], v[196:199], a[196:199], a[148:151]\n"
    "v_mfma_f32_16x16x32_bf16 a[24:27], v[192:195], a[180:183], a[24:27]\n"
    "v_perm_b32 v196, v167, v163, s68\n"
    "v_perm_b32 v197, v175, v171, s68\n"
    "v_mfma_f32_16x16x32_bf16 a[56:59], v[192:195], a[184:187], a[56:59]\n"
    "v_perm_b32 v198, v183, v179, s68\n"
    "v_perm_b32 v199, v191, v187, s68\n"
    "v_mfma_f32_16x16x32_bf16 a[88:91], v[192:195], a[188:191], a[88:91]\n"
    "buffer_load_dwordx4 v[152:155], v201, s[36:39], s65 offen\n"
    "v_mfma_f32_16x16x32_bf16 a[120:123], v[192:195], a[192:195], a[120:123]\n"
    "v_mfma_f32_16x16x32_bf16 a[152:155], v[192:195], a[196:199], a[152:155]\n"
    "s_waitcnt vmcnt(15)\n"
    "v_mfma_f32_16x16x32_bf16 a[28:31], v[196:199], a[180:183], a[28:31]\n"
    "v_perm_b32 v192, v68, v64, s67\n"
    "v_perm_b32 v193, v76, v72, s67\n"
    "v_mfma_f32_16x16x32_bf16 a[60:63], v[196:199], a[184:187], a[60:63]\n"
    "v_perm_b32 v194, v84, v80, s67\n"
    "v_perm_b32 v195, v92, v88, s67\n"
    "v_mfma_f32_16x16x32_bf16 a[92:95], v[196:199], a[188:191], a[92:95]\n"
    "buffer_load_dwordx4 v[156:159], v201, s[36:39], s66 offen\n"
    "v_mfma_f32_16x16x32_bf16 a[124:127], v[196:199], a[192:195], a[124:127]\n"
    "v_add_u32 v208, s73, v200\n"
    "v_mfma_f32_16x16x32_bf16 a[156:159], v[196:199], a[196:199], a[156:159]\n"
    "s_waitcnt lgkmcnt(0)\n"
    "s_add_u32 s73, s73, 0x40000\n"
    "s_cmp_eq_u32 s73, 0x480000\n"
    "s_cselect_b32 s73, 0, s73\n"
    "v_mfma_f32_16x16x32_bf16 a[0:3], v[192:195], a[160:163], a[0:3]\n"
    "v_perm_b32 v196, v68, v64, s68\n"
    "v_perm_b32 v197, v76, v72, s68\n"
    "v_mfma_f32_16x16x32_bf16 a[32:35], v[192:195], a[164:167], a[32:35]\n"
    "v_perm_b32 v198, v84, v80, s68\n"
    "v_perm_b32 v199, v92, v88, s68\n"
    "v_mfma_f32_16x16x32_bf16 a[64:67], v[192:195], a[168:171], a[64:67]\n"
    "buffer_load_dwordx4 v[160:163], v208, s[36:39], 0 offen\n"
    "v_mfma_f32_16x16x32_bf16 a[96:99], v[192:195], a[172:175], a[96:99]\n"
    "v_mfma_f32_16x16x32_bf16 a[128:131], v[192:195], a[176:179], a[128:131]\n"
    "v_mfma_f32_16x16x32_bf16 a[4:7], v[196:199], a[160:163], a[4:7]\n"
    "v_perm_b32 v192, v69, v65, s67\n"
    "v_perm_b32 v193, v77, v73, s67\n"
    "v_mfma_f32_16x16x32_bf16 a[36:39], v[196:199], a[164:167], a[36:39]\n"
    "v_perm_b32 v194, v85, v81, s67\n"
    "v_perm_b32 v195, v93, v89, s67\n"
    "v_mfma_f32_16x16x32_bf16 a[68:71], v[196:199], a[168:171], a[68:71]\n"
    "buffer_load_dwordx4 v[164:167], v208, s[36:39], s60 offen\n"
    "v_mfma_f32_16x16x32_bf16 a[100:103], v[196:199], a[172:175], a[100:103]\n"
    "ds_read_b128 a[180:183], v204 offset:43008\n"
    "v_mfma_f32_16x16x32_bf16 a[132:135], v[196:199], a[176:179], a[132:135]\n"
    "v_mfma_f32_16x16x32_bf16 a[8:11], v[192:195], a[160:163], a[8:11]\n"
    "v_perm_b32 v196, v69, v65, s68\n"
    "v_perm_b32 v197, v77, v73, s68\n"
    "v_mfma_f32_16x16x32_bf16 a[40:43], v[192:195], a[164:167], a[40:43]\n"
    "v_perm_b32 v198, v85, v81, s68\n"
    "v_perm_b32 v199, v93, v89, s68\n"
    "v_mfma_f32_16x16x32_bf16 a[72:75], v[192:195], a[168:171], a[72:75]\n"
    "buffer_load_dwordx4 v[168:171], v208, s[36:39], s61 offen\n"
    "v_mfma_f32_16x16x32_bf16 a[104:107], v[192:195], a[172:175], a[104:107]\n"
    "ds_read_b128 a[184:187], v204 offset:45056\n"
    "v_mfma_f32_16x16x32_bf16 a[136:139], v[192:195], a[176:179], a[136:139]\n"
    "v_mfma_f32_16x16x32_bf16 a[12:15], v[196:199], a[160:163], a[12:15]\n"
    "v_perm_b32 v192, v70, v66, s67\n"
    "v_perm_b32 v193, v78, v74, s67\n"
    "v_mfma_f32_16x16x32_bf16 a[44:47], v[196:199], a[164:167], a[44:47]\n"
    "v_perm_b32 v194, v86, v82, s67\n"
    "v_perm_b32 v195, v94, v90, s67\n"
    "v_mfma_f32_16x16x32_bf16 a[76:79], v[196:199], a[168:171], a[76:79]\n"
    "buffer_load_dwordx4 v[172:175], v208, s[36:39], s62 offen\n"
    "v_mfma_f32_16x16x32_bf16 a[108:111], v[196:199], a[172:175], a[108:111]\n"
    "ds_read_b128 a[188:191], v204 offset:47104\n"
    "v_mfma_f32_16x16x32_bf16 a[140:143], v[196:199], a[176:179], a[140:143]\n"
    "v_mfma_f32_16x16x32_bf16 a[16:19], v[192:195], a[160:163], a[16:19]\n"
    "v_perm_b32 v196, v70, v66, s68\n"
    "v_perm_b32 v197, v78, v74, s68\n"
    "v_mfma_f32_16x16x32_bf16 a[48:51], v[192:195], a[164:167], a[48:51]\n"
    "v_perm_b32 v198, v86, v82, s68\n"
    "v_perm_b32 v199, v94, v90, s68\n"
    "v_mfma_f32_16x16x32_bf16 a[80:83], v[192:195], a[168:171], a[80:83]\n"
    "buffer_load_dwordx4 v[176:179], v208, s[36:39], s63 offen\n"
    "v_mfma_f32_16x16x32_bf16 a[112:115], v[192:195], a[172:175], a[112:115]\n"
    "ds_read_b128 a[192:195], v205 offset:0\n"
    "v_mfma_f32_16x16x32_bf16 a[144:147], v[192:195], a[176:179], a[144:147]\n"
    "v_mfma_f32_16x16x32_bf16 a[20:23], v[196:199], a[160:163], a[20:23]\n"
    "v_perm_b32 v192, v71, v67, s67\n"
    "v_perm_b32 v193, v79, v75, s67\n"
    "v_mfma_f32_16x16x32_bf16 a[52:55], v[196:199], a[164:167], a[52:55]\n"
    "v_perm_b32 v194, v87, v83, s67\n"
    "v_perm_b32 v195, v95, v91, s67\n"
    "v_mfma_f32_16x16x32_bf16 a[84:87], v[196:199], a[168:171], a[84:87]\n"
    "buffer_load_dwordx4 v[180:183], v208, s[36:39], s64 offen\n"
    "v_mfma_f32_16x16x32_bf16 a[116:119], v[196:199], a[172:175], a[116:119]\n"
    "ds_read_b128 a[196:199], v205 offset:2048\n"
    "v_mfma_f32_16x16x32_bf16 a[148:151], v[196:199], a[176:179], a[148:151]\n"
    "v_mfma_f32_16x16x32_bf16 a[24:27], v[192:195], a[160:163], a[24:27]\n"
    "v_perm_b32 v196, v71, v67, s68\n"
    "v_perm_b32 v197, v79, v75, s68\n"
    "v_mfma_f32_16x16x32_bf16 a[56:59], v[192:195], a[164:167], a[56:59]\n"
    "v_perm_b32 v198, v87, v83, s68\n"
    "v_perm_b32 v199, v95, v91, s68\n"
    "v_mfma_f32_16x16x32_bf16 a[88:91], v[192:195], a[168:171], a[88:91]\n"
    "buffer_load_dwordx4 v[184:187], v208, s[36:39], s65 offen\n"
    "v_mfma_f32_16x16x32_bf16 a[120:123], v[192:195], a[172:175], a[120:123]\n"
    "v_mfma_f32_16x16x32_bf16 a[152:155], v[192:195], a[176:179], a[152:155]\n"
    "s_waitcnt vmcnt(15)\n"
    "v_mfma_f32_16x16x32_bf16 a[28:31], v[196:199], a[160:163], a[28:31]\n"
    "v_perm_b32 v192, v100, v96, s67\n"
    "v_perm_b32 v193, v108, v104, s67\n"
    "v_mfma_f32_16x16x32_bf16 a[60:63], v[196:199], a[164:167], a[60:63]\n"
    "v_perm_b32 v194, v116, v112, s67\n"
    "v_perm_b32 v195, v124, v120, s67\n"
    "v_mfma_f32_16x16x32_bf16 a[92:95], v[196:199], a[168:171], a[92:95]\n"
    "buffer_load_dwordx4 v[188:191], v208, s[36:39], s66 offen\n"
    "v_mfma_f32_16x16x32_bf16 a[124:127], v[196:199], a[172:175], a[124:127]\n"
    "v_mfma_f32_16x16x32_bf16 a[156:159], v[196:199], a[176:179], a[156:159]\n"
    "s_waitcnt lgkmcnt(0)\n"
    "v_mfma_f32_16x16x32_bf16 a[0:3], v[192:195], a[180:183], a[0:3]\n"
    "v_perm_b32 v196, v100, v96, s68\n"
    "v_perm_b32 v197, v108, v104, s68\n"
    "v_mfma_f32_16x16x32_bf16 a[32:35], v[192:195], a[184:187], a[32:35]\n"
    "v_perm_b32 v198, v116, v112, s68\n"
    "v_perm_b32 v199, v124, v120, s68\n"
    "v_mfma_f32_16x16x32_bf16 a[64:67], v[192:195], a[188:191], a[64:67]\n"
    "v_mov_b32 v64, 0\n"
    "v_mov_b32 v77, 0\n"
    "v_mfma_f32_16x16x32_bf16 a[96:99], v[192:195], a[192:195], a[96:99]\n"
    "s_add_u32 m0, s71, 0x2000\n"
    "s_add_u32 s81, s75, 0\n"
    "s_and_b32 s81, s81, 0x1e000\n"
    "v_mov_b32 v76, 0\n"
    "v_mfma_f32_16x16x32_bf16 a[128:131], v[192:195], a[196:199], a[128:131]\n"
    "buffer_load_dwordx4 v226, s[40:43], s81 offen nt lds\n"
    "v_mov_b32 v78, 0\n"
    "v_mfma_f32_16x16x32_bf16 a[4:7], v[196:199], a[180:183], a[4:7]\n"
    "v_perm_b32 v192, v101, v97, s67\n"
    "v_perm_b32 v193, v109, v105, s67\n"
    "v_mfma_f32_16x16x32_bf16 a[36:39], v[196:199], a[184:187], a[36:39]\n"
    "v_perm_b32 v194, v117, v113, s67\n"
    "v_perm_b32 v195, v125, v121, s67\n"
    "v_mfma_f32_16x16x32_bf16 a[68:71], v[196:199], a[188:191], a[68:71]\n"
    "buffer_load_dwordx4 v227, s[40:43], s81 offen offset:1024 nt lds\n"
    "v_mov_b32 v79, 0\n"
    "v_mfma_f32_16x16x32_bf16 a[100:103], v[196:199], a[192:195], a[100:103]\n"
    "ds_read_b128 a[160:163], v203 offset:4096\n"
    "buffer_load_dwordx4 v228, s[40:43], s81 offen offset:2048 nt lds\n"
    "v_mfma_f32_16x16x32_bf16 a[132:135], v[196:199], a[196:199], a[132:135]\n"
    "buffer_load_dwordx4 v229, s[40:43], s81 offen offset:3072 nt lds\n"
    "v_mov_b32 v80, 0\n"
    "v_mfma_f32_16x16x32_bf16 a[8:11], v[192:195], a[180:183], a[8:11]\n"
    "v_perm_b32 v196, v101, v97, s68\n"
    "v_perm_b32 v197, v109, v105, s68\n"
    "v_mfma_f32_16x16x32_bf16 a[40:43], v[192:195], a[184:187], a[40:43]\n"
    "v_perm_b32 v198, v117, v113, s68\n"
    "v_perm_b32 v199, v125, v121, s68\n"
    "v_mfma_f32_16x16x32_bf16 a[72:75], v[192:195], a[188:191], a[72:75]\n"
    "s_add_u32 m0, s71, 0x3000\n"
    "s_add_u32 s81, s75, 0\n"
    "s_and_b32 s81, s81, 0x1e000\n"
    "s_add_u32 s81, s81, 0x1000\n"
    "v_mov_b32 v94, 0\n"
    "v_mfma_f32_16x16x32_bf16 a[104:107], v[192:195], a[192:195], a[104:107]\n"
    "ds_read_b128 a[164:167], v203 offset:6144\n"
    "buffer_load_dwordx4 v226, s[40:43], s81 offen nt lds\n"
    "v_mfma_f32_16x16x32_bf16 a[136:139], v[192:195], a[196:199], a[136:139]\n"
    "buffer_load_dwordx4 v227, s[40:43], s81 offen offset:1024 nt lds\n"
    "v_mov_b32 v81, 0\n"
    "v_mfma_f32_16x16x32_bf16 a[12:15], v[196:199], a[180:183], a[12:15]\n"
    "v_perm_b32 v192, v102, v98, s67\n"
    "v_perm_b32 v193, v110, v106, s67\n"
    "v_mfma_f32_16x16x32_bf16 a[44:47], v[196:199], a[184:187], a[44:47]\n"
    "v_perm_b32 v194, v118, v114, s67\n"
    "v_perm_b32 v195, v126, v122, s67\n"
    "v_mfma_f32_16x16x32_bf16 a[76:79], v[196:199], a[188:191], a[76:79]\n"
    "buffer_load_dwordx4 v228, s[40:43], s81 offen offset:2048 nt lds\n"
    "v_mov_b32 v82, 0\n"
    "v_mfma_f32_16x16x32_bf16 a[108:111], v[196:199], a[192:195], a[108:111]\n"
    "ds_read_b128 a[168:171], v203 offset:40960\n"
    "buffer_load_dwordx4 v229, s[40:43], s81 offen offset:3072 nt lds\n"
    "v_mfma_f32_16x16x32_bf16 a[140:143], v[196:199], a[196:199], a[140:143]\n"
    "v_mov_b32 v65, 0\n"
    "v_mov_b32 v83, 0\n"
    "v_mfma_f32_16x16x32_bf16 a[16:19], v[192:195], a[180:183], a[16:19]\n"
    "v_perm_b32 v196, v102, v98, s68\n"
    "v_perm_b32 v197, v110, v106, s68\n"
    "v_mfma_f32_16x16x32_bf16 a[48:51], v[192:195], a[184:187], a[48:51]\n"
    "v_perm_b32 v198, v118, v114, s68\n"
    "v_perm_b32 v199, v126, v122, s68\n"
    "v_mfma_f32_16x16x32_bf16 a[80:83], v[192:195], a[188:191], a[80:83]\n"
    "v_mov_b32 v66, 0\n"
    "v_mov_b32 v84, 0\n"
    "v_mfma_f32_16x16x32_bf16 a[112:115], v[192:195], a[192:195], a[112:115]\n"
    "ds_read_b128 a[172:175], v203 offset:43008\n"
    "v_mov_b32 v85, 0\n"
    "v_mfma_f32_16x16x32_bf16 a[144:147], v[192:195], a[196:199], a[144:147]\n"
    "v_mov_b32 v67, 0\n"
    "v_mov_b32 v86, 0\n"
    "v_mfma_f32_16x16x32_bf16 a[20:23], v[196:199], a[180:183], a[20:23]\n"
    "v_perm_b32 v192, v103, v99, s67\n"
    "v_perm_b32 v193, v111, v107, s67\n"
    "v_mfma_f32_16x16x32_bf16 a[52:55], v[196:199], a[184:187], a[52:55]\n"
    "v_perm_b32 v194, v119, v115, s67\n"
    "v_perm_b32 v195, v127, v123, s67\n"
    "v_mfma_f32_16x16x32_bf16 a[84:87], v[196:199], a[188:191], a[84:87]\n"
    "v_mov_b32 v68, 0\n"
    "v_mov_b32 v87, 0\n"
    "v_mfma_f32_16x16x32_bf16 a[116:119], v[196:199], a[192:195], a[116:119]\n"
    "ds_read_b128 a[176:179], v203 offset:45056\n"
    "v_mov_b32 v88, 0\n"
    "v_mfma_f32_16x16x32_bf16 a[148:151], v[196:199], a[196:199], a[148:151]\n"
    "v_mov_b32 v69, 0\n"
    "v_mov_b32 v89, 0\n"
    "v_mfma_f32_16x16x32_bf16 a[24:27], v[192:195], a[180:183], a[24:27]\n"
    "v_perm_b32 v196, v103, v99, s68\n"
    "v_perm_b32 v197, v111, v107, s68\n"
    "v_mfma_f32_16x16x32_bf16 a[56:59], v[192:195], a[184:187], a[56:59]\n"
    "v_perm_b32 v198, v119, v115, s68\n"
    "v_perm_b32 v199, v127, v123, s68\n"
    "v_mfma_f32_16x16x32_bf16 a[88:91], v[192:195], a[188:191], a[88:91]\n"
    "v_mov_b32 v70, 0\n"
    "v_mov_b32 v90, 0\n"
    "v_mfma_f32_16x16x32_bf16 a[120:123], v[192:195], a[192:195], a[120:123]\n"
    "v_mov_b32 v71, 0\n"
    "v_mov_b32 v91, 0\n"
    "v_mfma_f32_16x16x32_bf16 a[152:155], v[192:195], a[196:199], a[152:155]\n"
    "s_waitcnt vmcnt(16)\n"
    "v_mov_b32 v74, 0\n"
    "v_mov_b32 v95, 0\n"
    "v_mfma_f32_16x16x32_bf16 a[28:31], v[196:199], a[180:183], a[28:31]\n"
    "v_perm_b32 v192, v132, v128, s67\n"
    "v_perm_b32 v193, v140, v136, s67\n"
    "v_mfma_f32_16x16x32_bf16 a[60:63], v[196:199], a[184:187], a[60:63]\n"
    "v_perm_b32 v194, v148, v144, s67\n"
    "v_perm_b32 v195, v156, v152, s67\n"
    "v_mfma_f32_16x16x32_bf16 a[92:95], v[196:199], a[188:191], a[92:95]\n"
    "v_mov_b32 v72, 0\n"
    "v_mov_b32 v92, 0\n"
    "v_mfma_f32_16x16x32_bf16 a[124:127], v[196:199], a[192:195], a[124:127]\n"
    "v_mov_b32 v73, 0\n"
    "v_mov_b32 v93, 0\n"
    "v_mfma_f32_16x16x32_bf16 a[156:159], v[196:199], a[196:199], a[156:159]\n"
    "s_waitcnt lgkmcnt(0)\n"
    "v_mov_b32 v75, 0\n"
    "v_mfma_f32_16x16x32_bf16 a[0:3], v[192:195], a[160:163], a[0:3]\n"
    "v_perm_b32 v196, v132, v128, s68\n"
    "v_perm_b32 v197, v140, v136, s68\n"
    "v_mfma_f32_16x16x32_bf16 a[32:35], v[192:195], a[164:167], a[32:35]\n"
    "v_perm_b32 v198, v148, v144, s68\n"
    "v_perm_b32 v199, v156, v152, s68\n"
    "v_mfma_f32_16x16x32_bf16 a[64:67], v[192:195], a[168:171], a[64:67]\n"
    "v_mov_b32 v96, 0\n"
    "v_mov_b32 v113, 0\n"
    "v_mfma_f32_16x16x32_bf16 a[96:99], v[192:195], a[172:175], a[96:99]\n"
    "v_mov_b32 v97, 0\n"
    "v_mov_b32 v114, 0\n"
    "v_mfma_f32_16x16x32_bf16 a[128:131], v[192:195], a[176:179], a[128:131]\n"
    "v_mov_b32 v98, 0\n"
    "v_mov_b32 v115, 0\n"
    "v_mfma_f32_16x16x32_bf16 a[4:7], v[196:199], a[160:163], a[4:7]\n"
    "v_perm_b32 v192, v133, v129, s67\n"
    "v_perm_b32 v193, v141, v137, s67\n"
    "v_mfma_f32_16x16x32_bf16 a[36:39], v[196:199], a[164:167], a[36:39]\n"
    "v_perm_b32 v194, v149, v145, s67\n"
    "v_perm_b32 v195, v157, v153, s67\n"
    "v_mfma_f32_16x16x32_bf16 a[68:71], v[196:199], a[168:171], a[68:71]\n"
    "s_add_u32 m0, s71, 0\n"
    "s_add_u32 s81, s75, 0\n"
    "s_and_b32 s81, s81, 0x1e000\n"
    "v_mov_b32 v112, 0\n"
    "v_mfma_f32_16x16x32_bf16 a[100:103], v[196:199], a[172:175], a[100:103]\n"
    "ds_read_b128 a[180:183], v205 offset:4096\n"
    "v_mov_b32 v116, 0\n"
    "v_mfma_f32_16x16x32_bf16 a[132:135], v[196:199], a[176:179], a[132:135]\n"
    "v_mov_b32 v99, 0\n"
    "v_mov_b32 v117, 0\n"
    "v_mfma_f32_16x16x32_bf16 a[8:11], v[192:195], a[160:163], a[8:11]\n"
    "v_perm_b32 v196, v133, v129, s68\n"
    "v_perm_b32 v197, v141, v137, s68\n"
    "v_mfma_f32_16x16x32_bf16 a[40:43], v[192:195], a[164:167], a[40:43]\n"
    "v_perm_b32 v198, v149, v145, s68\n"
    "v_perm_b32 v199, v157, v153, s68\n"
    "v_mfma_f32_16x16x32_bf16 a[72:75], v[192:195], a[168:171], a[72:75]\n"
    "v_mov_b32 v100, 0\n"
    "v_mov_b32 v118, 0\n"
    "v_mfma_f32_16x16x32_bf16 a[104:107], v[192:195], a[172:175], a[104:107]\n"
    "ds_read_b128 a[184:187], v205 offset:6144\n"
    "v_mov_b32 v119, 0\n"
    "v_mfma_f32_16x16x32_bf16 a[136:139], v[192:195], a[176:179], a[136:139]\n"
    "v_mov_b32 v101, 0\n"
    "v_mov_b32 v120, 0\n"
    "v_mfma_f32_16x16x32_bf16 a[12:15], v[196:199], a[160:163], a[12:15]\n"
    "v_perm_b32 v192, v134, v130, s67\n"
    "v_perm_b32 v193, v142, v138, s67\n"
    "v_mfma_f32_16x16x32_bf16 a[44:47], v[196:199], a[164:167], a[44:47]\n"
    "v_perm_b32 v194, v150, v146, s67\n"
    "v_perm_b32 v195, v158, v154, s67\n"
    "v_mfma_f32_16x16x32_bf16 a[76:79], v[196:199], a[168:171], a[76:79]\n"
    "buffer_load_dwordx4 v226, s[44:47], s81 offen nt lds\n"
    "v_mov_b32 v121, 0\n"
    "v_mfma_f32_16x16x32_bf16 a[108:111], v[196:199], a[172:175], a[108:111]\n"
    "ds_read_b128 a[188:191], v205 offset:40960\n"
    "v_mov_b32 v122, 0\n"
    "v_mfma_f32_16x16x32_bf16 a[140:143], v[196:199], a[176:179], a[140:143]\n"
    "v_mov_b32 v102, 0\n"
    "v_mov_b32 v123, 0\n"
    "v_mfma_f32_16x16x32_bf16 a[16:19], v[192:195], a[160:163], a[16:19]\n"
    "v_perm_b32 v196, v134, v130, s68\n"
    "v_perm_b32 v197, v142, v138, s68\n"
    "v_mfma_f32_16x16x32_bf16 a[48:51], v[192:195], a[164:167], a[48:51]\n"
    "v_perm_b32 v198, v150, v146, s68\n"
    "v_perm_b32 v199, v158, v154, s68\n"
    "v_mfma_f32_16x16x32_bf16 a[80:83], v[192:195], a[168:171], a[80:83]\n"
    "v_mov_b32 v103, 0\n"
    "v_mov_b32 v124, 0\n"
    "v_mfma_f32_16x16x32_bf16 a[112:115], v[192:195], a[172:175], a[112:115]\n"
    "ds_read_b128 a[192:195], v205 offset:43008\n"
    "v_mov_b32 v125, 0\n"
    "v_mfma_f32_16x16x32_bf16 a[144:147], v[192:195], a[176:179], a[144:147]\n"
    "v_mov_b32 v104, 0\n"
    "v_mov_b32 v126, 0\n"
    "v_mfma_f32_16x16x32_bf16 a[20:23], v[196:199], a[160:163], a[20:23]\n"
    "v_perm_b32 v192, v135, v131, s67\n"
    "v_perm_b32 v193, v143, v139, s67\n"
    "v_mfma_f32_16x16x32_bf16 a[52:55], v[196:199], a[164:167], a[52:55]\n"
    "v_perm_b32 v194, v151, v147, s67\n"
    "v_perm_b32 v195, v159, v155, s67\n"
    "v_mfma_f32_16x16x32_bf16 a[84:87], v[196:199], a[168:171], a[84:87]\n"
    "buffer_load_dwordx4 v227, s[44:47], s81 offen offset:1024 nt lds\n"
    "v_mov_b32 v127, 0\n"
    "v_mfma_f32_16x16x32_bf16 a[116:119], v[196:199], a[172:175], a[116:119]\n"
    "ds_read_b128 a[196:199], v205 offset:45056\n"
    "v_mfma_f32_16x16x32_bf16 a[148:151], v[196:199], a[176:179], a[148:151]\n"
    "v_mov_b32 v105, 0\n"
    "v_mfma_f32_16x16x32_bf16 a[24:27], v[192:195], a[160:163], a[24:27]\n"
    "v_perm_b32 v196, v135, v131, s68\n"
    "v_perm_b32 v197, v143, v139, s68\n"
    "v_mfma_f32_16x16x32_bf16 a[56:59], v[192:195], a[164:167], a[56:59]\n"
    "v_perm_b32 v198, v151, v147, s68\n"
    "v_perm_b32 v199, v159, v155, s68\n"
    "v_mfma_f32_16x16x32_bf16 a[88:91], v[192:195], a[168:171], a[88:91]\n"
    "v_mov_b32 v106, 0\n"
    "v_mfma_f32_16x16x32_bf16 a[120:123], v[192:195], a[172:175], a[120:123]\n"
    "v_mov_b32 v107, 0\n"
    "v_mfma_f32_16x16x32_bf16 a[152:155], v[192:195], a[176:179], a[152:155]\n"
    "s_waitcnt vmcnt(10)\n"
    "v_mov_b32 v110, 0\n"
    "v_mfma_f32_16x16x32_bf16 a[28:31], v[196:199], a[160:163], a[28:31]\n"
    "v_perm_b32 v192, v164, v160, s67\n"
    "v_perm_b32 v193, v172, v168, s67\n"
    "v_mfma_f32_16x16x32_bf16 a[60:63], v[196:199], a[164:167], a[60:63]\n"
    "v_perm_b32 v194, v180, v176, s67\n"
    "v_perm_b32 v195, v188, v184, s67\n"
    "v_mfma_f32_16x16x32_bf16 a[92:95], v[196:199], a[168:171], a[92:95]\n"
    "v_mov_b32 v108, 0\n"
    "v_mfma_f32_16x16x32_bf16 a[124:127], v[196:199], a[172:175], a[124:127]\n"
    "v_mov_b32 v109, 0\n"
    "v_mfma_f32_16x16x32_bf16 a[156:159], v[196:199], a[176:179], a[156:159]\n"
    "s_waitcnt lgkmcnt(0)\n"
    "v_mov_b32 v111, 0\n"
    "v_mfma_f32_16x16x32_bf16 a[0:3], v[192:195], a[180:183], a[0:3]\n"
    "v_perm_b32 v196, v164, v160, s68\n"
    "v_perm_b32 v197, v172, v168, s68\n"
    "v_mfma_f32_16x16x32_bf16 a[32:35], v[192:195], a[184:187], a[32:35]\n"
    "v_perm_b32 v198, v180, v176, s68\n"
    "v_perm_b32 v199, v188, v184, s68\n"
    "v_mfma_f32_16x16x32_bf16 a[64:67], v[192:195], a[188:191], a[64:67]\n"
    "s_barrier\n"
    "v_mov_b32 v150, 0\n"
    "v_mfma_f32_16x16x32_bf16 a[96:99], v[192:195], a[192:195], a[96:99]\n"
    "v_mov_b32 v128, 0\n"
    "v_mov_b32 v151, 0\n"
    "v_mfma_f32_16x16x32_bf16 a[128:131], v[192:195], a[196:199], a[128:131]\n"
    "v_mov_b32 v129, 0\n"
    "v_mov_b32 v152, 0\n"
    "v_mfma_f32_16x16x32_bf16 a[4:7], v[196:199], a[180:183], a[4:7]\n"
    "v_perm_b32 v192, v165, v161, s67\n"
    "v_perm_b32 v193, v173, v169, s67\n"
    "v_mfma_f32_16x16x32_bf16 a[36:39], v[196:199], a[184:187], a[36:39]\n"
    "v_perm_b32 v194, v181, v177, s67\n"
    "v_perm_b32 v195, v189, v185, s67\n"
    "v_mfma_f32_16x16x32_bf16 a[68:71], v[196:199], a[188:191], a[68:71]\n"
    "buffer_load_dwordx4 v228, s[44:47], s81 offen offset:2048 nt lds\n"
    "v_mov_b32 v153, 0\n"
    "v_mfma_f32_16x16x32_bf16 a[100:103], v[196:199], a[192:195], a[100:103]\n"
    "v_mov_b32 v130, 0\n"
    "v_mov_b32 v154, 0\n"
    "v_mfma_f32_16x16x32_bf16 a[132:135], v[196:199], a[196:199], a[132:135]\n"
    "v_mov_b32 v131, 0\n"
    "v_mov_b32 v155, 0\n"
    "v_mfma_f32_16x16x32_bf16 a[8:11], v[192:195], a[180:183], a[8:11]\n"
    "v_perm_b32 v196, v165, v161, s68\n"
    "v_perm_b32 v197, v173, v169, s68\n"
    "v_mfma_f32_16x16x32_bf16 a[40:43], v[192:195], a[184:187], a[40:43]\n"
    "v_perm_b32 v198, v181, v177, s68\n"
    "v_perm_b32 v199, v189, v185, s68\n"
    "v_mfma_f32_16x16x32_bf16 a[72:75], v[192:195], a[188:191], a[72:75]\n"
    "v_mov_b32 v132, 0\n"
    "v_mov_b32 v156, 0\n"
    "v_mfma_f32_16x16x32_bf16 a[104:107], v[192:195], a[192:195], a[104:107]\n"
    "v_mov_b32 v133, 0\n"
    "v_mov_b32 v157, 0\n"
    "v_mfma_f32_16x16x32_bf16 a[136:139], v[192:195], a[196:199], a[136:139]\n"
    "v_mov_b32 v134, 0\n"
    "v_mov_b32 v158, 0\n"
    "v_mfma_f32_16x16x32_bf16 a[12:15], v[196:199], a[180:183], a[12:15]\n"
    "v_perm_b32 v192, v166, v162, s67\n"
    "v_perm_b32 v193, v174, v170, s67\n"
    "v_mfma_f32_16x16x32_bf16 a[44:47], v[196:199], a[184:187], a[44:47]\n"
    "v_perm_b32 v194, v182, v178, s67\n"
    "v_perm_b32 v195, v190, v186, s67\n"
    "v_mfma_f32_16x16x32_bf16 a[76:79], v[196:199], a[188:191], a[76:79]\n"
    "buffer_load_dwordx4 v229, s[44:47], s81 offen offset:3072 nt lds\n"
    "v_mov_b32 v159, 0\n"
    "v_mfma_f32_16x16x32_bf16 a[108:111], v[196:199], a[192:195], a[108:111]\n"
    "v_mov_b32 v135, 0\n"
    "v_mfma_f32_16x16x32_bf16 a[140:143], v[196:199], a[196:199], a[140:143]\n"
    "v_mov_b32 v136, 0\n"
    "v_mfma_f32_16x16x32_bf16 a[16:19], v[192:195], a[180:183], a[16:19]\n"
    "v_perm_b32 v196, v166, v162, s68\n"
    "v_perm_b32 v197, v174, v170, s68\n"
    "v_mfma_f32_16x16x32_bf16 a[48:51], v[192:195], a[184:187], a[48:51]\n"
    "v_perm_b32 v198, v182, v178, s68\n"
    "v_perm_b32 v199, v190, v186, s68\n"
    "v_mfma_f32_16x16x32_bf16 a[80:83], v[192:195], a[188:191], a[80:83]\n"
    "v_mov_b32 v137, 0\n"
    "v_mfma_f32_16x16x32_bf16 a[112:115], v[192:195], a[192:195], a[112:115]\n"
    "v_mov_b32 v138, 0\n"
    "v_mfma_f32_16x16x32_bf16 a[144:147], v[192:195], a[196:199], a[144:147]\n"
    "v_mov_b32 v139, 0\n"
    "v_mfma_f32_16x16x32_bf16 a[20:23], v[196:199], a[180:183], a[20:23]\n"
    "v_perm_b32 v192, v167, v163, s67\n"
    "v_perm_b32 v193, v175, v171, s67\n"
    "v_mfma_f32_16x16x32_bf16 a[52:55], v[196:199], a[184:187], a[52:55]\n"
    "v_perm_b32 v194, v183, v179, s67\n"
    "v_perm_b32 v195, v191, v187, s67\n"
    "v_mfma_f32_16x16x32_bf16 a[84:87], v[196:199], a[188:191], a[84:87]\n"
    "s_add_u32 m0, s71, 0x1000\n"
    "s_add_u32 s81, s75, 0\n"
    "s_and_b32 s81, s81, 0x1e000\n"
    "s_add_u32 s81, s81, 0x1000\n"
    "v_mfma_f32_16x16x32_bf16 a[116:119], v[196:199], a[192:195], a[116:119]\n"
    "v_mov_b32 v140, 0\n"
    "v_mfma_f32_16x16x32_bf16 a[148:151], v[196:199], a[196:199], a[148:151]\n"
    "v_mov_b32 v141, 0\n"
    "v_mfma_f32_16x16x32_bf16 a[24:27], v[192:195], a[180:183], a[24:27]\n"
    "v_perm_b32 v196, v167, v163, s68\n"
    "v_perm_b32 v197, v175, v171, s68\n"
    "v_mfma_f32_16x16x32_bf16 a[56:59], v[192:195], a[184:187], a[56:59]\n"
    "v_perm_b32 v198, v183, v179, s68\n"
    "v_perm_b32 v199, v191, v187, s68\n"
    "v_mfma_f32_16x16x32_bf16 a[88:91], v[192:195], a[188:191], a[88:91]\n"
    "v_mov_b32 v142, 0\n"
    "v_mfma_f32_16x16x32_bf16 a[120:123], v[192:195], a[192:195], a[120:123]\n"
    "v_mov_b32 v143, 0\n"
    "v_mfma_f32_16x16x32_bf16 a[152:155], v[192:195], a[196:199], a[152:155]\n"
    "v_mov_b32 v144, 0\n"
    "v_mfma_f32_16x16x32_bf16 a[28:31], v[196:199], a[180:183], a[28:31]\n"
    "v_mov_b32 v145, 0\n"
    "v_mfma_f32_16x16x32_bf16 a[60:63], v[196:199], a[184:187], a[60:63]\n"
    "v_mov_b32 v146, 0\n"
    "v_mfma_f32_16x16x32_bf16 a[92:95], v[196:199], a[188:191], a[92:95]\n"
    "v_mov_b32 v147, 0\n"
    "v_mfma_f32_16x16x32_bf16 a[124:127], v[196:199], a[192:195], a[124:127]\n"
    "v_mov_b32 v148, 0\n"
    "v_mfma_f32_16x16x32_bf16 a[156:159], v[196:199], a[196:199], a[156:159]\n"
    "v_mov_b32 v149, 0\n"
    "s_nop 7\n"
    "v_accvgpr_read_b32 v160, a0\n"
    "v_accvgpr_read_b32 v161, a4\n"
    "v_accvgpr_read_b32 v162, a8\n"
    "v_accvgpr_read_b32 v163, a12\n"
    "v_accvgpr_read_b32 v164, a16\n"
    "v_accvgpr_read_b32 v165, a20\n"
    "v_accvgpr_read_b32 v166, a24\n"
    "v_accvgpr_read_b32 v167, a28\n"
    "v_cvt_pk_bf16_f32 v168, v160, v161\n"
    "v_cvt_pk_bf16_f32 v169, v162, v163\n"
    "v_cvt_pk_bf16_f32 v170, v164, v165\n"
    "v_cvt_pk_bf16_f32 v171, v166, v167\n"
    "v_accvgpr_write_b32 a160, v168\n"
    "v_accvgpr_write_b32 a161, v169\n"
    "v_accvgpr_write_b32 a162, v170\n"
    "v_accvgpr_write_b32 a163, v171\n"
    "buffer_load_dwordx4 v226, s[44:47], s81 offen nt lds\n"
    "v_accvgpr_read_b32 v172, a1\n"
    "v_accvgpr_read_b32 v173, a5\n"
    "v_accvgpr_read_b32 v174, a9\n"
    "v_accvgpr_read_b32 v175, a13\n"
    "v_accvgpr_read_b32 v176, a17\n"
    "v_accvgpr_read_b32 v177, a21\n"
    "v_accvgpr_read_b32 v178, a25\n"
    "v_accvgpr_read_b32 v179, a29\n"
    "v_cvt_pk_bf16_f32 v180, v172, v173\n"
    "v_cvt_pk_bf16_f32 v181, v174, v175\n"
    "v_cvt_pk_bf16_f32 v182, v176, v177\n"
    "v_cvt_pk_bf16_f32 v183, v178, v179\n"
    "v_accvgpr_write_b32 a164, v180\n"
    "v_accvgpr_write_b32 a165, v181\n"
    "v_accvgpr_write_b32 a166, v182\n"
    "v_accvgpr_write_b32 a167, v183\n"
    "buffer_load_dwordx4 v227, s[44:47], s81 offen offset:1024 nt lds\n"
    "v_accvgpr_read_b32 v160, a2\n"
    "v_accvgpr_read_b32 v161, a6\n"
    "v_accvgpr_read_b32 v162, a10\n"
    "v_accvgpr_read_b32 v163, a14\n"
    "v_accvgpr_read_b32 v164, a18\n"
    "v_accvgpr_read_b32 v165, a22\n"
    "v_accvgpr_read_b32 v166, a26\n"
    "v_accvgpr_read_b32 v167, a30\n"
    "v_cvt_pk_bf16_f32 v168, v160, v161\n"
    "v_cvt_pk_bf16_f32 v169, v162, v163\n"
    "v_cvt_pk_bf16_f32 v170, v164, v165\n"
    "v_cvt_pk_bf16_f32 v171, v166, v167\n"
    "v_accvgpr_write_b32 a168, v168\n"
    "v_accvgpr_write_b32 a169, v169\n"
    "v_accvgpr_write_b32 a170, v170\n"
    "v_accvgpr_write_b32 a171, v171\n"
    "buffer_load_dwordx4 v228, s[44:47], s81 offen offset:2048 nt lds\n"
    "v_accvgpr_read_b32 v172, a3\n"
    "v_accvgpr_read_b32 v173, a7\n"
    "v_accvgpr_read_b32 v174, a11\n"
    "v_accvgpr_read_b32 v175, a15\n"
    "v_accvgpr_read_b32 v176, a19\n"
    "v_accvgpr_read_b32 v177, a23\n"
    "v_accvgpr_read_b32 v178, a27\n"
    "v_accvgpr_read_b32 v179, a31\n"
    "v_cvt_pk_bf16_f32 v180, v172, v173\n"
    "v_cvt_pk_bf16_f32 v181, v174, v175\n"
    "v_cvt_pk_bf16_f32 v182, v176, v177\n"
    "v_cvt_pk_bf16_f32 v183, v178, v179\n"
    "v_accvgpr_write_b32 a172, v180\n"
    "B_TH_FIRST3:\n"
    "B_TH_S3:\n"
    "s_cbranch_execz B_TH_RET\n"
    "v_accvgpr_write_b32 a173, v181\n"
    "v_accvgpr_write_b32 a174, v182\n"
    "v_accvgpr_write_b32 a175, v183\n"
    "s_waitcnt vmcnt(7)\n"
    "ds_read_b128 a[0:3], v214 offset:8192\n"
    "ds_read_b128 a[4:7], v215 offset:8192\n"
    "ds_read_b128 a[8:11], v216 offset:8192\n"
    "ds_read_b128 a[12:15], v217 offset:8192\n"
    "ds_read_b128 a[16:19], v214 offset:12288\n"
    "ds_read_b128 a[20:23], v215 offset:12288\n"
    "ds_read_b128 a[24:27], v216 offset:12288\n"
    "ds_read_b128 a[28:31], v217 offset:12288\n"
    "buffer_load_dwordx4 v229, s[44:47], s81 offen offset:3072 nt lds\n"
    "v_accvgpr_read_b32 v160, a32\n"
    "v_accvgpr_read_b32 v161, a36\n"
    "v_accvgpr_read_b32 v162, a40\n"
    "v_accvgpr_read_b32 v163, a44\n"
    "v_accvgpr_read_b32 v164, a48\n"
    "v_accvgpr_read_b32 v165, a52\n"
    "v_accvgpr_read_b32 v166, a56\n"
    "v_accvgpr_read_b32 v167, a60\n"
    "v_cvt_pk_bf16_f32 v168, v160, v161\n"
    "v_cvt_pk_bf16_f32 v169, v162, v163\n"
    "v_cvt_pk_bf16_f32 v170, v164, v165\n"
    "v_cvt_pk_bf16_f32 v171, v166, v167\n"
    "v_accvgpr_write_b32 a176, v168\n"
    "v_accvgpr_write_b32 a177, v169\n"
    "v_accvgpr_write_b32 a178, v170\n"
    "v_accvgpr_write_b32 a179, v171\n"
    "s_add_u32 m0, s71, 0x6000\n"
    "s_add_u32 s81, s75, 0x2000\n"
    "s_and_b32 s81, s81, 0x1e000\n"
    "v_accvgpr_read_b32 v172, a33\n"
    "v_accvgpr_read_b32 v173, a37\n"
    "v_accvgpr_read_b32 v174, a41\n"
    "v_accvgpr_read_b32 v175, a45\n"
    "v_accvgpr_read_b32 v176, a49\n"
    "v_accvgpr_read_b32 v177, a53\n"
    "v_accvgpr_read_b32 v178, a57\n"
    "v_accvgpr_read_b32 v179, a61\n"
    "v_cvt_pk_bf16_f32 v180, v172, v173\n"
    "v_cvt_pk_bf16_f32 v181, v174, v175\n"
    "v_cvt_pk_bf16_f32 v182, v176, v177\n"
    "v_cvt_pk_bf16_f32 v183, v178, v179\n"
    "v_accvgpr_write_b32 a180, v180\n"
    "v_accvgpr_write_b32 a181, v181\n"
    "v_accvgpr_write_b32 a182, v182\n"
    "v_accvgpr_write_b32 a183, v183\n"
    "s_waitcnt lgkmcnt(0)\n"
    "buffer_load_dwordx4 v226, s[40:43], s81 offen nt lds\n"
    "v_accvgpr_read_b32 v160, a34\n"
    "v_accvgpr_read_b32 v161, a38\n"
    "v_accvgpr_read_b32 v162, a42\n"
    "v_accvgpr_read_b32 v163, a46\n"
    "v_accvgpr_read_b32 v164, a50\n"
    "v_accvgpr_read_b32 v165, a54\n"
    "v_accvgpr_read_b32 v166, a58\n"
    "v_accvgpr_read_b32 v167, a62\n"
    "v_cvt_pk_bf16_f32 v168, v160, v161\n"
    "v_cvt_pk_bf16_f32 v169, v162, v163\n"
    "v_cvt_pk_bf16_f32 v170, v164, v165\n"
    "v_cvt_pk_bf16_f32 v171, v166, v167\n"
    "v_accvgpr_write_b32 a184, v168\n"
    "v_accvgpr_write_b32 a185, v169\n"
    "v_accvgpr_write_b32 a186, v170\n"
    "v_accvgpr_write_b32 a187, v171\n"
    "buffer_load_dwordx4 v227, s[40:43], s81 offen offset:1024 nt lds\n"
    "v_accvgpr_read_b32 v172, a35\n"
    "v_accvgpr_read_b32 v173, a39\n"
    "v_accvgpr_read_b32 v174, a43\n"
    "v_accvgpr_read_b32 v175, a47\n"
    "v_accvgpr_read_b32 v176, a51\n"
    "v_accvgpr_read_b32 v177, a55\n"
    "v_accvgpr_read_b32 v178, a59\n"
    "v_accvgpr_read_b32 v179, a63\n"
    "v_cvt_pk_bf16_f32 v180, v172, v173\n"
    "v_cvt_pk_bf16_f32 v181, v174, v175\n"
    "v_cvt_pk_bf16_f32 v182, v176, v177\n"
    "v_cvt_pk_bf16_f32 v183, v178, v179\n"
    "v_accvgpr_write_b32 a188, v180\n"
    "v_accvgpr_write_b32 a189, v181\n"
    "v_accvgpr_write_b32 a190, v182\n"
    "v_accvgpr_write_b32 a191, v183\n"
    "buffer_load_dwordx4 v228, s[40:43], s81 offen offset:2048 nt lds\n"
    "v_accvgpr_read_b32 v160, a64\n"
    "v_accvgpr_read_b32 v161, a68\n"
    "v_accvgpr_read_b32 v162, a72\n"
    "v_accvgpr_read_b32 v163, a76\n"
    "v_accvgpr_read_b32 v164, a80\n"
    "v_accvgpr_read_b32 v165, a84\n"
    "v_accvgpr_read_b32 v166, a88\n"
    "v_accvgpr_read_b32 v167, a92\n"
    "v_cvt_pk_bf16_f32 v168, v160, v161\n"
    "v_cvt_pk_bf16_f32 v169, v162, v163\n"
    "v_cvt_pk_bf16_f32 v170, v164, v165\n"
    "v_cvt_pk_bf16_f32 v171, v166, v167\n"
    "v_accvgpr_write_b32 a192, v168\n"
    "v_accvgpr_write_b32 a193, v169\n"
    "v_accvgpr_write_b32 a194, v170\n"
    "v_accvgpr_write_b32 a195, v171\n"
    "buffer_load_dwordx4 v229, s[40:43], s81 offen offset:3072 nt lds\n"
    "v_accvgpr_read_b32 v172, a65\n"
    "v_accvgpr_read_b32 v173, a69\n"
    "v_accvgpr_read_b32 v174, a73\n"
    "v_accvgpr_read_b32 v175, a77\n"
    "v_accvgpr_read_b32 v176, a81\n"
    "v_accvgpr_read_b32 v177, a85\n"
    "v_accvgpr_read_b32 v178, a89\n"
    "v_accvgpr_read_b32 v179, a93\n"
    "v_cvt_pk_bf16_f32 v180, v172, v173\n"
    "v_cvt_pk_bf16_f32 v181, v174, v175\n"
    "v_cvt_pk_bf16_f32 v182, v176, v177\n"
    "v_cvt_pk_bf16_f32 v183, v178, v179\n"
    "v_accvgpr_write_b32 a196, v180\n"
    "v_accvgpr_write_b32 a197, v181\n"
    "v_accvgpr_write_b32 a198, v182\n"
    "v_accvgpr_write_b32 a199, v183\n"
    "s_add_u32 m0, s71, 0x7000\n"
    "s_add_u32 s81, s75, 0x2000\n"
    "s_and_b32 s81, s81, 0x1e000\n"
    "s_add_u32 s81, s81, 0x1000\n"
    "v_accvgpr_read_b32 v160, a66\n"
    "v_accvgpr_read_b32 v161, a70\n"
    "v_accvgpr_read_b32 v162, a74\n"
    "v_accvgpr_read_b32 v163, a78\n"
    "v_accvgpr_read_b32 v164, a82\n"
    "v_accvgpr_read_b32 v165, a86\n"
    "v_accvgpr_read_b32 v166, a90\n"
    "v_accvgpr_read_b32 v167, a94\n"
    "v_cvt_pk_bf16_f32 v168, v160, v161\n"
    "v_cvt_pk_bf16_f32 v169, v162, v163\n"
    "v_cvt_pk_bf16_f32 v170, v164, v165\n"
    "v_cvt_pk_bf16_f32 v171, v166, v167\n"
    "v_accvgpr_write_b32 a200, v168\n"
    "v_accvgpr_write_b32 a201, v169\n"
    "v_accvgpr_write_b32 a202, v170\n"
    "v_accvgpr_write_b32 a203, v171\n"
    "buffer_load_dwordx4 v226, s[40:43], s81 offen nt lds\n"
    "v_accvgpr_read_b32 v172, a67\n"
    "v_accvgpr_read_b32 v173, a71\n"
    "v_accvgpr_read_b32 v174, a75\n"
    "v_accvgpr_read_b32 v175, a79\n"
    "v_accvgpr_read_b32 v176, a83\n"
    "v_accvgpr_read_b32 v177, a87\n"
    "v_accvgpr_read_b32 v178, a91\n"
    "v_accvgpr_read_b32 v179, a95\n"
    "v_cvt_pk_bf16_f32 v180, v172, v173\n"
    "v_cvt_pk_bf16_f32 v181, v174, v175\n"
    "v_cvt_pk_bf16_f32 v182, v176, v177\n"
    "v_cvt_pk_bf16_f32 v183, v178, v179\n"
    "v_accvgpr_write_b32 a204, v180\n"
    "v_accvgpr_write_b32 a205, v181\n"
    "v_accvgpr_write_b32 a206, v182\n"
    "v_accvgpr_write_b32 a207, v183\n"
    "buffer_load_dwordx4 v227, s[40:43], s81 offen offset:1024 nt lds\n"
    "v_accvgpr_read_b32 v160, a96\n"
    "v_accvgpr_read_b32 v161, a100\n"
    "v_accvgpr_read_b32 v162, a104\n"
    "v_accvgpr_read_b32 v163, a108\n"
    "v_accvgpr_read_b32 v164, a112\n"
    "v_accvgpr_read_b32 v165, a116\n"
    "v_accvgpr_read_b32 v166, a120\n"
    "v_accvgpr_read_b32 v167, a124\n"
    "v_cvt_pk_bf16_f32 v168, v160, v161\n"
    "v_cvt_pk_bf16_f32 v169, v162, v163\n"
    "v_cvt_pk_bf16_f32 v170, v164, v165\n"
    "v_cvt_pk_bf16_f32 v171, v166, v167\n"
    "v_accvgpr_write_b32 a208, v168\n"
    "v_accvgpr_write_b32 a209, v169\n"
    "v_accvgpr_write_b32 a210, v170\n"
    "v_accvgpr_write_b32 a211, v171\n"
    "buffer_load_dwordx4 v228, s[40:43], s81 offen offset:2048 nt lds\n"
    "v_accvgpr_read_b32 v172, a97\n"
    "v_accvgpr_read_b32 v173, a101\n"
    "v_accvgpr_read_b32 v174, a105\n"
    "v_accvgpr_read_b32 v175, a109\n"
    "v_accvgpr_read_b32 v176, a113\n"
    "v_accvgpr_read_b32 v177, a117\n"
    "v_accvgpr_read_b32 v178, a121\n"
    "v_accvgpr_read_b32 v179, a125\n"
    "v_cvt_pk_bf16_f32 v180, v172, v173\n"
    "v_cvt_pk_bf16_f32 v181, v174, v175\n"
    "v_cvt_pk_bf16_f32 v182, v176, v177\n"
    "v_cvt_pk_bf16_f32 v183, v178, v179\n"
    "v_accvgpr_write_b32 a212, v180\n"
    "v_accvgpr_write_b32 a213, v181\n"
    "v_accvgpr_write_b32 a214, v182\n"
    "v_accvgpr_write_b32 a215, v183\n"
    "buffer_load_dwordx4 v229, s[40:43], s81 offen offset:3072 nt lds\n"
    "v_accvgpr_read_b32 v160, a98\n"
    "v_accvgpr_read_b32 v161, a102\n"
    "v_accvgpr_read_b32 v162, a106\n"
    "v_accvgpr_read_b32 v163, a110\n"
    "v_accvgpr_read_b32 v164, a114\n"
    "v_accvgpr_read_b32 v165, a118\n"
    "v_accvgpr_read_b32 v166, a122\n"
    "v_accvgpr_read_b32 v167, a126\n"
    "v_cvt_pk_bf16_f32 v168, v160, v161\n"
    "v_cvt_pk_bf16_f32 v169, v162, v163\n"
    "v_cvt_pk_bf16_f32 v170, v164, v165\n"
    "v_cvt_pk_bf16_f32 v171, v166, v167\n"
    "v_accvgpr_write_b32 a216, v168\n"
    "v_accvgpr_write_b32 a217, v169\n"
    "v_accvgpr_write_b32 a218, v170\n"
    "v_accvgpr_write_b32 a219, v171\n"
    "s_add_u32 m0, s71, 0x8000\n"
    "s_add_u32 s81, s75, 0x2000\n"
    "s_and_b32 s81, s81, 0x1e000\n"
    "v_accvgpr_read_b32 v172, a99\n"
    "v_accvgpr_read_b32 v173, a103\n"
    "v_accvgpr_read_b32 v174, a107\n"
    "v_accvgpr_read_b32 v175, a111\n"
    "v_accvgpr_read_b32 v176, a115\n"
    "v_accvgpr_read_b32 v177, a119\n"
    "v_accvgpr_read_b32 v178, a123\n"
    "v_accvgpr_read_b32 v179, a127\n"
    "v_cvt_pk_bf16_f32 v180, v172, v173\n"
    "v_cvt_pk_bf16_f32 v181, v174, v175\n"
    "v_cvt_pk_bf16_f32 v182, v176, v177\n"
    "v_cvt_pk_bf16_f32 v183, v178, v179\n"
    "v_accvgpr_write_b32 a220, v180\n"
    "v_accvgpr_write_b32 a221, v181\n"
    "v_accvgpr_write_b32 a222, v182\n"
    "v_accvgpr_write_b32 a223, v183\n"
    "buffer_load_dwordx4 v226, s[44:47], s81 offen nt lds\n"
    "v_accvgpr_read_b32 v160, a128\n"
    "v_accvgpr_read_b32 v161, a132\n"
    "v_accvgpr_read_b32 v162, a136\n"
    "v_accvgpr_read_b32 v163, a140\n"
    "v_accvgpr_read_b32 v164, a144\n"
    "v_accvgpr_read_b32 v165, a148\n"
    "v_accvgpr_read_b32 v166, a152\n"
    "v_accvgpr_read_b32 v167, a156\n"
    "v_cvt_pk_bf16_f32 v168, v160, v161\n"
    "v_cvt_pk_bf16_f32 v169, v162, v163\n"
    "v_cvt_pk_bf16_f32 v170, v164, v165\n"
    "v_cvt_pk_bf16_f32 v171, v166, v167\n"
    "v_accvgpr_write_b32 a224, v168\n"
    "v_accvgpr_write_b32 a225, v169\n"
    "v_accvgpr_write_b32 a226, v170\n"
    "v_accvgpr_write_b32 a227, v171\n"
    "buffer_load_dwordx4 v227, s[44:47], s81 offen offset:1024 nt lds\n"
    "v_accvgpr_read_b32 v172, a129\n"
    "v_accvgpr_read_b32 v173, a133\n"
    "v_accvgpr_read_b32 v174, a137\n"
    "v_accvgpr_read_b32 v175, a141\n"
    "v_accvgpr_read_b32 v176, a145\n"
    "v_accvgpr_read_b32 v177, a149\n"
    "v_accvgpr_read_b32 v178, a153\n"
    "v_accvgpr_read_b32 v179, a157\n"
    "v_cvt_pk_bf16_f32 v180, v172, v173\n"
    "v_cvt_pk_bf16_f32 v181, v174, v175\n"
    "v_cvt_pk_bf16_f32 v182, v176, v177\n"
    "v_cvt_pk_bf16_f32 v183, v178, v179\n"
    "v_accvgpr_write_b32 a228, v180\n"
    "v_accvgpr_write_b32 a229, v181\n"
    "v_accvgpr_write_b32 a230, v182\n"
    "v_accvgpr_write_b32 a231, v183\n"
    "buffer_load_dwordx4 v228, s[44:47], s81 offen offset:2048 nt lds\n"
    "v_accvgpr_read_b32 v160, a130\n"
    "v_accvgpr_read_b32 v161, a134\n"
    "v_accvgpr_read_b32 v162, a138\n"
    "v_accvgpr_read_b32 v163, a142\n"
    "v_accvgpr_read_b32 v164, a146\n"
    "v_accvgpr_read_b32 v165, a150\n"
    "v_accvgpr_read_b32 v166, a154\n"
    "v_accvgpr_read_b32 v167, a158\n"
    "v_cvt_pk_bf16_f32 v168, v160, v161\n"
    "v_cvt_pk_bf16_f32 v169, v162, v163\n"
    "v_cvt_pk_bf16_f32 v170, v164, v165\n"
    "v_cvt_pk_bf16_f32 v171, v166, v167\n"
    "v_accvgpr_write_b32 a232, v168\n"
    "v_accvgpr_write_b32 a233, v169\n"
    "v_accvgpr_write_b32 a234, v170\n"
    "v_accvgpr_write_b32 a235, v171\n"
    "buffer_load_dwordx4 v229, s[44:47], s81 offen offset:3072 nt lds\n"
    "v_accvgpr_read_b32 v172, a131\n"
    "v_accvgpr_read_b32 v173, a135\n"
    "v_accvgpr_read_b32 v174, a139\n"
    "v_accvgpr_read_b32 v175, a143\n"
    "v_accvgpr_read_b32 v176, a147\n"
    "v_accvgpr_read_b32 v177, a151\n"
    "v_accvgpr_read_b32 v178, a155\n"
    "v_accvgpr_read_b32 v179, a159\n"
    "v_cvt_pk_bf16_f32 v180, v172, v173\n"
    "v_cvt_pk_bf16_f32 v181, v174, v175\n"
    "v_cvt_pk_bf16_f32 v182, v176, v177\n"
    "v_cvt_pk_bf16_f32 v183, v178, v179\n"
    "v_accvgpr_write_b32 a236, v180\n"
    "v_accvgpr_write_b32 a237, v181\n"
    "v_accvgpr_write_b32 a238, v182\n"
    "v_accvgpr_write_b32 a239, v183\n"
    "s_add_u32 m0, s71, 0x9000\n"
    "s_add_u32 s81, s75, 0x2000\n"
    "s_and_b32 s81, s81, 0x1e000\n"
    "s_add_u32 s81, s81, 0x1000\n"
    "buffer_load_dwordx4 v226, s[44:47], s81 offen nt lds\n"
    "buffer_load_dwordx4 v227, s[44:47], s81 offen offset:1024 nt lds\n"
    "buffer_load_dwordx4 v228, s[44:47], s81 offen offset:2048 nt lds\n"
    "buffer_load_dwordx4 v229, s[44:47], s81 offen offset:3072 nt lds\n"
    "s_waitcnt vmcnt(16)\n"
    "ds_read_b128 a[96:99], v214 offset:0\n"
    "ds_read_b128 a[100:103], v215 offset:0\n"
    "ds_read_b128 a[104:107], v216 offset:0\n"
    "ds_read_b128 a[108:111], v217 offset:0\n"
    "ds_read_b128 a[112:115], v214 offset:4096\n"
    "ds_read_b128 a[116:119], v215 offset:4096\n"
    "ds_read_b128 a[120:123], v216 offset:4096\n"
    "ds_read_b128 a[124:127], v217 offset:4096\n"
    "s_mov_b32 s80, 0\n"
    "s_mov_b64 s[56:57], s[40:41]\n"
    "s_mov_b32 s58, 0x20000\n"
    "s_mov_b32 s59, 0x20000\n"
    "s_mov_b32 s81, s75\n"
    "v_mfma_f32_16x16x32_bf16 v[160:163], a[0:3], a[160:163], 0\n"
    "v_accvgpr_write_b32 a128, 0\n"
    "v_accvgpr_write_b32 a129, 0\n"
    "v_mfma_f32_16x16x32_bf16 v[160:163], a[4:7], a[164:167], v[160:163]\n"
    "v_accvgpr_write_b32 a130, 0\n"
    "v_accvgpr_write_b32 a131, 0\n"
    "v_mfma_f32_16x16x32_bf16 v[160:163], a[8:11], a[168:171], v[160:163]\n"
    "v_accvgpr_write_b32 a132, 0\n"
    "v_accvgpr_write_b32 a133, 0\n"
    "v_mfma_f32_16x16x32_bf16 v[160:163], a[12:15], a[172:175], v[160:163]\n"
    "v_accvgpr_write_b32 a134, 0\n"
    "v_accvgpr_write_b32 a135, 0\n"
    "v_mfma_f32_16x16x32_bf16 v[164:167], a[16:19], a[160:163], 0\n"
    "v_accvgpr_write_b32 a136, 0\n"
    "v_accvgpr_write_b32 a137, 0\n"
    "v_mfma_f32_16x16x32_bf16 v[164:167], a[20:23], a[164:167], v[164:167]\n"
    "v_accvgpr_write_b32 a138, 0\n"
    "v_accvgpr_write_b32 a139, 0\n"
    "v_mfma_f32_16x16x32_bf16 v[164:167], a[24:27], a[168:171], v[164:167]\n"
    "v_accvgpr_write_b32 a140, 0\n"
    "v_accvgpr_write_b32 a141, 0\n"
    "v_mfma_f32_16x16x32_bf16 v[164:167], a[28:31], a[172:175], v[164:167]\n"
    "v_accvgpr_write_b32 a142, 0\n"
    "v_accvgpr_write_b32 a143, 0\n"
    "v_accvgpr_write_b32 a144, 0\n"
    "v_accvgpr_write_b32 a145, 0\n"
    "v_accvgpr_write_b32 a146, 0\n"
    "v_accvgpr_write_b32 a147, 0\n"
    "v_mov_b32 v197, 0x3f803f80\n"
    "v_accvgpr_write_b32 a148, v197\n"
    "v_accvgpr_write_b32 a149, v197\n"
    "v_accvgpr_write_b32 a150, v197\n"
    "v_accvgpr_write_b32 a151, v197\n"
    "v_mov_b32 v180, 0\n"
    "v_mov_b32 v181, 0\n"
    "v_mov_b32 v182, 0\n"
    "v_mov_b32 v183, 0\n"
    "v_mov_b32 v184, 0xff800000\n"
    "v_mov_b32 v185, 0xff800000\n"
    "v_mov_b32 v186, 0xff800000\n"
    "v_mov_b32 v187, 0xff800000\n"
    "v_mov_b32 v188, 0xff800000\n"
    "v_mov_b32 v189, 0xff800000\n"
    "v_mov_b32 v190, 0xff800000\n"
    "v_mov_b32 v191, 0xff800000\n"
    "v_mov_b32 v192, 0xff800000\n"
    "v_mov_b32 v193, 0xff800000\n"
    "s_waitcnt lgkmcnt(0)\n"
    "B_ATT_LOOPD:\n"
    "s_add_u32 s82, s81, 0x6000\n"
    "s_and_b32 s82, s82, 0x1e000\n"
    "s_cmp_lt_u32 s80, 5\n"
    "s_cselect_b32 s82, s82, 0x40000000\n"
    "s_cselect_b32 s83, 8, 4\n"
    "s_lshl_b32 s83, s83, 12\n"
    "s_add_u32 s83, s83, s71\n"
    "s_cmp_lg_u32 s80, 1\n"
    "s_cbranch_scc1 B_HS0\n"
    "s_mul_i32 s76, s72, 0xa00\n"
    "s_mul_i32 s62, s35, 0x14000\n"
    "s_add_u32 s62, s62, s76\n"
    "s_lshl_b32 s77, s35, 9\n"
    "s_add_u32 s77, s77, s76\n"
    "s_add_u32 s66, s77, 0xa0000\n"
    "s_lshl_b32 s60, 1, s35\n"
    "s_or_b32 s77, s60, 0xff00\n"
    "s_cmp_lt_u32 s35, 5\n"
    "s_cselect_b32 s66, s66, 0x40000000\n"
    "s_cselect_b32 s60, s60, s77\n"
    "s_mov_b32 s28, -1\n"
    "s_mov_b32 s29, 0\n"
    "s_sub_u32 s30, s74, 0x80\n"
    "s_cselect_b32 s30, 0x400, s30\n"
    "s_add_u32 s31, s34, 8\n"
    "s_sub_u32 s77, s31, 9\n"
    "s_cselect_b32 s31, s31, s77\n"
    "s_sub_u32 s77, s31, 9\n"
    "s_cselect_b32 s31, s31, s77\n"
    "s_lshl_b32 s31, s31, 7\n"
    "s_mul_i32 s77, s35, 0x9000\n"
    "s_add_u32 s31, s31, s77\n"
    "s_cmp_lt_u32 s35, 5\n"
    "s_cselect_b32 s31, s31, 0x40000000\n"
    "s_mul_i32 s77, s34, 0x2d000\n"
    "s_lshl_b32 s76, s72, 5\n"
    "s_add_u32 s77, s77, 0\n"
    "s_add_u32 s77, s77, 0x480\n"
    "s_add_u32 s92, s26, s77\n"
    "s_addc_u32 s93, s27, 0\n"
    "s_and_b32 s93, s93, 0xffff\n"
    "s_mov_b32 s94, 0x2cb80\n"
    "s_mov_b32 s95, 0x20000\n"
    "v_and_b32 v209, 1, v239\n"
    "v_lshlrev_b32 v209, 4, v209\n"
    "v_mov_b32 v210, 0x3f80\n"
    "v_lshlrev_b32 v209, v209, v210\n"
    "v_lshrrev_b32 v208, 4, v239\n"
    "v_bfe_u32 v210, v239, 2, 2\n"
    "v_xor_b32 v210, v208, v210\n"
    "v_bfe_u32 v211, v239, 1, 1\n"
    "v_lshl_or_b32 v210, v210, 1, v211\n"
    "v_cmp_eq_u32 vcc, 0, v210\n"
    "s_nop 1\n"
    "v_cndmask_b32 v212, 0, v209, vcc\n"
    "v_accvgpr_write_b32 a240, v212\n"
    "v_accvgpr_write_b32 a242, v212\n"
    "v_cmp_eq_u32 vcc, 1, v210\n"
    "s_nop 1\n"
    "v_cndmask_b32 v212, 0, v209, vcc\n"
    "v_accvgpr_write_b32 a241, v212\n"
    "v_accvgpr_write_b32 a243, v212\n"
    "v_bfe_u32 v210, v239, 2, 1\n"
    "v_xor_b32 v208, v208, v210\n"
    "v_lshl_or_b32 v208, v208, 2, v211\n"
    "v_bfe_u32 v210, v239, 3, 1\n"
    "v_lshl_or_b32 v208, v210, 1, v208\n"
    "v_cmp_eq_u32 vcc, 0, v208\n"
    "s_nop 1\n"
    "v_cndmask_b32 v212, 0, v209, vcc\n"
    "v_accvgpr_write_b32 a244, v212\n"
    "v_cmp_eq_u32 vcc, 8, v208\n"
    "s_nop 1\n"
    "v_cndmask_b32 v212, 0, v209, vcc\n"
    "v_accvgpr_write_b32 a248, v212\n"
    "v_cmp_eq_u32 vcc, 1, v208\n"
    "s_nop 1\n"
    "v_cndmask_b32 v212, 0, v209, vcc\n"
    "v_accvgpr_write_b32 a245, v212\n"
    "v_cmp_eq_u32 vcc, 9, v208\n"
    "s_nop 1\n"
    "v_cndmask_b32 v212, 0, v209, vcc\n"
    "v_accvgpr_write_b32 a249, v212\n"
    "v_cmp_eq_u32 vcc, 2, v208\n"
    "s_nop 1\n"
    "v_cndmask_b32 v212, 0, v209, vcc\n"
    "v_accvgpr_write_b32 a246, v212\n"
    "v_cmp_eq_u32 vcc, 10, v208\n"
    "s_nop 1\n"
    "v_cndmask_b32 v212, 0, v209, vcc\n"
    "v_accvgpr_write_b32 a250, v212\n"
    "v_cmp_eq_u32 vcc, 3, v208\n"
    "s_nop 1\n"
    "v_cndmask_b32 v212, 0, v209, vcc\n"
    "v_accvgpr_write_b32 a247, v212\n"
    "v_cmp_eq_u32 vcc, 11, v208\n"
    "s_nop 1\n"
    "v_cndmask_b32 v212, 0, v209, vcc\n"
    "v_accvgpr_write_b32 a251, v212\n"
    "s_add_u32 s76, s34, 16\n"
    "s_lshl_b32 s76, s76, 12\n"
    "s_add_u32 s76, s76, 0x2000000\n"
    "s_add_u32 s36, s24, s76\n"
    "s_addc_u32 s37, s25, 0\n"
    "s_and_b32 s37, s37, 0xffff\n"
    "s_mov_b32 s38, 0x101000\n"
    "s_mov_b32 s39, 0x20000\n"
    "s_mov_b32 s67, 0x100000\n"
    "buffer_load_dword v251, v249, s[36:39], 0 offen sc1\n"
    "B_HS0:\n"
    "s_cmp_lg_u32 s80, 2\n"
    "s_cbranch_scc1 B_HS1\n"
    "v_add_u32 v251, 0x9e3779b9, v251\n"
    "buffer_store_dword v251, v249, s[36:39], s67 offen sc1\n"
    "B_HS1:\n"
    "s_cmp_lg_u32 s80, 5\n"
    "s_cbranch_scc1 B_HS2\n"
    "buffer_load_dword v252, v250, s[36:39], 0 offen sc1\n"
    "buffer_load_dword v253, v250, s[36:39], s67 offen sc1\n"
    "B_HS2:\n"
    "s_cmp_lg_u32 s80, 4\n"
    "s_cbranch_scc1 B_TT\n"
    "s_mov_b64 exec, 0\n"
    "s_cmp_eq_u32 s72, 0\n"
    "s_cbranch_scc1 B_TT_FIRST0\n"
    "s_cmp_eq_u32 s72, 1\n"
    "s_cbranch_scc1 B_TT_FIRST1\n"
    "s_cmp_eq_u32 s72, 2\n"
    "s_cbranch_scc1 B_TT_FIRST2\n"
    "s_cmp_eq_u32 s72, 3\n"
    "s_cbranch_scc1 B_TT_FIRST3\n"
    "B_TT_RET:\n"
    "s_mov_b64 exec, -1\n"
    "B_TT:\n"
    "v_mfma_f32_16x16x32_bf16 v[168:171], a[0:3], a[176:179], 0\n"
    "s_add_u32 s78, s81, 0x4000\n"
    "s_and_b32 s78, s78, 0x1e000\n"
    "s_add_u32 m0, s71, 0x2000\n"
    "s_waitcnt vmcnt(16)\n"
    "v_max3_f32 v196, v160, v161, v162\n"
    "v_max3_f32 v197, v163, v164, v165\n"
    "v_mfma_f32_16x16x32_bf16 v[168:171], a[4:7], a[180:183], v[168:171]\n"
    "buffer_load_dwordx4 v226, s[40:43], s78 offen nt lds\n"
    "v_max3_f32 v196, v196, v166, v167\n"
    "v_max_f32 v196, v196, v197\n"
    "v_mfma_f32_16x16x32_bf16 v[168:171], a[8:11], a[184:187], v[168:171]\n"
    "buffer_load_dwordx4 v227, s[40:43], s78 offen offset:1024 nt lds\n"
    "ds_read_b64_tr_b16 a[64:65], v218 offset:0\n"
    "v_cmp_gt_f32 vcc, v196, v189\n"
    "v_mfma_f32_16x16x32_bf16 v[168:171], a[12:15], a[188:191], v[168:171]\n"
    "s_add_u32 s79, s81, 0x4000\n"
    "s_cbranch_vccnz B_RARE_ED_0\n"
    "B_BACK_ED_0:\n"
    "v_fma_f32 v160, v160, s69, v200\n"
    "v_fma_f32 v161, v161, s69, v200\n"
    "v_mfma_f32_16x16x32_bf16 v[172:175], a[16:19], a[176:179], 0\n"
    "s_and_b32 s79, s79, 0x1e000\n"
    "buffer_load_dwordx4 v228, s[40:43], s78 offen offset:2048 nt lds\n"
    "v_exp_f32 v160, v160\n"
    "v_mfma_f32_16x16x32_bf16 v[172:175], a[20:23], a[180:183], v[172:175]\n"
    "s_add_u32 s79, s79, 0x1000\n"
    "buffer_load_dwordx4 v229, s[40:43], s78 offen offset:3072 nt lds\n"
    "v_exp_f32 v161, v161\n"
    "v_mfma_f32_16x16x32_bf16 v[172:175], a[24:27], a[184:187], v[172:175]\n"
    "s_add_u32 m0, s71, 0x3000\n"
    "ds_read_b64_tr_b16 a[66:67], v218 offset:4096\n"
    "v_fma_f32 v162, v162, s69, v200\n"
    "v_fma_f32 v163, v163, s69, v200\n"
    "v_mfma_f32_16x16x32_bf16 v[172:175], a[28:31], a[188:191], v[172:175]\n"
    "buffer_load_dwordx4 v226, s[40:43], s79 offen nt lds\n"
    "ds_read_b64_tr_b16 a[68:69], v219 offset:0\n"
    "v_exp_f32 v162, v162\n"
    "v_mfma_f32_16x16x32_bf16 a[144:147], a[148:151], v[180:183], a[144:147]\n"
    "buffer_load_dwordx4 v227, s[40:43], s79 offen offset:1024 nt lds\n"
    "ds_read_b64_tr_b16 a[70:71], v219 offset:4096\n"
    "v_exp_f32 v163, v163\n"
    "v_mfma_f32_16x16x32_bf16 v[128:131], a[96:99], v[180:183], v[128:131]\n"
    "buffer_load_dwordx4 v228, s[40:43], s79 offen offset:2048 nt lds\n"
    "v_fma_f32 v164, v164, s69, v200\n"
    "v_fma_f32 v165, v165, s69, v200\n"
    "v_mfma_f32_16x16x32_bf16 v[132:135], a[100:103], v[180:183], v[132:135]\n"
    "buffer_load_dwordx4 v229, s[40:43], s79 offen offset:3072 nt lds\n"
    "v_cvt_pk_bf16_f32 v176, v160, v161\n"
    "v_cvt_pk_bf16_f32 v177, v162, v163\n"
    "v_mfma_f32_16x16x32_bf16 v[136:139], a[104:107], v[180:183], v[136:139]\n"
    "ds_read_b64_tr_b16 a[72:73], v220 offset:0\n"
    "ds_read_b64_tr_b16 a[74:75], v220 offset:4096\n"
    "v_exp_f32 v164, v164\n"
    "v_mfma_f32_16x16x32_bf16 v[140:143], a[108:111], v[180:183], v[140:143]\n"
    "ds_read_b64_tr_b16 a[76:77], v221 offset:0\n"
    "v_exp_f32 v165, v165\n"
    "v_mfma_f32_16x16x32_bf16 v[144:147], a[112:115], v[180:183], v[144:147]\n"
    "ds_read_b64_tr_b16 a[78:79], v221 offset:4096\n"
    "v_fma_f32 v166, v166, s69, v200\n"
    "v_fma_f32 v167, v167, s69, v200\n"
    "v_mfma_f32_16x16x32_bf16 v[148:151], a[116:119], v[180:183], v[148:151]\n"
    "ds_read_b64_tr_b16 a[80:81], v222 offset:0\n"
    "v_exp_f32 v166, v166\n"
    "v_mfma_f32_16x16x32_bf16 v[152:155], a[120:123], v[180:183], v[152:155]\n"
    "ds_read_b64_tr_b16 a[82:83], v222 offset:4096\n"
    "v_exp_f32 v167, v167\n"
    "v_mfma_f32_16x16x32_bf16 v[156:159], a[124:127], v[180:183], v[156:159]\n"
    "ds_read_b64_tr_b16 a[84:85], v223 offset:0\n"
    "v_cvt_pk_bf16_f32 v178, v164, v165\n"
    "v_cvt_pk_bf16_f32 v179, v166, v167\n"
    "v_mfma_f32_16x16x32_bf16 v[160:163], a[0:3], a[192:195], 0\n"
    "ds_read_b64_tr_b16 a[86:87], v223 offset:4096\n"
    "v_max3_f32 v196, v168, v169, v170\n"
    "v_max3_f32 v197, v171, v172, v173\n"
    "v_mfma_f32_16x16x32_bf16 v[160:163], a[4:7], a[196:199], v[160:163]\n"
    "ds_read_b64_tr_b16 a[88:89], v224 offset:0\n"
    "v_max3_f32 v196, v196, v174, v175\n"
    "v_max_f32 v196, v196, v197\n"
    "v_mfma_f32_16x16x32_bf16 v[160:163], a[8:11], a[200:203], v[160:163]\n"
    "ds_read_b64_tr_b16 a[90:91], v224 offset:4096\n"
    "ds_read_b64_tr_b16 a[92:93], v225 offset:0\n"
    "v_cmp_gt_f32 vcc, v196, v190\n"
    "v_mfma_f32_16x16x32_bf16 v[160:163], a[12:15], a[204:207], v[160:163]\n"
    "s_cbranch_vccnz B_RARE_ED_1\n"
    "B_BACK_ED_1:\n"
    "v_fma_f32 v168, v168, s69, v201\n"
    "v_fma_f32 v169, v169, s69, v201\n"
    "v_mfma_f32_16x16x32_bf16 v[164:167], a[16:19], a[192:195], 0\n"
    "ds_read_b64_tr_b16 a[94:95], v225 offset:4096\n"
    "v_exp_f32 v168, v168\n"
    "v_mfma_f32_16x16x32_bf16 v[164:167], a[20:23], a[196:199], v[164:167]\n"
    "v_exp_f32 v169, v169\n"
    "v_mfma_f32_16x16x32_bf16 v[164:167], a[24:27], a[200:203], v[164:167]\n"
    "v_fma_f32 v170, v170, s69, v201\n"
    "v_fma_f32 v171, v171, s69, v201\n"
    "v_mfma_f32_16x16x32_bf16 v[164:167], a[28:31], a[204:207], v[164:167]\n"
    "v_exp_f32 v170, v170\n"
    "v_mfma_f32_16x16x32_bf16 a[128:131], a[148:151], v[176:179], a[128:131]\n"
    "s_waitcnt lgkmcnt(0)\n"
    "v_exp_f32 v171, v171\n"
    "v_mfma_f32_16x16x32_bf16 v[0:3], a[64:67], v[176:179], v[0:3]\n"
    "v_fma_f32 v172, v172, s69, v201\n"
    "v_fma_f32 v173, v173, s69, v201\n"
    "v_mfma_f32_16x16x32_bf16 v[4:7], a[68:71], v[176:179], v[4:7]\n"
    "v_cvt_pk_bf16_f32 v180, v168, v169\n"
    "v_cvt_pk_bf16_f32 v181, v170, v171\n"
    "v_mfma_f32_16x16x32_bf16 v[8:11], a[72:75], v[176:179], v[8:11]\n"
    "v_exp_f32 v172, v172\n"
    "v_mfma_f32_16x16x32_bf16 v[12:15], a[76:79], v[176:179], v[12:15]\n"
    "v_exp_f32 v173, v173\n"
    "v_mfma_f32_16x16x32_bf16 v[16:19], a[80:83], v[176:179], v[16:19]\n"
    "v_fma_f32 v174, v174, s69, v201\n"
    "v_fma_f32 v175, v175, s69, v201\n"
    "v_mfma_f32_16x16x32_bf16 v[20:23], a[84:87], v[176:179], v[20:23]\n"
    "v_exp_f32 v174, v174\n"
    "v_mfma_f32_16x16x32_bf16 v[24:27], a[88:91], v[176:179], v[24:27]\n"
    "v_exp_f32 v175, v175\n"
    "v_mfma_f32_16x16x32_bf16 v[28:31], a[92:95], v[176:179], v[28:31]\n"
    "v_cvt_pk_bf16_f32 v182, v172, v173\n"
    "v_cvt_pk_bf16_f32 v183, v174, v175\n"
    "v_mfma_f32_16x16x32_bf16 v[168:171], a[0:3], a[208:211], 0\n"
    "v_max3_f32 v196, v160, v161, v162\n"
    "v_max3_f32 v197, v163, v164, v165\n"
    "v_mfma_f32_16x16x32_bf16 v[168:171], a[4:7], a[212:215], v[168:171]\n"
    "v_max3_f32 v196, v196, v166, v167\n"
    "v_max_f32 v196, v196, v197\n"
    "v_mfma_f32_16x16x32_bf16 v[168:171], a[8:11], a[216:219], v[168:171]\n"
    "s_add_u32 s78, s81, 0x4000\n"
    "v_cmp_gt_f32 vcc, v196, v191\n"
    "v_mfma_f32_16x16x32_bf16 v[168:171], a[12:15], a[220:223], v[168:171]\n"
    "s_and_b32 s78, s78, 0x1e000\n"
    "s_cbranch_vccnz B_RARE_ED_2\n"
    "B_BACK_ED_2:\n"
    "v_fma_f32 v160, v160, s69, v202\n"
    "v_fma_f32 v161, v161, s69, v202\n"
    "v_mfma_f32_16x16x32_bf16 v[172:175], a[16:19], a[208:211], 0\n"
    "s_add_u32 m0, s71, 0\n"
    "s_waitcnt vmcnt(16)\n"
    "v_exp_f32 v160, v160\n"
    "v_mfma_f32_16x16x32_bf16 v[172:175], a[20:23], a[212:215], v[172:175]\n"
    "buffer_load_dwordx4 v226, s[44:47], s78 offen nt lds\n"
    "v_exp_f32 v161, v161\n"
    "v_mfma_f32_16x16x32_bf16 v[172:175], a[24:27], a[216:219], v[172:175]\n"
    "buffer_load_dwordx4 v227, s[44:47], s78 offen offset:1024 nt lds\n"
    "v_fma_f32 v162, v162, s69, v202\n"
    "v_fma_f32 v163, v163, s69, v202\n"
    "v_mfma_f32_16x16x32_bf16 v[172:175], a[28:31], a[220:223], v[172:175]\n"
    "s_add_u32 s79, s81, 0x4000\n"
    "buffer_load_dwordx4 v228, s[44:47], s78 offen offset:2048 nt lds\n"
    "v_exp_f32 v162, v162\n"
    "v_mfma_f32_16x16x32_bf16 a[132:135], a[148:151], v[180:183], a[132:135]\n"
    "s_and_b32 s79, s79, 0x1e000\n"
    "buffer_load_dwordx4 v229, s[44:47], s78 offen offset:3072 nt lds\n"
    "v_exp_f32 v163, v163\n"
    "v_mfma_f32_16x16x32_bf16 v[32:35], a[64:67], v[180:183], v[32:35]\n"
    "s_add_u32 s79, s79, 0x1000\n"
    "v_fma_f32 v164, v164, s69, v202\n"
    "v_fma_f32 v165, v165, s69, v202\n"
    "v_mfma_f32_16x16x32_bf16 v[36:39], a[68:71], v[180:183], v[36:39]\n"
    "s_add_u32 m0, s71, 0x1000\n"
    "v_cvt_pk_bf16_f32 v176, v160, v161\n"
    "v_cvt_pk_bf16_f32 v177, v162, v163\n"
    "v_mfma_f32_16x16x32_bf16 v[40:43], a[72:75], v[180:183], v[40:43]\n"
    "buffer_load_dwordx4 v226, s[44:47], s79 offen nt lds\n"
    "v_exp_f32 v164, v164\n"
    "v_mfma_f32_16x16x32_bf16 v[44:47], a[76:79], v[180:183], v[44:47]\n"
    "buffer_load_dwordx4 v227, s[44:47], s79 offen offset:1024 nt lds\n"
    "v_exp_f32 v165, v165\n"
    "v_mfma_f32_16x16x32_bf16 v[48:51], a[80:83], v[180:183], v[48:51]\n"
    "buffer_load_dwordx4 v228, s[44:47], s79 offen offset:2048 nt lds\n"
    "v_fma_f32 v166, v166, s69, v202\n"
    "v_fma_f32 v167, v167, s69, v202\n"
    "v_mfma_f32_16x16x32_bf16 v[52:55], a[84:87], v[180:183], v[52:55]\n"
    "buffer_load_dwordx4 v229, s[44:47], s79 offen offset:3072 nt lds\n"
    "v_exp_f32 v166, v166\n"
    "v_mfma_f32_16x16x32_bf16 v[56:59], a[88:91], v[180:183], v[56:59]\n"
    "ds_read_b128 a[32:35], v214 offset:24576\n"
    "v_exp_f32 v167, v167\n"
    "v_mfma_f32_16x16x32_bf16 v[60:63], a[92:95], v[180:183], v[60:63]\n"
    "ds_read_b128 a[36:39], v215 offset:24576\n"
    "v_cvt_pk_bf16_f32 v178, v164, v165\n"
    "v_cvt_pk_bf16_f32 v179, v166, v167\n"
    "v_mfma_f32_16x16x32_bf16 v[160:163], a[0:3], a[224:227], 0\n"
    "ds_read_b128 a[40:43], v216 offset:24576\n"
    "v_max3_f32 v196, v168, v169, v170\n"
    "v_max3_f32 v197, v171, v172, v173\n"
    "v_mfma_f32_16x16x32_bf16 v[160:163], a[4:7], a[228:231], v[160:163]\n"
    "ds_read_b128 a[44:47], v217 offset:24576\n"
    "v_max3_f32 v196, v196, v174, v175\n"
    "v_max_f32 v196, v196, v197\n"
    "v_mfma_f32_16x16x32_bf16 v[160:163], a[8:11], a[232:235], v[160:163]\n"
    "ds_read_b128 a[48:51], v214 offset:28672\n"
    "ds_read_b128 a[52:55], v215 offset:28672\n"
    "v_cmp_gt_f32 vcc, v196, v192\n"
    "v_mfma_f32_16x16x32_bf16 v[160:163], a[12:15], a[236:239], v[160:163]\n"
    "s_cbranch_vccnz B_RARE_ED_3\n"
    "B_BACK_ED_3:\n"
    "v_fma_f32 v168, v168, s69, v203\n"
    "v_fma_f32 v169, v169, s69, v203\n"
    "v_mfma_f32_16x16x32_bf16 v[164:167], a[16:19], a[224:227], 0\n"
    "ds_read_b128 a[56:59], v216 offset:28672\n"
    "v_exp_f32 v168, v168\n"
    "v_mfma_f32_16x16x32_bf16 v[164:167], a[20:23], a[228:231], v[164:167]\n"
    "ds_read_b128 a[60:63], v217 offset:28672\n"
    "v_exp_f32 v169, v169\n"
    "v_mfma_f32_16x16x32_bf16 v[164:167], a[24:27], a[232:235], v[164:167]\n"
    "v_fma_f32 v170, v170, s69, v203\n"
    "v_fma_f32 v171, v171, s69, v203\n"
    "v_mfma_f32_16x16x32_bf16 v[164:167], a[28:31], a[236:239], v[164:167]\n"
    "v_exp_f32 v170, v170\n"
    "v_mfma_f32_16x16x32_bf16 a[136:139], a[148:151], v[176:179], a[136:139]\n"
    "v_exp_f32 v171, v171\n"
    "v_mfma_f32_16x16x32_bf16 v[64:67], a[64:67], v[176:179], v[64:67]\n"
    "v_fma_f32 v172, v172, s69, v203\n"
    "v_fma_f32 v173, v173, s69, v203\n"
    "v_mfma_f32_16x16x32_bf16 v[68:71], a[68:71], v[176:179], v[68:71]\n"
    "v_cvt_pk_bf16_f32 v180, v168, v169\n"
    "v_cvt_pk_bf16_f32 v181, v170, v171\n"
    "v_mfma_f32_16x16x32_bf16 v[72:75], a[72:75], v[176:179], v[72:75]\n"
    "v_exp_f32 v172, v172\n"
    "v_mfma_f32_16x16x32_bf16 v[76:79], a[76:79], v[176:179], v[76:79]\n"
    "v_exp_f32 v173, v173\n"
    "v_mfma_f32_16x16x32_bf16 v[80:83], a[80:83], v[176:179], v[80:83]\n"
    "v_fma_f32 v174, v174, s69, v203\n"
    "v_fma_f32 v175, v175, s69, v203\n"
    "v_mfma_f32_16x16x32_bf16 v[84:87], a[84:87], v[176:179], v[84:87]\n"
    "v_exp_f32 v174, v174\n"
    "v_mfma_f32_16x16x32_bf16 v[88:91], a[88:91], v[176:179], v[88:91]\n"
    "v_exp_f32 v175, v175\n"
    "v_mfma_f32_16x16x32_bf16 v[92:95], a[92:95], v[176:179], v[92:95]\n"
    "s_waitcnt lgkmcnt(0)\n"
    "v_cvt_pk_bf16_f32 v182, v172, v173\n"
    "v_cvt_pk_bf16_f32 v183, v174, v175\n"
    "v_mfma_f32_16x16x32_bf16 v[168:171], a[32:35], a[160:163], 0\n"
    "v_max3_f32 v196, v160, v161, v162\n"
    "v_max3_f32 v197, v163, v164, v165\n"
    "v_mfma_f32_16x16x32_bf16 v[168:171], a[36:39], a[164:167], v[168:171]\n"
    "v_max3_f32 v196, v196, v166, v167\n"
    "v_max_f32 v196, v196, v197\n"
    "v_mfma_f32_16x16x32_bf16 v[168:171], a[40:43], a[168:171], v[168:171]\n"
    "v_cmp_gt_f32 vcc, v196, v193\n"
    "v_mfma_f32_16x16x32_bf16 v[168:171], a[44:47], a[172:175], v[168:171]\n"
    "s_cbranch_vccnz B_RARE_ED_4\n"
    "B_BACK_ED_4:\n"
    "v_fma_f32 v160, v160, s69, v204\n"
    "v_fma_f32 v161, v161, s69, v204\n"
    "v_mfma_f32_16x16x32_bf16 v[172:175], a[48:51], a[160:163], 0\n"
    "v_exp_f32 v160, v160\n"
    "v_mfma_f32_16x16x32_bf16 v[172:175], a[52:55], a[164:167], v[172:175]\n"
    "v_exp_f32 v161, v161\n"
    "v_mfma_f32_16x16x32_bf16 v[172:175], a[56:59], a[168:171], v[172:175]\n"
    "v_fma_f32 v162, v162, s69, v204\n"
    "v_fma_f32 v163, v163, s69, v204\n"
    "v_mfma_f32_16x16x32_bf16 v[172:175], a[60:63], a[172:175], v[172:175]\n"
    "v_exp_f32 v162, v162\n"
    "v_mfma_f32_16x16x32_bf16 a[140:143], a[148:151], v[180:183], a[140:143]\n"
    "v_exp_f32 v163, v163\n"
    "v_mfma_f32_16x16x32_bf16 v[96:99], a[64:67], v[180:183], v[96:99]\n"
    "v_fma_f32 v164, v164, s69, v204\n"
    "v_fma_f32 v165, v165, s69, v204\n"
    "v_mfma_f32_16x16x32_bf16 v[100:103], a[68:71], v[180:183], v[100:103]\n"
    "v_cvt_pk_bf16_f32 v176, v160, v161\n"
    "v_cvt_pk_bf16_f32 v177, v162, v163\n"
    "v_mfma_f32_16x16x32_bf16 v[104:107], a[72:75], v[180:183], v[104:107]\n"
    "v_exp_f32 v164, v164\n"
    "v_mfma_f32_16x16x32_bf16 v[108:111], a[76:79], v[180:183], v[108:111]\n"
    "v_exp_f32 v165, v165\n"
    "v_mfma_f32_16x16x32_bf16 v[112:115], a[80:83], v[180:183], v[112:115]\n"
    "v_fma_f32 v166, v166, s69, v204\n"
    "v_fma_f32 v167, v167, s69, v204\n"
    "v_mfma_f32_16x16x32_bf16 v[116:119], a[84:87], v[180:183], v[116:119]\n"
    "v_exp_f32 v166, v166\n"
    "v_mfma_f32_16x16x32_bf16 v[120:123], a[88:91], v[180:183], v[120:123]\n"
    "v_exp_f32 v167, v167\n"
    "v_mfma_f32_16x16x32_bf16 v[124:127], a[92:95], v[180:183], v[124:127]\n"
    "v_cvt_pk_bf16_f32 v178, v164, v165\n"
    "v_cvt_pk_bf16_f32 v179, v166, v167\n"
    "v_mfma_f32_16x16x32_bf16 v[160:163], a[32:35], a[176:179], 0\n"
    "s_add_u32 s78, s81, 0x6000\n"
    "s_and_b32 s78, s78, 0x1e000\n"
    "s_add_u32 m0, s71, 0x6000\n"
    "s_waitcnt vmcnt(16)\n"
    "v_max3_f32 v196, v168, v169, v170\n"
    "v_max3_f32 v197, v171, v172, v173\n"
    "v_mfma_f32_16x16x32_bf16 v[160:163], a[36:39], a[180:183], v[160:163]\n"
    "buffer_load_dwordx4 v226, s[40:43], s78 offen nt lds\n"
    "v_max3_f32 v196, v196, v174, v175\n"
    "v_max_f32 v196, v196, v197\n"
    "v_mfma_f32_16x16x32_bf16 v[160:163], a[40:43], a[184:187], v[160:163]\n"
    "buffer_load_dwordx4 v227, s[40:43], s78 offen offset:1024 nt lds\n"
    "ds_read_b64_tr_b16 a[96:97], v218 offset:32768\n"
    "v_cmp_gt_f32 vcc, v196, v189\n"
    "v_mfma_f32_16x16x32_bf16 v[160:163], a[44:47], a[188:191], v[160:163]\n"
    "s_add_u32 s79, s81, 0x6000\n"
    "s_cbranch_vccnz B_RARE_DD_0\n"
    "B_BACK_DD_0:\n"
    "v_fma_f32 v168, v168, s69, v200\n"
    "v_fma_f32 v169, v169, s69, v200\n"
    "v_mfma_f32_16x16x32_bf16 v[164:167], a[48:51], a[176:179], 0\n"
    "s_and_b32 s79, s79, 0x1e000\n"
    "buffer_load_dwordx4 v228, s[40:43], s78 offen offset:2048 nt lds\n"
    "v_exp_f32 v168, v168\n"
    "v_mfma_f32_16x16x32_bf16 v[164:167], a[52:55], a[180:183], v[164:167]\n"
    "s_add_u32 s79, s79, 0x1000\n"
    "buffer_load_dwordx4 v229, s[40:43], s78 offen offset:3072 nt lds\n"
    "v_exp_f32 v169, v169\n"
    "v_mfma_f32_16x16x32_bf16 v[164:167], a[56:59], a[184:187], v[164:167]\n"
    "s_add_u32 m0, s71, 0x7000\n"
    "ds_read_b64_tr_b16 a[98:99], v218 offset:36864\n"
    "v_fma_f32 v170, v170, s69, v200\n"
    "v_fma_f32 v171, v171, s69, v200\n"
    "v_mfma_f32_16x16x32_bf16 v[164:167], a[60:63], a[188:191], v[164:167]\n"
    "buffer_load_dwordx4 v226, s[40:43], s79 offen nt lds\n"
    "ds_read_b64_tr_b16 a[100:101], v219 offset:32768\n"
    "v_exp_f32 v170, v170\n"
    "v_mfma_f32_16x16x32_bf16 a[144:147], a[148:151], v[176:179], a[144:147]\n"
    "buffer_load_dwordx4 v227, s[40:43], s79 offen offset:1024 nt lds\n"
    "ds_read_b64_tr_b16 a[102:103], v219 offset:36864\n"
    "v_exp_f32 v171, v171\n"
    "v_mfma_f32_16x16x32_bf16 v[128:131], a[64:67], v[176:179], v[128:131]\n"
    "buffer_load_dwordx4 v228, s[40:43], s79 offen offset:2048 nt lds\n"
    "v_fma_f32 v172, v172, s69, v200\n"
    "v_fma_f32 v173, v173, s69, v200\n"
    "v_mfma_f32_16x16x32_bf16 v[132:135], a[68:71], v[176:179], v[132:135]\n"
    "buffer_load_dwordx4 v229, s[40:43], s79 offen offset:3072 nt lds\n"
    "v_cvt_pk_bf16_f32 v180, v168, v169\n"
    "v_cvt_pk_bf16_f32 v181, v170, v171\n"
    "v_mfma_f32_16x16x32_bf16 v[136:139], a[72:75], v[176:179], v[136:139]\n"
    "ds_read_b64_tr_b16 a[104:105], v220 offset:32768\n"
    "ds_read_b64_tr_b16 a[106:107], v220 offset:36864\n"
    "v_exp_f32 v172, v172\n"
    "v_mfma_f32_16x16x32_bf16 v[140:143], a[76:79], v[176:179], v[140:143]\n"
    "ds_read_b64_tr_b16 a[108:109], v221 offset:32768\n"
    "v_exp_f32 v173, v173\n"
    "v_mfma_f32_16x16x32_bf16 v[144:147], a[80:83], v[176:179], v[144:147]\n"
    "ds_read_b64_tr_b16 a[110:111], v221 offset:36864\n"
    "v_fma_f32 v174, v174, s69, v200\n"
    "v_fma_f32 v175, v175, s69, v200\n"
    "v_mfma_f32_16x16x32_bf16 v[148:151], a[84:87], v[176:179], v[148:151]\n"
    "ds_read_b64_tr_b16 a[112:113], v222 offset:32768\n"
    "v_exp_f32 v174, v174\n"
    "v_mfma_f32_16x16x32_bf16 v[152:155], a[88:91], v[176:179], v[152:155]\n"
    "ds_read_b64_tr_b16 a[114:115], v222 offset:36864\n"
    "v_exp_f32 v175, v175\n"
    "v_mfma_f32_16x16x32_bf16 v[156:159], a[92:95], v[176:179], v[156:159]\n"
    "ds_read_b64_tr_b16 a[116:117], v223 offset:32768\n"
    "v_cvt_pk_bf16_f32 v182, v172, v173\n"
    "v_cvt_pk_bf16_f32 v183, v174, v175\n"
    "v_mfma_f32_16x16x32_bf16 v[168:171], a[32:35], a[192:195], 0\n"
    "ds_read_b64_tr_b16 a[118:119], v223 offset:36864\n"
    "v_max3_f32 v196, v160, v161, v162\n"
    "v_max3_f32 v197, v163, v164, v165\n"
    "v_mfma_f32_16x16x32_bf16 v[168:171], a[36:39], a[196:199], v[168:171]\n"
    "ds_read_b64_tr_b16 a[120:121], v224 offset:32768\n"
    "v_max3_f32 v196, v196, v166, v167\n"
    "v_max_f32 v196, v196, v197\n"
    "v_mfma_f32_16x16x32_bf16 v[168:171], a[40:43], a[200:203], v[168:171]\n"
    "ds_read_b64_tr_b16 a[122:123], v224 offset:36864\n"
    "ds_read_b64_tr_b16 a[124:125], v225 offset:32768\n"
    "v_cmp_gt_f32 vcc, v196, v190\n"
    "v_mfma_f32_16x16x32_bf16 v[168:171], a[44:47], a[204:207], v[168:171]\n"
    "s_cbranch_vccnz B_RARE_DD_1\n"
    "B_BACK_DD_1:\n"
    "v_fma_f32 v160, v160, s69, v201\n"
    "v_fma_f32 v161, v161, s69, v201\n"
    "v_mfma_f32_16x16x32_bf16 v[172:175], a[48:51], a[192:195], 0\n"
    "ds_read_b64_tr_b16 a[126:127], v225 offset:36864\n"
    "v_exp_f32 v160, v160\n"
    "v_mfma_f32_16x16x32_bf16 v[172:175], a[52:55], a[196:199], v[172:175]\n"
    "v_exp_f32 v161, v161\n"
    "v_mfma_f32_16x16x32_bf16 v[172:175], a[56:59], a[200:203], v[172:175]\n"
    "v_fma_f32 v162, v162, s69, v201\n"
    "v_fma_f32 v163, v163, s69, v201\n"
    "v_mfma_f32_16x16x32_bf16 v[172:175], a[60:63], a[204:207], v[172:175]\n"
    "v_exp_f32 v162, v162\n"
    "v_mfma_f32_16x16x32_bf16 a[128:131], a[148:151], v[180:183], a[128:131]\n"
    "s_waitcnt lgkmcnt(0)\n"
    "v_exp_f32 v163, v163\n"
    "v_mfma_f32_16x16x32_bf16 v[0:3], a[96:99], v[180:183], v[0:3]\n"
    "v_fma_f32 v164, v164, s69, v201\n"
    "v_fma_f32 v165, v165, s69, v201\n"
    "v_mfma_f32_16x16x32_bf16 v[4:7], a[100:103], v[180:183], v[4:7]\n"
    "v_cvt_pk_bf16_f32 v176, v160, v161\n"
    "v_cvt_pk_bf16_f32 v177, v162, v163\n"
    "v_mfma_f32_16x16x32_bf16 v[8:11], a[104:107], v[180:183], v[8:11]\n"
    "v_exp_f32 v164, v164\n"
    "v_mfma_f32_16x16x32_bf16 v[12:15], a[108:111], v[180:183], v[12:15]\n"
    "v_exp_f32 v165, v165\n"
    "v_mfma_f32_16x16x32_bf16 v[16:19], a[112:115], v[180:183], v[16:19]\n"
    "v_fma_f32 v166, v166, s69, v201\n"
    "v_fma_f32 v167, v167, s69, v201\n"
    "v_mfma_f32_16x16x32_bf16 v[20:23], a[116:119], v[180:183], v[20:23]\n"
    "v_exp_f32 v166, v166\n"
    "v_mfma_f32_16x16x32_bf16 v[24:27], a[120:123], v[180:183], v[24:27]\n"
    "v_exp_f32 v167, v167\n"
    "v_mfma_f32_16x16x32_bf16 v[28:31], a[124:127], v[180:183], v[28:31]\n"
    "v_cvt_pk_bf16_f32 v178, v164, v165\n"
    "v_cvt_pk_bf16_f32 v179, v166, v167\n"
    "v_mfma_f32_16x16x32_bf16 v[160:163], a[32:35], a[208:211], 0\n"
    "v_max3_f32 v196, v168, v169, v170\n"
    "v_max3_f32 v197, v171, v172, v173\n"
    "v_mfma_f32_16x16x32_bf16 v[160:163], a[36:39], a[212:215], v[160:163]\n"
    "v_max3_f32 v196, v196, v174, v175\n"
    "v_max_f32 v196, v196, v197\n"
    "v_mfma_f32_16x16x32_bf16 v[160:163], a[40:43], a[216:219], v[160:163]\n"
    "v_cmp_gt_f32 vcc, v196, v191\n"
    "v_mfma_f32_16x16x32_bf16 v[160:163], a[44:47], a[220:223], v[160:163]\n"
    "s_cbranch_vccnz B_RARE_DD_2\n"
    "B_BACK_DD_2:\n"
    "v_fma_f32 v168, v168, s69, v202\n"
    "v_fma_f32 v169, v169, s69, v202\n"
    "v_mfma_f32_16x16x32_bf16 v[164:167], a[48:51], a[208:211], 0\n"
    "s_add_u32 m0, s83, 0\n"
    "s_waitcnt vmcnt(16)\n"
    "v_exp_f32 v168, v168\n"
    "v_mfma_f32_16x16x32_bf16 v[164:167], a[52:55], a[212:215], v[164:167]\n"
    "buffer_load_dwordx4 v226, s[44:47], s82 offen nt lds\n"
    "v_exp_f32 v169, v169\n"
    "v_mfma_f32_16x16x32_bf16 v[164:167], a[56:59], a[216:219], v[164:167]\n"
    "buffer_load_dwordx4 v227, s[44:47], s82 offen offset:1024 nt lds\n"
    "v_fma_f32 v170, v170, s69, v202\n"
    "v_fma_f32 v171, v171, s69, v202\n"
    "v_mfma_f32_16x16x32_bf16 v[164:167], a[60:63], a[220:223], v[164:167]\n"
    "buffer_load_dwordx4 v228, s[44:47], s82 offen offset:2048 nt lds\n"
    "v_exp_f32 v170, v170\n"
    "v_mfma_f32_16x16x32_bf16 a[132:135], a[148:151], v[176:179], a[132:135]\n"
    "buffer_load_dwordx4 v229, s[44:47], s82 offen offset:3072 nt lds\n"
    "v_exp_f32 v171, v171\n"
    "v_mfma_f32_16x16x32_bf16 v[32:35], a[96:99], v[176:179], v[32:35]\n"
    "s_add_u32 s79, s82, 0x1000\n"
    "v_fma_f32 v172, v172, s69, v202\n"
    "v_fma_f32 v173, v173, s69, v202\n"
    "v_mfma_f32_16x16x32_bf16 v[36:39], a[100:103], v[176:179], v[36:39]\n"
    "s_add_u32 m0, s83, 0x1000\n"
    "v_cvt_pk_bf16_f32 v180, v168, v169\n"
    "v_cvt_pk_bf16_f32 v181, v170, v171\n"
    "v_mfma_f32_16x16x32_bf16 v[40:43], a[104:107], v[176:179], v[40:43]\n"
    "buffer_load_dwordx4 v226, s[44:47], s79 offen nt lds\n"
    "v_exp_f32 v172, v172\n"
    "v_mfma_f32_16x16x32_bf16 v[44:47], a[108:111], v[176:179], v[44:47]\n"
    "buffer_load_dwordx4 v227, s[44:47], s79 offen offset:1024 nt lds\n"
    "v_exp_f32 v173, v173\n"
    "v_mfma_f32_16x16x32_bf16 v[48:51], a[112:115], v[176:179], v[48:51]\n"
    "buffer_load_dwordx4 v228, s[44:47], s79 offen offset:2048 nt lds\n"
    "v_fma_f32 v174, v174, s69, v202\n"
    "v_fma_f32 v175, v175, s69, v202\n"
    "v_mfma_f32_16x16x32_bf16 v[52:55], a[116:119], v[176:179], v[52:55]\n"
    "buffer_load_dwordx4 v229, s[44:47], s79 offen offset:3072 nt lds\n"
    "v_exp_f32 v174, v174\n"
    "v_mfma_f32_16x16x32_bf16 v[56:59], a[120:123], v[176:179], v[56:59]\n"
    "ds_read_b128 a[0:3], v214 offset:8192\n"
    "v_exp_f32 v175, v175\n"
    "v_mfma_f32_16x16x32_bf16 v[60:63], a[124:127], v[176:179], v[60:63]\n"
    "ds_read_b128 a[4:7], v215 offset:8192\n"
    "v_cvt_pk_bf16_f32 v182, v172, v173\n"
    "v_cvt_pk_bf16_f32 v183, v174, v175\n"
    "v_mfma_f32_16x16x32_bf16 v[168:171], a[32:35], a[224:227], 0\n"
    "ds_read_b128 a[8:11], v216 offset:8192\n"
    "v_max3_f32 v196, v160, v161, v162\n"
    "v_max3_f32 v197, v163, v164, v165\n"
    "v_mfma_f32_16x16x32_bf16 v[168:171], a[36:39], a[228:231], v[168:171]\n"
    "ds_read_b128 a[12:15], v217 offset:8192\n"
    "v_max3_f32 v196, v196, v166, v167\n"
    "v_max_f32 v196, v196, v197\n"
    "v_mfma_f32_16x16x32_bf16 v[168:171], a[40:43], a[232:235], v[168:171]\n"
    "ds_read_b128 a[16:19], v214 offset:12288\n"
    "ds_read_b128 a[20:23], v215 offset:12288\n"
    "v_cmp_gt_f32 vcc, v196, v192\n"
    "v_mfma_f32_16x16x32_bf16 v[168:171], a[44:47], a[236:239], v[168:171]\n"
    "s_cbranch_vccnz B_RARE_DD_3\n"
    "B_BACK_DD_3:\n"
    "v_fma_f32 v160, v160, s69, v203\n"
    "v_fma_f32 v161, v161, s69, v203\n"
    "v_mfma_f32_16x16x32_bf16 v[172:175], a[48:51], a[224:227], 0\n"
    "ds_read_b128 a[24:27], v216 offset:12288\n"
    "v_exp_f32 v160, v160\n"
    "v_mfma_f32_16x16x32_bf16 v[172:175], a[52:55], a[228:231], v[172:175]\n"
    "ds_read_b128 a[28:31], v217 offset:12288\n"
    "v_exp_f32 v161, v161\n"
    "v_mfma_f32_16x16x32_bf16 v[172:175], a[56:59], a[232:235], v[172:175]\n"
    "v_fma_f32 v162, v162, s69, v203\n"
    "v_fma_f32 v163, v163, s69, v203\n"
    "v_mfma_f32_16x16x32_bf16 v[172:175], a[60:63], a[236:239], v[172:175]\n"
    "v_exp_f32 v162, v162\n"
    "v_mfma_f32_16x16x32_bf16 a[136:139], a[148:151], v[180:183], a[136:139]\n"
    "v_exp_f32 v163, v163\n"
    "v_mfma_f32_16x16x32_bf16 v[64:67], a[96:99], v[180:183], v[64:67]\n"
    "v_fma_f32 v164, v164, s69, v203\n"
    "v_fma_f32 v165, v165, s69, v203\n"
    "v_mfma_f32_16x16x32_bf16 v[68:71], a[100:103], v[180:183], v[68:71]\n"
    "v_cvt_pk_bf16_f32 v176, v160, v161\n"
    "v_cvt_pk_bf16_f32 v177, v162, v163\n"
    "v_mfma_f32_16x16x32_bf16 v[72:75], a[104:107], v[180:183], v[72:75]\n"
    "v_exp_f32 v164, v164\n"
    "v_mfma_f32_16x16x32_bf16 v[76:79], a[108:111], v[180:183], v[76:79]\n"
    "v_exp_f32 v165, v165\n"
    "v_mfma_f32_16x16x32_bf16 v[80:83], a[112:115], v[180:183], v[80:83]\n"
    "v_fma_f32 v166, v166, s69, v203\n"
    "v_fma_f32 v167, v167, s69, v203\n"
    "v_mfma_f32_16x16x32_bf16 v[84:87], a[116:119], v[180:183], v[84:87]\n"
    "v_exp_f32 v166, v166\n"
    "v_mfma_f32_16x16x32_bf16 v[88:91], a[120:123], v[180:183], v[88:91]\n"
    "v_exp_f32 v167, v167\n"
    "v_mfma_f32_16x16x32_bf16 v[92:95], a[124:127], v[180:183], v[92:95]\n"
    "s_waitcnt lgkmcnt(0)\n"
    "v_cvt_pk_bf16_f32 v178, v164, v165\n"
    "v_cvt_pk_bf16_f32 v179, v166, v167\n"
    "v_mfma_f32_16x16x32_bf16 v[160:163], a[0:3], a[160:163], 0\n"
    "v_max3_f32 v196, v168, v169, v170\n"
    "v_max3_f32 v197, v171, v172, v173\n"
    "v_mfma_f32_16x16x32_bf16 v[160:163], a[4:7], a[164:167], v[160:163]\n"
    "v_max3_f32 v196, v196, v174, v175\n"
    "v_max_f32 v196, v196, v197\n"
    "v_mfma_f32_16x16x32_bf16 v[160:163], a[8:11], a[168:171], v[160:163]\n"
    "v_cmp_gt_f32 vcc, v196, v193\n"
    "v_mfma_f32_16x16x32_bf16 v[160:163], a[12:15], a[172:175], v[160:163]\n"
    "s_cbranch_vccnz B_RARE_DD_4\n"
    "B_BACK_DD_4:\n"
    "v_fma_f32 v168, v168, s69, v204\n"
    "v_fma_f32 v169, v169, s69, v204\n"
    "v_mfma_f32_16x16x32_bf16 v[164:167], a[16:19], a[160:163], 0\n"
    "v_exp_f32 v168, v168\n"
    "v_mfma_f32_16x16x32_bf16 v[164:167], a[20:23], a[164:167], v[164:167]\n"
    "v_exp_f32 v169, v169\n"
    "v_mfma_f32_16x16x32_bf16 v[164:167], a[24:27], a[168:171], v[164:167]\n"
    "v_fma_f32 v170, v170, s69, v204\n"
    "v_fma_f32 v171, v171, s69, v204\n"
    "v_mfma_f32_16x16x32_bf16 v[164:167], a[28:31], a[172:175], v[164:167]\n"
    "v_exp_f32 v170, v170\n"
    "v_mfma_f32_16x16x32_bf16 a[140:143], a[148:151], v[176:179], a[140:143]\n"
    "v_exp_f32 v171, v171\n"
    "v_mfma_f32_16x16x32_bf16 v[96:99], a[96:99], v[176:179], v[96:99]\n"
    "v_fma_f32 v172, v172, s69, v204\n"
    "v_fma_f32 v173, v173, s69, v204\n"
    "v_mfma_f32_16x16x32_bf16 v[100:103], a[100:103], v[176:179], v[100:103]\n"
    "v_cvt_pk_bf16_f32 v180, v168, v169\n"
    "v_cvt_pk_bf16_f32 v181, v170, v171\n"
    "v_mfma_f32_16x16x32_bf16 v[104:107], a[104:107], v[176:179], v[104:107]\n"
    "v_exp_f32 v172, v172\n"
    "v_mfma_f32_16x16x32_bf16 v[108:111], a[108:111], v[176:179], v[108:111]\n"
    "v_exp_f32 v173, v173\n"
    "v_mfma_f32_16x16x32_bf16 v[112:115], a[112:115], v[176:179], v[112:115]\n"
    "v_fma_f32 v174, v174, s69, v204\n"
    "v_fma_f32 v175, v175, s69, v204\n"
    "v_mfma_f32_16x16x32_bf16 v[116:119], a[116:119], v[176:179], v[116:119]\n"
    "v_exp_f32 v174, v174\n"
    "v_mfma_f32_16x16x32_bf16 v[120:123], a[120:123], v[176:179], v[120:123]\n"
    "v_exp_f32 v175, v175\n"
    "v_mfma_f32_16x16x32_bf16 v[124:127], a[124:127], v[176:179], v[124:127]\n"
    "v_cvt_pk_bf16_f32 v182, v172, v173\n"
    "v_cvt_pk_bf16_f32 v183, v174, v175\n"
    "s_add_u32 s81, s81, 0x4000\n"
    "s_and_b32 s81, s81, 0x1e000\n"
    "s_add_u32 s80, s80, 1\n"
    "s_cmp_lt_u32 s80, 6\n"
    "s_cbranch_scc1 B_ATT_LOOPD\n"
    "B_ATT_LOOP:\n"
    "v_mfma_f32_16x16x32_bf16 v[168:171], a[0:3], a[176:179], 0\n"
    "s_add_u32 s78, s81, 0x2000\n"
    "s_and_b32 s78, s78, 0x1e000\n"
    "s_add_u32 m0, s71, 0x8000\n"
    "s_waitcnt vmcnt(20)\n"
    "v_max3_f32 v196, v160, v161, v162\n"
    "v_max3_f32 v197, v163, v164, v165\n"
    "v_mfma_f32_16x16x32_bf16 v[168:171], a[4:7], a[180:183], v[168:171]\n"
    "buffer_load_dwordx4 v226, s[44:47], s78 offen nt lds\n"
    "v_max3_f32 v196, v196, v166, v167\n"
    "v_max_f32 v196, v196, v197\n"
    "v_mfma_f32_16x16x32_bf16 v[168:171], a[8:11], a[184:187], v[168:171]\n"
    "buffer_load_dwordx4 v227, s[44:47], s78 offen offset:1024 nt lds\n"
    "ds_read_b64_tr_b16 a[64:65], v218 offset:0\n"
    "v_cmp_gt_f32 vcc, v196, v189\n"
    "v_mfma_f32_16x16x32_bf16 v[168:171], a[12:15], a[188:191], v[168:171]\n"
    "s_add_u32 s79, s81, 0x2000\n"
    "s_cbranch_vccnz B_RARE_EP_0\n"
    "B_BACK_EP_0:\n"
    "v_fma_f32 v160, v160, s69, v200\n"
    "v_fma_f32 v161, v161, s69, v200\n"
    "v_mfma_f32_16x16x32_bf16 v[172:175], a[16:19], a[176:179], 0\n"
    "s_and_b32 s79, s79, 0x1e000\n"
    "buffer_load_dwordx4 v228, s[44:47], s78 offen offset:2048 nt lds\n"
    "v_exp_f32 v160, v160\n"
    "v_mfma_f32_16x16x32_bf16 v[172:175], a[20:23], a[180:183], v[172:175]\n"
    "s_add_u32 s79, s79, 0x1000\n"
    "buffer_load_dwordx4 v229, s[44:47], s78 offen offset:3072 nt lds\n"
    "v_exp_f32 v161, v161\n"
    "v_mfma_f32_16x16x32_bf16 v[172:175], a[24:27], a[184:187], v[172:175]\n"
    "s_add_u32 m0, s71, 0x9000\n"
    "s_waitcnt vmcnt(20)\n"
    "ds_read_b64_tr_b16 a[68:69], v219 offset:0\n"
    "v_fma_f32 v162, v162, s69, v200\n"
    "v_fma_f32 v163, v163, s69, v200\n"
    "v_mfma_f32_16x16x32_bf16 v[172:175], a[28:31], a[188:191], v[172:175]\n"
    "buffer_load_dwordx4 v226, s[44:47], s79 offen nt lds\n"
    "ds_read_b64_tr_b16 a[72:73], v220 offset:0\n"
    "v_exp_f32 v162, v162\n"
    "v_mfma_f32_16x16x32_bf16 a[144:147], a[148:151], v[180:183], a[144:147]\n"
    "buffer_load_dwordx4 v227, s[44:47], s79 offen offset:1024 nt lds\n"
    "ds_read_b64_tr_b16 a[76:77], v221 offset:0\n"
    "v_exp_f32 v163, v163\n"
    "v_mfma_f32_16x16x32_bf16 v[128:131], a[96:99], v[180:183], v[128:131]\n"
    "buffer_load_dwordx4 v228, s[44:47], s79 offen offset:2048 nt lds\n"
    "v_fma_f32 v164, v164, s69, v200\n"
    "v_fma_f32 v165, v165, s69, v200\n"
    "v_mfma_f32_16x16x32_bf16 v[132:135], a[100:103], v[180:183], v[132:135]\n"
    "buffer_load_dwordx4 v229, s[44:47], s79 offen offset:3072 nt lds\n"
    "v_cvt_pk_bf16_f32 v176, v160, v161\n"
    "v_cvt_pk_bf16_f32 v177, v162, v163\n"
    "v_mfma_f32_16x16x32_bf16 v[136:139], a[104:107], v[180:183], v[136:139]\n"
    "ds_read_b64_tr_b16 a[80:81], v222 offset:0\n"
    "ds_read_b64_tr_b16 a[84:85], v223 offset:0\n"
    "v_exp_f32 v164, v164\n"
    "v_mfma_f32_16x16x32_bf16 v[140:143], a[108:111], v[180:183], v[140:143]\n"
    "ds_read_b64_tr_b16 a[88:89], v224 offset:0\n"
    "ds_read_b64_tr_b16 a[92:93], v225 offset:0\n"
    "v_exp_f32 v165, v165\n"
    "v_mfma_f32_16x16x32_bf16 v[144:147], a[112:115], v[180:183], v[144:147]\n"
    "ds_read_b64_tr_b16 a[66:67], v218 offset:4096\n"
    "v_fma_f32 v166, v166, s69, v200\n"
    "v_fma_f32 v167, v167, s69, v200\n"
    "v_mfma_f32_16x16x32_bf16 v[148:151], a[116:119], v[180:183], v[148:151]\n"
    "ds_read_b64_tr_b16 a[70:71], v219 offset:4096\n"
    "v_exp_f32 v166, v166\n"
    "v_mfma_f32_16x16x32_bf16 v[152:155], a[120:123], v[180:183], v[152:155]\n"
    "ds_read_b64_tr_b16 a[74:75], v220 offset:4096\n"
    "v_exp_f32 v167, v167\n"
    "v_mfma_f32_16x16x32_bf16 v[156:159], a[124:127], v[180:183], v[156:159]\n"
    "ds_read_b64_tr_b16 a[78:79], v221 offset:4096\n"
    "v_cvt_pk_bf16_f32 v178, v164, v165\n"
    "v_cvt_pk_bf16_f32 v179, v166, v167\n"
    "v_mfma_f32_16x16x32_bf16 v[160:163], a[0:3], a[192:195], 0\n"
    "ds_read_b64_tr_b16 a[82:83], v222 offset:4096\n"
    "v_max3_f32 v196, v168, v169, v170\n"
    "v_max3_f32 v197, v171, v172, v173\n"
    "v_mfma_f32_16x16x32_bf16 v[160:163], a[4:7], a[196:199], v[160:163]\n"
    "ds_read_b64_tr_b16 a[86:87], v223 offset:4096\n"
    "v_max3_f32 v196, v196, v174, v175\n"
    "v_max_f32 v196, v196, v197\n"
    "v_mfma_f32_16x16x32_bf16 v[160:163], a[8:11], a[200:203], v[160:163]\n"
    "ds_read_b64_tr_b16 a[90:91], v224 offset:4096\n"
    "ds_read_b64_tr_b16 a[94:95], v225 offset:4096\n"
    "v_cmp_gt_f32 vcc, v196, v190\n"
    "v_mfma_f32_16x16x32_bf16 v[160:163], a[12:15], a[204:207], v[160:163]\n"
    "s_cbranch_vccnz B_RARE_EP_1\n"
    "B_BACK_EP_1:\n"
    "v_fma_f32 v168, v168, s69, v201\n"
    "v_fma_f32 v169, v169, s69, v201\n"
    "v_mfma_f32_16x16x32_bf16 v[164:167], a[16:19], a[192:195], 0\n"
    "v_exp_f32 v168, v168\n"
    "v_mfma_f32_16x16x32_bf16 v[164:167], a[20:23], a[196:199], v[164:167]\n"
    "v_exp_f32 v169, v169\n"
    "v_mfma_f32_16x16x32_bf16 v[164:167], a[24:27], a[200:203], v[164:167]\n"
    "v_fma_f32 v170, v170, s69, v201\n"
    "v_fma_f32 v171, v171, s69, v201\n"
    "v_mfma_f32_16x16x32_bf16 v[164:167], a[28:31], a[204:207], v[164:167]\n"
    "v_exp_f32 v170, v170\n"
    "v_mfma_f32_16x16x32_bf16 a[128:131], a[148:151], v[176:179], a[128:131]\n"
    "s_waitcnt lgkmcnt(0)\n"
    "v_exp_f32 v171, v171\n"
    "v_mfma_f32_16x16x32_bf16 v[0:3], a[64:67], v[176:179], v[0:3]\n"
    "v_fma_f32 v172, v172, s69, v201\n"
    "v_fma_f32 v173, v173, s69, v201\n"
    "v_mfma_f32_16x16x32_bf16 v[4:7], a[68:71], v[176:179], v[4:7]\n"
    "v_cvt_pk_bf16_f32 v180, v168, v169\n"
    "v_cvt_pk_bf16_f32 v181, v170, v171\n"
    "v_mfma_f32_16x16x32_bf16 v[8:11], a[72:75], v[176:179], v[8:11]\n"
    "v_exp_f32 v172, v172\n"
    "v_mfma_f32_16x16x32_bf16 v[12:15], a[76:79], v[176:179], v[12:15]\n"
    "v_exp_f32 v173, v173\n"
    "v_mfma_f32_16x16x32_bf16 v[16:19], a[80:83], v[176:179], v[16:19]\n"
    "v_fma_f32 v174, v174, s69, v201\n"
    "v_fma_f32 v175, v175, s69, v201\n"
    "v_mfma_f32_16x16x32_bf16 v[20:23], a[84:87], v[176:179], v[20:23]\n"
    "v_exp_f32 v174, v174\n"
    "v_mfma_f32_16x16x32_bf16 v[24:27], a[88:91], v[176:179], v[24:27]\n"
    "v_exp_f32 v175, v175\n"
    "v_mfma_f32_16x16x32_bf16 v[28:31], a[92:95], v[176:179], v[28:31]\n"
    "v_cvt_pk_bf16_f32 v182, v172, v173\n"
    "v_cvt_pk_bf16_f32 v183, v174, v175\n"
    "v_mfma_f32_16x16x32_bf16 v[168:171], a[0:3], a[208:211], 0\n"
    "v_max3_f32 v196, v160, v161, v162\n"
    "v_max3_f32 v197, v163, v164, v165\n"
    "v_mfma_f32_16x16x32_bf16 v[168:171], a[4:7], a[212:215], v[168:171]\n"
    "v_max3_f32 v196, v196, v166, v167\n"
    "v_max_f32 v196, v196, v197\n"
    "v_mfma_f32_16x16x32_bf16 v[168:171], a[8:11], a[216:219], v[168:171]\n"
    "s_add_u32 s78, s81, 0x4000\n"
    "v_cmp_gt_f32 vcc, v196, v191\n"
    "v_mfma_f32_16x16x32_bf16 v[168:171], a[12:15], a[220:223], v[168:171]\n"
    "s_and_b32 s78, s78, 0x1e000\n"
    "s_cbranch_vccnz B_RARE_EP_2\n"
    "B_BACK_EP_2:\n"
    "v_fma_f32 v160, v160, s69, v202\n"
    "v_fma_f32 v161, v161, s69, v202\n"
    "v_mfma_f32_16x16x32_bf16 v[172:175], a[16:19], a[208:211], 0\n"
    "s_add_u32 m0, s71, 0x2000\n"
    "s_waitcnt vmcnt(20)\n"
    "v_exp_f32 v160, v160\n"
    "v_mfma_f32_16x16x32_bf16 v[172:175], a[20:23], a[212:215], v[172:175]\n"
    "buffer_load_dwordx4 v226, s[40:43], s78 offen nt lds\n"
    "v_exp_f32 v161, v161\n"
    "v_mfma_f32_16x16x32_bf16 v[172:175], a[24:27], a[216:219], v[172:175]\n"
    "buffer_load_dwordx4 v227, s[40:43], s78 offen offset:1024 nt lds\n"
    "v_fma_f32 v162, v162, s69, v202\n"
    "v_fma_f32 v163, v163, s69, v202\n"
    "v_mfma_f32_16x16x32_bf16 v[172:175], a[28:31], a[220:223], v[172:175]\n"
    "s_add_u32 s79, s81, 0x4000\n"
    "buffer_load_dwordx4 v228, s[40:43], s78 offen offset:2048 nt lds\n"
    "v_exp_f32 v162, v162\n"
    "v_mfma_f32_16x16x32_bf16 a[132:135], a[148:151], v[180:183], a[132:135]\n"
    "s_and_b32 s79, s79, 0x1e000\n"
    "buffer_load_dwordx4 v229, s[40:43], s78 offen offset:3072 nt lds\n"
    "v_exp_f32 v163, v163\n"
    "v_mfma_f32_16x16x32_bf16 v[32:35], a[64:67], v[180:183], v[32:35]\n"
    "s_add_u32 s79, s79, 0x1000\n"
    "v_fma_f32 v164, v164, s69, v202\n"
    "v_fma_f32 v165, v165, s69, v202\n"
    "v_mfma_f32_16x16x32_bf16 v[36:39], a[68:71], v[180:183], v[36:39]\n"
    "s_add_u32 m0, s71, 0x3000\n"
    "s_waitcnt vmcnt(20)\n"
    "v_cvt_pk_bf16_f32 v176, v160, v161\n"
    "v_cvt_pk_bf16_f32 v177, v162, v163\n"
    "v_mfma_f32_16x16x32_bf16 v[40:43], a[72:75], v[180:183], v[40:43]\n"
    "buffer_load_dwordx4 v226, s[40:43], s79 offen nt lds\n"
    "v_exp_f32 v164, v164\n"
    "v_mfma_f32_16x16x32_bf16 v[44:47], a[76:79], v[180:183], v[44:47]\n"
    "buffer_load_dwordx4 v227, s[40:43], s79 offen offset:1024 nt lds\n"
    "v_exp_f32 v165, v165\n"
    "v_mfma_f32_16x16x32_bf16 v[48:51], a[80:83], v[180:183], v[48:51]\n"
    "buffer_load_dwordx4 v228, s[40:43], s79 offen offset:2048 nt lds\n"
    "v_fma_f32 v166, v166, s69, v202\n"
    "v_fma_f32 v167, v167, s69, v202\n"
    "v_mfma_f32_16x16x32_bf16 v[52:55], a[84:87], v[180:183], v[52:55]\n"
    "buffer_load_dwordx4 v229, s[40:43], s79 offen offset:3072 nt lds\n"
    "v_exp_f32 v166, v166\n"
    "v_mfma_f32_16x16x32_bf16 v[56:59], a[88:91], v[180:183], v[56:59]\n"
    "ds_read_b128 a[32:35], v214 offset:24576\n"
    "v_exp_f32 v167, v167\n"
    "v_mfma_f32_16x16x32_bf16 v[60:63], a[92:95], v[180:183], v[60:63]\n"
    "ds_read_b128 a[36:39], v215 offset:24576\n"
    "v_cvt_pk_bf16_f32 v178, v164, v165\n"
    "v_cvt_pk_bf16_f32 v179, v166, v167\n"
    "v_mfma_f32_16x16x32_bf16 v[160:163], a[0:3], a[224:227], 0\n"
    "ds_read_b128 a[40:43], v216 offset:24576\n"
    "v_max3_f32 v196, v168, v169, v170\n"
    "v_max3_f32 v197, v171, v172, v173\n"
    "v_mfma_f32_16x16x32_bf16 v[160:163], a[4:7], a[228:231], v[160:163]\n"
    "ds_read_b128 a[44:47], v217 offset:24576\n"
    "v_max3_f32 v196, v196, v174, v175\n"
    "v_max_f32 v196, v196, v197\n"
    "v_mfma_f32_16x16x32_bf16 v[160:163], a[8:11], a[232:235], v[160:163]\n"
    "ds_read_b128 a[48:51], v214 offset:28672\n"
    "ds_read_b128 a[52:55], v215 offset:28672\n"
    "v_cmp_gt_f32 vcc, v196, v192\n"
    "v_mfma_f32_16x16x32_bf16 v[160:163], a[12:15], a[236:239], v[160:163]\n"
    "s_cbranch_vccnz B_RARE_EP_3\n"
    "B_BACK_EP_3:\n"
    "v_fma_f32 v168, v168, s69, v203\n"
    "v_fma_f32 v169, v169, s69, v203\n"
    "v_mfma_f32_16x16x32_bf16 v[164:167], a[16:19], a[224:227], 0\n"
    "ds_read_b128 a[56:59], v216 offset:28672\n"
    "v_exp_f32 v168, v168\n"
    "v_mfma_f32_16x16x32_bf16 v[164:167], a[20:23], a[228:231], v[164:167]\n"
    "ds_read_b128 a[60:63], v217 offset:28672\n"
    "v_exp_f32 v169, v169\n"
    "v_mfma_f32_16x16x32_bf16 v[164:167], a[24:27], a[232:235], v[164:167]\n"
    "v_fma_f32 v170, v170, s69, v203\n"
    "v_fma_f32 v171, v171, s69, v203\n"
    "v_mfma_f32_16x16x32_bf16 v[164:167], a[28:31], a[236:239], v[164:167]\n"
    "v_exp_f32 v170, v170\n"
    "v_mfma_f32_16x16x32_bf16 a[136:139], a[148:151], v[176:179], a[136:139]\n"
    "v_exp_f32 v171, v171\n"
    "v_mfma_f32_16x16x32_bf16 v[64:67], a[64:67], v[176:179], v[64:67]\n"
    "v_fma_f32 v172, v172, s69, v203\n"
    "v_fma_f32 v173, v173, s69, v203\n"
    "v_mfma_f32_16x16x32_bf16 v[68:71], a[68:71], v[176:179], v[68:71]\n"
    "v_cvt_pk_bf16_f32 v180, v168, v169\n"
    "v_cvt_pk_bf16_f32 v181, v170, v171\n"
    "v_mfma_f32_16x16x32_bf16 v[72:75], a[72:75], v[176:179], v[72:75]\n"
    "v_exp_f32 v172, v172\n"
    "v_mfma_f32_16x16x32_bf16 v[76:79], a[76:79], v[176:179], v[76:79]\n"
    "v_exp_f32 v173, v173\n"
    "v_mfma_f32_16x16x32_bf16 v[80:83], a[80:83], v[176:179], v[80:83]\n"
    "v_fma_f32 v174, v174, s69, v203\n"
    "v_fma_f32 v175, v175, s69, v203\n"
    "v_mfma_f32_16x16x32_bf16 v[84:87], a[84:87], v[176:179], v[84:87]\n"
    "v_exp_f32 v174, v174\n"
    "v_mfma_f32_16x16x32_bf16 v[88:91], a[88:91], v[176:179], v[88:91]\n"
    "v_exp_f32 v175, v175\n"
    "v_mfma_f32_16x16x32_bf16 v[92:95], a[92:95], v[176:179], v[92:95]\n"
    "s_waitcnt lgkmcnt(0)\n"
    "v_cvt_pk_bf16_f32 v182, v172, v173\n"
    "v_cvt_pk_bf16_f32 v183, v174, v175\n"
    "v_mfma_f32_16x16x32_bf16 v[168:171], a[32:35], a[160:163], 0\n"
    "v_max3_f32 v196, v160, v161, v162\n"
    "v_max3_f32 v197, v163, v164, v165\n"
    "v_mfma_f32_16x16x32_bf16 v[168:171], a[36:39], a[164:167], v[168:171]\n"
    "v_max3_f32 v196, v196, v166, v167\n"
    "v_max_f32 v196, v196, v197\n"
    "v_mfma_f32_16x16x32_bf16 v[168:171], a[40:43], a[168:171], v[168:171]\n"
    "v_cmp_gt_f32 vcc, v196, v193\n"
    "v_mfma_f32_16x16x32_bf16 v[168:171], a[44:47], a[172:175], v[168:171]\n"
    "s_cbranch_vccnz B_RARE_EP_4\n"
    "B_BACK_EP_4:\n"
    "v_fma_f32 v160, v160, s69, v204\n"
    "v_fma_f32 v161, v161, s69, v204\n"
    "v_mfma_f32_16x16x32_bf16 v[172:175], a[48:51], a[160:163], 0\n"
    "v_exp_f32 v160, v160\n"
    "v_mfma_f32_16x16x32_bf16 v[172:175], a[52:55], a[164:167], v[172:175]\n"
    "v_exp_f32 v161, v161\n"
    "v_mfma_f32_16x16x32_bf16 v[172:175], a[56:59], a[168:171], v[172:175]\n"
    "v_fma_f32 v162, v162, s69, v204\n"
    "v_fma_f32 v163, v163, s69, v204\n"
    "v_mfma_f32_16x16x32_bf16 v[172:175], a[60:63], a[172:175], v[172:175]\n"
    "v_exp_f32 v162, v162\n"
    "v_mfma_f32_16x16x32_bf16 a[140:143], a[148:151], v[180:183], a[140:143]\n"
    "v_exp_f32 v163, v163\n"
    "v_mfma_f32_16x16x32_bf16 v[96:99], a[64:67], v[180:183], v[96:99]\n"
    "v_fma_f32 v164, v164, s69, v204\n"
    "v_fma_f32 v165, v165, s69, v204\n"
    "v_mfma_f32_16x16x32_bf16 v[100:103], a[68:71], v[180:183], v[100:103]\n"
    "v_cvt_pk_bf16_f32 v176, v160, v161\n"
    "v_cvt_pk_bf16_f32 v177, v162, v163\n"
    "v_mfma_f32_16x16x32_bf16 v[104:107], a[72:75], v[180:183], v[104:107]\n"
    "v_exp_f32 v164, v164\n"
    "v_mfma_f32_16x16x32_bf16 v[108:111], a[76:79], v[180:183], v[108:111]\n"
    "v_exp_f32 v165, v165\n"
    "v_mfma_f32_16x16x32_bf16 v[112:115], a[80:83], v[180:183], v[112:115]\n"
    "v_fma_f32 v166, v166, s69, v204\n"
    "v_fma_f32 v167, v167, s69, v204\n"
    "v_mfma_f32_16x16x32_bf16 v[116:119], a[84:87], v[180:183], v[116:119]\n"
    "v_exp_f32 v166, v166\n"
    "v_mfma_f32_16x16x32_bf16 v[120:123], a[88:91], v[180:183], v[120:123]\n"
    "v_exp_f32 v167, v167\n"
    "v_mfma_f32_16x16x32_bf16 v[124:127], a[92:95], v[180:183], v[124:127]\n"
    "v_cvt_pk_bf16_f32 v178, v164, v165\n"
    "v_cvt_pk_bf16_f32 v179, v166, v167\n"
    "v_mfma_f32_16x16x32_bf16 v[160:163], a[32:35], a[176:179], 0\n"
    "s_add_u32 s78, s81, 0x4000\n"
    "s_and_b32 s78, s78, 0x1e000\n"
    "s_add_u32 m0, s71, 0\n"
    "s_waitcnt vmcnt(12)\n"
    "v_max3_f32 v196, v168, v169, v170\n"
    "v_max3_f32 v197, v171, v172, v173\n"
    "v_mfma_f32_16x16x32_bf16 v[160:163], a[36:39], a[180:183], v[160:163]\n"
    "buffer_load_dwordx4 v226, s[44:47], s78 offen nt lds\n"
    "v_max3_f32 v196, v196, v174, v175\n"
    "v_max_f32 v196, v196, v197\n"
    "v_mfma_f32_16x16x32_bf16 v[160:163], a[40:43], a[184:187], v[160:163]\n"
    "buffer_load_dwordx4 v227, s[44:47], s78 offen offset:1024 nt lds\n"
    "ds_read_b64_tr_b16 a[96:97], v218 offset:32768\n"
    "v_cmp_gt_f32 vcc, v196, v189\n"
    "v_mfma_f32_16x16x32_bf16 v[160:163], a[44:47], a[188:191], v[160:163]\n"
    "s_add_u32 s79, s81, 0x4000\n"
    "s_cbranch_vccnz B_RARE_DP_0\n"
    "B_BACK_DP_0:\n"
    "v_fma_f32 v168, v168, s69, v200\n"
    "v_fma_f32 v169, v169, s69, v200\n"
    "v_mfma_f32_16x16x32_bf16 v[164:167], a[48:51], a[176:179], 0\n"
    "s_and_b32 s79, s79, 0x1e000\n"
    "buffer_load_dwordx4 v228, s[44:47], s78 offen offset:2048 nt lds\n"
    "v_exp_f32 v168, v168\n"
    "v_mfma_f32_16x16x32_bf16 v[164:167], a[52:55], a[180:183], v[164:167]\n"
    "s_add_u32 s79, s79, 0x1000\n"
    "buffer_load_dwordx4 v229, s[44:47], s78 offen offset:3072 nt lds\n"
    "v_exp_f32 v169, v169\n"
    "v_mfma_f32_16x16x32_bf16 v[164:167], a[56:59], a[184:187], v[164:167]\n"
    "s_add_u32 m0, s71, 0x1000\n"
    "s_waitcnt vmcnt(12)\n"
    "ds_read_b64_tr_b16 a[100:101], v219 offset:32768\n"
    "v_fma_f32 v170, v170, s69, v200\n"
    "v_fma_f32 v171, v171, s69, v200\n"
    "v_mfma_f32_16x16x32_bf16 v[164:167], a[60:63], a[188:191], v[164:167]\n"
    "buffer_load_dwordx4 v226, s[44:47], s79 offen nt lds\n"
    "ds_read_b64_tr_b16 a[104:105], v220 offset:32768\n"
    "v_exp_f32 v170, v170\n"
    "v_mfma_f32_16x16x32_bf16 a[144:147], a[148:151], v[176:179], a[144:147]\n"
    "buffer_load_dwordx4 v227, s[44:47], s79 offen offset:1024 nt lds\n"
    "ds_read_b64_tr_b16 a[108:109], v221 offset:32768\n"
    "v_exp_f32 v171, v171\n"
    "v_mfma_f32_16x16x32_bf16 v[128:131], a[64:67], v[176:179], v[128:131]\n"
    "buffer_load_dwordx4 v228, s[44:47], s79 offen offset:2048 nt lds\n"
    "v_fma_f32 v172, v172, s69, v200\n"
    "v_fma_f32 v173, v173, s69, v200\n"
    "v_mfma_f32_16x16x32_bf16 v[132:135], a[68:71], v[176:179], v[132:135]\n"
    "buffer_load_dwordx4 v229, s[44:47], s79 offen offset:3072 nt lds\n"
    "v_cvt_pk_bf16_f32 v180, v168, v169\n"
    "v_cvt_pk_bf16_f32 v181, v170, v171\n"
    "v_mfma_f32_16x16x32_bf16 v[136:139], a[72:75], v[176:179], v[136:139]\n"
    "ds_read_b64_tr_b16 a[112:113], v222 offset:32768\n"
    "ds_read_b64_tr_b16 a[116:117], v223 offset:32768\n"
    "v_exp_f32 v172, v172\n"
    "v_mfma_f32_16x16x32_bf16 v[140:143], a[76:79], v[176:179], v[140:143]\n"
    "ds_read_b64_tr_b16 a[120:121], v224 offset:32768\n"
    "ds_read_b64_tr_b16 a[124:125], v225 offset:32768\n"
    "v_exp_f32 v173, v173\n"
    "v_mfma_f32_16x16x32_bf16 v[144:147], a[80:83], v[176:179], v[144:147]\n"
    "ds_read_b64_tr_b16 a[98:99], v218 offset:36864\n"
    "v_fma_f32 v174, v174, s69, v200\n"
    "v_fma_f32 v175, v175, s69, v200\n"
    "v_mfma_f32_16x16x32_bf16 v[148:151], a[84:87], v[176:179], v[148:151]\n"
    "ds_read_b64_tr_b16 a[102:103], v219 offset:36864\n"
    "v_exp_f32 v174, v174\n"
    "v_mfma_f32_16x16x32_bf16 v[152:155], a[88:91], v[176:179], v[152:155]\n"
    "ds_read_b64_tr_b16 a[106:107], v220 offset:36864\n"
    "v_exp_f32 v175, v175\n"
    "v_mfma_f32_16x16x32_bf16 v[156:159], a[92:95], v[176:179], v[156:159]\n"
    "ds_read_b64_tr_b16 a[110:111], v221 offset:36864\n"
    "v_cvt_pk_bf16_f32 v182, v172, v173\n"
    "v_cvt_pk_bf16_f32 v183, v174, v175\n"
    "v_mfma_f32_16x16x32_bf16 v[168:171], a[32:35], a[192:195], 0\n"
    "ds_read_b64_tr_b16 a[114:115], v222 offset:36864\n"
    "v_max3_f32 v196, v160, v161, v162\n"
    "v_max3_f32 v197, v163, v164, v165\n"
    "v_mfma_f32_16x16x32_bf16 v[168:171], a[36:39], a[196:199], v[168:171]\n"
    "ds_read_b64_tr_b16 a[118:119], v223 offset:36864\n"
    "v_max3_f32 v196, v196, v166, v167\n"
    "v_max_f32 v196, v196, v197\n"
    "v_mfma_f32_16x16x32_bf16 v[168:171], a[40:43], a[200:203], v[168:171]\n"
    "ds_read_b64_tr_b16 a[122:123], v224 offset:36864\n"
    "ds_read_b64_tr_b16 a[126:127], v225 offset:36864\n"
    "v_cmp_gt_f32 vcc, v196, v190\n"
    "v_mfma_f32_16x16x32_bf16 v[168:171], a[44:47], a[204:207], v[168:171]\n"
    "s_cbranch_vccnz B_RARE_DP_1\n"
    "B_BACK_DP_1:\n"
    "v_fma_f32 v160, v160, s69, v201\n"
    "v_fma_f32 v161, v161, s69, v201\n"
    "v_mfma_f32_16x16x32_bf16 v[172:175], a[48:51], a[192:195], 0\n"
    "v_exp_f32 v160, v160\n"
    "v_mfma_f32_16x16x32_bf16 v[172:175], a[52:55], a[196:199], v[172:175]\n"
    "v_exp_f32 v161, v161\n"
    "v_mfma_f32_16x16x32_bf16 v[172:175], a[56:59], a[200:203], v[172:175]\n"
    "v_fma_f32 v162, v162, s69, v201\n"
    "v_fma_f32 v163, v163, s69, v201\n"
    "v_mfma_f32_16x16x32_bf16 v[172:175], a[60:63], a[204:207], v[172:175]\n"
    "v_exp_f32 v162, v162\n"
    "v_mfma_f32_16x16x32_bf16 a[128:131], a[148:151], v[180:183], a[128:131]\n"
    "s_waitcnt lgkmcnt(0)\n"
    "v_exp_f32 v163, v163\n"
    "v_mfma_f32_16x16x32_bf16 v[0:3], a[96:99], v[180:183], v[0:3]\n"
    "v_fma_f32 v164, v164, s69, v201\n"
    "v_fma_f32 v165, v165, s69, v201\n"
    "v_mfma_f32_16x16x32_bf16 v[4:7], a[100:103], v[180:183], v[4:7]\n"
    "v_cvt_pk_bf16_f32 v176, v160, v161\n"
    "v_cvt_pk_bf16_f32 v177, v162, v163\n"
    "v_mfma_f32_16x16x32_bf16 v[8:11], a[104:107], v[180:183], v[8:11]\n"
    "v_exp_f32 v164, v164\n"
    "v_mfma_f32_16x16x32_bf16 v[12:15], a[108:111], v[180:183], v[12:15]\n"
    "v_exp_f32 v165, v165\n"
    "v_mfma_f32_16x16x32_bf16 v[16:19], a[112:115], v[180:183], v[16:19]\n"
    "v_fma_f32 v166, v166, s69, v201\n"
    "v_fma_f32 v167, v167, s69, v201\n"
    "v_mfma_f32_16x16x32_bf16 v[20:23], a[116:119], v[180:183], v[20:23]\n"
    "v_exp_f32 v166, v166\n"
    "v_mfma_f32_16x16x32_bf16 v[24:27], a[120:123], v[180:183], v[24:27]\n"
    "v_exp_f32 v167, v167\n"
    "v_mfma_f32_16x16x32_bf16 v[28:31], a[124:127], v[180:183], v[28:31]\n"
    "v_cvt_pk_bf16_f32 v178, v164, v165\n"
    "v_cvt_pk_bf16_f32 v179, v166, v167\n"
    "v_mfma_f32_16x16x32_bf16 v[160:163], a[32:35], a[208:211], 0\n"
    "v_max3_f32 v196, v168, v169, v170\n"
    "v_max3_f32 v197, v171, v172, v173\n"
    "v_mfma_f32_16x16x32_bf16 v[160:163], a[36:39], a[212:215], v[160:163]\n"
    "v_max3_f32 v196, v196, v174, v175\n"
    "v_max_f32 v196, v196, v197\n"
    "v_mfma_f32_16x16x32_bf16 v[160:163], a[40:43], a[216:219], v[160:163]\n"
    "s_add_u32 s78, s81, 0x6000\n"
    "v_cmp_gt_f32 vcc, v196, v191\n"
    "v_mfma_f32_16x16x32_bf16 v[160:163], a[44:47], a[220:223], v[160:163]\n"
    "s_and_b32 s78, s78, 0x1e000\n"
    "s_cbranch_vccnz B_RARE_DP_2\n"
    "B_BACK_DP_2:\n"
    "v_fma_f32 v168, v168, s69, v202\n"
    "v_fma_f32 v169, v169, s69, v202\n"
    "v_mfma_f32_16x16x32_bf16 v[164:167], a[48:51], a[208:211], 0\n"
    "s_add_u32 m0, s71, 0x6000\n"
    "s_waitcnt vmcnt(12)\n"
    "v_exp_f32 v168, v168\n"
    "v_mfma_f32_16x16x32_bf16 v[164:167], a[52:55], a[212:215], v[164:167]\n"
    "buffer_load_dwordx4 v226, s[40:43], s78 offen nt lds\n"
    "v_exp_f32 v169, v169\n"
    "v_mfma_f32_16x16x32_bf16 v[164:167], a[56:59], a[216:219], v[164:167]\n"
    "buffer_load_dwordx4 v227, s[40:43], s78 offen offset:1024 nt lds\n"
    "v_fma_f32 v170, v170, s69, v202\n"
    "v_fma_f32 v171, v171, s69, v202\n"
    "v_mfma_f32_16x16x32_bf16 v[164:167], a[60:63], a[220:223], v[164:167]\n"
    "s_add_u32 s79, s81, 0x6000\n"
    "buffer_load_dwordx4 v228, s[40:43], s78 offen offset:2048 nt lds\n"
    "v_exp_f32 v170, v170\n"
    "v_mfma_f32_16x16x32_bf16 a[132:135], a[148:151], v[176:179], a[132:135]\n"
    "s_and_b32 s79, s79, 0x1e000\n"
    "buffer_load_dwordx4 v229, s[40:43], s78 offen offset:3072 nt lds\n"
    "v_exp_f32 v171, v171\n"
    "v_mfma_f32_16x16x32_bf16 v[32:35], a[96:99], v[176:179], v[32:35]\n"
    "s_add_u32 s79, s79, 0x1000\n"
    "v_fma_f32 v172, v172, s69, v202\n"
    "v_fma_f32 v173, v173, s69, v202\n"
    "v_mfma_f32_16x16x32_bf16 v[36:39], a[100:103], v[176:179], v[36:39]\n"
    "s_add_u32 m0, s71, 0x7000\n"
    "s_waitcnt vmcnt(12)\n"
    "v_cvt_pk_bf16_f32 v180, v168, v169\n"
    "v_cvt_pk_bf16_f32 v181, v170, v171\n"
    "v_mfma_f32_16x16x32_bf16 v[40:43], a[104:107], v[176:179], v[40:43]\n"
    "buffer_load_dwordx4 v226, s[40:43], s79 offen nt lds\n"
    "v_exp_f32 v172, v172\n"
    "v_mfma_f32_16x16x32_bf16 v[44:47], a[108:111], v[176:179], v[44:47]\n"
    "buffer_load_dwordx4 v227, s[40:43], s79 offen offset:1024 nt lds\n"
    "v_exp_f32 v173, v173\n"
    "v_mfma_f32_16x16x32_bf16 v[48:51], a[112:115], v[176:179], v[48:51]\n"
    "buffer_load_dwordx4 v228, s[40:43], s79 offen offset:2048 nt lds\n"
    "v_fma_f32 v174, v174, s69, v202\n"
    "v_fma_f32 v175, v175, s69, v202\n"
    "v_mfma_f32_16x16x32_bf16 v[52:55], a[116:119], v[176:179], v[52:55]\n"
    "buffer_load_dwordx4 v229, s[40:43], s79 offen offset:3072 nt lds\n"
    "v_exp_f32 v174, v174\n"
    "v_mfma_f32_16x16x32_bf16 v[56:59], a[120:123], v[176:179], v[56:59]\n"
    "ds_read_b128 a[0:3], v214 offset:8192\n"
    "v_exp_f32 v175, v175\n"
    "v_mfma_f32_16x16x32_bf16 v[60:63], a[124:127], v[176:179], v[60:63]\n"
    "ds_read_b128 a[4:7], v215 offset:8192\n"
    "v_cvt_pk_bf16_f32 v182, v172, v173\n"
    "v_cvt_pk_bf16_f32 v183, v174, v175\n"
    "v_mfma_f32_16x16x32_bf16 v[168:171], a[32:35], a[224:227], 0\n"
    "ds_read_b128 a[8:11], v216 offset:8192\n"
    "v_max3_f32 v196, v160, v161, v162\n"
    "v_max3_f32 v197, v163, v164, v165\n"
    "v_mfma_f32_16x16x32_bf16 v[168:171], a[36:39], a[228:231], v[168:171]\n"
    "ds_read_b128 a[12:15], v217 offset:8192\n"
    "v_max3_f32 v196, v196, v166, v167\n"
    "v_max_f32 v196, v196, v197\n"
    "v_mfma_f32_16x16x32_bf16 v[168:171], a[40:43], a[232:235], v[168:171]\n"
    "ds_read_b128 a[16:19], v214 offset:12288\n"
    "ds_read_b128 a[20:23], v215 offset:12288\n"
    "v_cmp_gt_f32 vcc, v196, v192\n"
    "v_mfma_f32_16x16x32_bf16 v[168:171], a[44:47], a[236:239], v[168:171]\n"
    "s_cbranch_vccnz B_RARE_DP_3\n"
    "B_BACK_DP_3:\n"
    "v_fma_f32 v160, v160, s69, v203\n"
    "v_fma_f32 v161, v161, s69, v203\n"
    "v_mfma_f32_16x16x32_bf16 v[172:175], a[48:51], a[224:227], 0\n"
    "ds_read_b128 a[24:27], v216 offset:12288\n"
    "v_exp_f32 v160, v160\n"
    "v_mfma_f32_16x16x32_bf16 v[172:175], a[52:55], a[228:231], v[172:175]\n"
    "ds_read_b128 a[28:31], v217 offset:12288\n"
    "v_exp_f32 v161, v161\n"
    "v_mfma_f32_16x16x32_bf16 v[172:175], a[56:59], a[232:235], v[172:175]\n"
    "v_fma_f32 v162, v162, s69, v203\n"
    "v_fma_f32 v163, v163, s69, v203\n"
    "v_mfma_f32_16x16x32_bf16 v[172:175], a[60:63], a[236:239], v[172:175]\n"
    "v_exp_f32 v162, v162\n"
    "v_mfma_f32_16x16x32_bf16 a[136:139], a[148:151], v[180:183], a[136:139]\n"
    "v_exp_f32 v163, v163\n"
    "v_mfma_f32_16x16x32_bf16 v[64:67], a[96:99], v[180:183], v[64:67]\n"
    "v_fma_f32 v164, v164, s69, v203\n"
    "v_fma_f32 v165, v165, s69, v203\n"
    "v_mfma_f32_16x16x32_bf16 v[68:71], a[100:103], v[180:183], v[68:71]\n"
    "v_cvt_pk_bf16_f32 v176, v160, v161\n"
    "v_cvt_pk_bf16_f32 v177, v162, v163\n"
    "v_mfma_f32_16x16x32_bf16 v[72:75], a[104:107], v[180:183], v[72:75]\n"
    "v_exp_f32 v164, v164\n"
    "v_mfma_f32_16x16x32_bf16 v[76:79], a[108:111], v[180:183], v[76:79]\n"
    "v_exp_f32 v165, v165\n"
    "v_mfma_f32_16x16x32_bf16 v[80:83], a[112:115], v[180:183], v[80:83]\n"
    "v_fma_f32 v166, v166, s69, v203\n"
    "v_fma_f32 v167, v167, s69, v203\n"
    "v_mfma_f32_16x16x32_bf16 v[84:87], a[116:119], v[180:183], v[84:87]\n"
    "v_exp_f32 v166, v166\n"
    "v_mfma_f32_16x16x32_bf16 v[88:91], a[120:123], v[180:183], v[88:91]\n"
    "v_exp_f32 v167, v167\n"
    "v_mfma_f32_16x16x32_bf16 v[92:95], a[124:127], v[180:183], v[92:95]\n"
    "s_waitcnt lgkmcnt(0)\n"
    "v_cvt_pk_bf16_f32 v178, v164, v165\n"
    "v_cvt_pk_bf16_f32 v179, v166, v167\n"
    "v_mfma_f32_16x16x32_bf16 v[160:163], a[0:3], a[160:163], 0\n"
    "v_max3_f32 v196, v168, v169, v170\n"
    "v_max3_f32 v197, v171, v172, v173\n"
    "v_mfma_f32_16x16x32_bf16 v[160:163], a[4:7], a[164:167], v[160:163]\n"
    "v_max3_f32 v196, v196, v174, v175\n"
    "v_max_f32 v196, v196, v197\n"
    "v_mfma_f32_16x16x32_bf16 v[160:163], a[8:11], a[168:171], v[160:163]\n"
    "v_cmp_gt_f32 vcc, v196, v193\n"
    "v_mfma_f32_16x16x32_bf16 v[160:163], a[12:15], a[172:175], v[160:163]\n"
    "s_cbranch_vccnz B_RARE_DP_4\n"
    "B_BACK_DP_4:\n"
    "v_fma_f32 v168, v168, s69, v204\n"
    "v_fma_f32 v169, v169, s69, v204\n"
    "v_mfma_f32_16x16x32_bf16 v[164:167], a[16:19], a[160:163], 0\n"
    "v_exp_f32 v168, v168\n"
    "v_mfma_f32_16x16x32_bf16 v[164:167], a[20:23], a[164:167], v[164:167]\n"
    "v_exp_f32 v169, v169\n"
    "v_mfma_f32_16x16x32_bf16 v[164:167], a[24:27], a[168:171], v[164:167]\n"
    "v_fma_f32 v170, v170, s69, v204\n"
    "v_fma_f32 v171, v171, s69, v204\n"
    "v_mfma_f32_16x16x32_bf16 v[164:167], a[28:31], a[172:175], v[164:167]\n"
    "v_exp_f32 v170, v170\n"
    "v_mfma_f32_16x16x32_bf16 a[140:143], a[148:151], v[176:179], a[140:143]\n"
    "v_exp_f32 v171, v171\n"
    "v_mfma_f32_16x16x32_bf16 v[96:99], a[96:99], v[176:179], v[96:99]\n"
    "v_fma_f32 v172, v172, s69, v204\n"
    "v_fma_f32 v173, v173, s69, v204\n"
    "v_mfma_f32_16x16x32_bf16 v[100:103], a[100:103], v[176:179], v[100:103]\n"
    "v_cvt_pk_bf16_f32 v180, v168, v169\n"
    "v_cvt_pk_bf16_f32 v181, v170, v171\n"
    "v_mfma_f32_16x16x32_bf16 v[104:107], a[104:107], v[176:179], v[104:107]\n"
    "v_exp_f32 v172, v172\n"
    "v_mfma_f32_16x16x32_bf16 v[108:111], a[108:111], v[176:179], v[108:111]\n"
    "v_exp_f32 v173, v173\n"
    "v_mfma_f32_16x16x32_bf16 v[112:115], a[112:115], v[176:179], v[112:115]\n"
    "v_fma_f32 v174, v174, s69, v204\n"
    "v_fma_f32 v175, v175, s69, v204\n"
    "v_mfma_f32_16x16x32_bf16 v[116:119], a[116:119], v[176:179], v[116:119]\n"
    "v_exp_f32 v174, v174\n"
    "v_mfma_f32_16x16x32_bf16 v[120:123], a[120:123], v[176:179], v[120:123]\n"
    "v_exp_f32 v175, v175\n"
    "v_mfma_f32_16x16x32_bf16 v[124:127], a[124:127], v[176:179], v[124:127]\n"
    "v_cvt_pk_bf16_f32 v182, v172, v173\n"
    "v_cvt_pk_bf16_f32 v183, v174, v175\n"
    "s_add_u32 s81, s81, 0x4000\n"
    "s_and_b32 s81, s81, 0x1e000\n"
    "s_add_u32 s80, s80, 1\n"
    "s_cmp_lt_u32 s80, 7\n"
    "s_cbranch_scc1 B_ATT_LOOP\n"
    "B_TT_BEGIN:\n"
    "v_add_u32 v252, 0x9e3779b9, v252\n"
    "s_sub_u32 s61, 7, s35\n"
    "s_lshl_b32 s76, 1, s61\n"
    "s_andn2_b32 s76, 0xffff, s76\n"
    "s_mov_b32 s65, 0\n"
    "B_HS_CHK:\n"
    "v_cmp_ne_u32 vcc, v252, v253\n"
    "s_and_b32 s77, vcc_lo, s76\n"
    "s_cbranch_scc1 B_HS_SLOW\n"
    "B_HS_OK:\n"
    "v_mfma_f32_16x16x32_bf16 v[168:171], a[0:3], a[176:179], 0\n"
    "s_add_u32 s78, s81, 0x2000\n"
    "s_and_b32 s78, s78, 0x1e000\n"
    "s_add_u32 m0, s71, 0x8000\n"
    "s_waitcnt vmcnt(12)\n"
    "v_max3_f32 v196, v160, v161, v162\n"
    "v_max3_f32 v197, v163, v164, v165\n"
    "v_mfma_f32_16x16x32_bf16 v[168:171], a[4:7], a[180:183], v[168:171]\n"
    "buffer_load_dwordx4 v226, s[44:47], s78 offen nt lds\n"
    "v_max3_f32 v196, v196, v166, v167\n"
    "v_max_f32 v196, v196, v197\n"
    "v_mfma_f32_16x16x32_bf16 v[168:171], a[8:11], a[184:187], v[168:171]\n"
    "buffer_load_dwordx4 v227, s[44:47], s78 offen offset:1024 nt lds\n"
    "ds_read_b64_tr_b16 a[64:65], v218 offset:0\n"
    "v_cmp_gt_f32 vcc, v196, v189\n"
    "v_mfma_f32_16x16x32_bf16 v[168:171], a[12:15], a[188:191], v[168:171]\n"
    "s_add_u32 s79, s81, 0x2000\n"
    "s_cbranch_vccnz B_RARE_F_0\n"
    "B_BACK_F_0:\n"
    "v_fma_f32 v160, v160, s69, v200\n"
    "v_fma_f32 v161, v161, s69, v200\n"
    "v_mfma_f32_16x16x32_bf16 v[172:175], a[16:19], a[176:179], 0\n"
    "s_and_b32 s79, s79, 0x1e000\n"
    "buffer_load_dwordx4 v228, s[44:47], s78 offen offset:2048 nt lds\n"
    "v_exp_f32 v160, v160\n"
    "v_mfma_f32_16x16x32_bf16 v[172:175], a[20:23], a[180:183], v[172:175]\n"
    "s_add_u32 s79, s79, 0x1000\n"
    "buffer_load_dwordx4 v229, s[44:47], s78 offen offset:3072 nt lds\n"
    "v_exp_f32 v161, v161\n"
    "v_mfma_f32_16x16x32_bf16 v[172:175], a[24:27], a[184:187], v[172:175]\n"
    "s_add_u32 m0, s71, 0x9000\n"
    "s_waitcnt vmcnt(12)\n"
    "ds_read_b64_tr_b16 a[68:69], v219 offset:0\n"
    "v_fma_f32 v162, v162, s69, v200\n"
    "v_fma_f32 v163, v163, s69, v200\n"
    "v_mfma_f32_16x16x32_bf16 v[172:175], a[28:31], a[188:191], v[172:175]\n"
    "buffer_load_dwordx4 v226, s[44:47], s79 offen nt lds\n"
    "ds_read_b64_tr_b16 a[72:73], v220 offset:0\n"
    "v_exp_f32 v162, v162\n"
    "v_mfma_f32_16x16x32_bf16 a[144:147], a[148:151], v[180:183], a[144:147]\n"
    "buffer_load_dwordx4 v227, s[44:47], s79 offen offset:1024 nt lds\n"
    "ds_read_b64_tr_b16 a[76:77], v221 offset:0\n"
    "v_exp_f32 v163, v163\n"
    "v_mfma_f32_16x16x32_bf16 v[128:131], a[96:99], v[180:183], v[128:131]\n"
    "buffer_load_dwordx4 v228, s[44:47], s79 offen offset:2048 nt lds\n"
    "v_fma_f32 v164, v164, s69, v200\n"
    "v_fma_f32 v165, v165, s69, v200\n"
    "v_mfma_f32_16x16x32_bf16 v[132:135], a[100:103], v[180:183], v[132:135]\n"
    "buffer_load_dwordx4 v229, s[44:47], s79 offen offset:3072 nt lds\n"
    "v_cvt_pk_bf16_f32 v176, v160, v161\n"
    "v_cvt_pk_bf16_f32 v177, v162, v163\n"
    "v_mfma_f32_16x16x32_bf16 v[136:139], a[104:107], v[180:183], v[136:139]\n"
    "ds_read_b64_tr_b16 a[80:81], v222 offset:0\n"
    "ds_read_b64_tr_b16 a[84:85], v223 offset:0\n"
    "v_exp_f32 v164, v164\n"
    "v_mfma_f32_16x16x32_bf16 v[140:143], a[108:111], v[180:183], v[140:143]\n"
    "ds_read_b64_tr_b16 a[88:89], v224 offset:0\n"
    "ds_read_b64_tr_b16 a[92:93], v225 offset:0\n"
    "v_exp_f32 v165, v165\n"
    "v_mfma_f32_16x16x32_bf16 v[144:147], a[112:115], v[180:183], v[144:147]\n"
    "ds_read_b64_tr_b16 a[66:67], v218 offset:4096\n"
    "v_fma_f32 v166, v166, s69, v200\n"
    "v_fma_f32 v167, v167, s69, v200\n"
    "v_mfma_f32_16x16x32_bf16 v[148:151], a[116:119], v[180:183], v[148:151]\n"
    "ds_read_b64_tr_b16 a[70:71], v219 offset:4096\n"
    "v_exp_f32 v166, v166\n"
    "v_mfma_f32_16x16x32_bf16 v[152:155], a[120:123], v[180:183], v[152:155]\n"
    "ds_read_b64_tr_b16 a[74:75], v220 offset:4096\n"
    "v_exp_f32 v167, v167\n"
    "v_mfma_f32_16x16x32_bf16 v[156:159], a[124:127], v[180:183], v[156:159]\n"
    "ds_read_b64_tr_b16 a[78:79], v221 offset:4096\n"
    "v_cvt_pk_bf16_f32 v178, v164, v165\n"
    "v_cvt_pk_bf16_f32 v179, v166, v167\n"
    "v_mfma_f32_16x16x32_bf16 v[160:163], a[0:3], a[192:195], 0\n"
    "ds_read_b64_tr_b16 a[82:83], v222 offset:4096\n"
    "v_max3_f32 v196, v168, v169, v170\n"
    "v_max3_f32 v197, v171, v172, v173\n"
    "v_mfma_f32_16x16x32_bf16 v[160:163], a[4:7], a[196:199], v[160:163]\n"
    "ds_read_b64_tr_b16 a[86:87], v223 offset:4096\n"
    "v_max3_f32 v196, v196, v174, v175\n"
    "v_max_f32 v196, v196, v197\n"
    "v_mfma_f32_16x16x32_bf16 v[160:163], a[8:11], a[200:203], v[160:163]\n"
    "ds_read_b64_tr_b16 a[90:91], v224 offset:4096\n"
    "ds_read_b64_tr_b16 a[94:95], v225 offset:4096\n"
    "v_cmp_gt_f32 vcc, v196, v190\n"
    "v_mfma_f32_16x16x32_bf16 v[160:163], a[12:15], a[204:207], v[160:163]\n"
    "s_cbranch_vccnz B_RARE_F_1\n"
    "B_BACK_F_1:\n"
    "v_fma_f32 v168, v168, s69, v201\n"
    "v_fma_f32 v169, v169, s69, v201\n"
    "v_mfma_f32_16x16x32_bf16 v[164:167], a[16:19], a[192:195], 0\n"
    "v_exp_f32 v168, v168\n"
    "v_mfma_f32_16x16x32_bf16 v[164:167], a[20:23], a[196:199], v[164:167]\n"
    "v_exp_f32 v169, v169\n"
    "v_mfma_f32_16x16x32_bf16 v[164:167], a[24:27], a[200:203], v[164:167]\n"
    "v_fma_f32 v170, v170, s69, v201\n"
    "v_fma_f32 v171, v171, s69, v201\n"
    "v_mfma_f32_16x16x32_bf16 v[164:167], a[28:31], a[204:207], v[164:167]\n"
    "v_exp_f32 v170, v170\n"
    "v_mfma_f32_16x16x32_bf16 a[128:131], a[148:151], v[176:179], a[128:131]\n"
    "s_waitcnt lgkmcnt(0)\n"
    "v_exp_f32 v171, v171\n"
    "v_mfma_f32_16x16x32_bf16 v[0:3], a[64:67], v[176:179], v[0:3]\n"
    "s_add_u32 s85, s74, s86\n"
    "v_fma_f32 v172, v172, s69, v201\n"
    "v_fma_f32 v173, v173, s69, v201\n"
    "v_mfma_f32_16x16x32_bf16 v[4:7], a[68:71], v[176:179], v[4:7]\n"
    "v_cvt_pk_bf16_f32 v180, v168, v169\n"
    "v_cvt_pk_bf16_f32 v181, v170, v171\n"
    "v_mfma_f32_16x16x32_bf16 v[8:11], a[72:75], v[176:179], v[8:11]\n"
    "s_add_u32 s87, s74, 0x80\n"
    "s_cmp_eq_u32 s87, 0x480\n"
    "s_cselect_b32 s87, 0, s87\n"
    "v_exp_f32 v172, v172\n"
    "v_mfma_f32_16x16x32_bf16 v[12:15], a[76:79], v[176:179], v[12:15]\n"
    "v_exp_f32 v173, v173\n"
    "v_mfma_f32_16x16x32_bf16 v[16:19], a[80:83], v[176:179], v[16:19]\n"
    "v_fma_f32 v174, v174, s69, v201\n"
    "v_fma_f32 v175, v175, s69, v201\n"
    "v_mfma_f32_16x16x32_bf16 v[20:23], a[84:87], v[176:179], v[20:23]\n"
    "v_exp_f32 v174, v174\n"
    "v_mfma_f32_16x16x32_bf16 v[24:27], a[88:91], v[176:179], v[24:27]\n"
    "v_exp_f32 v175, v175\n"
    "v_mfma_f32_16x16x32_bf16 v[28:31], a[92:95], v[176:179], v[28:31]\n"
    "v_cvt_pk_bf16_f32 v182, v172, v173\n"
    "B_TT_FIRST0:\n"
    "B_TT_S0:\n"
    "s_cbranch_execz B_TT_RET\n"
    "v_cvt_pk_bf16_f32 v183, v174, v175\n"
    "v_mfma_f32_16x16x32_bf16 v[168:171], a[0:3], a[208:211], 0\n"
    "v_max3_f32 v196, v160, v161, v162\n"
    "v_max3_f32 v197, v163, v164, v165\n"
    "v_mfma_f32_16x16x32_bf16 v[168:171], a[4:7], a[212:215], v[168:171]\n"
    "v_max3_f32 v196, v196, v166, v167\n"
    "v_max_f32 v196, v196, v197\n"
    "v_mfma_f32_16x16x32_bf16 v[168:171], a[8:11], a[216:219], v[168:171]\n"
    "v_cmp_gt_f32 vcc, v196, v191\n"
    "v_mfma_f32_16x16x32_bf16 v[168:171], a[12:15], a[220:223], v[168:171]\n"
    "s_cbranch_vccnz B_RARE_F_2\n"
    "B_BACK_F_2:\n"
    "v_fma_f32 v160, v160, s69, v202\n"
    "v_fma_f32 v161, v161, s69, v202\n"
    "v_mfma_f32_16x16x32_bf16 v[172:175], a[16:19], a[208:211], 0\n"
    "v_exp_f32 v160, v160\n"
    "v_mfma_f32_16x16x32_bf16 v[172:175], a[20:23], a[212:215], v[172:175]\n"
    "v_exp_f32 v161, v161\n"
    "v_mfma_f32_16x16x32_bf16 v[172:175], a[24:27], a[216:219], v[172:175]\n"
    "v_fma_f32 v162, v162, s69, v202\n"
    "v_fma_f32 v163, v163, s69, v202\n"
    "v_mfma_f32_16x16x32_bf16 v[172:175], a[28:31], a[220:223], v[172:175]\n"
    "v_exp_f32 v162, v162\n"
    "v_mfma_f32_16x16x32_bf16 a[132:135], a[148:151], v[180:183], a[132:135]\n"
    "v_exp_f32 v163, v163\n"
    "v_mfma_f32_16x16x32_bf16 v[32:35], a[64:67], v[180:183], v[32:35]\n"
    "v_fma_f32 v164, v164, s69, v202\n"
    "v_fma_f32 v165, v165, s69, v202\n"
    "v_mfma_f32_16x16x32_bf16 v[36:39], a[68:71], v[180:183], v[36:39]\n"
    "v_cvt_pk_bf16_f32 v176, v160, v161\n"
    "v_cvt_pk_bf16_f32 v177, v162, v163\n"
    "v_mfma_f32_16x16x32_bf16 v[40:43], a[72:75], v[180:183], v[40:43]\n"
    "v_exp_f32 v164, v164\n"
    "v_mfma_f32_16x16x32_bf16 v[44:47], a[76:79], v[180:183], v[44:47]\n"
    "v_exp_f32 v165, v165\n"
    "v_mfma_f32_16x16x32_bf16 v[48:51], a[80:83], v[180:183], v[48:51]\n"
    "v_fma_f32 v166, v166, s69, v202\n"
    "v_fma_f32 v167, v167, s69, v202\n"
    "v_mfma_f32_16x16x32_bf16 v[52:55], a[84:87], v[180:183], v[52:55]\n"
    "v_exp_f32 v166, v166\n"
    "v_mfma_f32_16x16x32_bf16 v[56:59], a[88:91], v[180:183], v[56:59]\n"
    "v_exp_f32 v167, v167\n"
    "v_mfma_f32_16x16x32_bf16 v[60:63], a[92:95], v[180:183], v[60:63]\n"
    "s_waitcnt vmcnt(12)\n"
    "v_cvt_pk_bf16_f32 v178, v164, v165\n"
    "v_cvt_pk_bf16_f32 v179, v166, v167\n"
    "v_mfma_f32_16x16x32_bf16 v[160:163], a[0:3], a[224:227], 0\n"
    "v_max3_f32 v196, v168, v169, v170\n"
    "v_max3_f32 v197, v171, v172, v173\n"
    "v_mfma_f32_16x16x32_bf16 v[160:163], a[4:7], a[228:231], v[160:163]\n"
    "v_max3_f32 v196, v196, v174, v175\n"
    "v_max_f32 v196, v196, v197\n"
    "v_mfma_f32_16x16x32_bf16 v[160:163], a[8:11], a[232:235], v[160:163]\n"
    "s_add_u32 s78, s85, 0\n"
    "v_cmp_gt_f32 vcc, v196, v192\n"
    "v_mfma_f32_16x16x32_bf16 v[160:163], a[12:15], a[236:239], v[160:163]\n"
    "s_add_u32 m0, s71, 0x2000\n"
    "s_cbranch_vccnz B_RARE_F_3\n"
    "B_BACK_F_3:\n"
    "v_fma_f32 v168, v168, s69, v203\n"
    "v_fma_f32 v169, v169, s69, v203\n"
    "v_mfma_f32_16x16x32_bf16 v[164:167], a[16:19], a[224:227], 0\n"
    "buffer_load_dwordx4 v234, s[52:55], s78 offen lds\n"
    "v_exp_f32 v168, v168\n"
    "v_mfma_f32_16x16x32_bf16 v[164:167], a[20:23], a[228:231], v[164:167]\n"
    "s_waitcnt vmcnt(9)\n"
    "ds_read_b128 a[32:35], v214 offset:24576\n"
    "buffer_load_dwordx4 v235, s[52:55], s78 offen offset:1024 lds\n"
    "v_exp_f32 v169, v169\n"
    "v_mfma_f32_16x16x32_bf16 v[164:167], a[24:27], a[232:235], v[164:167]\n"
    "ds_read_b128 a[36:39], v215 offset:24576\n"
    "v_fma_f32 v170, v170, s69, v203\n"
    "v_fma_f32 v171, v171, s69, v203\n"
    "v_mfma_f32_16x16x32_bf16 v[164:167], a[28:31], a[236:239], v[164:167]\n"
    "ds_read_b128 a[40:43], v216 offset:24576\n"
    "buffer_load_dwordx4 v236, s[52:55], s78 offen offset:2048 lds\n"
    "v_exp_f32 v170, v170\n"
    "v_mfma_f32_16x16x32_bf16 a[136:139], a[148:151], v[176:179], a[136:139]\n"
    "ds_read_b128 a[44:47], v217 offset:24576\n"
    "buffer_load_dwordx4 v237, s[52:55], s78 offen offset:3072 lds\n"
    "v_exp_f32 v171, v171\n"
    "v_mfma_f32_16x16x32_bf16 v[64:67], a[64:67], v[176:179], v[64:67]\n"
    "ds_read_b128 a[48:51], v214 offset:28672\n"
    "v_fma_f32 v172, v172, s69, v203\n"
    "v_fma_f32 v173, v173, s69, v203\n"
    "v_mfma_f32_16x16x32_bf16 v[68:71], a[68:71], v[176:179], v[68:71]\n"
    "ds_read_b128 a[52:55], v215 offset:28672\n"
    "v_cvt_pk_bf16_f32 v180, v168, v169\n"
    "v_cvt_pk_bf16_f32 v181, v170, v171\n"
    "v_mfma_f32_16x16x32_bf16 v[72:75], a[72:75], v[176:179], v[72:75]\n"
    "s_add_u32 s79, s85, 0x9000\n"
    "ds_read_b128 a[56:59], v216 offset:28672\n"
    "v_exp_f32 v172, v172\n"
    "v_mfma_f32_16x16x32_bf16 v[76:79], a[76:79], v[176:179], v[76:79]\n"
    "s_add_u32 m0, s71, 0x3000\n"
    "ds_read_b128 a[60:63], v217 offset:28672\n"
    "v_exp_f32 v173, v173\n"
    "v_mfma_f32_16x16x32_bf16 v[80:83], a[80:83], v[176:179], v[80:83]\n"
    "buffer_load_dwordx4 v234, s[52:55], s79 offen lds\n"
    "v_fma_f32 v174, v174, s69, v203\n"
    "v_fma_f32 v175, v175, s69, v203\n"
    "v_mfma_f32_16x16x32_bf16 v[84:87], a[84:87], v[176:179], v[84:87]\n"
    "buffer_load_dwordx4 v235, s[52:55], s79 offen offset:1024 lds\n"
    "v_exp_f32 v174, v174\n"
    "v_mfma_f32_16x16x32_bf16 v[88:91], a[88:91], v[176:179], v[88:91]\n"
    "buffer_load_dwordx4 v236, s[52:55], s79 offen offset:2048 lds\n"
    "v_exp_f32 v175, v175\n"
    "v_mfma_f32_16x16x32_bf16 v[92:95], a[92:95], v[176:179], v[92:95]\n"
    "s_waitcnt lgkmcnt(0)\n"
    "v_cvt_pk_bf16_f32 v182, v172, v173\n"
    "v_cvt_pk_bf16_f32 v183, v174, v175\n"
    "v_mfma_f32_16x16x32_bf16 v[168:171], a[32:35], a[160:163], 0\n"
    "s_add_u32 s78, s85, 0x12000\n"
    "buffer_load_dwordx4 v237, s[52:55], s79 offen offset:3072 lds\n"
    "v_max3_f32 v196, v160, v161, v162\n"
    "v_max3_f32 v197, v163, v164, v165\n"
    "v_mfma_f32_16x16x32_bf16 v[168:171], a[36:39], a[164:167], v[168:171]\n"
    "s_add_u32 m0, s71, 0x4000\n"
    "v_max3_f32 v196, v196, v166, v167\n"
    "v_max_f32 v196, v196, v197\n"
    "v_mfma_f32_16x16x32_bf16 v[168:171], a[40:43], a[168:171], v[168:171]\n"
    "buffer_load_dwordx4 v234, s[52:55], s78 offen lds\n"
    "v_cmp_gt_f32 vcc, v196, v193\n"
    "v_mfma_f32_16x16x32_bf16 v[168:171], a[44:47], a[172:175], v[168:171]\n"
    "s_cbranch_vccnz B_RARE_F_4\n"
    "B_BACK_F_4:\n"
    "v_fma_f32 v160, v160, s69, v204\n"
    "v_fma_f32 v161, v161, s69, v204\n"
    "v_mfma_f32_16x16x32_bf16 v[172:175], a[48:51], a[160:163], 0\n"
    "buffer_load_dwordx4 v235, s[52:55], s78 offen offset:1024 lds\n"
    "v_exp_f32 v160, v160\n"
    "v_mfma_f32_16x16x32_bf16 v[172:175], a[52:55], a[164:167], v[172:175]\n"
    "buffer_load_dwordx4 v236, s[52:55], s78 offen offset:2048 lds\n"
    "v_exp_f32 v161, v161\n"
    "v_mfma_f32_16x16x32_bf16 v[172:175], a[56:59], a[168:171], v[172:175]\n"
    "buffer_load_dwordx4 v237, s[52:55], s78 offen offset:3072 lds\n"
    "v_fma_f32 v162, v162, s69, v204\n"
    "v_fma_f32 v163, v163, s69, v204\n"
    "v_mfma_f32_16x16x32_bf16 v[172:175], a[60:63], a[172:175], v[172:175]\n"
    "v_exp_f32 v162, v162\n"
    "v_mfma_f32_16x16x32_bf16 a[140:143], a[148:151], v[180:183], a[140:143]\n"
    "s_add_u32 s79, s85, 0x1b000\n"
    "v_exp_f32 v163, v163\n"
    "v_mfma_f32_16x16x32_bf16 v[96:99], a[64:67], v[180:183], v[96:99]\n"
    "s_add_u32 m0, s71, 0x5000\n"
    "v_fma_f32 v164, v164, s69, v204\n"
    "v_fma_f32 v165, v165, s69, v204\n"
    "v_mfma_f32_16x16x32_bf16 v[100:103], a[68:71], v[180:183], v[100:103]\n"
    "buffer_load_dwordx4 v234, s[52:55], s79 offen lds\n"
    "v_cvt_pk_bf16_f32 v176, v160, v161\n"
    "v_cvt_pk_bf16_f32 v177, v162, v163\n"
    "v_mfma_f32_16x16x32_bf16 v[104:107], a[72:75], v[180:183], v[104:107]\n"
    "buffer_load_dwordx4 v235, s[52:55], s79 offen offset:1024 lds\n"
    "v_exp_f32 v164, v164\n"
    "v_mfma_f32_16x16x32_bf16 v[108:111], a[76:79], v[180:183], v[108:111]\n"
    "buffer_load_dwordx4 v236, s[52:55], s79 offen offset:2048 lds\n"
    "v_exp_f32 v165, v165\n"
    "v_mfma_f32_16x16x32_bf16 v[112:115], a[80:83], v[180:183], v[112:115]\n"
    "buffer_load_dwordx4 v237, s[52:55], s79 offen offset:3072 lds\n"
    "v_fma_f32 v166, v166, s69, v204\n"
    "v_fma_f32 v167, v167, s69, v204\n"
    "v_mfma_f32_16x16x32_bf16 v[116:119], a[84:87], v[180:183], v[116:119]\n"
    "v_exp_f32 v166, v166\n"
    "v_mfma_f32_16x16x32_bf16 v[120:123], a[88:91], v[180:183], v[120:123]\n"
    "v_exp_f32 v167, v167\n"
    "v_mfma_f32_16x16x32_bf16 v[124:127], a[92:95], v[180:183], v[124:127]\n"
    "v_cvt_pk_bf16_f32 v178, v164, v165\n"
    "v_cvt_pk_bf16_f32 v179, v166, v167\n"
    "v_mfma_f32_16x16x32_bf16 v[160:163], a[32:35], a[176:179], 0\n"
    "s_add_u32 s78, s87, s86\n"
    "v_max3_f32 v196, v168, v169, v170\n"
    "v_max3_f32 v197, v171, v172, v173\n"
    "v_mfma_f32_16x16x32_bf16 v[160:163], a[36:39], a[180:183], v[160:163]\n"
    "s_add_u32 m0, s71, 0x1000\n"
    "v_max3_f32 v196, v196, v174, v175\n"
    "v_max_f32 v196, v196, v197\n"
    "v_mfma_f32_16x16x32_bf16 v[160:163], a[40:43], a[184:187], v[160:163]\n"
    "buffer_load_dwordx4 v234, s[52:55], s78 offen lds\n"
    "v_cmp_gt_f32 vcc, v196, v189\n"
    "v_mfma_f32_16x16x32_bf16 v[160:163], a[44:47], a[188:191], v[160:163]\n"
    "s_cbranch_vccnz B_RARE_G_0\n"
    "B_BACK_G_0:\n"
    "v_fma_f32 v168, v168, s69, v200\n"
    "v_fma_f32 v169, v169, s69, v200\n"
    "v_mfma_f32_16x16x32_bf16 v[164:167], a[48:51], a[176:179], 0\n"
    "buffer_load_dwordx4 v235, s[52:55], s78 offen offset:1024 lds\n"
    "v_exp_f32 v168, v168\n"
    "v_mfma_f32_16x16x32_bf16 v[164:167], a[52:55], a[180:183], v[164:167]\n"
    "buffer_load_dwordx4 v236, s[52:55], s78 offen offset:2048 lds\n"
    "v_exp_f32 v169, v169\n"
    "v_mfma_f32_16x16x32_bf16 v[164:167], a[56:59], a[184:187], v[164:167]\n"
    "buffer_load_dwordx4 v237, s[52:55], s78 offen offset:3072 lds\n"
    "v_fma_f32 v170, v170, s69, v200\n"
    "v_fma_f32 v171, v171, s69, v200\n"
    "v_mfma_f32_16x16x32_bf16 v[164:167], a[60:63], a[188:191], v[164:167]\n"
    "v_exp_f32 v170, v170\n"
    "v_mfma_f32_16x16x32_bf16 a[144:147], a[148:151], v[176:179], a[144:147]\n"
    "v_exp_f32 v171, v171\n"
    "v_mfma_f32_16x16x32_bf16 v[128:131], a[64:67], v[176:179], v[128:131]\n"
    "v_fma_f32 v172, v172, s69, v200\n"
    "v_fma_f32 v173, v173, s69, v200\n"
    "v_mfma_f32_16x16x32_bf16 v[132:135], a[68:71], v[176:179], v[132:135]\n"
    "v_cvt_pk_bf16_f32 v180, v168, v169\n"
    "v_cvt_pk_bf16_f32 v181, v170, v171\n"
    "v_mfma_f32_16x16x32_bf16 v[136:139], a[72:75], v[176:179], v[136:139]\n"
    "v_exp_f32 v172, v172\n"
    "v_mfma_f32_16x16x32_bf16 v[140:143], a[76:79], v[176:179], v[140:143]\n"
    "v_exp_f32 v173, v173\n"
    "v_mfma_f32_16x16x32_bf16 v[144:147], a[80:83], v[176:179], v[144:147]\n"
    "v_fma_f32 v174, v174, s69, v200\n"
    "v_fma_f32 v175, v175, s69, v200\n"
    "v_mfma_f32_16x16x32_bf16 v[148:151], a[84:87], v[176:179], v[148:151]\n"
    "v_exp_f32 v174, v174\n"
    "v_mfma_f32_16x16x32_bf16 v[152:155], a[88:91], v[176:179], v[152:155]\n"
    "v_exp_f32 v175, v175\n"
    "v_mfma_f32_16x16x32_bf16 v[156:159], a[92:95], v[176:179], v[156:159]\n"
    "v_cvt_pk_bf16_f32 v182, v172, v173\n"
    "v_cvt_pk_bf16_f32 v183, v174, v175\n"
    "v_mfma_f32_16x16x32_bf16 v[168:171], a[32:35], a[192:195], 0\n"
    "v_max3_f32 v196, v160, v161, v162\n"
    "v_max3_f32 v197, v163, v164, v165\n"
    "v_mfma_f32_16x16x32_bf16 v[168:171], a[36:39], a[196:199], v[168:171]\n"
    "v_max3_f32 v196, v196, v166, v167\n"
    "v_max_f32 v196, v196, v197\n"
    "v_mfma_f32_16x16x32_bf16 v[168:171], a[40:43], a[200:203], v[168:171]\n"
    "v_cmp_gt_f32 vcc, v196, v190\n"
    "v_mfma_f32_16x16x32_bf16 v[168:171], a[44:47], a[204:207], v[168:171]\n"
    "s_cbranch_vccnz B_RARE_G_1\n"
    "B_BACK_G_1:\n"
    "v_fma_f32 v160, v160, s69, v201\n"
    "v_fma_f32 v161, v161, s69, v201\n"
    "v_mfma_f32_16x16x32_bf16 v[172:175], a[48:51], a[192:195], 0\n"
    "v_exp_f32 v160, v160\n"
    "v_mfma_f32_16x16x32_bf16 v[172:175], a[52:55], a[196:199], v[172:175]\n"
    "v_exp_f32 v161, v161\n"
    "v_mfma_f32_16x16x32_bf16 v[172:175], a[56:59], a[200:203], v[172:175]\n"
    "v_fma_f32 v162, v162, s69, v201\n"
    "v_fma_f32 v163, v163, s69, v201\n"
    "v_mfma_f32_16x16x32_bf16 v[172:175], a[60:63], a[204:207], v[172:175]\n"
    "v_exp_f32 v162, v162\n"
    "v_exp_f32 v163, v163\n"
    "v_fma_f32 v164, v164, s69, v201\n"
    "v_fma_f32 v165, v165, s69, v201\n"
    "v_cvt_pk_bf16_f32 v214, v160, v161\n"
    "v_cvt_pk_bf16_f32 v215, v162, v163\n"
    "v_exp_f32 v164, v164\n"
    "v_exp_f32 v165, v165\n"
    "v_fma_f32 v166, v166, s69, v201\n"
    "v_fma_f32 v167, v167, s69, v201\n"
    "v_exp_f32 v166, v166\n"
    "v_exp_f32 v167, v167\n"
    "v_cvt_pk_bf16_f32 v216, v164, v165\n"
    "v_cvt_pk_bf16_f32 v217, v166, v167\n"
    "v_mfma_f32_16x16x32_bf16 v[160:163], a[32:35], a[208:211], 0\n"
    "v_max3_f32 v196, v168, v169, v170\n"
    "v_max3_f32 v197, v171, v172, v173\n"
    "v_mfma_f32_16x16x32_bf16 v[160:163], a[36:39], a[212:215], v[160:163]\n"
    "v_max3_f32 v196, v196, v174, v175\n"
    "v_max_f32 v196, v196, v197\n"
    "v_mfma_f32_16x16x32_bf16 v[160:163], a[40:43], a[216:219], v[160:163]\n"
    "v_cmp_gt_f32 vcc, v196, v191\n"
    "v_mfma_f32_16x16x32_bf16 v[160:163], a[44:47], a[220:223], v[160:163]\n"
    "s_cbranch_vccnz B_RARE_G_2\n"
    "B_BACK_G_2:\n"
    "v_fma_f32 v168, v168, s69, v202\n"
    "v_fma_f32 v169, v169, s69, v202\n"
    "v_mfma_f32_16x16x32_bf16 v[164:167], a[48:51], a[208:211], 0\n"
    "v_exp_f32 v168, v168\n"
    "v_mfma_f32_16x16x32_bf16 v[164:167], a[52:55], a[212:215], v[164:167]\n"
    "v_exp_f32 v169, v169\n"
    "v_mfma_f32_16x16x32_bf16 v[164:167], a[56:59], a[216:219], v[164:167]\n"
    "v_fma_f32 v170, v170, s69, v202\n"
    "v_fma_f32 v171, v171, s69, v202\n"
    "v_mfma_f32_16x16x32_bf16 v[164:167], a[60:63], a[220:223], v[164:167]\n"
    "v_exp_f32 v170, v170\n"
    "v_exp_f32 v171, v171\n"
    "v_fma_f32 v172, v172, s69, v202\n"
    "v_fma_f32 v173, v173, s69, v202\n"
    "v_cvt_pk_bf16_f32 v226, v168, v169\n"
    "v_cvt_pk_bf16_f32 v227, v170, v171\n"
    "v_exp_f32 v172, v172\n"
    "v_exp_f32 v173, v173\n"
    "v_fma_f32 v174, v174, s69, v202\n"
    "v_fma_f32 v175, v175, s69, v202\n"
    "v_exp_f32 v174, v174\n"
    "v_exp_f32 v175, v175\n"
    "v_cvt_pk_bf16_f32 v228, v172, v173\n"
    "v_cvt_pk_bf16_f32 v229, v174, v175\n"
    "s_waitcnt vmcnt(20)\n"
    "v_mfma_f32_16x16x32_bf16 v[168:171], a[32:35], a[224:227], 0\n"
    "ds_read_b64_tr_b16 a[96:97], v218 offset:32768\n"
    "ds_read_b64_tr_b16 a[98:99], v218 offset:36864\n"
    "v_max3_f32 v196, v160, v161, v162\n"
    "v_max3_f32 v197, v163, v164, v165\n"
    "v_mfma_f32_16x16x32_bf16 v[168:171], a[36:39], a[228:231], v[168:171]\n"
    "ds_read_b64_tr_b16 a[100:101], v219 offset:32768\n"
    "ds_read_b64_tr_b16 a[102:103], v219 offset:36864\n"
    "v_max3_f32 v196, v196, v166, v167\n"
    "v_max_f32 v196, v196, v197\n"
    "v_mfma_f32_16x16x32_bf16 v[168:171], a[40:43], a[232:235], v[168:171]\n"
    "ds_read_b64_tr_b16 a[104:105], v220 offset:32768\n"
    "ds_read_b64_tr_b16 a[106:107], v220 offset:36864\n"
    "v_cmp_gt_f32 vcc, v196, v192\n"
    "v_mfma_f32_16x16x32_bf16 v[168:171], a[44:47], a[236:239], v[168:171]\n"
    "ds_read_b64_tr_b16 a[108:109], v221 offset:32768\n"
    "ds_read_b64_tr_b16 a[110:111], v221 offset:36864\n"
    "s_cbranch_vccnz B_RARE_G_3\n"
    "B_BACK_G_3:\n"
    "v_fma_f32 v160, v160, s69, v203\n"
    "v_fma_f32 v161, v161, s69, v203\n"
    "v_mfma_f32_16x16x32_bf16 v[172:175], a[48:51], a[224:227], 0\n"
    "ds_read_b64_tr_b16 a[112:113], v222 offset:32768\n"
    "ds_read_b64_tr_b16 a[114:115], v222 offset:36864\n"
    "v_exp_f32 v160, v160\n"
    "v_mfma_f32_16x16x32_bf16 v[172:175], a[52:55], a[228:231], v[172:175]\n"
    "ds_read_b64_tr_b16 a[116:117], v223 offset:32768\n"
    "ds_read_b64_tr_b16 a[118:119], v223 offset:36864\n"
    "v_exp_f32 v161, v161\n"
    "v_mfma_f32_16x16x32_bf16 v[172:175], a[56:59], a[232:235], v[172:175]\n"
    "ds_read_b64_tr_b16 a[120:121], v224 offset:32768\n"
    "ds_read_b64_tr_b16 a[122:123], v224 offset:36864\n"
    "v_fma_f32 v162, v162, s69, v203\n"
    "v_fma_f32 v163, v163, s69, v203\n"
    "v_mfma_f32_16x16x32_bf16 v[172:175], a[60:63], a[236:239], v[172:175]\n"
    "ds_read_b64_tr_b16 a[124:125], v225 offset:32768\n"
    "ds_read_b64_tr_b16 a[126:127], v225 offset:36864\n"
    "v_exp_f32 v162, v162\n"
    "v_exp_f32 v163, v163\n"
    "v_fma_f32 v164, v164, s69, v203\n"
    "v_fma_f32 v165, v165, s69, v203\n"
    "v_cvt_pk_bf16_f32 v230, v160, v161\n"
    "v_cvt_pk_bf16_f32 v231, v162, v163\n"
    "v_exp_f32 v164, v164\n"
    "v_exp_f32 v165, v165\n"
    "v_fma_f32 v166, v166, s69, v203\n"
    "v_fma_f32 v167, v167, s69, v203\n"
    "v_exp_f32 v166, v166\n"
    "v_exp_f32 v167, v167\n"
    "v_cvt_pk_bf16_f32 v232, v164, v165\n"
    "v_cvt_pk_bf16_f32 v233, v166, v167\n"
    "s_waitcnt lgkmcnt(0)\n"
    "v_mfma_f32_16x16x32_bf16 a[128:131], a[148:151], v[180:183], a[128:131]\n"
    "v_max3_f32 v196, v168, v169, v170\n"
    "v_max3_f32 v197, v171, v172, v173\n"
    "v_mfma_f32_16x16x32_bf16 v[0:3], a[96:99], v[180:183], v[0:3]\n"
    "v_max3_f32 v196, v196, v174, v175\n"
    "v_max_f32 v196, v196, v197\n"
    "v_mfma_f32_16x16x32_bf16 v[4:7], a[100:103], v[180:183], v[4:7]\n"
    "v_cmp_gt_f32 vcc, v196, v193\n"
    "v_mfma_f32_16x16x32_bf16 v[8:11], a[104:107], v[180:183], v[8:11]\n"
    "s_cbranch_vccnz B_RARE_G_4\n"
    "B_BACK_G_4:\n"
    "v_fma_f32 v168, v168, s69, v204\n"
    "v_fma_f32 v169, v169, s69, v204\n"
    "v_mfma_f32_16x16x32_bf16 v[12:15], a[108:111], v[180:183], v[12:15]\n"
    "v_exp_f32 v168, v168\n"
    "v_exp_f32 v169, v169\n"
    "v_mfma_f32_16x16x32_bf16 v[16:19], a[112:115], v[180:183], v[16:19]\n"
    "v_fma_f32 v170, v170, s69, v204\n"
    "v_fma_f32 v171, v171, s69, v204\n"
    "v_exp_f32 v170, v170\n"
    "v_exp_f32 v171, v171\n"
    "v_mfma_f32_16x16x32_bf16 v[20:23], a[116:119], v[180:183], v[20:23]\n"
    "v_fma_f32 v172, v172, s69, v204\n"
    "v_fma_f32 v173, v173, s69, v204\n"
    "v_cvt_pk_bf16_f32 v176, v168, v169\n"
    "v_cvt_pk_bf16_f32 v177, v170, v171\n"
    "v_exp_f32 v172, v172\n"
    "v_mfma_f32_16x16x32_bf16 v[24:27], a[120:123], v[180:183], v[24:27]\n"
    "v_exp_f32 v173, v173\n"
    "v_fma_f32 v174, v174, s69, v204\n"
    "v_fma_f32 v175, v175, s69, v204\n"
    "v_exp_f32 v174, v174\n"
    "v_mfma_f32_16x16x32_bf16 v[28:31], a[124:127], v[180:183], v[28:31]\n"
    "v_exp_f32 v175, v175\n"
    "v_cvt_pk_bf16_f32 v178, v172, v173\n"
    "v_cvt_pk_bf16_f32 v179, v174, v175\n"
    "v_mfma_f32_16x16x32_bf16 a[132:135], a[148:151], v[214:217], a[132:135]\n"
    "v_accvgpr_read_b32 v254, a128\n"
    "v_rcp_f32 v254, v254\n"
    "s_nop 0\n"
    "v_mul_f32 v0, v254, v0\n"
    "v_mul_f32 v1, v254, v1\n"
    "v_mul_f32 v2, v254, v2\n"
    "v_mul_f32 v3, v254, v3\n"
    "v_mfma_f32_16x16x32_bf16 v[32:35], a[96:99], v[214:217], v[32:35]\n"
    "v_mul_f32 v4, v254, v4\n"
    "v_mul_f32 v5, v254, v5\n"
    "v_mul_f32 v6, v254, v6\n"
    "v_mul_f32 v7, v254, v7\n"
    "v_cvt_pk_bf16_f32 v0, v0, v1\n"
    "v_cvt_pk_bf16_f32 v1, v2, v3\n"
    "v_cvt_pk_bf16_f32 v2, v4, v5\n"
    "v_cvt_pk_bf16_f32 v3, v6, v7\n"
    "v_mfma_f32_16x16x32_bf16 v[36:39], a[100:103], v[214:217], v[36:39]\n"
    "ds_write_b128 v240, v[0:3] offset:24576\n"
    "v_mul_f32 v8, v254, v8\n"
    "v_mul_f32 v9, v254, v9\n"
    "v_mul_f32 v10, v254, v10\n"
    "v_mul_f32 v11, v254, v11\n"
    "v_mfma_f32_16x16x32_bf16 v[40:43], a[104:107], v[214:217], v[40:43]\n"
    "v_mul_f32 v12, v254, v12\n"
    "v_mul_f32 v13, v254, v13\n"
    "v_mul_f32 v14, v254, v14\n"
    "v_mul_f32 v15, v254, v15\n"
    "v_cvt_pk_bf16_f32 v8, v8, v9\n"
    "v_cvt_pk_bf16_f32 v9, v10, v11\n"
    "v_cvt_pk_bf16_f32 v10, v12, v13\n"
    "v_cvt_pk_bf16_f32 v11, v14, v15\n"
    "v_mfma_f32_16x16x32_bf16 v[44:47], a[108:111], v[214:217], v[44:47]\n"
    "ds_write_b128 v240, v[8:11] offset:29696\n"
    "v_mul_f32 v16, v254, v16\n"
    "v_mul_f32 v17, v254, v17\n"
    "v_mul_f32 v18, v254, v18\n"
    "v_mul_f32 v19, v254, v19\n"
    "v_mfma_f32_16x16x32_bf16 v[48:51], a[112:115], v[214:217], v[48:51]\n"
    "v_mul_f32 v20, v254, v20\n"
    "v_mul_f32 v21, v254, v21\n"
    "v_mul_f32 v22, v254, v22\n"
    "v_mul_f32 v23, v254, v23\n"
    "v_cvt_pk_bf16_f32 v16, v16, v17\n"
    "v_cvt_pk_bf16_f32 v17, v18, v19\n"
    "v_cvt_pk_bf16_f32 v18, v20, v21\n"
    "v_cvt_pk_bf16_f32 v19, v22, v23\n"
    "v_mfma_f32_16x16x32_bf16 v[52:55], a[116:119], v[214:217], v[52:55]\n"
    "ds_write_b128 v240, v[16:19] offset:34816\n"
    "v_mul_f32 v24, v254, v24\n"
    "v_mul_f32 v25, v254, v25\n"
    "v_mul_f32 v26, v254, v26\n"
    "v_mul_f32 v27, v254, v27\n"
    "v_mfma_f32_16x16x32_bf16 v[56:59], a[120:123], v[214:217], v[56:59]\n"
    "v_mul_f32 v28, v254, v28\n"
    "v_mul_f32 v29, v254, v29\n"
    "v_mul_f32 v30, v254, v30\n"
    "v_mul_f32 v31, v254, v31\n"
    "v_cvt_pk_bf16_f32 v24, v24, v25\n"
    "v_cvt_pk_bf16_f32 v25, v26, v27\n"
    "v_cvt_pk_bf16_f32 v26, v28, v29\n"
    "v_cvt_pk_bf16_f32 v27, v30, v31\n"
    "v_mfma_f32_16x16x32_bf16 v[60:63], a[124:127], v[214:217], v[60:63]\n"
    "ds_write_b128 v240, v[24:27] offset:39936\n"
    "v_mfma_f32_16x16x32_bf16 a[136:139], a[148:151], v[226:229], a[136:139]\n"
    "v_accvgpr_read_b32 v254, a132\n"
    "v_rcp_f32 v254, v254\n"
    "s_nop 0\n"
    "v_mul_f32 v32, v254, v32\n"
    "v_mul_f32 v33, v254, v33\n"
    "v_mul_f32 v34, v254, v34\n"
    "v_mul_f32 v35, v254, v35\n"
    "v_mfma_f32_16x16x32_bf16 v[64:67], a[96:99], v[226:229], v[64:67]\n"
    "v_mul_f32 v36, v254, v36\n"
    "v_mul_f32 v37, v254, v37\n"
    "v_mul_f32 v38, v254, v38\n"
    "v_mul_f32 v39, v254, v39\n"
    "v_cvt_pk_bf16_f32 v32, v32, v33\n"
    "v_cvt_pk_bf16_f32 v33, v34, v35\n"
    "v_cvt_pk_bf16_f32 v34, v36, v37\n"
    "v_cvt_pk_bf16_f32 v35, v38, v39\n"
    "v_mfma_f32_16x16x32_bf16 v[68:71], a[100:103], v[226:229], v[68:71]\n"
    "ds_write_b128 v240, v[32:35] offset:25600\n"
    "v_mul_f32 v40, v254, v40\n"
    "v_mul_f32 v41, v254, v41\n"
    "v_mul_f32 v42, v254, v42\n"
    "v_mul_f32 v43, v254, v43\n"
    "v_mfma_f32_16x16x32_bf16 v[72:75], a[104:107], v[226:229], v[72:75]\n"
    "v_mul_f32 v44, v254, v44\n"
    "v_mul_f32 v45, v254, v45\n"
    "v_mul_f32 v46, v254, v46\n"
    "v_mul_f32 v47, v254, v47\n"
    "v_cvt_pk_bf16_f32 v40, v40, v41\n"
    "v_cvt_pk_bf16_f32 v41, v42, v43\n"
    "v_cvt_pk_bf16_f32 v42, v44, v45\n"
    "v_cvt_pk_bf16_f32 v43, v46, v47\n"
    "v_mfma_f32_16x16x32_bf16 v[76:79], a[108:111], v[226:229], v[76:79]\n"
    "ds_write_b128 v240, v[40:43] offset:30720\n"
    "v_mul_f32 v48, v254, v48\n"
    "v_mul_f32 v49, v254, v49\n"
    "v_mul_f32 v50, v254, v50\n"
    "v_mul_f32 v51, v254, v51\n"
    "v_mfma_f32_16x16x32_bf16 v[80:83], a[112:115], v[226:229], v[80:83]\n"
    "v_mul_f32 v52, v254, v52\n"
    "v_mul_f32 v53, v254, v53\n"
    "v_mul_f32 v54, v254, v54\n"
    "v_mul_f32 v55, v254, v55\n"
    "v_cvt_pk_bf16_f32 v48, v48, v49\n"
    "v_cvt_pk_bf16_f32 v49, v50, v51\n"
    "v_cvt_pk_bf16_f32 v50, v52, v53\n"
    "v_cvt_pk_bf16_f32 v51, v54, v55\n"
    "v_mfma_f32_16x16x32_bf16 v[84:87], a[116:119], v[226:229], v[84:87]\n"
    "ds_write_b128 v240, v[48:51] offset:35840\n"
    "v_mul_f32 v56, v254, v56\n"
    "v_mul_f32 v57, v254, v57\n"
    "v_mul_f32 v58, v254, v58\n"
    "v_mul_f32 v59, v254, v59\n"
    "v_mfma_f32_16x16x32_bf16 v[88:91], a[120:123], v[226:229], v[88:91]\n"
    "v_mul_f32 v60, v254, v60\n"
    "v_mul_f32 v61, v254, v61\n"
    "v_mul_f32 v62, v254, v62\n"
    "v_mul_f32 v63, v254, v63\n"
    "v_cvt_pk_bf16_f32 v56, v56, v57\n"
    "v_cvt_pk_bf16_f32 v57, v58, v59\n"
    "v_cvt_pk_bf16_f32 v58, v60, v61\n"
    "v_cvt_pk_bf16_f32 v59, v62, v63\n"
    "v_mfma_f32_16x16x32_bf16 v[92:95], a[124:127], v[226:229], v[92:95]\n"
    "ds_write_b128 v240, v[56:59] offset:0\n"
    "v_mfma_f32_16x16x32_bf16 a[140:143], a[148:151], v[230:233], a[140:143]\n"
    "v_accvgpr_read_b32 v254, a136\n"
    "v_rcp_f32 v254, v254\n"
    "s_nop 0\n"
    "v_mul_f32 v64, v254, v64\n"
    "v_mul_f32 v65, v254, v65\n"
    "v_mul_f32 v66, v254, v66\n"
    "v_mul_f32 v67, v254, v67\n"
    "v_mfma_f32_16x16x32_bf16 v[96:99], a[96:99], v[230:233], v[96:99]\n"
    "v_mul_f32 v68, v254, v68\n"
    "v_mul_f32 v69, v254, v69\n"
    "v_mul_f32 v70, v254, v70\n"
    "v_mul_f32 v71, v254, v71\n"
    "v_cvt_pk_bf16_f32 v64, v64, v65\n"
    "v_cvt_pk_bf16_f32 v65, v66, v67\n"
    "v_cvt_pk_bf16_f32 v66, v68, v69\n"
    "v_cvt_pk_bf16_f32 v67, v70, v71\n"
    "v_mfma_f32_16x16x32_bf16 v[100:103], a[100:103], v[230:233], v[100:103]\n"
    "ds_write_b128 v240, v[64:67] offset:26624\n"
    "v_mul_f32 v72, v254, v72\n"
    "v_mul_f32 v73, v254, v73\n"
    "v_mul_f32 v74, v254, v74\n"
    "v_mul_f32 v75, v254, v75\n"
    "v_mfma_f32_16x16x32_bf16 v[104:107], a[104:107], v[230:233], v[104:107]\n"
    "v_mul_f32 v76, v254, v76\n"
    "v_mul_f32 v77, v254, v77\n"
    "v_mul_f32 v78, v254, v78\n"
    "v_mul_f32 v79, v254, v79\n"
    "v_cvt_pk_bf16_f32 v72, v72, v73\n"
    "v_cvt_pk_bf16_f32 v73, v74, v75\n"
    "v_cvt_pk_bf16_f32 v74, v76, v77\n"
    "v_cvt_pk_bf16_f32 v75, v78, v79\n"
    "v_mfma_f32_16x16x32_bf16 v[108:111], a[108:111], v[230:233], v[108:111]\n"
    "ds_write_b128 v240, v[72:75] offset:31744\n"
    "v_mul_f32 v80, v254, v80\n"
    "v_mul_f32 v81, v254, v81\n"
    "v_mul_f32 v82, v254, v82\n"
    "v_mul_f32 v83, v254, v83\n"
    "v_mfma_f32_16x16x32_bf16 v[112:115], a[112:115], v[230:233], v[112:115]\n"
    "v_mul_f32 v84, v254, v84\n"
    "v_mul_f32 v85, v254, v85\n"
    "v_mul_f32 v86, v254, v86\n"
    "v_mul_f32 v87, v254, v87\n"
    "v_cvt_pk_bf16_f32 v80, v80, v81\n"
    "v_cvt_pk_bf16_f32 v81, v82, v83\n"
    "v_cvt_pk_bf16_f32 v82, v84, v85\n"
    "v_cvt_pk_bf16_f32 v83, v86, v87\n"
    "v_mfma_f32_16x16x32_bf16 v[116:119], a[116:119], v[230:233], v[116:119]\n"
    "ds_write_b128 v240, v[80:83] offset:36864\n"
    "v_mul_f32 v88, v254, v88\n"
    "v_mul_f32 v89, v254, v89\n"
    "v_mul_f32 v90, v254, v90\n"
    "v_mul_f32 v91, v254, v91\n"
    "v_mfma_f32_16x16x32_bf16 v[120:123], a[120:123], v[230:233], v[120:123]\n"
    "v_mul_f32 v92, v254, v92\n"
    "v_mul_f32 v93, v254, v93\n"
    "v_mul_f32 v94, v254, v94\n"
    "v_mul_f32 v95, v254, v95\n"
    "v_cvt_pk_bf16_f32 v88, v88, v89\n"
    "v_cvt_pk_bf16_f32 v89, v90, v91\n"
    "v_cvt_pk_bf16_f32 v90, v92, v93\n"
    "v_cvt_pk_bf16_f32 v91, v94, v95\n"
    "v_mfma_f32_16x16x32_bf16 v[124:127], a[124:127], v[230:233], v[124:127]\n"
    "ds_write_b128 v240, v[88:91] offset:1024\n"
    "v_mfma_f32_16x16x32_bf16 a[144:147], a[148:151], v[176:179], a[144:147]\n"
    "v_accvgpr_read_b32 v254, a140\n"
    "v_rcp_f32 v254, v254\n"
    "s_nop 0\n"
    "v_mul_f32 v96, v254, v96\n"
    "v_mul_f32 v97, v254, v97\n"
    "v_mul_f32 v98, v254, v98\n"
    "v_mul_f32 v99, v254, v99\n"
    "v_mfma_f32_16x16x32_bf16 v[128:131], a[96:99], v[176:179], v[128:131]\n"
    "v_mul_f32 v100, v254, v100\n"
    "v_mul_f32 v101, v254, v101\n"
    "v_mul_f32 v102, v254, v102\n"
    "v_mul_f32 v103, v254, v103\n"
    "v_cvt_pk_bf16_f32 v96, v96, v97\n"
    "v_cvt_pk_bf16_f32 v97, v98, v99\n"
    "v_cvt_pk_bf16_f32 v98, v100, v101\n"
    "v_cvt_pk_bf16_f32 v99, v102, v103\n"
    "v_mfma_f32_16x16x32_bf16 v[132:135], a[100:103], v[176:179], v[132:135]\n"
    "ds_write_b128 v240, v[96:99] offset:27648\n"
    "v_mul_f32 v104, v254, v104\n"
    "v_mul_f32 v105, v254, v105\n"
    "v_mul_f32 v106, v254, v106\n"
    "v_mul_f32 v107, v254, v107\n"
    "v_mfma_f32_16x16x32_bf16 v[136:139], a[104:107], v[176:179], v[136:139]\n"
    "v_mul_f32 v108, v254, v108\n"
    "v_mul_f32 v109, v254, v109\n"
    "v_mul_f32 v110, v254, v110\n"
    "v_mul_f32 v111, v254, v111\n"
    "v_cvt_pk_bf16_f32 v104, v104, v105\n"
    "v_cvt_pk_bf16_f32 v105, v106, v107\n"
    "v_cvt_pk_bf16_f32 v106, v108, v109\n"
    "v_cvt_pk_bf16_f32 v107, v110, v111\n"
    "v_mfma_f32_16x16x32_bf16 v[140:143], a[108:111], v[176:179], v[140:143]\n"
    "ds_write_b128 v240, v[104:107] offset:32768\n"
    "v_mul_f32 v112, v254, v112\n"
    "v_mul_f32 v113, v254, v113\n"
    "v_mul_f32 v114, v254, v114\n"
    "v_mul_f32 v115, v254, v115\n"
    "v_mfma_f32_16x16x32_bf16 v[144:147], a[112:115], v[176:179], v[144:147]\n"
    "v_mul_f32 v116, v254, v116\n"
    "v_mul_f32 v117, v254, v117\n"
    "v_mul_f32 v118, v254, v118\n"
    "v_mul_f32 v119, v254, v119\n"
    "v_cvt_pk_bf16_f32 v112, v112, v113\n"
    "v_cvt_pk_bf16_f32 v113, v114, v115\n"
    "v_cvt_pk_bf16_f32 v114, v116, v117\n"
    "v_cvt_pk_bf16_f32 v115, v118, v119\n"
    "v_mfma_f32_16x16x32_bf16 v[148:151], a[116:119], v[176:179], v[148:151]\n"
    "ds_write_b128 v240, v[112:115] offset:37888\n"
    "v_mul_f32 v120, v254, v120\n"
    "v_mul_f32 v121, v254, v121\n"
    "v_mul_f32 v122, v254, v122\n"
    "v_mul_f32 v123, v254, v123\n"
    "v_mfma_f32_16x16x32_bf16 v[152:155], a[120:123], v[176:179], v[152:155]\n"
    "v_mul_f32 v124, v254, v124\n"
    "v_mul_f32 v125, v254, v125\n"
    "v_mul_f32 v126, v254, v126\n"
    "v_mul_f32 v127, v254, v127\n"
    "v_cvt_pk_bf16_f32 v120, v120, v121\n"
    "v_cvt_pk_bf16_f32 v121, v122, v123\n"
    "v_cvt_pk_bf16_f32 v122, v124, v125\n"
    "v_cvt_pk_bf16_f32 v123, v126, v127\n"
    "v_mfma_f32_16x16x32_bf16 v[156:159], a[124:127], v[176:179], v[156:159]\n"
    "ds_write_b128 v240, v[120:123] offset:2048\n"
    "s_nop 1\n"
    "v_accvgpr_read_b32 v254, a144\n"
    "v_rcp_f32 v254, v254\n"
    "B_TT_FIRST1:\n"
    "B_TT_S1:\n"
    "s_cbranch_execz B_TT_RET\n"
    "s_nop 0\n"
    "v_mul_f32 v128, v254, v128\n"
    "v_mul_f32 v129, v254, v129\n"
    "v_mul_f32 v130, v254, v130\n"
    "v_mul_f32 v131, v254, v131\n"
    "v_mul_f32 v132, v254, v132\n"
    "v_mul_f32 v133, v254, v133\n"
    "v_mul_f32 v134, v254, v134\n"
    "v_mul_f32 v135, v254, v135\n"
    "v_cvt_pk_bf16_f32 v128, v128, v129\n"
    "v_cvt_pk_bf16_f32 v129, v130, v131\n"
    "v_cvt_pk_bf16_f32 v130, v132, v133\n"
    "v_cvt_pk_bf16_f32 v131, v134, v135\n"
    "ds_write_b128 v240, v[128:131] offset:28672\n"
    "v_mul_f32 v136, v254, v136\n"
    "v_mul_f32 v137, v254, v137\n"
    "v_mul_f32 v138, v254, v138\n"
    "v_mul_f32 v139, v254, v139\n"
    "v_mul_f32 v140, v254, v140\n"
    "v_mul_f32 v141, v254, v141\n"
    "v_mul_f32 v142, v254, v142\n"
    "v_mul_f32 v143, v254, v143\n"
    "v_cvt_pk_bf16_f32 v136, v136, v137\n"
    "v_cvt_pk_bf16_f32 v137, v138, v139\n"
    "v_cvt_pk_bf16_f32 v138, v140, v141\n"
    "v_cvt_pk_bf16_f32 v139, v142, v143\n"
    "ds_write_b128 v240, v[136:139] offset:33792\n"
    "v_mul_f32 v144, v254, v144\n"
    "v_mul_f32 v145, v254, v145\n"
    "v_mul_f32 v146, v254, v146\n"
    "v_mul_f32 v147, v254, v147\n"
    "v_mul_f32 v148, v254, v148\n"
    "v_mul_f32 v149, v254, v149\n"
    "v_mul_f32 v150, v254, v150\n"
    "v_mul_f32 v151, v254, v151\n"
    "v_cvt_pk_bf16_f32 v144, v144, v145\n"
    "v_cvt_pk_bf16_f32 v145, v146, v147\n"
    "v_cvt_pk_bf16_f32 v146, v148, v149\n"
    "v_cvt_pk_bf16_f32 v147, v150, v151\n"
    "ds_write_b128 v240, v[144:147] offset:38912\n"
    "v_mul_f32 v152, v254, v152\n"
    "v_mul_f32 v153, v254, v153\n"
    "v_mul_f32 v154, v254, v154\n"
    "v_mul_f32 v155, v254, v155\n"
    "v_mul_f32 v156, v254, v156\n"
    "v_mul_f32 v157, v254, v157\n"
    "v_mul_f32 v158, v254, v158\n"
    "v_mul_f32 v159, v254, v159\n"
    "v_cvt_pk_bf16_f32 v152, v152, v153\n"
    "v_cvt_pk_bf16_f32 v153, v154, v155\n"
    "v_cvt_pk_bf16_f32 v154, v156, v157\n"
    "v_cvt_pk_bf16_f32 v155, v158, v159\n"
    "ds_write_b128 v240, v[152:155] offset:3072\n"
    "s_add_u32 s85, s87, s86\n"
    "s_mov_b32 s80, 1\n"
    "s_mov_b32 s84, 0x2000\n"
    "s_mov_b32 s88, 0x3000\n"
    "s_add_u32 s89, s35, 1\n"
    "s_mul_i32 s89, s89, 0x14000\n"
    "s_mov_b32 s81, 0\n"
    "v_add_u32 v230, 0x2000, v244\n"
    "v_add_u32 v231, 0x2000, v245\n"
    "v_add_u32 v232, 0x2000, v246\n"
    "v_add_u32 v233, 0x2000, v247\n"
    "s_waitcnt vmcnt(0) lgkmcnt(0)\n"
    "s_barrier\n"
    "ds_read_b64_tr_b16 v[84:85], v230 offset:0\n"
    "ds_read_b64_tr_b16 v[86:87], v230 offset:2048\n"
    "ds_read_b64_tr_b16 v[92:93], v231 offset:0\n"
    "ds_read_b64_tr_b16 v[94:95], v231 offset:2048\n"
    "ds_read_b128 a[0:3], v241 offset:24576\n"
    "ds_read_b128 a[4:7], v241 offset:25600\n"
    "ds_read_b128 a[8:11], v241 offset:26624\n"
    "ds_read_b128 a[12:15], v241 offset:27648\n"
    "ds_read_b128 a[16:19], v241 offset:28672\n"
    "s_waitcnt lgkmcnt(4)\n"
    "v_mfma_f32_16x16x32_bf16 v[4:7], v[84:87], v[0:3], 0\n"
    "ds_read_b64_tr_b16 v[100:101], v232 offset:0\n"
    "ds_read_b64_tr_b16 v[102:103], v232 offset:2048\n"
    "v_mfma_f32_16x16x32_bf16 v[4:7], v[92:95], a[0:3], v[4:7]\n"
    "s_waitcnt lgkmcnt(5)\n"
    "ds_read_b128 a[20:23], v242 offset:24576\n"
    "ds_read_b128 a[40:43], v243 offset:24576\n"
    "v_mfma_f32_16x16x32_bf16 v[12:15], v[92:95], a[4:7], 0\n"
    "ds_read_b64_tr_b16 v[108:109], v233 offset:0\n"
    "ds_read_b64_tr_b16 v[110:111], v233 offset:2048\n"
    "v_add_u32 v230, s88, v244\n"
    "v_mfma_f32_16x16x32_bf16 v[12:15], v[84:87], v[32:35], v[12:15]\n"
    "s_waitcnt lgkmcnt(8)\n"
    "ds_read_b128 a[24:27], v242 offset:25600\n"
    "ds_read_b128 a[44:47], v243 offset:25600\n"
    "v_mfma_f32_16x16x32_bf16 v[20:23], v[84:87], v[64:67], 0\n"
    "ds_read_b128 a[28:31], v242 offset:26624\n"
    "ds_read_b128 a[48:51], v243 offset:26624\n"
    "v_add_u32 v231, s88, v245\n"
    "v_mfma_f32_16x16x32_bf16 v[20:23], v[92:95], a[8:11], v[20:23]\n"
    "s_waitcnt lgkmcnt(11)\n"
    "ds_read_b128 a[32:35], v242 offset:27648\n"
    "ds_read_b128 a[52:55], v243 offset:27648\n"
    "v_mfma_f32_16x16x32_bf16 v[28:31], v[92:95], a[12:15], 0\n"
    "ds_read_b128 a[36:39], v242 offset:28672\n"
    "ds_read_b128 a[56:59], v243 offset:28672\n"
    "v_mfma_f32_16x16x32_bf16 v[28:31], v[84:87], v[96:99], v[28:31]\n"
    "s_waitcnt lgkmcnt(14)\n"
    "ds_read_b128 a[60:63], v241 offset:29696\n"
    "ds_read_b128 a[64:67], v241 offset:30720\n"
    "v_mfma_f32_16x16x32_bf16 v[36:39], v[84:87], v[128:131], 0\n"
    "ds_read_b128 a[68:71], v241 offset:31744\n"
    "ds_read_b128 a[72:75], v241 offset:32768\n"
    "v_mfma_f32_16x16x32_bf16 v[36:39], v[92:95], a[16:19], v[36:39]\n"
    "s_waitcnt lgkmcnt(14)\n"
    "s_waitcnt vmcnt(12) lgkmcnt(12)\n"
    "s_barrier\n"
    "v_add_u32 v233, s88, v247\n"
    "v_mfma_f32_16x16x32_bf16 v[4:7], v[100:103], a[20:23], v[4:7]\n"
    "ds_read_b64_tr_b16 v[84:85], v230 offset:0\n"
    "ds_read_b64_tr_b16 v[86:87], v230 offset:2048\n"
    "v_mfma_f32_16x16x32_bf16 v[4:7], v[108:111], a[40:43], v[4:7]\n"
    "s_waitcnt lgkmcnt(12)\n"
    "ds_read_b128 a[76:79], v241 offset:33792\n"
    "ds_read_b128 a[80:83], v242 offset:29696\n"
    "v_mfma_f32_16x16x32_bf16 v[12:15], v[108:111], a[44:47], v[12:15]\n"
    "ds_read_b64_tr_b16 v[92:93], v231 offset:0\n"
    "ds_read_b64_tr_b16 v[94:95], v231 offset:2048\n"
    "v_mfma_f32_16x16x32_bf16 v[12:15], v[100:103], a[24:27], v[12:15]\n"
    "s_waitcnt lgkmcnt(14)\n"
    "ds_read_b128 a[100:103], v243 offset:29696\n"
    "ds_read_b128 a[84:87], v242 offset:30720\n"
    "v_mfma_f32_16x16x32_bf16 v[20:23], v[100:103], a[28:31], v[20:23]\n"
    "ds_read_b128 a[104:107], v243 offset:30720\n"
    "v_add_u32 v232, s88, v246\n"
    "v_mfma_f32_16x16x32_bf16 v[20:23], v[108:111], a[48:51], v[20:23]\n"
    "s_waitcnt lgkmcnt(15)\n"
    "s_add_u32 s78, s85, 0x9000\n"
    "ds_read_b128 a[88:91], v242 offset:31744\n"
    "v_mfma_f32_16x16x32_bf16 v[28:31], v[108:111], a[52:55], v[28:31]\n"
    "s_add_u32 m0, s71, s84\n"
    "s_add_u32 s88, s88, 0x1000\n"
    "ds_read_b128 a[108:111], v243 offset:31744\n"
    "v_mfma_f32_16x16x32_bf16 v[28:31], v[100:103], a[32:35], v[28:31]\n"
    "s_waitcnt lgkmcnt(15)\n"
    "s_cmp_eq_u32 s88, 0x6000\n"
    "s_cselect_b32 s88, 0x1000, s88\n"
    "buffer_load_dwordx4 v234, s[52:55], s78 offen lds\n"
    "v_mfma_f32_16x16x32_bf16 v[36:39], v[100:103], a[36:39], v[36:39]\n"
    "ds_read_b128 a[92:95], v242 offset:32768\n"
    "ds_read_b128 a[112:115], v243 offset:32768\n"
    "v_mfma_f32_16x16x32_bf16 v[36:39], v[108:111], a[56:59], v[36:39]\n"
    "s_waitcnt lgkmcnt(15)\n"
    "s_waitcnt lgkmcnt(7)\n"
    "ds_read_b128 a[96:99], v242 offset:33792\n"
    "v_mfma_f32_16x16x32_bf16 v[4:7], v[84:87], v[8:11], v[4:7]\n"
    "s_add_u32 s84, s84, 0x1000\n"
    "ds_read_b64_tr_b16 v[100:101], v232 offset:0\n"
    "ds_read_b64_tr_b16 v[102:103], v232 offset:2048\n"
    "v_mfma_f32_16x16x32_bf16 v[4:7], v[92:95], a[60:63], v[4:7]\n"
    "s_waitcnt lgkmcnt(15)\n"
    "ds_read_b128 a[116:119], v243 offset:33792\n"
    "ds_read_b128 a[120:123], v241 offset:34816\n"
    "v_mfma_f32_16x16x32_bf16 v[12:15], v[92:95], a[64:67], v[12:15]\n"
    "ds_read_b64_tr_b16 v[108:109], v233 offset:0\n"
    "ds_read_b64_tr_b16 v[110:111], v233 offset:2048\n"
    "v_mfma_f32_16x16x32_bf16 v[12:15], v[84:87], v[40:43], v[12:15]\n"
    "s_waitcnt lgkmcnt(15)\n"
    "buffer_load_dwordx4 v235, s[52:55], s78 offen offset:1024 lds\n"
    "v_mfma_f32_16x16x32_bf16 v[20:23], v[84:87], v[72:75], v[20:23]\n"
    "ds_read_b128 a[124:127], v241 offset:35840\n"
    "v_add_u32 v230, s88, v244\n"
    "v_mfma_f32_16x16x32_bf16 v[20:23], v[92:95], a[68:71], v[20:23]\n"
    "s_waitcnt lgkmcnt(15)\n"
    "ds_read_b128 a[128:131], v241 offset:36864\n"
    "v_add_u32 v231, s88, v245\n"
    "v_mfma_f32_16x16x32_bf16 v[28:31], v[92:95], a[72:75], v[28:31]\n"
    "s_cmp_eq_u32 s84, 0x6000\n"
    "s_cselect_b32 s84, 0x1000, s84\n"
    "v_add_u32 v232, s88, v246\n"
    "v_mfma_f32_16x16x32_bf16 v[28:31], v[84:87], v[104:107], v[28:31]\n"
    "s_waitcnt lgkmcnt(15)\n"
    "buffer_load_dwordx4 v236, s[52:55], s78 offen offset:2048 lds\n"
    "v_mfma_f32_16x16x32_bf16 v[36:39], v[84:87], v[136:139], v[36:39]\n"
    "ds_read_b128 a[132:135], v241 offset:37888\n"
    "v_and_b32 v229, 15, v239\n"
    "v_mfma_f32_16x16x32_bf16 v[36:39], v[92:95], a[76:79], v[36:39]\n"
    "s_waitcnt lgkmcnt(15)\n"
    "s_waitcnt vmcnt(11) lgkmcnt(3)\n"
    "s_barrier\n"
    "v_mfma_f32_16x16x32_bf16 v[4:7], v[100:103], a[80:83], v[4:7]\n"
    "ds_read_b64_tr_b16 v[84:85], v230 offset:0\n"
    "ds_read_b64_tr_b16 v[86:87], v230 offset:2048\n"
    "v_mfma_f32_16x16x32_bf16 v[4:7], v[108:111], a[100:103], v[4:7]\n"
    "s_waitcnt lgkmcnt(15)\n"
    "buffer_load_dwordx4 v237, s[52:55], s78 offen offset:3072 lds\n"
    "v_add_u32 v233, s88, v247\n"
    "v_mfma_f32_16x16x32_bf16 v[12:15], v[108:111], a[104:107], v[12:15]\n"
    "ds_read_b64_tr_b16 v[92:93], v231 offset:0\n"
    "ds_read_b64_tr_b16 v[94:95], v231 offset:2048\n"
    "v_mfma_f32_16x16x32_bf16 v[12:15], v[100:103], a[84:87], v[12:15]\n"
    "s_waitcnt lgkmcnt(15)\n"
    "ds_read_b128 a[136:139], v241 offset:38912\n"
    "ds_read_b128 a[140:143], v242 offset:34816\n"
    "v_mfma_f32_16x16x32_bf16 v[20:23], v[100:103], a[88:91], v[20:23]\n"
    "ds_read_b128 a[160:163], v243 offset:34816\n"
    "ds_read_b128 a[144:147], v242 offset:35840\n"
    "v_mfma_f32_16x16x32_bf16 v[20:23], v[108:111], a[108:111], v[20:23]\n"
    "s_waitcnt lgkmcnt(15)\n"
    "s_add_u32 s78, s85, 0x12000\n"
    "ds_read_b128 a[164:167], v243 offset:35840\n"
    "v_mfma_f32_16x16x32_bf16 v[28:31], v[108:111], a[112:115], v[28:31]\n"
    "s_add_u32 m0, s71, s84\n"
    "ds_read_b128 a[148:151], v242 offset:36864\n"
    "ds_read_b128 a[168:171], v243 offset:36864\n"
    "v_mfma_f32_16x16x32_bf16 v[28:31], v[100:103], a[92:95], v[28:31]\n"
    "s_waitcnt lgkmcnt(15)\n"
    "s_add_u32 s88, s88, 0x1000\n"
    "buffer_load_dwordx4 v234, s[52:55], s78 offen lds\n"
    "v_mfma_f32_16x16x32_bf16 v[36:39], v[100:103], a[96:99], v[36:39]\n"
    "s_cmp_eq_u32 s88, 0x6000\n"
    "s_cselect_b32 s88, 0x1000, s88\n"
    "ds_read_b128 a[152:155], v242 offset:37888\n"
    "ds_read_b128 a[172:175], v243 offset:37888\n"
    "v_mfma_f32_16x16x32_bf16 v[36:39], v[108:111], a[116:119], v[36:39]\n"
    "s_waitcnt lgkmcnt(15)\n"
    "s_waitcnt lgkmcnt(9)\n"
    "ds_read_b128 a[156:159], v242 offset:38912\n"
    "v_mfma_f32_16x16x32_bf16 v[4:7], v[84:87], v[16:19], v[4:7]\n"
    "s_add_u32 s84, s84, 0x1000\n"
    "ds_read_b64_tr_b16 v[100:101], v232 offset:0\n"
    "ds_read_b64_tr_b16 v[102:103], v232 offset:2048\n"
    "v_mfma_f32_16x16x32_bf16 v[4:7], v[92:95], a[120:123], v[4:7]\n"
    "s_waitcnt lgkmcnt(15)\n"
    "ds_read_b128 a[176:179], v243 offset:38912\n"
    "ds_read_b128 a[180:183], v241 offset:39936\n"
    "v_mfma_f32_16x16x32_bf16 v[12:15], v[92:95], a[124:127], v[12:15]\n"
    "ds_read_b64_tr_b16 v[108:109], v233 offset:0\n"
    "ds_read_b64_tr_b16 v[110:111], v233 offset:2048\n"
    "v_mfma_f32_16x16x32_bf16 v[12:15], v[84:87], v[48:51], v[12:15]\n"
    "s_waitcnt lgkmcnt(15)\n"
    "buffer_load_dwordx4 v235, s[52:55], s78 offen offset:1024 lds\n"
    "v_mfma_f32_16x16x32_bf16 v[20:23], v[84:87], v[80:83], v[20:23]\n"
    "ds_read_b128 a[184:187], v241 offset:0\n"
    "v_add_u32 v230, s88, v244\n"
    "v_mfma_f32_16x16x32_bf16 v[20:23], v[92:95], a[128:131], v[20:23]\n"
    "s_waitcnt lgkmcnt(15)\n"
    "ds_read_b128 a[188:191], v241 offset:1024\n"
    "v_add_u32 v231, s88, v245\n"
    "v_mfma_f32_16x16x32_bf16 v[28:31], v[92:95], a[132:135], v[28:31]\n"
    "s_cmp_eq_u32 s84, 0x6000\n"
    "s_cselect_b32 s84, 0x1000, s84\n"
    "v_add_u32 v232, s88, v246\n"
    "v_mfma_f32_16x16x32_bf16 v[28:31], v[84:87], v[112:115], v[28:31]\n"
    "s_waitcnt lgkmcnt(15)\n"
    "buffer_load_dwordx4 v236, s[52:55], s78 offen offset:2048 lds\n"
    "v_mfma_f32_16x16x32_bf16 v[36:39], v[84:87], v[144:147], v[36:39]\n"
    "ds_read_b128 a[192:195], v241 offset:2048\n"
    "v_mul_u32_u24 v229, 0x90, v229\n"
    "v_mfma_f32_16x16x32_bf16 v[36:39], v[92:95], a[136:139], v[36:39]\n"
    "s_waitcnt lgkmcnt(15)\n"
    "s_waitcnt vmcnt(11) lgkmcnt(3)\n"
    "s_barrier\n"
    "v_lshrrev_b32 v228, 4, v239\n"
    "v_mfma_f32_16x16x32_bf16 v[4:7], v[100:103], a[140:143], v[4:7]\n"
    "ds_read_b64_tr_b16 v[84:85], v230 offset:0\n"
    "ds_read_b64_tr_b16 v[86:87], v230 offset:2048\n"
    "v_lshl_add_u32 v229, v228, 3, v229\n"
    "v_mfma_f32_16x16x32_bf16 v[4:7], v[108:111], a[160:163], v[4:7]\n"
    "s_waitcnt lgkmcnt(15)\n"
    "buffer_load_dwordx4 v237, s[52:55], s78 offen offset:3072 lds\n"
    "v_add_u32 v233, s88, v247\n"
    "v_mfma_f32_16x16x32_bf16 v[12:15], v[108:111], a[164:167], v[12:15]\n"
    "ds_read_b64_tr_b16 v[92:93], v231 offset:0\n"
    "ds_read_b64_tr_b16 v[94:95], v231 offset:2048\n"
    "v_lshl_add_u32 v229, s72, 5, v229\n"
    "v_mfma_f32_16x16x32_bf16 v[12:15], v[100:103], a[144:147], v[12:15]\n"
    "s_waitcnt lgkmcnt(15)\n"
    "ds_read_b128 a[196:199], v241 offset:3072\n"
    "ds_read_b128 a[200:203], v242 offset:39936\n"
    "v_mfma_f32_16x16x32_bf16 v[20:23], v[100:103], a[148:151], v[20:23]\n"
    "ds_read_b128 a[220:223], v243 offset:39936\n"
    "ds_read_b128 a[204:207], v242 offset:0\n"
    "v_lshrrev_b32 v228, 3, v239\n"
    "v_mfma_f32_16x16x32_bf16 v[20:23], v[108:111], a[168:171], v[20:23]\n"
    "s_waitcnt lgkmcnt(15)\n"
    "s_add_u32 s78, s85, 0x1b000\n"
    "ds_read_b128 a[224:227], v243 offset:0\n"
    "v_mul_u32_u24 v228, 0x880, v228\n"
    "v_mfma_f32_16x16x32_bf16 v[28:31], v[108:111], a[172:175], v[28:31]\n"
    "s_add_u32 m0, s71, s84\n"
    "ds_read_b128 a[208:211], v242 offset:1024\n"
    "ds_read_b128 a[228:231], v243 offset:1024\n"
    "v_mfma_f32_16x16x32_bf16 v[28:31], v[100:103], a[152:155], v[28:31]\n"
    "s_waitcnt lgkmcnt(15)\n"
    "s_add_u32 s88, s88, 0x1000\n"
    "buffer_load_dwordx4 v234, s[52:55], s78 offen lds\n"
    "v_mfma_f32_16x16x32_bf16 v[36:39], v[100:103], a[156:159], v[36:39]\n"
    "s_cmp_eq_u32 s88, 0x6000\n"
    "s_cselect_b32 s88, 0x1000, s88\n"
    "ds_read_b128 a[212:215], v242 offset:2048\n"
    "ds_read_b128 a[232:235], v243 offset:2048\n"
    "v_mfma_f32_16x16x32_bf16 v[36:39], v[108:111], a[176:179], v[36:39]\n"
    "s_waitcnt lgkmcnt(15)\n"
    "s_waitcnt lgkmcnt(9)\n"
    "ds_read_b128 a[216:219], v242 offset:3072\n"
    "v_add_u32 v228, v228, v238\n"
    "v_mfma_f32_16x16x32_bf16 v[4:7], v[84:87], v[24:27], v[4:7]\n"
    "s_add_u32 s84, s84, 0x1000\n"
    "ds_read_b64_tr_b16 v[100:101], v232 offset:0\n"
    "ds_read_b64_tr_b16 v[102:103], v232 offset:2048\n"
    "v_mfma_f32_16x16x32_bf16 v[4:7], v[92:95], a[180:183], v[4:7]\n"
    "s_waitcnt lgkmcnt(15)\n"
    "ds_read_b128 a[236:239], v243 offset:3072\n"
    "v_add_u32 v230, s88, v244\n"
    "v_mfma_f32_16x16x32_bf16 v[12:15], v[92:95], a[184:187], v[12:15]\n"
    "s_add_u32 s87, s87, 0x80\n"
    "ds_read_b64_tr_b16 v[108:109], v233 offset:0\n"
    "ds_read_b64_tr_b16 v[110:111], v233 offset:2048\n"
    "v_mfma_f32_16x16x32_bf16 v[12:15], v[84:87], v[56:59], v[12:15]\n"
    "s_waitcnt lgkmcnt(15)\n"
    "buffer_load_dwordx4 v235, s[52:55], s78 offen offset:1024 lds\n"
    "v_mfma_f32_16x16x32_bf16 v[20:23], v[84:87], v[88:91], v[20:23]\n"
    "v_add_u32 v231, s88, v245\n"
    "v_add_u32 v232, s88, v246\n"
    "v_mfma_f32_16x16x32_bf16 v[20:23], v[92:95], a[188:191], v[20:23]\n"
    "s_waitcnt lgkmcnt(15)\n"
    "s_cmp_eq_u32 s84, 0x6000\n"
    "s_cselect_b32 s84, 0x1000, s84\n"
    "v_mfma_f32_16x16x32_bf16 v[28:31], v[92:95], a[192:195], v[28:31]\n"
    "s_cmp_eq_u32 s87, 0x480\n"
    "s_cselect_b32 s87, 0, s87\n"
    "v_mfma_f32_16x16x32_bf16 v[28:31], v[84:87], v[120:123], v[28:31]\n"
    "s_waitcnt lgkmcnt(14)\n"
    "s_add_u32 s85, s87, s86\n"
    "buffer_load_dwordx4 v236, s[52:55], s78 offen offset:2048 lds\n"
    "v_mfma_f32_16x16x32_bf16 v[36:39], v[84:87], v[152:155], v[36:39]\n"
    "s_add_u32 s80, s80, 1\n"
    "v_mfma_f32_16x16x32_bf16 v[36:39], v[92:95], a[196:199], v[36:39]\n"
    "s_waitcnt lgkmcnt(12)\n"
    "s_waitcnt vmcnt(11) lgkmcnt(0)\n"
    "s_barrier\n"
    "v_mfma_f32_16x16x32_bf16 v[4:7], v[100:103], a[200:203], v[4:7]\n"
    "ds_read_b64_tr_b16 v[84:85], v230 offset:0\n"
    "ds_read_b64_tr_b16 v[86:87], v230 offset:2048\n"
    "v_mfma_f32_16x16x32_bf16 v[4:7], v[108:111], a[220:223], v[4:7]\n"
    "s_waitcnt lgkmcnt(12)\n"
    "buffer_load_dwordx4 v237, s[52:55], s78 offen offset:3072 lds\n"
    "v_mfma_f32_16x16x32_bf16 v[12:15], v[108:111], a[224:227], v[12:15]\n"
    "ds_read_b64_tr_b16 v[92:93], v231 offset:0\n"
    "ds_read_b64_tr_b16 v[94:95], v231 offset:2048\n"
    "v_mfma_f32_16x16x32_bf16 v[12:15], v[100:103], a[204:207], v[12:15]\n"
    "s_waitcnt lgkmcnt(12)\n"
    "v_add_u32 v233, s88, v247\n"
    "v_mov_b32 v241, 0x40000000\n"
    "v_mfma_f32_16x16x32_bf16 v[20:23], v[100:103], a[208:211], v[20:23]\n"
    "s_cmp_lt_u32 s80, 9\n"
    "s_cselect_b32 s85, s85, 0x40000000\n"
    "v_mov_b32 v253, 0x40000000\n"
    "v_mfma_f32_16x16x32_bf16 v[20:23], v[108:111], a[228:231], v[20:23]\n"
    "s_waitcnt lgkmcnt(10)\n"
    "s_add_u32 s78, s85, 0\n"
    "s_add_u32 s88, s88, 0x1000\n"
    "v_cndmask_b32 v242, v241, v238, s[28:29]\n"
    "v_mfma_f32_16x16x32_bf16 v[28:31], v[108:111], a[232:235], v[28:31]\n"
    "s_add_u32 m0, s71, s84\n"
    "s_cmp_eq_u32 s88, 0x6000\n"
    "s_cselect_b32 s88, 0x1000, s88\n"
    "v_lshrrev_b32 v243, 3, v239\n"
    "v_mfma_f32_16x16x32_bf16 v[28:31], v[100:103], a[212:215], v[28:31]\n"
    "s_waitcnt lgkmcnt(6)\n"
    "buffer_load_dwordx4 v234, s[52:55], s78 offen lds\n"
    "v_lshl_add_u32 v243, v243, 4, v238\n"
    "v_mfma_f32_16x16x32_bf16 v[36:39], v[100:103], a[216:219], v[36:39]\n"
    "v_mov_b32 v254, 0xd80\n"
    "v_mfma_f32_16x16x32_bf16 v[36:39], v[108:111], a[236:239], v[36:39]\n"
    "s_waitcnt lgkmcnt(0)\n"
    "v_mad_u32_u24 v243, s72, v254, v243\n"
    "B_OP_LOOP:\n"
    "v_mfma_f32_16x16x32_bf16 v[44:47], v[84:87], v[0:3], 0\n"
    "s_add_u32 s77, s89, s90\n"
    "ds_read_b64_tr_b16 v[100:101], v232 offset:0\n"
    "ds_read_b64_tr_b16 v[102:103], v232 offset:2048\n"
    "v_mfma_f32_16x16x32_bf16 v[44:47], v[92:95], a[0:3], v[44:47]\n"
    "s_add_u32 s84, s84, 0x1000\n"
    "v_add_u32 v230, s88, v244\n"
    "v_add_u32 v231, s88, v245\n"
    "v_mfma_f32_16x16x32_bf16 v[52:55], v[92:95], a[4:7], 0\n"
    "ds_read_b64_tr_b16 v[108:109], v233 offset:0\n"
    "ds_read_b64_tr_b16 v[110:111], v233 offset:2048\n"
    "v_mfma_f32_16x16x32_bf16 v[52:55], v[84:87], v[32:35], v[52:55]\n"
    "s_cmp_eq_u32 s84, 0x6000\n"
    "s_cselect_b32 s84, 0x1000, s84\n"
    "buffer_load_dwordx4 v235, s[52:55], s78 offen offset:1024 lds\n"
    "v_mfma_f32_16x16x32_bf16 v[60:63], v[84:87], v[64:67], 0\n"
    "v_cvt_pk_bf16_f32 v116, v4, v5\n"
    "v_cvt_pk_bf16_f32 v117, v6, v7\n"
    "v_mfma_f32_16x16x32_bf16 v[60:63], v[92:95], a[8:11], v[60:63]\n"
    "v_cvt_pk_bf16_f32 v118, v12, v13\n"
    "v_cvt_pk_bf16_f32 v119, v14, v15\n"
    "v_mfma_f32_16x16x32_bf16 v[68:71], v[92:95], a[12:15], 0\n"
    "v_cvt_pk_bf16_f32 v124, v20, v21\n"
    "v_cvt_pk_bf16_f32 v125, v22, v23\n"
    "v_mfma_f32_16x16x32_bf16 v[68:71], v[84:87], v[96:99], v[68:71]\n"
    "buffer_load_dwordx4 v236, s[52:55], s78 offen offset:2048 lds\n"
    "v_add_u32 v232, s88, v246\n"
    "v_mfma_f32_16x16x32_bf16 v[76:79], v[84:87], v[128:131], 0\n"
    "v_permlane32_swap_b32 v116, v118\n"
    "v_permlane32_swap_b32 v117, v119\n"
    "v_mfma_f32_16x16x32_bf16 v[76:79], v[92:95], a[16:19], v[76:79]\n"
    "s_waitcnt vmcnt(11) lgkmcnt(0)\n"
    "s_barrier\n"
    "v_cvt_pk_bf16_f32 v126, v28, v29\n"
    "v_mfma_f32_16x16x32_bf16 v[44:47], v[100:103], a[20:23], v[44:47]\n"
    "ds_read_b64_tr_b16 v[84:85], v230 offset:0\n"
    "ds_read_b64_tr_b16 v[86:87], v230 offset:2048\n"
    "v_mfma_f32_16x16x32_bf16 v[44:47], v[108:111], a[40:43], v[44:47]\n"
    "buffer_load_dwordx4 v237, s[52:55], s78 offen offset:3072 lds\n"
    "v_add_u32 v233, s88, v247\n"
    "v_mfma_f32_16x16x32_bf16 v[52:55], v[108:111], a[44:47], v[52:55]\n"
    "ds_read_b64_tr_b16 v[92:93], v231 offset:0\n"
    "ds_read_b64_tr_b16 v[94:95], v231 offset:2048\n"
    "v_mfma_f32_16x16x32_bf16 v[52:55], v[100:103], a[24:27], v[52:55]\n"
    "s_cmp_eq_u32 s81, 6\n"
    "s_cselect_b32 s79, 0, 0x40000000\n"
    "buffer_store_dword v252, v253, s[36:39], 0 offen sc1\n"
    "v_mfma_f32_16x16x32_bf16 v[60:63], v[100:103], a[28:31], v[60:63]\n"
    "v_cvt_pk_bf16_f32 v127, v30, v31\n"
    "v_cvt_pk_bf16_f32 v132, v36, v37\n"
    "v_mfma_f32_16x16x32_bf16 v[60:63], v[108:111], a[48:51], v[60:63]\n"
    "s_add_u32 s78, s85, 0x9000\n"
    "s_add_u32 s88, s88, 0x1000\n"
    "buffer_store_dwordx4 v[116:119], v238, s[48:51], s77 offen sc1\n"
    "v_mfma_f32_16x16x32_bf16 v[68:71], v[108:111], a[52:55], v[68:71]\n"
    "s_add_u32 m0, s71, s84\n"
    "v_permlane32_swap_b32 v124, v126\n"
    "v_permlane32_swap_b32 v125, v127\n"
    "v_mfma_f32_16x16x32_bf16 v[68:71], v[100:103], a[32:35], v[68:71]\n"
    "s_cmp_eq_u32 s88, 0x6000\n"
    "s_cselect_b32 s88, 0x1000, s88\n"
    "buffer_load_dwordx4 v234, s[52:55], s78 offen lds\n"
    "v_mfma_f32_16x16x32_bf16 v[76:79], v[100:103], a[36:39], v[76:79]\n"
    "v_cvt_pk_bf16_f32 v133, v38, v39\n"
    "v_mfma_f32_16x16x32_bf16 v[76:79], v[108:111], a[56:59], v[76:79]\n"
    "s_waitcnt lgkmcnt(0)\n"
    "buffer_store_dwordx4 v[124:127], v238, s[48:51], s77 offen offset:1024 sc1\n"
    "v_mfma_f32_16x16x32_bf16 v[44:47], v[84:87], v[8:11], v[44:47]\n"
    "s_add_u32 s84, s84, 0x1000\n"
    "ds_read_b64_tr_b16 v[100:101], v232 offset:0\n"
    "ds_read_b64_tr_b16 v[102:103], v232 offset:2048\n"
    "v_mfma_f32_16x16x32_bf16 v[44:47], v[92:95], a[60:63], v[44:47]\n"
    "v_permlane32_swap_b32 v132, v134\n"
    "v_permlane32_swap_b32 v133, v135\n"
    "v_mfma_f32_16x16x32_bf16 v[52:55], v[92:95], a[64:67], v[52:55]\n"
    "s_lshl_b32 s28, 1, s81\n"
    "ds_read_b64_tr_b16 v[108:109], v233 offset:0\n"
    "ds_read_b64_tr_b16 v[110:111], v233 offset:2048\n"
    "v_mfma_f32_16x16x32_bf16 v[52:55], v[84:87], v[40:43], v[52:55]\n"
    "s_cmp_eq_u32 s84, 0x6000\n"
    "s_cselect_b32 s84, 0x1000, s84\n"
    "buffer_load_dwordx4 v235, s[52:55], s78 offen offset:1024 lds\n"
    "v_mfma_f32_16x16x32_bf16 v[60:63], v[84:87], v[72:75], v[60:63]\n"
    "v_add_u32 v230, s88, v244\n"
    "v_add_u32 v231, s88, v245\n"
    "v_mfma_f32_16x16x32_bf16 v[60:63], v[92:95], a[68:71], v[60:63]\n"
    "s_cmp_eq_u32 s81, s61\n"
    "s_cselect_b32 s28, 0xff00, s28\n"
    "buffer_store_dwordx4 v[132:135], v242, s[48:51], s77 offen offset:2048 sc1\n"
    "v_mfma_f32_16x16x32_bf16 v[68:71], v[92:95], a[72:75], v[68:71]\n"
    "v_add_u32 v232, s88, v246\n"
    "v_cndmask_b32 v253, v241, v250, s[28:29]\n"
    "v_mfma_f32_16x16x32_bf16 v[68:71], v[84:87], v[104:107], v[68:71]\n"
    "buffer_load_dwordx4 v236, s[52:55], s78 offen offset:2048 lds\n"
    "v_mfma_f32_16x16x32_bf16 v[76:79], v[84:87], v[136:139], v[76:79]\n"
    "v_mfma_f32_16x16x32_bf16 v[76:79], v[92:95], a[76:79], v[76:79]\n"
    "s_waitcnt vmcnt(15) lgkmcnt(0)\n"
    "s_barrier\n"
    "v_add_u32 v233, s88, v247\n"
    "v_mfma_f32_16x16x32_bf16 v[44:47], v[100:103], a[80:83], v[44:47]\n"
    "ds_read_b64_tr_b16 v[84:85], v230 offset:0\n"
    "ds_read_b64_tr_b16 v[86:87], v230 offset:2048\n"
    "v_mfma_f32_16x16x32_bf16 v[44:47], v[108:111], a[100:103], v[44:47]\n"
    "buffer_load_dwordx4 v237, s[52:55], s78 offen offset:3072 lds\n"
    "v_mfma_f32_16x16x32_bf16 v[52:55], v[108:111], a[104:107], v[52:55]\n"
    "ds_read_b64_tr_b16 v[92:93], v231 offset:0\n"
    "ds_read_b64_tr_b16 v[94:95], v231 offset:2048\n"
    "v_mfma_f32_16x16x32_bf16 v[52:55], v[100:103], a[84:87], v[52:55]\n"
    "v_mfma_f32_16x16x32_bf16 v[60:63], v[100:103], a[88:91], v[60:63]\n"
    "v_mfma_f32_16x16x32_bf16 v[60:63], v[108:111], a[108:111], v[60:63]\n"
    "s_add_u32 s78, s85, 0x12000\n"
    "s_add_u32 s88, s88, 0x1000\n"
    "buffer_load_dword v254, v249, s[36:39], s79 offen sc1\n"
    "v_mfma_f32_16x16x32_bf16 v[68:71], v[108:111], a[112:115], v[68:71]\n"
    "s_add_u32 m0, s71, s84\n"
    "s_cmp_eq_u32 s88, 0x6000\n"
    "s_cselect_b32 s88, 0x1000, s88\n"
    "v_mfma_f32_16x16x32_bf16 v[68:71], v[100:103], a[92:95], v[68:71]\n"
    "buffer_load_dwordx4 v234, s[52:55], s78 offen lds\n"
    "v_mfma_f32_16x16x32_bf16 v[76:79], v[100:103], a[96:99], v[76:79]\n"
    "v_mfma_f32_16x16x32_bf16 v[76:79], v[108:111], a[116:119], v[76:79]\n"
    "s_waitcnt lgkmcnt(0)\n"
    "v_mfma_f32_16x16x32_bf16 v[44:47], v[84:87], v[16:19], v[44:47]\n"
    "s_add_u32 s84, s84, 0x1000\n"
    "ds_read_b64_tr_b16 v[100:101], v232 offset:0\n"
    "ds_read_b64_tr_b16 v[102:103], v232 offset:2048\n"
    "v_mfma_f32_16x16x32_bf16 v[44:47], v[92:95], a[120:123], v[44:47]\n"
    "s_add_u32 s89, s89, 0x14000\n"
    "v_add_u32 v230, s88, v244\n"
    "v_add_u32 v231, s88, v245\n"
    "v_mfma_f32_16x16x32_bf16 v[52:55], v[92:95], a[124:127], v[52:55]\n"
    "ds_read_b64_tr_b16 v[108:109], v233 offset:0\n"
    "ds_read_b64_tr_b16 v[110:111], v233 offset:2048\n"
    "v_mfma_f32_16x16x32_bf16 v[52:55], v[84:87], v[48:51], v[52:55]\n"
    "s_cmp_eq_u32 s84, 0x6000\n"
    "s_cselect_b32 s84, 0x1000, s84\n"
    "buffer_load_dwordx4 v235, s[52:55], s78 offen offset:1024 lds\n"
    "v_mfma_f32_16x16x32_bf16 v[60:63], v[84:87], v[80:83], v[60:63]\n"
    "s_cmp_eq_u32 s89, 0xb4000\n"
    "s_cselect_b32 s89, 0, s89\n"
    "v_add_u32 v232, s88, v246\n"
    "v_mfma_f32_16x16x32_bf16 v[60:63], v[92:95], a[128:131], v[60:63]\n"
    "s_add_u32 s90, s90, 0x2800\n"
    "v_mfma_f32_16x16x32_bf16 v[68:71], v[92:95], a[132:135], v[68:71]\n"
    "s_add_u32 s81, s81, 1\n"
    "v_mfma_f32_16x16x32_bf16 v[68:71], v[84:87], v[112:115], v[68:71]\n"
    "buffer_load_dwordx4 v236, s[52:55], s78 offen offset:2048 lds\n"
    "v_mfma_f32_16x16x32_bf16 v[76:79], v[84:87], v[144:147], v[76:79]\n"
    "v_mfma_f32_16x16x32_bf16 v[76:79], v[92:95], a[136:139], v[76:79]\n"
    "s_waitcnt vmcnt(16) lgkmcnt(0)\n"
    "s_barrier\n"
    "v_add_u32 v233, s88, v247\n"
    "v_mfma_f32_16x16x32_bf16 v[44:47], v[100:103], a[140:143], v[44:47]\n"
    "ds_read_b64_tr_b16 v[84:85], v230 offset:0\n"
    "ds_read_b64_tr_b16 v[86:87], v230 offset:2048\n"
    "v_mfma_f32_16x16x32_bf16 v[44:47], v[108:111], a[160:163], v[44:47]\n"
    "buffer_load_dwordx4 v237, s[52:55], s78 offen offset:3072 lds\n"
    "v_mfma_f32_16x16x32_bf16 v[52:55], v[108:111], a[164:167], v[52:55]\n"
    "ds_read_b64_tr_b16 v[92:93], v231 offset:0\n"
    "ds_read_b64_tr_b16 v[94:95], v231 offset:2048\n"
    "v_mfma_f32_16x16x32_bf16 v[52:55], v[100:103], a[144:147], v[52:55]\n"
    "v_mfma_f32_16x16x32_bf16 v[60:63], v[100:103], a[148:151], v[60:63]\n"
    "v_mfma_f32_16x16x32_bf16 v[60:63], v[108:111], a[168:171], v[60:63]\n"
    "s_add_u32 s78, s85, 0x1b000\n"
    "s_add_u32 s88, s88, 0x1000\n"
    "v_mfma_f32_16x16x32_bf16 v[68:71], v[108:111], a[172:175], v[68:71]\n"
    "s_add_u32 m0, s71, s84\n"
    "s_cmp_eq_u32 s88, 0x6000\n"
    "s_cselect_b32 s88, 0x1000, s88\n"
    "v_mfma_f32_16x16x32_bf16 v[68:71], v[100:103], a[152:155], v[68:71]\n"
    "s_add_u32 s87, s87, 0x80\n"
    "buffer_load_dwordx4 v234, s[52:55], s78 offen lds\n"
    "v_mfma_f32_16x16x32_bf16 v[76:79], v[100:103], a[156:159], v[76:79]\n"
    "s_cmp_eq_u32 s87, 0x480\n"
    "s_cselect_b32 s87, 0, s87\n"
    "v_mfma_f32_16x16x32_bf16 v[76:79], v[108:111], a[176:179], v[76:79]\n"
    "s_waitcnt lgkmcnt(0)\n"
    "s_add_u32 s85, s87, s86\n"
    "v_mfma_f32_16x16x32_bf16 v[44:47], v[84:87], v[24:27], v[44:47]\n"
    "s_add_u32 s84, s84, 0x1000\n"
    "ds_read_b64_tr_b16 v[100:101], v232 offset:0\n"
    "ds_read_b64_tr_b16 v[102:103], v232 offset:2048\n"
    "v_mfma_f32_16x16x32_bf16 v[44:47], v[92:95], a[180:183], v[44:47]\n"
    "s_add_u32 s80, s80, 1\n"
    "v_add_u32 v230, s88, v244\n"
    "v_add_u32 v231, s88, v245\n"
    "v_mfma_f32_16x16x32_bf16 v[52:55], v[92:95], a[184:187], v[52:55]\n"
    "ds_read_b64_tr_b16 v[108:109], v233 offset:0\n"
    "ds_read_b64_tr_b16 v[110:111], v233 offset:2048\n"
    "v_mfma_f32_16x16x32_bf16 v[52:55], v[84:87], v[56:59], v[52:55]\n"
    "s_cmp_eq_u32 s84, 0x6000\n"
    "s_cselect_b32 s84, 0x1000, s84\n"
    "buffer_load_dwordx4 v235, s[52:55], s78 offen offset:1024 lds\n"
    "v_mfma_f32_16x16x32_bf16 v[60:63], v[84:87], v[88:91], v[60:63]\n"
    "s_cmp_lt_u32 s80, 9\n"
    "s_cselect_b32 s85, s85, 0x40000000\n"
    "v_add_u32 v232, s88, v246\n"
    "v_mfma_f32_16x16x32_bf16 v[60:63], v[92:95], a[188:191], v[60:63]\n"
    "v_mfma_f32_16x16x32_bf16 v[68:71], v[92:95], a[192:195], v[68:71]\n"
    "v_mfma_f32_16x16x32_bf16 v[68:71], v[84:87], v[120:123], v[68:71]\n"
    "buffer_load_dwordx4 v236, s[52:55], s78 offen offset:2048 lds\n"
    "v_mfma_f32_16x16x32_bf16 v[76:79], v[84:87], v[152:155], v[76:79]\n"
    "v_mfma_f32_16x16x32_bf16 v[76:79], v[92:95], a[196:199], v[76:79]\n"
    "s_waitcnt vmcnt(16) lgkmcnt(0)\n"
    "s_barrier\n"
    "v_add_u32 v233, s88, v247\n"
    "v_mfma_f32_16x16x32_bf16 v[44:47], v[100:103], a[200:203], v[44:47]\n"
    "ds_read_b64_tr_b16 v[84:85], v230 offset:0\n"
    "ds_read_b64_tr_b16 v[86:87], v230 offset:2048\n"
    "v_mfma_f32_16x16x32_bf16 v[44:47], v[108:111], a[220:223], v[44:47]\n"
    "buffer_load_dwordx4 v237, s[52:55], s78 offen offset:3072 lds\n"
    "v_mfma_f32_16x16x32_bf16 v[52:55], v[108:111], a[224:227], v[52:55]\n"
    "ds_read_b64_tr_b16 v[92:93], v231 offset:0\n"
    "ds_read_b64_tr_b16 v[94:95], v231 offset:2048\n"
    "v_mfma_f32_16x16x32_bf16 v[52:55], v[100:103], a[204:207], v[52:55]\n"
    "v_mfma_f32_16x16x32_bf16 v[60:63], v[100:103], a[208:211], v[60:63]\n"
    "v_mfma_f32_16x16x32_bf16 v[60:63], v[108:111], a[228:231], v[60:63]\n"
    "s_add_u32 s78, s85, 0\n"
    "s_add_u32 s88, s88, 0x1000\n"
    "v_mfma_f32_16x16x32_bf16 v[68:71], v[108:111], a[232:235], v[68:71]\n"
    "s_add_u32 m0, s71, s84\n"
    "s_cmp_eq_u32 s88, 0x6000\n"
    "s_cselect_b32 s88, 0x1000, s88\n"
    "v_mfma_f32_16x16x32_bf16 v[68:71], v[100:103], a[212:215], v[68:71]\n"
    "buffer_load_dwordx4 v234, s[52:55], s78 offen lds\n"
    "B_TT_FIRST2:\n"
    "B_TT_S2:\n"
    "s_cbranch_execz B_TT_RET\n"
    "v_mfma_f32_16x16x32_bf16 v[76:79], v[100:103], a[216:219], v[76:79]\n"
    "v_mfma_f32_16x16x32_bf16 v[76:79], v[108:111], a[236:239], v[76:79]\n"
    "s_waitcnt lgkmcnt(0)\n"
    "s_cmp_eq_u32 s81, 7\n"
    "s_cbranch_scc1 B_OP_LAST\n"
    "v_mfma_f32_16x16x32_bf16 v[4:7], v[84:87], v[0:3], 0\n"
    "s_add_u32 s77, s89, s90\n"
    "ds_read_b64_tr_b16 v[100:101], v232 offset:0\n"
    "ds_read_b64_tr_b16 v[102:103], v232 offset:2048\n"
    "v_mfma_f32_16x16x32_bf16 v[4:7], v[92:95], a[0:3], v[4:7]\n"
    "s_add_u32 s84, s84, 0x1000\n"
    "v_add_u32 v230, s88, v244\n"
    "v_add_u32 v231, s88, v245\n"
    "v_mfma_f32_16x16x32_bf16 v[12:15], v[92:95], a[4:7], 0\n"
    "ds_read_b64_tr_b16 v[108:109], v233 offset:0\n"
    "ds_read_b64_tr_b16 v[110:111], v233 offset:2048\n"
    "v_mfma_f32_16x16x32_bf16 v[12:15], v[84:87], v[32:35], v[12:15]\n"
    "s_cmp_eq_u32 s84, 0x6000\n"
    "s_cselect_b32 s84, 0x1000, s84\n"
    "buffer_load_dwordx4 v235, s[52:55], s78 offen offset:1024 lds\n"
    "v_mfma_f32_16x16x32_bf16 v[20:23], v[84:87], v[64:67], 0\n"
    "v_cvt_pk_bf16_f32 v116, v44, v45\n"
    "v_cvt_pk_bf16_f32 v117, v46, v47\n"
    "v_mfma_f32_16x16x32_bf16 v[20:23], v[92:95], a[8:11], v[20:23]\n"
    "v_cvt_pk_bf16_f32 v118, v52, v53\n"
    "v_cvt_pk_bf16_f32 v119, v54, v55\n"
    "v_mfma_f32_16x16x32_bf16 v[28:31], v[92:95], a[12:15], 0\n"
    "v_cvt_pk_bf16_f32 v124, v60, v61\n"
    "v_cvt_pk_bf16_f32 v125, v62, v63\n"
    "v_mfma_f32_16x16x32_bf16 v[28:31], v[84:87], v[96:99], v[28:31]\n"
    "buffer_load_dwordx4 v236, s[52:55], s78 offen offset:2048 lds\n"
    "v_add_u32 v232, s88, v246\n"
    "v_mfma_f32_16x16x32_bf16 v[36:39], v[84:87], v[128:131], 0\n"
    "v_permlane32_swap_b32 v116, v118\n"
    "v_permlane32_swap_b32 v117, v119\n"
    "v_mfma_f32_16x16x32_bf16 v[36:39], v[92:95], a[16:19], v[36:39]\n"
    "s_waitcnt vmcnt(12) lgkmcnt(0)\n"
    "s_barrier\n"
    "v_cvt_pk_bf16_f32 v126, v68, v69\n"
    "v_mfma_f32_16x16x32_bf16 v[4:7], v[100:103], a[20:23], v[4:7]\n"
    "ds_read_b64_tr_b16 v[84:85], v230 offset:0\n"
    "ds_read_b64_tr_b16 v[86:87], v230 offset:2048\n"
    "v_mfma_f32_16x16x32_bf16 v[4:7], v[108:111], a[40:43], v[4:7]\n"
    "buffer_load_dwordx4 v237, s[52:55], s78 offen offset:3072 lds\n"
    "v_add_u32 v233, s88, v247\n"
    "v_mfma_f32_16x16x32_bf16 v[12:15], v[108:111], a[44:47], v[12:15]\n"
    "ds_read_b64_tr_b16 v[92:93], v231 offset:0\n"
    "ds_read_b64_tr_b16 v[94:95], v231 offset:2048\n"
    "v_mfma_f32_16x16x32_bf16 v[12:15], v[100:103], a[24:27], v[12:15]\n"
    "buffer_store_dword v252, v253, s[36:39], 0 offen sc1\n"
    "v_mfma_f32_16x16x32_bf16 v[20:23], v[100:103], a[28:31], v[20:23]\n"
    "v_cvt_pk_bf16_f32 v127, v70, v71\n"
    "v_cvt_pk_bf16_f32 v132, v76, v77\n"
    "v_mfma_f32_16x16x32_bf16 v[20:23], v[108:111], a[48:51], v[20:23]\n"
    "s_add_u32 s78, s85, 0x9000\n"
    "s_add_u32 s88, s88, 0x1000\n"
    "buffer_store_dwordx4 v[116:119], v238, s[48:51], s77 offen sc1\n"
    "v_mfma_f32_16x16x32_bf16 v[28:31], v[108:111], a[52:55], v[28:31]\n"
    "s_add_u32 m0, s71, s84\n"
    "v_permlane32_swap_b32 v124, v126\n"
    "v_permlane32_swap_b32 v125, v127\n"
    "v_mfma_f32_16x16x32_bf16 v[28:31], v[100:103], a[32:35], v[28:31]\n"
    "s_cmp_eq_u32 s88, 0x6000\n"
    "s_cselect_b32 s88, 0x1000, s88\n"
    "buffer_load_dwordx4 v234, s[52:55], s78 offen lds\n"
    "v_mfma_f32_16x16x32_bf16 v[36:39], v[100:103], a[36:39], v[36:39]\n"
    "v_cvt_pk_bf16_f32 v133, v78, v79\n"
    "v_mfma_f32_16x16x32_bf16 v[36:39], v[108:111], a[56:59], v[36:39]\n"
    "s_waitcnt lgkmcnt(0)\n"
    "buffer_store_dwordx4 v[124:127], v238, s[48:51], s77 offen offset:1024 sc1\n"
    "v_mfma_f32_16x16x32_bf16 v[4:7], v[84:87], v[8:11], v[4:7]\n"
    "s_add_u32 s84, s84, 0x1000\n"
    "ds_read_b64_tr_b16 v[100:101], v232 offset:0\n"
    "ds_read_b64_tr_b16 v[102:103], v232 offset:2048\n"
    "v_mfma_f32_16x16x32_bf16 v[4:7], v[92:95], a[60:63], v[4:7]\n"
    "v_permlane32_swap_b32 v132, v134\n"
    "v_permlane32_swap_b32 v133, v135\n"
    "v_mfma_f32_16x16x32_bf16 v[12:15], v[92:95], a[64:67], v[12:15]\n"
    "s_lshl_b32 s28, 1, s81\n"
    "ds_read_b64_tr_b16 v[108:109], v233 offset:0\n"
    "ds_read_b64_tr_b16 v[110:111], v233 offset:2048\n"
    "v_mfma_f32_16x16x32_bf16 v[12:15], v[84:87], v[40:43], v[12:15]\n"
    "s_cmp_eq_u32 s84, 0x6000\n"
    "s_cselect_b32 s84, 0x1000, s84\n"
    "buffer_load_dwordx4 v235, s[52:55], s78 offen offset:1024 lds\n"
    "v_mfma_f32_16x16x32_bf16 v[20:23], v[84:87], v[72:75], v[20:23]\n"
    "v_add_u32 v230, s88, v244\n"
    "v_add_u32 v231, s88, v245\n"
    "v_mfma_f32_16x16x32_bf16 v[20:23], v[92:95], a[68:71], v[20:23]\n"
    "s_cmp_eq_u32 s81, s61\n"
    "s_cselect_b32 s28, 0xff00, s28\n"
    "buffer_store_dwordx4 v[132:135], v242, s[48:51], s77 offen offset:2048 sc1\n"
    "v_mfma_f32_16x16x32_bf16 v[28:31], v[92:95], a[72:75], v[28:31]\n"
    "v_add_u32 v232, s88, v246\n"
    "v_cndmask_b32 v253, v241, v250, s[28:29]\n"
    "v_mfma_f32_16x16x32_bf16 v[28:31], v[84:87], v[104:107], v[28:31]\n"
    "buffer_load_dwordx4 v236, s[52:55], s78 offen offset:2048 lds\n"
    "v_mfma_f32_16x16x32_bf16 v[36:39], v[84:87], v[136:139], v[36:39]\n"
    "v_mfma_f32_16x16x32_bf16 v[36:39], v[92:95], a[76:79], v[36:39]\n"
    "s_waitcnt vmcnt(15) lgkmcnt(0)\n"
    "s_barrier\n"
    "v_add_u32 v233, s88, v247\n"
    "v_mfma_f32_16x16x32_bf16 v[4:7], v[100:103], a[80:83], v[4:7]\n"
    "ds_read_b64_tr_b16 v[84:85], v230 offset:0\n"
    "ds_read_b64_tr_b16 v[86:87], v230 offset:2048\n"
    "v_mfma_f32_16x16x32_bf16 v[4:7], v[108:111], a[100:103], v[4:7]\n"
    "buffer_load_dwordx4 v237, s[52:55], s78 offen offset:3072 lds\n"
    "v_mfma_f32_16x16x32_bf16 v[12:15], v[108:111], a[104:107], v[12:15]\n"
    "ds_read_b64_tr_b16 v[92:93], v231 offset:0\n"
    "ds_read_b64_tr_b16 v[94:95], v231 offset:2048\n"
    "v_mfma_f32_16x16x32_bf16 v[12:15], v[100:103], a[84:87], v[12:15]\n"
    "v_mfma_f32_16x16x32_bf16 v[20:23], v[100:103], a[88:91], v[20:23]\n"
    "v_mfma_f32_16x16x32_bf16 v[20:23], v[108:111], a[108:111], v[20:23]\n"
    "s_add_u32 s78, s85, 0x12000\n"
    "s_add_u32 s88, s88, 0x1000\n"
    "v_mfma_f32_16x16x32_bf16 v[28:31], v[108:111], a[112:115], v[28:31]\n"
    "s_add_u32 m0, s71, s84\n"
    "s_cmp_eq_u32 s88, 0x6000\n"
    "s_cselect_b32 s88, 0x1000, s88\n"
    "v_mfma_f32_16x16x32_bf16 v[28:31], v[100:103], a[92:95], v[28:31]\n"
    "buffer_load_dwordx4 v234, s[52:55], s78 offen lds\n"
    "v_mfma_f32_16x16x32_bf16 v[36:39], v[100:103], a[96:99], v[36:39]\n"
    "v_mfma_f32_16x16x32_bf16 v[36:39], v[108:111], a[116:119], v[36:39]\n"
    "s_waitcnt lgkmcnt(0)\n"
    "v_mfma_f32_16x16x32_bf16 v[4:7], v[84:87], v[16:19], v[4:7]\n"
    "s_add_u32 s84, s84, 0x1000\n"
    "ds_read_b64_tr_b16 v[100:101], v232 offset:0\n"
    "ds_read_b64_tr_b16 v[102:103], v232 offset:2048\n"
    "v_mfma_f32_16x16x32_bf16 v[4:7], v[92:95], a[120:123], v[4:7]\n"
    "s_add_u32 s89, s89, 0x14000\n"
    "v_add_u32 v230, s88, v244\n"
    "v_add_u32 v231, s88, v245\n"
    "v_mfma_f32_16x16x32_bf16 v[12:15], v[92:95], a[124:127], v[12:15]\n"
    "ds_read_b64_tr_b16 v[108:109], v233 offset:0\n"
    "ds_read_b64_tr_b16 v[110:111], v233 offset:2048\n"
    "v_mfma_f32_16x16x32_bf16 v[12:15], v[84:87], v[48:51], v[12:15]\n"
    "s_cmp_eq_u32 s84, 0x6000\n"
    "s_cselect_b32 s84, 0x1000, s84\n"
    "buffer_load_dwordx4 v235, s[52:55], s78 offen offset:1024 lds\n"
    "v_mfma_f32_16x16x32_bf16 v[20:23], v[84:87], v[80:83], v[20:23]\n"
    "s_cmp_eq_u32 s89, 0xb4000\n"
    "s_cselect_b32 s89, 0, s89\n"
    "v_add_u32 v232, s88, v246\n"
    "v_mfma_f32_16x16x32_bf16 v[20:23], v[92:95], a[128:131], v[20:23]\n"
    "s_add_u32 s90, s90, 0x2800\n"
    "v_mfma_f32_16x16x32_bf16 v[28:31], v[92:95], a[132:135], v[28:31]\n"
    "s_add_u32 s81, s81, 1\n"
    "v_mfma_f32_16x16x32_bf16 v[28:31], v[84:87], v[112:115], v[28:31]\n"
    "buffer_load_dwordx4 v236, s[52:55], s78 offen offset:2048 lds\n"
    "v_mfma_f32_16x16x32_bf16 v[36:39], v[84:87], v[144:147], v[36:39]\n"
    "v_mfma_f32_16x16x32_bf16 v[36:39], v[92:95], a[136:139], v[36:39]\n"
    "s_waitcnt vmcnt(15) lgkmcnt(0)\n"
    "s_barrier\n"
    "v_add_u32 v233, s88, v247\n"
    "v_mfma_f32_16x16x32_bf16 v[4:7], v[100:103], a[140:143], v[4:7]\n"
    "ds_read_b64_tr_b16 v[84:85], v230 offset:0\n"
    "ds_read_b64_tr_b16 v[86:87], v230 offset:2048\n"
    "v_mfma_f32_16x16x32_bf16 v[4:7], v[108:111], a[160:163], v[4:7]\n"
    "buffer_load_dwordx4 v237, s[52:55], s78 offen offset:3072 lds\n"
    "v_mfma_f32_16x16x32_bf16 v[12:15], v[108:111], a[164:167], v[12:15]\n"
    "ds_read_b64_tr_b16 v[92:93], v231 offset:0\n"
    "ds_read_b64_tr_b16 v[94:95], v231 offset:2048\n"
    "v_mfma_f32_16x16x32_bf16 v[12:15], v[100:103], a[144:147], v[12:15]\n"
    "v_mfma_f32_16x16x32_bf16 v[20:23], v[100:103], a[148:151], v[20:23]\n"
    "v_mfma_f32_16x16x32_bf16 v[20:23], v[108:111], a[168:171], v[20:23]\n"
    "s_add_u32 s78, s85, 0x1b000\n"
    "s_add_u32 s88, s88, 0x1000\n"
    "v_mfma_f32_16x16x32_bf16 v[28:31], v[108:111], a[172:175], v[28:31]\n"
    "s_add_u32 m0, s71, s84\n"
    "s_cmp_eq_u32 s88, 0x6000\n"
    "s_cselect_b32 s88, 0x1000, s88\n"
    "v_mfma_f32_16x16x32_bf16 v[28:31], v[100:103], a[152:155], v[28:31]\n"
    "s_add_u32 s87, s87, 0x80\n"
    "buffer_load_dwordx4 v234, s[52:55], s78 offen lds\n"
    "v_mfma_f32_16x16x32_bf16 v[36:39], v[100:103], a[156:159], v[36:39]\n"
    "s_cmp_eq_u32 s87, 0x480\n"
    "s_cselect_b32 s87, 0, s87\n"
    "v_mfma_f32_16x16x32_bf16 v[36:39], v[108:111], a[176:179], v[36:39]\n"
    "s_waitcnt lgkmcnt(0)\n"
    "s_add_u32 s85, s87, s86\n"
    "v_mfma_f32_16x16x32_bf16 v[4:7], v[84:87], v[24:27], v[4:7]\n"
    "s_add_u32 s84, s84, 0x1000\n"
    "ds_read_b64_tr_b16 v[100:101], v232 offset:0\n"
    "ds_read_b64_tr_b16 v[102:103], v232 offset:2048\n"
    "v_mfma_f32_16x16x32_bf16 v[4:7], v[92:95], a[180:183], v[4:7]\n"
    "s_add_u32 s80, s80, 1\n"
    "v_add_u32 v230, s88, v244\n"
    "v_add_u32 v231, s88, v245\n"
    "v_mfma_f32_16x16x32_bf16 v[12:15], v[92:95], a[184:187], v[12:15]\n"
    "ds_read_b64_tr_b16 v[108:109], v233 offset:0\n"
    "ds_read_b64_tr_b16 v[110:111], v233 offset:2048\n"
    "v_mfma_f32_16x16x32_bf16 v[12:15], v[84:87], v[56:59], v[12:15]\n"
    "s_cmp_eq_u32 s84, 0x6000\n"
    "s_cselect_b32 s84, 0x1000, s84\n"
    "buffer_load_dwordx4 v235, s[52:55], s78 offen offset:1024 lds\n"
    "v_mfma_f32_16x16x32_bf16 v[20:23], v[84:87], v[88:91], v[20:23]\n"
    "s_cmp_lt_u32 s80, 9\n"
    "s_cselect_b32 s85, s85, 0x40000000\n"
    "v_add_u32 v232, s88, v246\n"
    "v_mfma_f32_16x16x32_bf16 v[20:23], v[92:95], a[188:191], v[20:23]\n"
    "v_mfma_f32_16x16x32_bf16 v[28:31], v[92:95], a[192:195], v[28:31]\n"
    "v_mfma_f32_16x16x32_bf16 v[28:31], v[84:87], v[120:123], v[28:31]\n"
    "buffer_load_dwordx4 v236, s[52:55], s78 offen offset:2048 lds\n"
    "v_mfma_f32_16x16x32_bf16 v[36:39], v[84:87], v[152:155], v[36:39]\n"
    "v_mfma_f32_16x16x32_bf16 v[36:39], v[92:95], a[196:199], v[36:39]\n"
    "s_waitcnt vmcnt(15) lgkmcnt(0)\n"
    "s_barrier\n"
    "v_add_u32 v233, s88, v247\n"
    "v_mfma_f32_16x16x32_bf16 v[4:7], v[100:103], a[200:203], v[4:7]\n"
    "ds_read_b64_tr_b16 v[84:85], v230 offset:0\n"
    "ds_read_b64_tr_b16 v[86:87], v230 offset:2048\n"
    "v_mfma_f32_16x16x32_bf16 v[4:7], v[108:111], a[220:223], v[4:7]\n"
    "buffer_load_dwordx4 v237, s[52:55], s78 offen offset:3072 lds\n"
    "v_mfma_f32_16x16x32_bf16 v[12:15], v[108:111], a[224:227], v[12:15]\n"
    "ds_read_b64_tr_b16 v[92:93], v231 offset:0\n"
    "ds_read_b64_tr_b16 v[94:95], v231 offset:2048\n"
    "v_mfma_f32_16x16x32_bf16 v[12:15], v[100:103], a[204:207], v[12:15]\n"
    "v_mfma_f32_16x16x32_bf16 v[20:23], v[100:103], a[208:211], v[20:23]\n"
    "v_mfma_f32_16x16x32_bf16 v[20:23], v[108:111], a[228:231], v[20:23]\n"
    "s_add_u32 s78, s85, 0\n"
    "s_add_u32 s88, s88, 0x1000\n"
    "v_mfma_f32_16x16x32_bf16 v[28:31], v[108:111], a[232:235], v[28:31]\n"
    "s_add_u32 m0, s71, s84\n"
    "s_cmp_eq_u32 s88, 0x6000\n"
    "s_cselect_b32 s88, 0x1000, s88\n"
    "v_mfma_f32_16x16x32_bf16 v[28:31], v[100:103], a[212:215], v[28:31]\n"
    "buffer_load_dwordx4 v234, s[52:55], s78 offen lds\n"
    "v_mfma_f32_16x16x32_bf16 v[36:39], v[100:103], a[216:219], v[36:39]\n"
    "v_mfma_f32_16x16x32_bf16 v[36:39], v[108:111], a[236:239], v[36:39]\n"
    "s_waitcnt lgkmcnt(0)\n"
    "s_branch B_OP_LOOP\n"
    "B_OP_LAST:\n"
    "s_waitcnt vmcnt(9)\n"
    "s_mov_b32 s65, 0\n"
    "B_C1_CHK:\n"
    "v_cmp_eq_u32 vcc, v254, v251\n"
    "s_or_b32 s63, vcc_lo, s60\n"
    "s_andn2_b32 s76, 0xf0f, s63\n"
    "s_cbranch_scc1 B_C1_SLOW\n"
    "B_C1_OK:\n"
    "v_mfma_f32_16x16x32_bf16 v[4:7], v[84:87], v[0:3], 0\n"
    "s_add_u32 s77, s89, s90\n"
    "ds_read_b64_tr_b16 v[100:101], v232 offset:0\n"
    "ds_read_b64_tr_b16 v[102:103], v232 offset:2048\n"
    "v_mfma_f32_16x16x32_bf16 v[4:7], v[92:95], a[0:3], v[4:7]\n"
    "v_add_u32 v230, s88, v244\n"
    "v_add_u32 v231, s88, v245\n"
    "v_mfma_f32_16x16x32_bf16 v[12:15], v[92:95], a[4:7], 0\n"
    "ds_read_b64_tr_b16 v[108:109], v233 offset:0\n"
    "ds_read_b64_tr_b16 v[110:111], v233 offset:2048\n"
    "v_mfma_f32_16x16x32_bf16 v[12:15], v[84:87], v[32:35], v[12:15]\n"
    "v_cvt_pk_bf16_f32 v116, v44, v45\n"
    "v_cvt_pk_bf16_f32 v117, v46, v47\n"
    "v_mfma_f32_16x16x32_bf16 v[20:23], v[84:87], v[64:67], 0\n"
    "v_cvt_pk_bf16_f32 v118, v52, v53\n"
    "v_cvt_pk_bf16_f32 v119, v54, v55\n"
    "v_mfma_f32_16x16x32_bf16 v[20:23], v[92:95], a[8:11], v[20:23]\n"
    "v_cvt_pk_bf16_f32 v124, v60, v61\n"
    "v_cvt_pk_bf16_f32 v125, v62, v63\n"
    "v_mfma_f32_16x16x32_bf16 v[28:31], v[92:95], a[12:15], 0\n"
    "v_permlane32_swap_b32 v116, v118\n"
    "v_permlane32_swap_b32 v117, v119\n"
    "v_mfma_f32_16x16x32_bf16 v[28:31], v[84:87], v[96:99], v[28:31]\n"
    "buffer_store_dwordx4 v[116:119], v238, s[48:51], s77 offen sc1\n"
    "v_add_u32 v232, s88, v246\n"
    "v_mfma_f32_16x16x32_bf16 v[36:39], v[84:87], v[128:131], 0\n"
    "v_cvt_pk_bf16_f32 v126, v68, v69\n"
    "v_cvt_pk_bf16_f32 v127, v70, v71\n"
    "v_mfma_f32_16x16x32_bf16 v[36:39], v[92:95], a[16:19], v[36:39]\n"
    "s_waitcnt vmcnt(11) lgkmcnt(0)\n"
    "s_barrier\n"
    "v_cvt_pk_bf16_f32 v132, v76, v77\n"
    "v_mfma_f32_16x16x32_bf16 v[4:7], v[100:103], a[20:23], v[4:7]\n"
    "ds_read_b64_tr_b16 v[84:85], v230 offset:0\n"
    "ds_read_b64_tr_b16 v[86:87], v230 offset:2048\n"
    "v_permlane32_swap_b32 v124, v126\n"
    "v_permlane32_swap_b32 v125, v127\n"
    "v_mfma_f32_16x16x32_bf16 v[4:7], v[108:111], a[40:43], v[4:7]\n"
    "buffer_store_dwordx4 v[124:127], v238, s[48:51], s77 offen offset:1024 sc1\n"
    "v_cvt_pk_bf16_f32 v133, v78, v79\n"
    "v_mfma_f32_16x16x32_bf16 v[12:15], v[108:111], a[44:47], v[12:15]\n"
    "ds_read_b64_tr_b16 v[92:93], v231 offset:0\n"
    "ds_read_b64_tr_b16 v[94:95], v231 offset:2048\n"
    "v_mfma_f32_16x16x32_bf16 v[12:15], v[100:103], a[24:27], v[12:15]\n"
    "buffer_store_dword v252, v253, s[36:39], 0 offen sc1\n"
    "v_add_u32 v233, s88, v247\n"
    "v_mfma_f32_16x16x32_bf16 v[20:23], v[100:103], a[28:31], v[20:23]\n"
    "v_permlane32_swap_b32 v132, v134\n"
    "v_permlane32_swap_b32 v133, v135\n"
    "v_mfma_f32_16x16x32_bf16 v[20:23], v[108:111], a[48:51], v[20:23]\n"
    "s_add_u32 s88, s88, 0x1000\n"
    "buffer_store_dwordx4 v[132:135], v242, s[48:51], s77 offen offset:2048 sc1\n"
    "v_mfma_f32_16x16x32_bf16 v[28:31], v[108:111], a[52:55], v[28:31]\n"
    "s_cmp_eq_u32 s88, 0x6000\n"
    "s_cselect_b32 s88, 0x1000, s88\n"
    "v_mfma_f32_16x16x32_bf16 v[28:31], v[100:103], a[32:35], v[28:31]\n"
    "s_lshl_b32 s28, 1, s81\n"
    "v_mfma_f32_16x16x32_bf16 v[36:39], v[100:103], a[36:39], v[36:39]\n"
    "s_cmp_eq_u32 s81, s61\n"
    "s_cselect_b32 s28, 0xff00, s28\n"
    "v_mfma_f32_16x16x32_bf16 v[36:39], v[108:111], a[56:59], v[36:39]\n"
    "s_waitcnt lgkmcnt(0)\n"
    "v_mfma_f32_16x16x32_bf16 v[4:7], v[84:87], v[8:11], v[4:7]\n"
    "ds_read_b64_tr_b16 v[100:101], v232 offset:0\n"
    "ds_read_b64_tr_b16 v[102:103], v232 offset:2048\n"
    "v_mfma_f32_16x16x32_bf16 v[4:7], v[92:95], a[60:63], v[4:7]\n"
    "v_add_u32 v230, s88, v244\n"
    "v_add_u32 v231, s88, v245\n"
    "v_mfma_f32_16x16x32_bf16 v[12:15], v[92:95], a[64:67], v[12:15]\n"
    "ds_read_b64_tr_b16 v[108:109], v233 offset:0\n"
    "ds_read_b64_tr_b16 v[110:111], v233 offset:2048\n"
    "v_mfma_f32_16x16x32_bf16 v[12:15], v[84:87], v[40:43], v[12:15]\n"
    "v_cndmask_b32 v253, v241, v250, s[28:29]\n"
    "v_mfma_f32_16x16x32_bf16 v[20:23], v[84:87], v[72:75], v[20:23]\n"
    "v_add_u32 v232, s88, v246\n"
    "v_mfma_f32_16x16x32_bf16 v[20:23], v[92:95], a[68:71], v[20:23]\n"
    "s_add_u32 s78, s62, 0\n"
    "s_cmp_eq_u32 s35, 0\n"
    "s_cselect_b32 s78, 0x40000000, s78\n"
    "v_mfma_f32_16x16x32_bf16 v[28:31], v[92:95], a[72:75], v[28:31]\n"
    "s_add_u32 s79, s66, 0\n"
    "buffer_load_dwordx4 v[168:171], v238, s[48:51], s78 offen sc1\n"
    "v_mfma_f32_16x16x32_bf16 v[28:31], v[84:87], v[104:107], v[28:31]\n"
    "buffer_load_dwordx4 v[172:175], v238, s[48:51], s78 offen offset:1024 sc1\n"
    "v_mfma_f32_16x16x32_bf16 v[36:39], v[84:87], v[136:139], v[36:39]\n"
    "buffer_load_dwordx2 v[184:185], v248, s[48:51], s78 offen offset:2048 sc1\n"
    "v_mfma_f32_16x16x32_bf16 v[36:39], v[92:95], a[76:79], v[36:39]\n"
    "s_waitcnt vmcnt(12) lgkmcnt(0)\n"
    "s_barrier\n"
    "v_add_u32 v233, s88, v247\n"
    "v_mfma_f32_16x16x32_bf16 v[4:7], v[100:103], a[80:83], v[4:7]\n"
    "ds_read_b64_tr_b16 v[84:85], v230 offset:0\n"
    "ds_read_b64_tr_b16 v[86:87], v230 offset:2048\n"
    "v_mfma_f32_16x16x32_bf16 v[4:7], v[108:111], a[100:103], v[4:7]\n"
    "buffer_load_dwordx2 v[148:149], v248, s[48:51], s79 offen sc1\n"
    "v_mfma_f32_16x16x32_bf16 v[12:15], v[108:111], a[104:107], v[12:15]\n"
    "ds_read_b64_tr_b16 v[92:93], v231 offset:0\n"
    "ds_read_b64_tr_b16 v[94:95], v231 offset:2048\n"
    "v_mfma_f32_16x16x32_bf16 v[12:15], v[100:103], a[84:87], v[12:15]\n"
    "s_add_u32 s85, s62, 0x2800\n"
    "s_cmp_eq_u32 s35, 1\n"
    "s_cselect_b32 s85, 0x40000000, s85\n"
    "v_mfma_f32_16x16x32_bf16 v[20:23], v[100:103], a[88:91], v[20:23]\n"
    "s_add_u32 s87, s66, 0x2800\n"
    "buffer_load_dwordx4 v[176:179], v238, s[48:51], s85 offen sc1\n"
    "v_mfma_f32_16x16x32_bf16 v[20:23], v[108:111], a[108:111], v[20:23]\n"
    "s_add_u32 s88, s88, 0x1000\n"
    "buffer_load_dwordx4 v[180:183], v238, s[48:51], s85 offen offset:1024 sc1\n"
    "v_mfma_f32_16x16x32_bf16 v[28:31], v[108:111], a[112:115], v[28:31]\n"
    "s_cmp_eq_u32 s88, 0x6000\n"
    "s_cselect_b32 s88, 0x1000, s88\n"
    "buffer_load_dwordx2 v[186:187], v248, s[48:51], s85 offen offset:2048 sc1\n"
    "v_mfma_f32_16x16x32_bf16 v[28:31], v[100:103], a[92:95], v[28:31]\n"
    "buffer_load_dwordx2 v[150:151], v248, s[48:51], s87 offen sc1\n"
    "v_mfma_f32_16x16x32_bf16 v[36:39], v[100:103], a[96:99], v[36:39]\n"
    "s_add_u32 s78, s62, 0x5000\n"
    "s_cmp_eq_u32 s35, 2\n"
    "s_cselect_b32 s78, 0x40000000, s78\n"
    "v_mfma_f32_16x16x32_bf16 v[36:39], v[108:111], a[116:119], v[36:39]\n"
    "s_waitcnt lgkmcnt(0)\n"
    "s_add_u32 s79, s66, 0x5000\n"
    "buffer_load_dwordx4 v[188:191], v238, s[48:51], s78 offen sc1\n"
    "v_mfma_f32_16x16x32_bf16 v[4:7], v[84:87], v[16:19], v[4:7]\n"
    "ds_read_b64_tr_b16 v[100:101], v232 offset:0\n"
    "ds_read_b64_tr_b16 v[102:103], v232 offset:2048\n"
    "v_mfma_f32_16x16x32_bf16 v[4:7], v[92:95], a[120:123], v[4:7]\n"
    "v_add_u32 v230, s88, v244\n"
    "v_add_u32 v231, s88, v245\n"
    "v_mfma_f32_16x16x32_bf16 v[12:15], v[92:95], a[124:127], v[12:15]\n"
    "ds_read_b64_tr_b16 v[108:109], v233 offset:0\n"
    "ds_read_b64_tr_b16 v[110:111], v233 offset:2048\n"
    "v_mfma_f32_16x16x32_bf16 v[12:15], v[84:87], v[48:51], v[12:15]\n"
    "buffer_load_dwordx4 v[192:195], v238, s[48:51], s78 offen offset:1024 sc1\n"
    "v_mfma_f32_16x16x32_bf16 v[20:23], v[84:87], v[80:83], v[20:23]\n"
    "v_add_u32 v232, s88, v246\n"
    "v_mfma_f32_16x16x32_bf16 v[20:23], v[92:95], a[128:131], v[20:23]\n"
    "buffer_load_dwordx2 v[204:205], v248, s[48:51], s78 offen offset:2048 sc1\n"
    "v_mfma_f32_16x16x32_bf16 v[28:31], v[92:95], a[132:135], v[28:31]\n"
    "buffer_load_dwordx2 v[156:157], v248, s[48:51], s79 offen sc1\n"
    "v_mfma_f32_16x16x32_bf16 v[28:31], v[84:87], v[112:115], v[28:31]\n"
    "v_mfma_f32_16x16x32_bf16 v[36:39], v[84:87], v[144:147], v[36:39]\n"
    "v_mfma_f32_16x16x32_bf16 v[36:39], v[92:95], a[136:139], v[36:39]\n"
    "s_waitcnt vmcnt(17) lgkmcnt(0)\n"
    "s_barrier\n"
    "v_add_u32 v233, s88, v247\n"
    "v_mfma_f32_16x16x32_bf16 v[4:7], v[100:103], a[140:143], v[4:7]\n"
    "ds_read_b64_tr_b16 v[84:85], v230 offset:0\n"
    "ds_read_b64_tr_b16 v[86:87], v230 offset:2048\n"
    "v_mfma_f32_16x16x32_bf16 v[4:7], v[108:111], a[160:163], v[4:7]\n"
    "v_mfma_f32_16x16x32_bf16 v[12:15], v[108:111], a[164:167], v[12:15]\n"
    "ds_read_b64_tr_b16 v[92:93], v231 offset:0\n"
    "ds_read_b64_tr_b16 v[94:95], v231 offset:2048\n"
    "v_mfma_f32_16x16x32_bf16 v[12:15], v[100:103], a[144:147], v[12:15]\n"
    "v_mfma_f32_16x16x32_bf16 v[20:23], v[100:103], a[148:151], v[20:23]\n"
    "v_mfma_f32_16x16x32_bf16 v[20:23], v[108:111], a[168:171], v[20:23]\n"
    "s_add_u32 s88, s88, 0x1000\n"
    "v_mfma_f32_16x16x32_bf16 v[28:31], v[108:111], a[172:175], v[28:31]\n"
    "s_cmp_eq_u32 s88, 0x6000\n"
    "s_cselect_b32 s88, 0x1000, s88\n"
    "v_mfma_f32_16x16x32_bf16 v[28:31], v[100:103], a[152:155], v[28:31]\n"
    "v_mfma_f32_16x16x32_bf16 v[36:39], v[100:103], a[156:159], v[36:39]\n"
    "v_mfma_f32_16x16x32_bf16 v[36:39], v[108:111], a[176:179], v[36:39]\n"
    "s_waitcnt lgkmcnt(0)\n"
    "v_mfma_f32_16x16x32_bf16 v[4:7], v[84:87], v[24:27], v[4:7]\n"
    "ds_read_b64_tr_b16 v[100:101], v232 offset:0\n"
    "ds_read_b64_tr_b16 v[102:103], v232 offset:2048\n"
    "v_mfma_f32_16x16x32_bf16 v[4:7], v[92:95], a[180:183], v[4:7]\n"
    "v_mfma_f32_16x16x32_bf16 v[12:15], v[92:95], a[184:187], v[12:15]\n"
    "ds_read_b64_tr_b16 v[108:109], v233 offset:0\n"
    "ds_read_b64_tr_b16 v[110:111], v233 offset:2048\n"
    "v_mfma_f32_16x16x32_bf16 v[12:15], v[84:87], v[56:59], v[12:15]\n"
    "v_mfma_f32_16x16x32_bf16 v[20:23], v[84:87], v[88:91], v[20:23]\n"
    "v_mfma_f32_16x16x32_bf16 v[20:23], v[92:95], a[188:191], v[20:23]\n"
    "v_mfma_f32_16x16x32_bf16 v[28:31], v[92:95], a[192:195], v[28:31]\n"
    "v_mfma_f32_16x16x32_bf16 v[28:31], v[84:87], v[120:123], v[28:31]\n"
    "s_waitcnt vmcnt(12)\n"
    "buffer_store_dword v252, v253, s[36:39], 0 offen sc1\n"
    "v_mfma_f32_16x16x32_bf16 v[36:39], v[84:87], v[152:155], v[36:39]\n"
    "s_add_u32 s85, s62, 0x7800\n"
    "s_cmp_eq_u32 s35, 3\n"
    "s_cselect_b32 s85, 0x40000000, s85\n"
    "v_mfma_f32_16x16x32_bf16 v[36:39], v[92:95], a[196:199], v[36:39]\n"
    "s_waitcnt lgkmcnt(0)\n"
    "s_add_u32 s87, s66, 0x7800\n"
    "buffer_load_dwordx4 v[196:199], v238, s[48:51], s85 offen sc1\n"
    "v_mfma_f32_16x16x32_bf16 v[4:7], v[100:103], a[200:203], v[4:7]\n"
    "buffer_load_dwordx4 v[200:203], v238, s[48:51], s85 offen offset:1024 sc1\n"
    "v_mfma_f32_16x16x32_bf16 v[4:7], v[108:111], a[220:223], v[4:7]\n"
    "buffer_load_dwordx2 v[206:207], v248, s[48:51], s85 offen offset:2048 sc1\n"
    "v_mfma_f32_16x16x32_bf16 v[12:15], v[108:111], a[224:227], v[12:15]\n"
    "buffer_load_dwordx2 v[158:159], v248, s[48:51], s87 offen sc1\n"
    "v_mfma_f32_16x16x32_bf16 v[12:15], v[100:103], a[204:207], v[12:15]\n"
    "v_mfma_f32_16x16x32_bf16 v[20:23], v[100:103], a[208:211], v[20:23]\n"
    "v_mfma_f32_16x16x32_bf16 v[20:23], v[108:111], a[228:231], v[20:23]\n"
    "v_mfma_f32_16x16x32_bf16 v[28:31], v[108:111], a[232:235], v[28:31]\n"
    "v_mfma_f32_16x16x32_bf16 v[28:31], v[100:103], a[212:215], v[28:31]\n"
    "v_mfma_f32_16x16x32_bf16 v[36:39], v[100:103], a[216:219], v[36:39]\n"
    "v_mfma_f32_16x16x32_bf16 v[36:39], v[108:111], a[236:239], v[36:39]\n"
    "s_andn2_b32 s64, 0xf0f0, s63\n"
    "s_cbranch_scc1 B_C2_LATE\n"
    "s_add_u32 s78, s62, 0xa000\n"
    "s_cmp_eq_u32 s35, 4\n"
    "s_cselect_b32 s78, 0x40000000, s78\n"
    "buffer_load_dwordx4 v[208:211], v238, s[48:51], s78 offen sc1\n"
    "s_add_u32 s79, s66, 0xa000\n"
    "buffer_load_dwordx4 v[212:215], v238, s[48:51], s78 offen offset:1024 sc1\n"
    "buffer_load_dwordx2 v[224:225], v248, s[48:51], s78 offen offset:2048 sc1\n"
    "buffer_load_dwordx2 v[160:161], v248, s[48:51], s79 offen sc1\n"
    "s_add_u32 s85, s62, 0xc800\n"
    "s_cmp_eq_u32 s35, 5\n"
    "s_cselect_b32 s85, 0x40000000, s85\n"
    "buffer_load_dwordx4 v[216:219], v238, s[48:51], s85 offen sc1\n"
    "s_add_u32 s87, s66, 0xc800\n"
    "buffer_load_dwordx4 v[220:223], v238, s[48:51], s85 offen offset:1024 sc1\n"
    "buffer_load_dwordx2 v[226:227], v248, s[48:51], s85 offen offset:2048 sc1\n"
    "buffer_load_dwordx2 v[162:163], v248, s[48:51], s87 offen sc1\n"
    "s_add_u32 s78, s62, 0xf000\n"
    "s_cmp_eq_u32 s35, 6\n"
    "s_cselect_b32 s78, 0x40000000, s78\n"
    "buffer_load_dwordx4 v[44:47], v238, s[48:51], s78 offen sc1\n"
    "s_add_u32 s79, s66, 0xf000\n"
    "buffer_load_dwordx4 v[52:55], v238, s[48:51], s78 offen offset:1024 sc1\n"
    "buffer_load_dwordx2 v[76:77], v248, s[48:51], s78 offen offset:2048 sc1\n"
    "buffer_load_dwordx2 v[164:165], v248, s[48:51], s79 offen sc1\n"
    "s_add_u32 s85, s62, 0x11800\n"
    "s_cmp_eq_u32 s35, 7\n"
    "s_cselect_b32 s85, 0x40000000, s85\n"
    "buffer_load_dwordx4 v[60:63], v238, s[48:51], s85 offen sc1\n"
    "s_add_u32 s87, s66, 0x11800\n"
    "buffer_load_dwordx4 v[68:71], v238, s[48:51], s85 offen offset:1024 sc1\n"
    "buffer_load_dwordx2 v[78:79], v248, s[48:51], s85 offen offset:2048 sc1\n"
    "buffer_load_dwordx2 v[166:167], v248, s[48:51], s87 offen sc1\n"
    "s_waitcnt vmcnt(29)\n"
    "v_mfma_f32_16x16x32_bf16 v[4:7], a[244:247], v[168:171], v[4:7]\n"
    "v_mfma_f32_16x16x32_bf16 v[12:15], a[248:251], v[168:171], v[12:15]\n"
    "v_mfma_f32_16x16x32_bf16 v[20:23], a[244:247], v[172:175], v[20:23]\n"
    "v_mfma_f32_16x16x32_bf16 v[28:31], a[248:251], v[172:175], v[28:31]\n"
    "s_waitcnt vmcnt(25)\n"
    "v_mfma_f32_16x16x32_bf16 v[4:7], a[244:247], v[176:179], v[4:7]\n"
    "v_mfma_f32_16x16x32_bf16 v[12:15], a[248:251], v[176:179], v[12:15]\n"
    "v_mfma_f32_16x16x32_bf16 v[20:23], a[244:247], v[180:183], v[20:23]\n"
    "v_mfma_f32_16x16x32_bf16 v[28:31], a[248:251], v[180:183], v[28:31]\n"
    "v_mfma_f32_16x16x32_bf16 v[36:39], a[240:243], v[184:187], v[36:39]\n"
    "v_mfma_f32_16x16x32_bf16 v[140:143], a[240:243], v[148:151], 0\n"
    "s_waitcnt vmcnt(21)\n"
    "v_mfma_f32_16x16x32_bf16 v[4:7], a[244:247], v[188:191], v[4:7]\n"
    "v_mfma_f32_16x16x32_bf16 v[12:15], a[248:251], v[188:191], v[12:15]\n"
    "v_mfma_f32_16x16x32_bf16 v[20:23], a[244:247], v[192:195], v[20:23]\n"
    "v_mfma_f32_16x16x32_bf16 v[28:31], a[248:251], v[192:195], v[28:31]\n"
    "s_waitcnt vmcnt(16)\n"
    "v_mfma_f32_16x16x32_bf16 v[4:7], a[244:247], v[196:199], v[4:7]\n"
    "v_mfma_f32_16x16x32_bf16 v[12:15], a[248:251], v[196:199], v[12:15]\n"
    "v_mfma_f32_16x16x32_bf16 v[20:23], a[244:247], v[200:203], v[20:23]\n"
    "v_mfma_f32_16x16x32_bf16 v[28:31], a[248:251], v[200:203], v[28:31]\n"
    "v_mfma_f32_16x16x32_bf16 v[36:39], a[240:243], v[204:207], v[36:39]\n"
    "v_mfma_f32_16x16x32_bf16 v[140:143], a[240:243], v[156:159], v[140:143]\n"
    "B_C2_BACK:\n"
    "s_waitcnt vmcnt(12)\n"
    "v_mfma_f32_16x16x32_bf16 v[4:7], a[244:247], v[208:211], v[4:7]\n"
    "v_mfma_f32_16x16x32_bf16 v[12:15], a[248:251], v[208:211], v[12:15]\n"
    "v_mfma_f32_16x16x32_bf16 v[20:23], a[244:247], v[212:215], v[20:23]\n"
    "v_mfma_f32_16x16x32_bf16 v[28:31], a[248:251], v[212:215], v[28:31]\n"
    "s_waitcnt vmcnt(8)\n"
    "v_mfma_f32_16x16x32_bf16 v[4:7], a[244:247], v[216:219], v[4:7]\n"
    "v_mfma_f32_16x16x32_bf16 v[12:15], a[248:251], v[216:219], v[12:15]\n"
    "v_mfma_f32_16x16x32_bf16 v[20:23], a[244:247], v[220:223], v[20:23]\n"
    "v_mfma_f32_16x16x32_bf16 v[28:31], a[248:251], v[220:223], v[28:31]\n"
    "v_mfma_f32_16x16x32_bf16 v[36:39], a[240:243], v[224:227], v[36:39]\n"
    "v_mfma_f32_16x16x32_bf16 v[140:143], a[240:243], v[160:163], v[140:143]\n"
    "s_waitcnt vmcnt(4)\n"
    "v_mfma_f32_16x16x32_bf16 v[4:7], a[244:247], v[44:47], v[4:7]\n"
    "v_mfma_f32_16x16x32_bf16 v[12:15], a[248:251], v[44:47], v[12:15]\n"
    "v_mfma_f32_16x16x32_bf16 v[20:23], a[244:247], v[52:55], v[20:23]\n"
    "v_mfma_f32_16x16x32_bf16 v[28:31], a[248:251], v[52:55], v[28:31]\n"
    "s_waitcnt vmcnt(0)\n"
    "v_mfma_f32_16x16x32_bf16 v[4:7], a[244:247], v[60:63], v[4:7]\n"
    "v_mfma_f32_16x16x32_bf16 v[12:15], a[248:251], v[60:63], v[12:15]\n"
    "v_mfma_f32_16x16x32_bf16 v[20:23], a[244:247], v[68:71], v[20:23]\n"
    "v_mfma_f32_16x16x32_bf16 v[28:31], a[248:251], v[68:71], v[28:31]\n"
    "v_mfma_f32_16x16x32_bf16 v[36:39], a[240:243], v[76:79], v[36:39]\n"
    "v_mfma_f32_16x16x32_bf16 v[140:143], a[240:243], v[164:167], v[140:143]\n"
    "s_mul_i32 s76, s72, 0xd800\n"
    "s_add_u32 s60, s30, s76\n"
    "s_add_u32 s61, s60, 0x4800\n"
    "s_add_u32 s62, s60, 0x9000\n"
    "s_add_u32 s76, s31, 0x4800\n"
    "s_cmp_eq_u32 s72, 3\n"
    "s_cselect_b32 s61, s31, s61\n"
    "s_cselect_b32 s62, s76, s62\n"
    "v_cvt_pk_bf16_f32 v4, v4, v5\n"
    "v_cvt_pk_bf16_f32 v5, v6, v7\n"
    "ds_write_b64 v229, v[4:5] offset:24576\n"
    "v_cvt_pk_bf16_f32 v12, v12, v13\n"
    "v_cvt_pk_bf16_f32 v13, v14, v15\n"
    "ds_write_b64 v229, v[12:13] offset:26880\n"
    "v_cvt_pk_bf16_f32 v20, v20, v21\n"
    "v_cvt_pk_bf16_f32 v21, v22, v23\n"
    "ds_write_b64 v229, v[20:21] offset:29184\n"
    "v_cvt_pk_bf16_f32 v28, v28, v29\n"
    "v_cvt_pk_bf16_f32 v29, v30, v31\n"
    "ds_write_b64 v229, v[28:29] offset:31488\n"
    "v_cvt_pk_bf16_f32 v36, v36, v37\n"
    "v_cvt_pk_bf16_f32 v37, v38, v39\n"
    "ds_write_b64 v229, v[36:37] offset:33792\n"
    "v_cvt_pk_bf16_f32 v140, v140, v141\n"
    "v_cvt_pk_bf16_f32 v141, v142, v143\n"
    "ds_write_b64 v229, v[140:141] offset:36096\n"
    "s_waitcnt lgkmcnt(0)\n"
    "s_barrier\n"
    "ds_read_b128 v[84:87], v243 offset:24576\n"
    "ds_read_b128 v[92:95], v243 offset:25728\n"
    "ds_read_b128 v[100:103], v243 offset:26880\n"
    "s_waitcnt lgkmcnt(2)\n"
    "buffer_store_dwordx4 v[84:87], v228, s[92:95], s60 offen nt\n"
    "s_waitcnt lgkmcnt(1)\n"
    "buffer_store_dwordx4 v[92:95], v228, s[92:95], s61 offen nt\n"
    "s_waitcnt lgkmcnt(0)\n"
    "buffer_store_dwordx4 v[100:103], v228, s[92:95], s62 offen nt\n"
    "s_endpgm\n"
    "B_HS_SLOW:\n"
    "s_add_u32 s65, s65, 1\n"
    "s_cmp_gt_u32 s65, 0x80000\n"
    "s_cbranch_scc1 B_HS_OK\n"
    "s_sleep 1\n"
    "buffer_load_dword v253, v250, s[36:39], s67 offen sc1\n"
    "s_waitcnt vmcnt(0)\n"
    "s_branch B_HS_CHK\n"
    "B_C1_SLOW:\n"
    "s_add_u32 s65, s65, 1\n"
    "s_cmp_gt_u32 s65, 0x80000\n"
    "s_cbranch_scc1 B_C1_OK\n"
    "s_sleep 1\n"
    "buffer_load_dword v254, v249, s[36:39], 0 offen sc1\n"
    "s_waitcnt vmcnt(0)\n"
    "s_branch B_C1_CHK\n"
    "B_C2_LATE:\n"
    "buffer_load_dword v254, v249, s[36:39], 0 offen sc1\n"
    "s_waitcnt vmcnt(13)\n"
    "v_mfma_f32_16x16x32_bf16 v[4:7], a[244:247], v[168:171], v[4:7]\n"
    "v_mfma_f32_16x16x32_bf16 v[12:15], a[248:251], v[168:171], v[12:15]\n"
    "v_mfma_f32_16x16x32_bf16 v[20:23], a[244:247], v[172:175], v[20:23]\n"
    "v_mfma_f32_16x16x32_bf16 v[28:31], a[248:251], v[172:175], v[28:31]\n"
    "s_waitcnt vmcnt(9)\n"
    "v_mfma_f32_16x16x32_bf16 v[4:7], a[244:247], v[176:179], v[4:7]\n"
    "v_mfma_f32_16x16x32_bf16 v[12:15], a[248:251], v[176:179], v[12:15]\n"
    "v_mfma_f32_16x16x32_bf16 v[20:23], a[244:247], v[180:183], v[20:23]\n"
    "v_mfma_f32_16x16x32_bf16 v[28:31], a[248:251], v[180:183], v[28:31]\n"
    "v_mfma_f32_16x16x32_bf16 v[36:39], a[240:243], v[184:187], v[36:39]\n"
    "B_TT_FIRST3:\n"
    "B_TT_S3:\n"
    "s_cbranch_execz B_TT_RET\n"
    "v_mfma_f32_16x16x32_bf16 v[140:143], a[240:243], v[148:151], 0\n"
    "s_waitcnt vmcnt(5)\n"
    "v_mfma_f32_16x16x32_bf16 v[4:7], a[244:247], v[188:191], v[4:7]\n"
    "v_mfma_f32_16x16x32_bf16 v[12:15], a[248:251], v[188:191], v[12:15]\n"
    "v_mfma_f32_16x16x32_bf16 v[20:23], a[244:247], v[192:195], v[20:23]\n"
    "v_mfma_f32_16x16x32_bf16 v[28:31], a[248:251], v[192:195], v[28:31]\n"
    "s_waitcnt vmcnt(1)\n"
    "v_mfma_f32_16x16x32_bf16 v[4:7], a[244:247], v[196:199], v[4:7]\n"
    "v_mfma_f32_16x16x32_bf16 v[12:15], a[248:251], v[196:199], v[12:15]\n"
    "v_mfma_f32_16x16x32_bf16 v[20:23], a[244:247], v[200:203], v[20:23]\n"
    "v_mfma_f32_16x16x32_bf16 v[28:31], a[248:251], v[200:203], v[28:31]\n"
    "v_mfma_f32_16x16x32_bf16 v[36:39], a[240:243], v[204:207], v[36:39]\n"
    "v_mfma_f32_16x16x32_bf16 v[140:143], a[240:243], v[156:159], v[140:143]\n"
    "s_waitcnt vmcnt(0)\n"
    "s_mov_b32 s65, 0\n"
    "B_C3_CHK4:\n"
    "v_cmp_eq_u32 vcc, v254, v251\n"
    "s_or_b32 s63, vcc_lo, s60\n"
    "s_andn2_b32 s76, 0x1010, s63\n"
    "s_cbranch_scc1 B_C3_SLOW4\n"
    "B_C3_OK4:\n"
    "s_add_u32 s78, s62, 0xa000\n"
    "s_cmp_eq_u32 s35, 4\n"
    "s_cselect_b32 s78, 0x40000000, s78\n"
    "buffer_load_dwordx4 v[208:211], v238, s[48:51], s78 offen sc1\n"
    "s_add_u32 s79, s66, 0xa000\n"
    "buffer_load_dwordx4 v[212:215], v238, s[48:51], s78 offen offset:1024 sc1\n"
    "buffer_load_dwordx2 v[224:225], v248, s[48:51], s78 offen offset:2048 sc1\n"
    "buffer_load_dwordx2 v[160:161], v248, s[48:51], s79 offen sc1\n"
    "B_C3_CHK5:\n"
    "v_cmp_eq_u32 vcc, v254, v251\n"
    "s_or_b32 s63, vcc_lo, s60\n"
    "s_andn2_b32 s76, 0x2020, s63\n"
    "s_cbranch_scc1 B_C3_SLOW5\n"
    "B_C3_OK5:\n"
    "s_add_u32 s85, s62, 0xc800\n"
    "s_cmp_eq_u32 s35, 5\n"
    "s_cselect_b32 s85, 0x40000000, s85\n"
    "buffer_load_dwordx4 v[216:219], v238, s[48:51], s85 offen sc1\n"
    "s_add_u32 s87, s66, 0xc800\n"
    "buffer_load_dwordx4 v[220:223], v238, s[48:51], s85 offen offset:1024 sc1\n"
    "buffer_load_dwordx2 v[226:227], v248, s[48:51], s85 offen offset:2048 sc1\n"
    "buffer_load_dwordx2 v[162:163], v248, s[48:51], s87 offen sc1\n"
    "B_C3_CHK6:\n"
    "v_cmp_eq_u32 vcc, v254, v251\n"
    "s_or_b32 s63, vcc_lo, s60\n"
    "s_andn2_b32 s76, 0x4040, s63\n"
    "s_cbranch_scc1 B_C3_SLOW6\n"
    "B_C3_OK6:\n"
    "s_add_u32 s78, s62, 0xf000\n"
    "s_cmp_eq_u32 s35, 6\n"
    "s_cselect_b32 s78, 0x40000000, s78\n"
    "buffer_load_dwordx4 v[44:47], v238, s[48:51], s78 offen sc1\n"
    "s_add_u32 s79, s66, 0xf000\n"
    "buffer_load_dwordx4 v[52:55], v238, s[48:51], s78 offen offset:1024 sc1\n"
    "buffer_load_dwordx2 v[76:77], v248, s[48:51], s78 offen offset:2048 sc1\n"
    "buffer_load_dwordx2 v[164:165], v248, s[48:51], s79 offen sc1\n"
    "B_C3_CHK7:\n"
    "v_cmp_eq_u32 vcc, v254, v251\n"
    "s_or_b32 s63, vcc_lo, s60\n"
    "s_andn2_b32 s76, 0x8080, s63\n"
    "s_cbranch_scc1 B_C3_SLOW7\n"
    "B_C3_OK7:\n"
    "s_add_u32 s85, s62, 0x11800\n"
    "s_cmp_eq_u32 s35, 7\n"
    "s_cselect_b32 s85, 0x40000000, s85\n"
    "buffer_load_dwordx4 v[60:63], v238, s[48:51], s85 offen sc1\n"
    "s_add_u32 s87, s66, 0x11800\n"
    "buffer_load_dwordx4 v[68:71], v238, s[48:51], s85 offen offset:1024 sc1\n"
    "buffer_load_dwordx2 v[78:79], v248, s[48:51], s85 offen offset:2048 sc1\n"
    "buffer_load_dwordx2 v[166:167], v248, s[48:51], s87 offen sc1\n"
    "s_branch B_C2_BACK\n"
    "B_C3_SLOW4:\n"
    "s_add_u32 s65, s65, 1\n"
    "s_cmp_gt_u32 s65, 0x80000\n"
    "s_cbranch_scc1 B_C3_OK4\n"
    "s_nop 0\n"
    "buffer_load_dword v254, v249, s[36:39], 0 offen sc1\n"
    "s_waitcnt vmcnt(0)\n"
    "s_branch B_C3_CHK4\n"
    "B_C3_SLOW5:\n"
    "s_add_u32 s65, s65, 1\n"
    "s_cmp_gt_u32 s65, 0x80000\n"
    "s_cbranch_scc1 B_C3_OK5\n"
    "s_nop 0\n"
    "buffer_load_dword v254, v249, s[36:39], 0 offen sc1\n"
    "s_waitcnt vmcnt(0)\n"
    "s_branch B_C3_CHK5\n"
    "B_C3_SLOW6:\n"
    "s_add_u32 s65, s65, 1\n"
    "s_cmp_gt_u32 s65, 0x80000\n"
    "s_cbranch_scc1 B_C3_OK6\n"
    "s_nop 0\n"
    "buffer_load_dword v254, v249, s[36:39], 0 offen sc1\n"
    "s_waitcnt vmcnt(0)\n"
    "s_branch B_C3_CHK6\n"
    "B_C3_SLOW7:\n"
    "s_add_u32 s65, s65, 1\n"
    "s_cmp_gt_u32 s65, 0x80000\n"
    "s_cbranch_scc1 B_C3_OK7\n"
    "s_nop 0\n"
    "buffer_load_dword v254, v249, s[36:39], 0 offen sc1\n"
    "s_waitcnt vmcnt(0)\n"
    "s_branch B_C3_CHK7\n"
    "B_TT_END:\n"
    "B_RARE_ED_0:\n"
    "v_mov_b32 v197, v196\n"
    "s_nop 1\n"
    "v_permlane16_swap_b32 v196, v197\n"
    "s_nop 0\n"
    "v_max_f32 v196, v196, v197\n"
    "v_mov_b32 v197, v196\n"
    "s_nop 1\n"
    "v_permlane32_swap_b32 v196, v197\n"
    "s_nop 0\n"
    "v_max3_f32 v196, v196, v197, v184\n"
    "v_sub_f32 v197, v184, v196\n"
    "v_mul_f32 v197, s69, v197\n"
    "v_exp_f32 v197, v197\n"
    "v_mov_b32 v184, v196\n"
    "v_add_f32 v189, 0x423c35a6, v196\n"
    "v_mul_f32 v200, s70, v196\n"
    "s_cmp_eq_u32 s80, 0\n"
    "s_cbranch_scc1 B_BACK_ED_0\n"
    "v_mul_f32 v0, v197, v0\n"
    "v_mul_f32 v1, v197, v1\n"
    "v_mul_f32 v2, v197, v2\n"
    "v_mul_f32 v3, v197, v3\n"
    "v_mul_f32 v4, v197, v4\n"
    "v_mul_f32 v5, v197, v5\n"
    "v_mul_f32 v6, v197, v6\n"
    "v_mul_f32 v7, v197, v7\n"
    "v_mul_f32 v8, v197, v8\n"
    "v_mul_f32 v9, v197, v9\n"
    "v_mul_f32 v10, v197, v10\n"
    "v_mul_f32 v11, v197, v11\n"
    "v_mul_f32 v12, v197, v12\n"
    "v_mul_f32 v13, v197, v13\n"
    "v_mul_f32 v14, v197, v14\n"
    "v_mul_f32 v15, v197, v15\n"
    "v_mul_f32 v16, v197, v16\n"
    "v_mul_f32 v17, v197, v17\n"
    "v_mul_f32 v18, v197, v18\n"
    "v_mul_f32 v19, v197, v19\n"
    "v_mul_f32 v20, v197, v20\n"
    "v_mul_f32 v21, v197, v21\n"
    "v_mul_f32 v22, v197, v22\n"
    "v_mul_f32 v23, v197, v23\n"
    "v_mul_f32 v24, v197, v24\n"
    "v_mul_f32 v25, v197, v25\n"
    "v_mul_f32 v26, v197, v26\n"
    "v_mul_f32 v27, v197, v27\n"
    "v_mul_f32 v28, v197, v28\n"
    "v_mul_f32 v29, v197, v29\n"
    "v_mul_f32 v30, v197, v30\n"
    "v_mul_f32 v31, v197, v31\n"
    "v_accvgpr_read_b32 v198, a128\n"
    "v_mul_f32 v198, v197, v198\n"
    "v_accvgpr_write_b32 a128, v198\n"
    "v_accvgpr_read_b32 v198, a129\n"
    "v_mul_f32 v198, v197, v198\n"
    "v_accvgpr_write_b32 a129, v198\n"
    "v_accvgpr_read_b32 v198, a130\n"
    "v_mul_f32 v198, v197, v198\n"
    "v_accvgpr_write_b32 a130, v198\n"
    "v_accvgpr_read_b32 v198, a131\n"
    "v_mul_f32 v198, v197, v198\n"
    "v_accvgpr_write_b32 a131, v198\n"
    "s_nop 1\n"
    "s_branch B_BACK_ED_0\n"
    "B_RARE_ED_1:\n"
    "v_mov_b32 v197, v196\n"
    "s_nop 1\n"
    "v_permlane16_swap_b32 v196, v197\n"
    "s_nop 0\n"
    "v_max_f32 v196, v196, v197\n"
    "v_mov_b32 v197, v196\n"
    "s_nop 1\n"
    "v_permlane32_swap_b32 v196, v197\n"
    "s_nop 0\n"
    "v_max3_f32 v196, v196, v197, v185\n"
    "v_sub_f32 v197, v185, v196\n"
    "v_mul_f32 v197, s69, v197\n"
    "v_exp_f32 v197, v197\n"
    "v_mov_b32 v185, v196\n"
    "v_add_f32 v190, 0x423c35a6, v196\n"
    "v_mul_f32 v201, s70, v196\n"
    "s_cmp_eq_u32 s80, 0\n"
    "s_cbranch_scc1 B_BACK_ED_1\n"
    "v_mul_f32 v32, v197, v32\n"
    "v_mul_f32 v33, v197, v33\n"
    "v_mul_f32 v34, v197, v34\n"
    "v_mul_f32 v35, v197, v35\n"
    "v_mul_f32 v36, v197, v36\n"
    "v_mul_f32 v37, v197, v37\n"
    "v_mul_f32 v38, v197, v38\n"
    "v_mul_f32 v39, v197, v39\n"
    "v_mul_f32 v40, v197, v40\n"
    "v_mul_f32 v41, v197, v41\n"
    "v_mul_f32 v42, v197, v42\n"
    "v_mul_f32 v43, v197, v43\n"
    "v_mul_f32 v44, v197, v44\n"
    "v_mul_f32 v45, v197, v45\n"
    "v_mul_f32 v46, v197, v46\n"
    "v_mul_f32 v47, v197, v47\n"
    "v_mul_f32 v48, v197, v48\n"
    "v_mul_f32 v49, v197, v49\n"
    "v_mul_f32 v50, v197, v50\n"
    "v_mul_f32 v51, v197, v51\n"
    "v_mul_f32 v52, v197, v52\n"
    "v_mul_f32 v53, v197, v53\n"
    "v_mul_f32 v54, v197, v54\n"
    "v_mul_f32 v55, v197, v55\n"
    "v_mul_f32 v56, v197, v56\n"
    "v_mul_f32 v57, v197, v57\n"
    "v_mul_f32 v58, v197, v58\n"
    "v_mul_f32 v59, v197, v59\n"
    "v_mul_f32 v60, v197, v60\n"
    "v_mul_f32 v61, v197, v61\n"
    "v_mul_f32 v62, v197, v62\n"
    "v_mul_f32 v63, v197, v63\n"
    "v_accvgpr_read_b32 v198, a132\n"
    "v_mul_f32 v198, v197, v198\n"
    "v_accvgpr_write_b32 a132, v198\n"
    "v_accvgpr_read_b32 v198, a133\n"
    "v_mul_f32 v198, v197, v198\n"
    "v_accvgpr_write_b32 a133, v198\n"
    "v_accvgpr_read_b32 v198, a134\n"
    "v_mul_f32 v198, v197, v198\n"
    "v_accvgpr_write_b32 a134, v198\n"
    "v_accvgpr_read_b32 v198, a135\n"
    "v_mul_f32 v198, v197, v198\n"
    "v_accvgpr_write_b32 a135, v198\n"
    "s_nop 1\n"
    "s_branch B_BACK_ED_1\n"
    "B_RARE_ED_2:\n"
    "v_mov_b32 v197, v196\n"
    "s_nop 1\n"
    "v_permlane16_swap_b32 v196, v197\n"
    "s_nop 0\n"
    "v_max_f32 v196, v196, v197\n"
    "v_mov_b32 v197, v196\n"
    "s_nop 1\n"
    "v_permlane32_swap_b32 v196, v197\n"
    "s_nop 0\n"
    "v_max3_f32 v196, v196, v197, v186\n"
    "v_sub_f32 v197, v186, v196\n"
    "v_mul_f32 v197, s69, v197\n"
    "v_exp_f32 v197, v197\n"
    "v_mov_b32 v186, v196\n"
    "v_add_f32 v191, 0x423c35a6, v196\n"
    "v_mul_f32 v202, s70, v196\n"
    "s_cmp_eq_u32 s80, 0\n"
    "s_cbranch_scc1 B_BACK_ED_2\n"
    "v_mul_f32 v64, v197, v64\n"
    "v_mul_f32 v65, v197, v65\n"
    "v_mul_f32 v66, v197, v66\n"
    "v_mul_f32 v67, v197, v67\n"
    "v_mul_f32 v68, v197, v68\n"
    "v_mul_f32 v69, v197, v69\n"
    "v_mul_f32 v70, v197, v70\n"
    "v_mul_f32 v71, v197, v71\n"
    "v_mul_f32 v72, v197, v72\n"
    "v_mul_f32 v73, v197, v73\n"
    "v_mul_f32 v74, v197, v74\n"
    "v_mul_f32 v75, v197, v75\n"
    "v_mul_f32 v76, v197, v76\n"
    "v_mul_f32 v77, v197, v77\n"
    "v_mul_f32 v78, v197, v78\n"
    "v_mul_f32 v79, v197, v79\n"
    "v_mul_f32 v80, v197, v80\n"
    "v_mul_f32 v81, v197, v81\n"
    "v_mul_f32 v82, v197, v82\n"
    "v_mul_f32 v83, v197, v83\n"
    "v_mul_f32 v84, v197, v84\n"
    "v_mul_f32 v85, v197, v85\n"
    "v_mul_f32 v86, v197, v86\n"
    "v_mul_f32 v87, v197, v87\n"
    "v_mul_f32 v88, v197, v88\n"
    "v_mul_f32 v89, v197, v89\n"
    "v_mul_f32 v90, v197, v90\n"
    "v_mul_f32 v91, v197, v91\n"
    "v_mul_f32 v92, v197, v92\n"
    "v_mul_f32 v93, v197, v93\n"
    "v_mul_f32 v94, v197, v94\n"
    "v_mul_f32 v95, v197, v95\n"
    "v_accvgpr_read_b32 v198, a136\n"
    "v_mul_f32 v198, v197, v198\n"
    "v_accvgpr_write_b32 a136, v198\n"
    "v_accvgpr_read_b32 v198, a137\n"
    "v_mul_f32 v198, v197, v198\n"
    "v_accvgpr_write_b32 a137, v198\n"
    "v_accvgpr_read_b32 v198, a138\n"
    "v_mul_f32 v198, v197, v198\n"
    "v_accvgpr_write_b32 a138, v198\n"
    "v_accvgpr_read_b32 v198, a139\n"
    "v_mul_f32 v198, v197, v198\n"
    "v_accvgpr_write_b32 a139, v198\n"
    "s_nop 1\n"
    "s_branch B_BACK_ED_2\n"
    "B_RARE_ED_3:\n"
    "v_mov_b32 v197, v196\n"
    "s_nop 1\n"
    "v_permlane16_swap_b32 v196, v197\n"
    "s_nop 0\n"
    "v_max_f32 v196, v196, v197\n"
    "v_mov_b32 v197, v196\n"
    "s_nop 1\n"
    "v_permlane32_swap_b32 v196, v197\n"
    "s_nop 0\n"
    "v_max3_f32 v196, v196, v197, v187\n"
    "v_sub_f32 v197, v187, v196\n"
    "v_mul_f32 v197, s69, v197\n"
    "v_exp_f32 v197, v197\n"
    "v_mov_b32 v187, v196\n"
    "v_add_f32 v192, 0x423c35a6, v196\n"
    "v_mul_f32 v203, s70, v196\n"
    "s_cmp_eq_u32 s80, 0\n"
    "s_cbranch_scc1 B_BACK_ED_3\n"
    "v_mul_f32 v96, v197, v96\n"
    "v_mul_f32 v97, v197, v97\n"
    "v_mul_f32 v98, v197, v98\n"
    "v_mul_f32 v99, v197, v99\n"
    "v_mul_f32 v100, v197, v100\n"
    "v_mul_f32 v101, v197, v101\n"
    "v_mul_f32 v102, v197, v102\n"
    "v_mul_f32 v103, v197, v103\n"
    "v_mul_f32 v104, v197, v104\n"
    "v_mul_f32 v105, v197, v105\n"
    "v_mul_f32 v106, v197, v106\n"
    "v_mul_f32 v107, v197, v107\n"
    "v_mul_f32 v108, v197, v108\n"
    "v_mul_f32 v109, v197, v109\n"
    "v_mul_f32 v110, v197, v110\n"
    "v_mul_f32 v111, v197, v111\n"
    "v_mul_f32 v112, v197, v112\n"
    "v_mul_f32 v113, v197, v113\n"
    "v_mul_f32 v114, v197, v114\n"
    "v_mul_f32 v115, v197, v115\n"
    "v_mul_f32 v116, v197, v116\n"
    "v_mul_f32 v117, v197, v117\n"
    "v_mul_f32 v118, v197, v118\n"
    "v_mul_f32 v119, v197, v119\n"
    "v_mul_f32 v120, v197, v120\n"
    "v_mul_f32 v121, v197, v121\n"
    "v_mul_f32 v122, v197, v122\n"
    "v_mul_f32 v123, v197, v123\n"
    "v_mul_f32 v124, v197, v124\n"
    "v_mul_f32 v125, v197, v125\n"
    "v_mul_f32 v126, v197, v126\n"
    "v_mul_f32 v127, v197, v127\n"
    "v_accvgpr_read_b32 v198, a140\n"
    "v_mul_f32 v198, v197, v198\n"
    "v_accvgpr_write_b32 a140, v198\n"
    "v_accvgpr_read_b32 v198, a141\n"
    "v_mul_f32 v198, v197, v198\n"
    "v_accvgpr_write_b32 a141, v198\n"
    "v_accvgpr_read_b32 v198, a142\n"
    "v_mul_f32 v198, v197, v198\n"
    "v_accvgpr_write_b32 a142, v198\n"
    "v_accvgpr_read_b32 v198, a143\n"
    "v_mul_f32 v198, v197, v198\n"
    "v_accvgpr_write_b32 a143, v198\n"
    "s_nop 1\n"
    "s_branch B_BACK_ED_3\n"
    "B_RARE_ED_4:\n"
    "v_mov_b32 v197, v196\n"
    "s_nop 1\n"
    "v_permlane16_swap_b32 v196, v197\n"
    "s_nop 0\n"
    "v_max_f32 v196, v196, v197\n"
    "v_mov_b32 v197, v196\n"
    "s_nop 1\n"
    "v_permlane32_swap_b32 v196, v197\n"
    "s_nop 0\n"
    "v_max3_f32 v196, v196, v197, v188\n"
    "v_sub_f32 v197, v188, v196\n"
    "v_mul_f32 v197, s69, v197\n"
    "v_exp_f32 v197, v197\n"
    "v_mov_b32 v188, v196\n"
    "v_add_f32 v193, 0x423c35a6, v196\n"
    "v_mul_f32 v204, s70, v196\n"
    "s_cmp_eq_u32 s80, 0\n"
    "s_cbranch_scc1 B_BACK_ED_4\n"
    "v_mul_f32 v128, v197, v128\n"
    "v_mul_f32 v129, v197, v129\n"
    "v_mul_f32 v130, v197, v130\n"
    "v_mul_f32 v131, v197, v131\n"
    "v_mul_f32 v132, v197, v132\n"
    "v_mul_f32 v133, v197, v133\n"
    "v_mul_f32 v134, v197, v134\n"
    "v_mul_f32 v135, v197, v135\n"
    "v_mul_f32 v136, v197, v136\n"
    "v_mul_f32 v137, v197, v137\n"
    "v_mul_f32 v138, v197, v138\n"
    "v_mul_f32 v139, v197, v139\n"
    "v_mul_f32 v140, v197, v140\n"
    "v_mul_f32 v141, v197, v141\n"
    "v_mul_f32 v142, v197, v142\n"
    "v_mul_f32 v143, v197, v143\n"
    "v_mul_f32 v144, v197, v144\n"
    "v_mul_f32 v145, v197, v145\n"
    "v_mul_f32 v146, v197, v146\n"
    "v_mul_f32 v147, v197, v147\n"
    "v_mul_f32 v148, v197, v148\n"
    "v_mul_f32 v149, v197, v149\n"
    "v_mul_f32 v150, v197, v150\n"
    "v_mul_f32 v151, v197, v151\n"
    "v_mul_f32 v152, v197, v152\n"
    "v_mul_f32 v153, v197, v153\n"
    "v_mul_f32 v154, v197, v154\n"
    "v_mul_f32 v155, v197, v155\n"
    "v_mul_f32 v156, v197, v156\n"
    "v_mul_f32 v157, v197, v157\n"
    "v_mul_f32 v158, v197, v158\n"
    "v_mul_f32 v159, v197, v159\n"
    "v_accvgpr_read_b32 v198, a144\n"
    "v_mul_f32 v198, v197, v198\n"
    "v_accvgpr_write_b32 a144, v198\n"
    "v_accvgpr_read_b32 v198, a145\n"
    "v_mul_f32 v198, v197, v198\n"
    "v_accvgpr_write_b32 a145, v198\n"
    "v_accvgpr_read_b32 v198, a146\n"
    "v_mul_f32 v198, v197, v198\n"
    "v_accvgpr_write_b32 a146, v198\n"
    "v_accvgpr_read_b32 v198, a147\n"
    "v_mul_f32 v198, v197, v198\n"
    "v_accvgpr_write_b32 a147, v198\n"
    "s_nop 1\n"
    "s_branch B_BACK_ED_4\n"
    "B_RARE_EP_0:\n"
    "v_mov_b32 v197, v196\n"
    "s_nop 1\n"
    "v_permlane16_swap_b32 v196, v197\n"
    "s_nop 0\n"
    "v_max_f32 v196, v196, v197\n"
    "v_mov_b32 v197, v196\n"
    "s_nop 1\n"
    "v_permlane32_swap_b32 v196, v197\n"
    "s_nop 0\n"
    "v_max3_f32 v196, v196, v197, v184\n"
    "v_sub_f32 v197, v184, v196\n"
    "v_mul_f32 v197, s69, v197\n"
    "v_exp_f32 v197, v197\n"
    "v_mov_b32 v184, v196\n"
    "v_add_f32 v189, 0x423c35a6, v196\n"
    "v_mul_f32 v200, s70, v196\n"
    "s_cmp_eq_u32 s80, 0\n"
    "s_cbranch_scc1 B_BACK_EP_0\n"
    "v_mul_f32 v0, v197, v0\n"
    "v_mul_f32 v1, v197, v1\n"
    "v_mul_f32 v2, v197, v2\n"
    "v_mul_f32 v3, v197, v3\n"
    "v_mul_f32 v4, v197, v4\n"
    "v_mul_f32 v5, v197, v5\n"
    "v_mul_f32 v6, v197, v6\n"
    "v_mul_f32 v7, v197, v7\n"
    "v_mul_f32 v8, v197, v8\n"
    "v_mul_f32 v9, v197, v9\n"
    "v_mul_f32 v10, v197, v10\n"
    "v_mul_f32 v11, v197, v11\n"
    "v_mul_f32 v12, v197, v12\n"
    "v_mul_f32 v13, v197, v13\n"
    "v_mul_f32 v14, v197, v14\n"
    "v_mul_f32 v15, v197, v15\n"
    "v_mul_f32 v16, v197, v16\n"
    "v_mul_f32 v17, v197, v17\n"
    "v_mul_f32 v18, v197, v18\n"
    "v_mul_f32 v19, v197, v19\n"
    "v_mul_f32 v20, v197, v20\n"
    "v_mul_f32 v21, v197, v21\n"
    "v_mul_f32 v22, v197, v22\n"
    "v_mul_f32 v23, v197, v23\n"
    "v_mul_f32 v24, v197, v24\n"
    "v_mul_f32 v25, v197, v25\n"
    "v_mul_f32 v26, v197, v26\n"
    "v_mul_f32 v27, v197, v27\n"
    "v_mul_f32 v28, v197, v28\n"
    "v_mul_f32 v29, v197, v29\n"
    "v_mul_f32 v30, v197, v30\n"
    "v_mul_f32 v31, v197, v31\n"
    "v_accvgpr_read_b32 v198, a128\n"
    "v_mul_f32 v198, v197, v198\n"
    "v_accvgpr_write_b32 a128, v198\n"
    "v_accvgpr_read_b32 v198, a129\n"
    "v_mul_f32 v198, v197, v198\n"
    "v_accvgpr_write_b32 a129, v198\n"
    "v_accvgpr_read_b32 v198, a130\n"
    "v_mul_f32 v198, v197, v198\n"
    "v_accvgpr_write_b32 a130, v198\n"
    "v_accvgpr_read_b32 v198, a131\n"
    "v_mul_f32 v198, v197, v198\n"
    "v_accvgpr_write_b32 a131, v198\n"
    "s_nop 1\n"
    "s_branch B_BACK_EP_0\n"
    "B_RARE_EP_1:\n"
    "v_mov_b32 v197, v196\n"
    "s_nop 1\n"
    "v_permlane16_swap_b32 v196, v197\n"
    "s_nop 0\n"
    "v_max_f32 v196, v196, v197\n"
    "v_mov_b32 v197, v196\n"
    "s_nop 1\n"
    "v_permlane32_swap_b32 v196, v197\n"
    "s_nop 0\n"
    "v_max3_f32 v196, v196, v197, v185\n"
    "v_sub_f32 v197, v185, v196\n"
    "v_mul_f32 v197, s69, v197\n"
    "v_exp_f32 v197, v197\n"
    "v_mov_b32 v185, v196\n"
    "v_add_f32 v190, 0x423c35a6, v196\n"
    "v_mul_f32 v201, s70, v196\n"
    "s_cmp_eq_u32 s80, 0\n"
    "s_cbranch_scc1 B_BACK_EP_1\n"
    "v_mul_f32 v32, v197, v32\n"
    "v_mul_f32 v33, v197, v33\n"
    "v_mul_f32 v34, v197, v34\n"
    "v_mul_f32 v35, v197, v35\n"
    "v_mul_f32 v36, v197, v36\n"
    "v_mul_f32 v37, v197, v37\n"
    "v_mul_f32 v38, v197, v38\n"
    "v_mul_f32 v39, v197, v39\n"
    "v_mul_f32 v40, v197, v40\n"
    "v_mul_f32 v41, v197, v41\n"
    "v_mul_f32 v42, v197, v42\n"
    "v_mul_f32 v43, v197, v43\n"
    "v_mul_f32 v44, v197, v44\n"
    "v_mul_f32 v45, v197, v45\n"
    "v_mul_f32 v46, v197, v46\n"
    "v_mul_f32 v47, v197, v47\n"
    "v_mul_f32 v48, v197, v48\n"
    "v_mul_f32 v49, v197, v49\n"
    "v_mul_f32 v50, v197, v50\n"
    "v_mul_f32 v51, v197, v51\n"
    "v_mul_f32 v52, v197, v52\n"
    "v_mul_f32 v53, v197, v53\n"
    "v_mul_f32 v54, v197, v54\n"
    "v_mul_f32 v55, v197, v55\n"
    "v_mul_f32 v56, v197, v56\n"
    "v_mul_f32 v57, v197, v57\n"
    "v_mul_f32 v58, v197, v58\n"
    "v_mul_f32 v59, v197, v59\n"
    "v_mul_f32 v60, v197, v60\n"
    "v_mul_f32 v61, v197, v61\n"
    "v_mul_f32 v62, v197, v62\n"
    "v_mul_f32 v63, v197, v63\n"
    "v_accvgpr_read_b32 v198, a132\n"
    "v_mul_f32 v198, v197, v198\n"
    "v_accvgpr_write_b32 a132, v198\n"
    "v_accvgpr_read_b32 v198, a133\n"
    "v_mul_f32 v198, v197, v198\n"
    "v_accvgpr_write_b32 a133, v198\n"
    "v_accvgpr_read_b32 v198, a134\n"
    "v_mul_f32 v198, v197, v198\n"
    "v_accvgpr_write_b32 a134, v198\n"
    "v_accvgpr_read_b32 v198, a135\n"
    "v_mul_f32 v198, v197, v198\n"
    "v_accvgpr_write_b32 a135, v198\n"
    "s_nop 1\n"
    "s_branch B_BACK_EP_1\n"
    "B_RARE_EP_2:\n"
    "v_mov_b32 v197, v196\n"
    "s_nop 1\n"
    "v_permlane16_swap_b32 v196, v197\n"
    "s_nop 0\n"
    "v_max_f32 v196, v196, v197\n"
    "v_mov_b32 v197, v196\n"
    "s_nop 1\n"
    "v_permlane32_swap_b32 v196, v197\n"
    "s_nop 0\n"
    "v_max3_f32 v196, v196, v197, v186\n"
    "v_sub_f32 v197, v186, v196\n"
    "v_mul_f32 v197, s69, v197\n"
    "v_exp_f32 v197, v197\n"
    "v_mov_b32 v186, v196\n"
    "v_add_f32 v191, 0x423c35a6, v196\n"
    "v_mul_f32 v202, s70, v196\n"
    "s_cmp_eq_u32 s80, 0\n"
    "s_cbranch_scc1 B_BACK_EP_2\n"
    "v_mul_f32 v64, v197, v64\n"
    "v_mul_f32 v65, v197, v65\n"
    "v_mul_f32 v66, v197, v66\n"
    "v_mul_f32 v67, v197, v67\n"
    "v_mul_f32 v68, v197, v68\n"
    "v_mul_f32 v69, v197, v69\n"
    "v_mul_f32 v70, v197, v70\n"
    "v_mul_f32 v71, v197, v71\n"
    "v_mul_f32 v72, v197, v72\n"
    "v_mul_f32 v73, v197, v73\n"
    "v_mul_f32 v74, v197, v74\n"
    "v_mul_f32 v75, v197, v75\n"
    "v_mul_f32 v76, v197, v76\n"
    "v_mul_f32 v77, v197, v77\n"
    "v_mul_f32 v78, v197, v78\n"
    "v_mul_f32 v79, v197, v79\n"
    "v_mul_f32 v80, v197, v80\n"
    "v_mul_f32 v81, v197, v81\n"
    "v_mul_f32 v82, v197, v82\n"
    "v_mul_f32 v83, v197, v83\n"
    "v_mul_f32 v84, v197, v84\n"
    "v_mul_f32 v85, v197, v85\n"
    "v_mul_f32 v86, v197, v86\n"
    "v_mul_f32 v87, v197, v87\n"
    "v_mul_f32 v88, v197, v88\n"
    "v_mul_f32 v89, v197, v89\n"
    "v_mul_f32 v90, v197, v90\n"
    "v_mul_f32 v91, v197, v91\n"
    "v_mul_f32 v92, v197, v92\n"
    "v_mul_f32 v93, v197, v93\n"
    "v_mul_f32 v94, v197, v94\n"
    "v_mul_f32 v95, v197, v95\n"
    "v_accvgpr_read_b32 v198, a136\n"
    "v_mul_f32 v198, v197, v198\n"
    "v_accvgpr_write_b32 a136, v198\n"
    "v_accvgpr_read_b32 v198, a137\n"
    "v_mul_f32 v198, v197, v198\n"
    "v_accvgpr_write_b32 a137, v198\n"
    "v_accvgpr_read_b32 v198, a138\n"
    "v_mul_f32 v198, v197, v198\n"
    "v_accvgpr_write_b32 a138, v198\n"
    "v_accvgpr_read_b32 v198, a139\n"
    "v_mul_f32 v198, v197, v198\n"
    "v_accvgpr_write_b32 a139, v198\n"
    "s_nop 1\n"
    "s_branch B_BACK_EP_2\n"
    "B_RARE_EP_3:\n"
    "v_mov_b32 v197, v196\n"
    "s_nop 1\n"
    "v_permlane16_swap_b32 v196, v197\n"
    "s_nop 0\n"
    "v_max_f32 v196, v196, v197\n"
    "v_mov_b32 v197, v196\n"
    "s_nop 1\n"
    "v_permlane32_swap_b32 v196, v197\n"
    "s_nop 0\n"
    "v_max3_f32 v196, v196, v197, v187\n"
    "v_sub_f32 v197, v187, v196\n"
    "v_mul_f32 v197, s69, v197\n"
    "v_exp_f32 v197, v197\n"
    "v_mov_b32 v187, v196\n"
    "v_add_f32 v192, 0x423c35a6, v196\n"
    "v_mul_f32 v203, s70, v196\n"
    "s_cmp_eq_u32 s80, 0\n"
    "s_cbranch_scc1 B_BACK_EP_3\n"
    "v_mul_f32 v96, v197, v96\n"
    "v_mul_f32 v97, v197, v97\n"
    "v_mul_f32 v98, v197, v98\n"
    "v_mul_f32 v99, v197, v99\n"
    "v_mul_f32 v100, v197, v100\n"
    "v_mul_f32 v101, v197, v101\n"
    "v_mul_f32 v102, v197, v102\n"
    "v_mul_f32 v103, v197, v103\n"
    "v_mul_f32 v104, v197, v104\n"
    "v_mul_f32 v105, v197, v105\n"
    "v_mul_f32 v106, v197, v106\n"
    "v_mul_f32 v107, v197, v107\n"
    "v_mul_f32 v108, v197, v108\n"
    "v_mul_f32 v109, v197, v109\n"
    "v_mul_f32 v110, v197, v110\n"
    "v_mul_f32 v111, v197, v111\n"
    "v_mul_f32 v112, v197, v112\n"
    "v_mul_f32 v113, v197, v113\n"
    "v_mul_f32 v114, v197, v114\n"
    "v_mul_f32 v115, v197, v115\n"
    "v_mul_f32 v116, v197, v116\n"
    "v_mul_f32 v117, v197, v117\n"
    "v_mul_f32 v118, v197, v118\n"
    "v_mul_f32 v119, v197, v119\n"
    "v_mul_f32 v120, v197, v120\n"
    "v_mul_f32 v121, v197, v121\n"
    "v_mul_f32 v122, v197, v122\n"
    "v_mul_f32 v123, v197, v123\n"
    "v_mul_f32 v124, v197, v124\n"
    "v_mul_f32 v125, v197, v125\n"
    "v_mul_f32 v126, v197, v126\n"
    "v_mul_f32 v127, v197, v127\n"
    "v_accvgpr_read_b32 v198, a140\n"
    "v_mul_f32 v198, v197, v198\n"
    "v_accvgpr_write_b32 a140, v198\n"
    "v_accvgpr_read_b32 v198, a141\n"
    "v_mul_f32 v198, v197, v198\n"
    "v_accvgpr_write_b32 a141, v198\n"
    "v_accvgpr_read_b32 v198, a142\n"
    "v_mul_f32 v198, v197, v198\n"
    "v_accvgpr_write_b32 a142, v198\n"
    "v_accvgpr_read_b32 v198, a143\n"
    "v_mul_f32 v198, v197, v198\n"
    "v_accvgpr_write_b32 a143, v198\n"
    "s_nop 1\n"
    "s_branch B_BACK_EP_3\n"
    "B_RARE_EP_4:\n"
    "v_mov_b32 v197, v196\n"
    "s_nop 1\n"
    "v_permlane16_swap_b32 v196, v197\n"
    "s_nop 0\n"
    "v_max_f32 v196, v196, v197\n"
    "v_mov_b32 v197, v196\n"
    "s_nop 1\n"
    "v_permlane32_swap_b32 v196, v197\n"
    "s_nop 0\n"
    "v_max3_f32 v196, v196, v197, v188\n"
    "v_sub_f32 v197, v188, v196\n"
    "v_mul_f32 v197, s69, v197\n"
    "v_exp_f32 v197, v197\n"
    "v_mov_b32 v188, v196\n"
    "v_add_f32 v193, 0x423c35a6, v196\n"
    "v_mul_f32 v204, s70, v196\n"
    "s_cmp_eq_u32 s80, 0\n"
    "s_cbranch_scc1 B_BACK_EP_4\n"
    "v_mul_f32 v128, v197, v128\n"
    "v_mul_f32 v129, v197, v129\n"
    "v_mul_f32 v130, v197, v130\n"
    "v_mul_f32 v131, v197, v131\n"
    "v_mul_f32 v132, v197, v132\n"
    "v_mul_f32 v133, v197, v133\n"
    "v_mul_f32 v134, v197, v134\n"
    "v_mul_f32 v135, v197, v135\n"
    "v_mul_f32 v136, v197, v136\n"
    "v_mul_f32 v137, v197, v137\n"
    "v_mul_f32 v138, v197, v138\n"
    "v_mul_f32 v139, v197, v139\n"
    "v_mul_f32 v140, v197, v140\n"
    "v_mul_f32 v141, v197, v141\n"
    "v_mul_f32 v142, v197, v142\n"
    "v_mul_f32 v143, v197, v143\n"
    "v_mul_f32 v144, v197, v144\n"
    "v_mul_f32 v145, v197, v145\n"
    "v_mul_f32 v146, v197, v146\n"
    "v_mul_f32 v147, v197, v147\n"
    "v_mul_f32 v148, v197, v148\n"
    "v_mul_f32 v149, v197, v149\n"
    "v_mul_f32 v150, v197, v150\n"
    "v_mul_f32 v151, v197, v151\n"
    "v_mul_f32 v152, v197, v152\n"
    "v_mul_f32 v153, v197, v153\n"
    "v_mul_f32 v154, v197, v154\n"
    "v_mul_f32 v155, v197, v155\n"
    "v_mul_f32 v156, v197, v156\n"
    "v_mul_f32 v157, v197, v157\n"
    "v_mul_f32 v158, v197, v158\n"
    "v_mul_f32 v159, v197, v159\n"
    "v_accvgpr_read_b32 v198, a144\n"
    "v_mul_f32 v198, v197, v198\n"
    "v_accvgpr_write_b32 a144, v198\n"
    "v_accvgpr_read_b32 v198, a145\n"
    "v_mul_f32 v198, v197, v198\n"
    "v_accvgpr_write_b32 a145, v198\n"
    "v_accvgpr_read_b32 v198, a146\n"
    "v_mul_f32 v198, v197, v198\n"
    "v_accvgpr_write_b32 a146, v198\n"
    "v_accvgpr_read_b32 v198, a147\n"
    "v_mul_f32 v198, v197, v198\n"
    "v_accvgpr_write_b32 a147, v198\n"
    "s_nop 1\n"
    "s_branch B_BACK_EP_4\n"
    "B_RARE_F_0:\n"
    "v_mov_b32 v197, v196\n"
    "s_nop 1\n"
    "v_permlane16_swap_b32 v196, v197\n"
    "s_nop 0\n"
    "v_max_f32 v196, v196, v197\n"
    "v_mov_b32 v197, v196\n"
    "s_nop 1\n"
    "v_permlane32_swap_b32 v196, v197\n"
    "s_nop 0\n"
    "v_max3_f32 v196, v196, v197, v184\n"
    "v_sub_f32 v197, v184, v196\n"
    "v_mul_f32 v197, s69, v197\n"
    "v_exp_f32 v197, v197\n"
    "v_mov_b32 v184, v196\n"
    "v_add_f32 v189, 0x423c35a6, v196\n"
    "v_mul_f32 v200, s70, v196\n"
    "v_mul_f32 v0, v197, v0\n"
    "v_mul_f32 v1, v197, v1\n"
    "v_mul_f32 v2, v197, v2\n"
    "v_mul_f32 v3, v197, v3\n"
    "v_mul_f32 v4, v197, v4\n"
    "v_mul_f32 v5, v197, v5\n"
    "v_mul_f32 v6, v197, v6\n"
    "v_mul_f32 v7, v197, v7\n"
    "v_mul_f32 v8, v197, v8\n"
    "v_mul_f32 v9, v197, v9\n"
    "v_mul_f32 v10, v197, v10\n"
    "v_mul_f32 v11, v197, v11\n"
    "v_mul_f32 v12, v197, v12\n"
    "v_mul_f32 v13, v197, v13\n"
    "v_mul_f32 v14, v197, v14\n"
    "v_mul_f32 v15, v197, v15\n"
    "v_mul_f32 v16, v197, v16\n"
    "v_mul_f32 v17, v197, v17\n"
    "v_mul_f32 v18, v197, v18\n"
    "v_mul_f32 v19, v197, v19\n"
    "v_mul_f32 v20, v197, v20\n"
    "v_mul_f32 v21, v197, v21\n"
    "v_mul_f32 v22, v197, v22\n"
    "v_mul_f32 v23, v197, v23\n"
    "v_mul_f32 v24, v197, v24\n"
    "v_mul_f32 v25, v197, v25\n"
    "v_mul_f32 v26, v197, v26\n"
    "v_mul_f32 v27, v197, v27\n"
    "v_mul_f32 v28, v197, v28\n"
    "v_mul_f32 v29, v197, v29\n"
    "v_mul_f32 v30, v197, v30\n"
    "v_mul_f32 v31, v197, v31\n"
    "v_accvgpr_read_b32 v198, a128\n"
    "v_mul_f32 v198, v197, v198\n"
    "v_accvgpr_write_b32 a128, v198\n"
    "v_accvgpr_read_b32 v198, a129\n"
    "v_mul_f32 v198, v197, v198\n"
    "v_accvgpr_write_b32 a129, v198\n"
    "v_accvgpr_read_b32 v198, a130\n"
    "v_mul_f32 v198, v197, v198\n"
    "v_accvgpr_write_b32 a130, v198\n"
    "v_accvgpr_read_b32 v198, a131\n"
    "v_mul_f32 v198, v197, v198\n"
    "v_accvgpr_write_b32 a131, v198\n"
    "s_nop 1\n"
    "s_branch B_BACK_F_0\n"
    "B_RARE_F_1:\n"
    "v_mov_b32 v197, v196\n"
    "s_nop 1\n"
    "v_permlane16_swap_b32 v196, v197\n"
    "s_nop 0\n"
    "v_max_f32 v196, v196, v197\n"
    "v_mov_b32 v197, v196\n"
    "s_nop 1\n"
    "v_permlane32_swap_b32 v196, v197\n"
    "s_nop 0\n"
    "v_max3_f32 v196, v196, v197, v185\n"
    "v_sub_f32 v197, v185, v196\n"
    "v_mul_f32 v197, s69, v197\n"
    "v_exp_f32 v197, v197\n"
    "v_mov_b32 v185, v196\n"
    "v_add_f32 v190, 0x423c35a6, v196\n"
    "v_mul_f32 v201, s70, v196\n"
    "v_mul_f32 v32, v197, v32\n"
    "v_mul_f32 v33, v197, v33\n"
    "v_mul_f32 v34, v197, v34\n"
    "v_mul_f32 v35, v197, v35\n"
    "v_mul_f32 v36, v197, v36\n"
    "v_mul_f32 v37, v197, v37\n"
    "v_mul_f32 v38, v197, v38\n"
    "v_mul_f32 v39, v197, v39\n"
    "v_mul_f32 v40, v197, v40\n"
    "v_mul_f32 v41, v197, v41\n"
    "v_mul_f32 v42, v197, v42\n"
    "v_mul_f32 v43, v197, v43\n"
    "v_mul_f32 v44, v197, v44\n"
    "v_mul_f32 v45, v197, v45\n"
    "v_mul_f32 v46, v197, v46\n"
    "v_mul_f32 v47, v197, v47\n"
    "v_mul_f32 v48, v197, v48\n"
    "v_mul_f32 v49, v197, v49\n"
    "v_mul_f32 v50, v197, v50\n"
    "v_mul_f32 v51, v197, v51\n"
    "v_mul_f32 v52, v197, v52\n"
    "v_mul_f32 v53, v197, v53\n"
    "v_mul_f32 v54, v197, v54\n"
    "v_mul_f32 v55, v197, v55\n"
    "v_mul_f32 v56, v197, v56\n"
    "v_mul_f32 v57, v197, v57\n"
    "v_mul_f32 v58, v197, v58\n"
    "v_mul_f32 v59, v197, v59\n"
    "v_mul_f32 v60, v197, v60\n"
    "v_mul_f32 v61, v197, v61\n"
    "v_mul_f32 v62, v197, v62\n"
    "v_mul_f32 v63, v197, v63\n"
    "v_accvgpr_read_b32 v198, a132\n"
    "v_mul_f32 v198, v197, v198\n"
    "v_accvgpr_write_b32 a132, v198\n"
    "v_accvgpr_read_b32 v198, a133\n"
    "v_mul_f32 v198, v197, v198\n"
    "v_accvgpr_write_b32 a133, v198\n"
    "v_accvgpr_read_b32 v198, a134\n"
    "v_mul_f32 v198, v197, v198\n"
    "v_accvgpr_write_b32 a134, v198\n"
    "v_accvgpr_read_b32 v198, a135\n"
    "v_mul_f32 v198, v197, v198\n"
    "v_accvgpr_write_b32 a135, v198\n"
    "s_nop 1\n"
    "s_branch B_BACK_F_1\n"
    "B_RARE_F_2:\n"
    "v_mov_b32 v197, v196\n"
    "s_nop 1\n"
    "v_permlane16_swap_b32 v196, v197\n"
    "s_nop 0\n"
    "v_max_f32 v196, v196, v197\n"
    "v_mov_b32 v197, v196\n"
    "s_nop 1\n"
    "v_permlane32_swap_b32 v196, v197\n"
    "s_nop 0\n"
    "v_max3_f32 v196, v196, v197, v186\n"
    "v_sub_f32 v197, v186, v196\n"
    "v_mul_f32 v197, s69, v197\n"
    "v_exp_f32 v197, v197\n"
    "v_mov_b32 v186, v196\n"
    "v_add_f32 v191, 0x423c35a6, v196\n"
    "v_mul_f32 v202, s70, v196\n"
    "v_mul_f32 v64, v197, v64\n"
    "v_mul_f32 v65, v197, v65\n"
    "v_mul_f32 v66, v197, v66\n"
    "v_mul_f32 v67, v197, v67\n"
    "v_mul_f32 v68, v197, v68\n"
    "v_mul_f32 v69, v197, v69\n"
    "v_mul_f32 v70, v197, v70\n"
    "v_mul_f32 v71, v197, v71\n"
    "v_mul_f32 v72, v197, v72\n"
    "v_mul_f32 v73, v197, v73\n"
    "v_mul_f32 v74, v197, v74\n"
    "v_mul_f32 v75, v197, v75\n"
    "v_mul_f32 v76, v197, v76\n"
    "v_mul_f32 v77, v197, v77\n"
    "v_mul_f32 v78, v197, v78\n"
    "v_mul_f32 v79, v197, v79\n"
    "v_mul_f32 v80, v197, v80\n"
    "v_mul_f32 v81, v197, v81\n"
    "v_mul_f32 v82, v197, v82\n"
    "v_mul_f32 v83, v197, v83\n"
    "v_mul_f32 v84, v197, v84\n"
    "v_mul_f32 v85, v197, v85\n"
    "v_mul_f32 v86, v197, v86\n"
    "v_mul_f32 v87, v197, v87\n"
    "v_mul_f32 v88, v197, v88\n"
    "v_mul_f32 v89, v197, v89\n"
    "v_mul_f32 v90, v197, v90\n"
    "v_mul_f32 v91, v197, v91\n"
    "v_mul_f32 v92, v197, v92\n"
    "v_mul_f32 v93, v197, v93\n"
    "v_mul_f32 v94, v197, v94\n"
    "v_mul_f32 v95, v197, v95\n"
    "v_accvgpr_read_b32 v198, a136\n"
    "v_mul_f32 v198, v197, v198\n"
    "v_accvgpr_write_b32 a136, v198\n"
    "v_accvgpr_read_b32 v198, a137\n"
    "v_mul_f32 v198, v197, v198\n"
    "v_accvgpr_write_b32 a137, v198\n"
    "v_accvgpr_read_b32 v198, a138\n"
    "v_mul_f32 v198, v197, v198\n"
    "v_accvgpr_write_b32 a138, v198\n"
    "v_accvgpr_read_b32 v198, a139\n"
    "v_mul_f32 v198, v197, v198\n"
    "v_accvgpr_write_b32 a139, v198\n"
    "s_nop 1\n"
    "s_branch B_BACK_F_2\n"
    "B_RARE_F_3:\n"
    "v_mov_b32 v197, v196\n"
    "s_nop 1\n"
    "v_permlane16_swap_b32 v196, v197\n"
    "s_nop 0\n"
    "v_max_f32 v196, v196, v197\n"
    "v_mov_b32 v197, v196\n"
    "s_nop 1\n"
    "v_permlane32_swap_b32 v196, v197\n"
    "s_nop 0\n"
    "v_max3_f32 v196, v196, v197, v187\n"
    "v_sub_f32 v197, v187, v196\n"
    "v_mul_f32 v197, s69, v197\n"
    "v_exp_f32 v197, v197\n"
    "v_mov_b32 v187, v196\n"
    "v_add_f32 v192, 0x423c35a6, v196\n"
    "v_mul_f32 v203, s70, v196\n"
    "v_mul_f32 v96, v197, v96\n"
    "v_mul_f32 v97, v197, v97\n"
    "v_mul_f32 v98, v197, v98\n"
    "v_mul_f32 v99, v197, v99\n"
    "v_mul_f32 v100, v197, v100\n"
    "v_mul_f32 v101, v197, v101\n"
    "v_mul_f32 v102, v197, v102\n"
    "v_mul_f32 v103, v197, v103\n"
    "v_mul_f32 v104, v197, v104\n"
    "v_mul_f32 v105, v197, v105\n"
    "v_mul_f32 v106, v197, v106\n"
    "v_mul_f32 v107, v197, v107\n"
    "v_mul_f32 v108, v197, v108\n"
    "v_mul_f32 v109, v197, v109\n"
    "v_mul_f32 v110, v197, v110\n"
    "v_mul_f32 v111, v197, v111\n"
    "v_mul_f32 v112, v197, v112\n"
    "v_mul_f32 v113, v197, v113\n"
    "v_mul_f32 v114, v197, v114\n"
    "v_mul_f32 v115, v197, v115\n"
    "v_mul_f32 v116, v197, v116\n"
    "v_mul_f32 v117, v197, v117\n"
    "v_mul_f32 v118, v197, v118\n"
    "v_mul_f32 v119, v197, v119\n"
    "v_mul_f32 v120, v197, v120\n"
    "v_mul_f32 v121, v197, v121\n"
    "v_mul_f32 v122, v197, v122\n"
    "v_mul_f32 v123, v197, v123\n"
    "v_mul_f32 v124, v197, v124\n"
    "v_mul_f32 v125, v197, v125\n"
    "v_mul_f32 v126, v197, v126\n"
    "v_mul_f32 v127, v197, v127\n"
    "v_accvgpr_read_b32 v198, a140\n"
    "v_mul_f32 v198, v197, v198\n"
    "v_accvgpr_write_b32 a140, v198\n"
    "v_accvgpr_read_b32 v198, a141\n"
    "v_mul_f32 v198, v197, v198\n"
    "v_accvgpr_write_b32 a141, v198\n"
    "v_accvgpr_read_b32 v198, a142\n"
    "v_mul_f32 v198, v197, v198\n"
    "v_accvgpr_write_b32 a142, v198\n"
    "v_accvgpr_read_b32 v198, a143\n"
    "v_mul_f32 v198, v197, v198\n"
    "v_accvgpr_write_b32 a143, v198\n"
    "s_nop 1\n"
    "s_branch B_BACK_F_3\n"
    "B_RARE_F_4:\n"
    "v_mov_b32 v197, v196\n"
    "s_nop 1\n"
    "v_permlane16_swap_b32 v196, v197\n"
    "s_nop 0\n"
    "v_max_f32 v196, v196, v197\n"
    "v_mov_b32 v197, v196\n"
    "s_nop 1\n"
    "v_permlane32_swap_b32 v196, v197\n"
    "s_nop 0\n"
    "v_max3_f32 v196, v196, v197, v188\n"
    "v_sub_f32 v197, v188, v196\n"
    "v_mul_f32 v197, s69, v197\n"
    "v_exp_f32 v197, v197\n"
    "v_mov_b32 v188, v196\n"
    "v_add_f32 v193, 0x423c35a6, v196\n"
    "v_mul_f32 v204, s70, v196\n"
    "v_mul_f32 v128, v197, v128\n"
    "v_mul_f32 v129, v197, v129\n"
    "v_mul_f32 v130, v197, v130\n"
    "v_mul_f32 v131, v197, v131\n"
    "v_mul_f32 v132, v197, v132\n"
    "v_mul_f32 v133, v197, v133\n"
    "v_mul_f32 v134, v197, v134\n"
    "v_mul_f32 v135, v197, v135\n"
    "v_mul_f32 v136, v197, v136\n"
    "v_mul_f32 v137, v197, v137\n"
    "v_mul_f32 v138, v197, v138\n"
    "v_mul_f32 v139, v197, v139\n"
    "v_mul_f32 v140, v197, v140\n"
    "v_mul_f32 v141, v197, v141\n"
    "v_mul_f32 v142, v197, v142\n"
    "v_mul_f32 v143, v197, v143\n"
    "v_mul_f32 v144, v197, v144\n"
    "v_mul_f32 v145, v197, v145\n"
    "v_mul_f32 v146, v197, v146\n"
    "v_mul_f32 v147, v197, v147\n"
    "v_mul_f32 v148, v197, v148\n"
    "v_mul_f32 v149, v197, v149\n"
    "v_mul_f32 v150, v197, v150\n"
    "v_mul_f32 v151, v197, v151\n"
    "v_mul_f32 v152, v197, v152\n"
    "v_mul_f32 v153, v197, v153\n"
    "v_mul_f32 v154, v197, v154\n"
    "v_mul_f32 v155, v197, v155\n"
    "v_mul_f32 v156, v197, v156\n"
    "v_mul_f32 v157, v197, v157\n"
    "v_mul_f32 v158, v197, v158\n"
    "v_mul_f32 v159, v197, v159\n"
    "v_accvgpr_read_b32 v198, a144\n"
    "v_mul_f32 v198, v197, v198\n"
    "v_accvgpr_write_b32 a144, v198\n"
    "v_accvgpr_read_b32 v198, a145\n"
    "v_mul_f32 v198, v197, v198\n"
    "v_accvgpr_write_b32 a145, v198\n"
    "v_accvgpr_read_b32 v198, a146\n"
    "v_mul_f32 v198, v197, v198\n"
    "v_accvgpr_write_b32 a146, v198\n"
    "v_accvgpr_read_b32 v198, a147\n"
    "v_mul_f32 v198, v197, v198\n"
    "v_accvgpr_write_b32 a147, v198\n"
    "s_nop 1\n"
    "s_branch B_BACK_F_4\n"
    "B_RARE_G_0:\n"
    "v_mov_b32 v197, v196\n"
    "s_nop 1\n"
    "v_permlane16_swap_b32 v196, v197\n"
    "s_nop 0\n"
    "v_max_f32 v196, v196, v197\n"
    "v_mov_b32 v197, v196\n"
    "s_nop 1\n"
    "v_permlane32_swap_b32 v196, v197\n"
    "s_nop 0\n"
    "v_max3_f32 v196, v196, v197, v184\n"
    "v_sub_f32 v197, v184, v196\n"
    "v_mul_f32 v197, s69, v197\n"
    "v_exp_f32 v197, v197\n"
    "v_mov_b32 v184, v196\n"
    "v_add_f32 v189, 0x423c35a6, v196\n"
    "v_mul_f32 v200, s70, v196\n"
    "v_mul_f32 v0, v197, v0\n"
    "v_mul_f32 v1, v197, v1\n"
    "v_mul_f32 v2, v197, v2\n"
    "v_mul_f32 v3, v197, v3\n"
    "v_mul_f32 v4, v197, v4\n"
    "v_mul_f32 v5, v197, v5\n"
    "v_mul_f32 v6, v197, v6\n"
    "v_mul_f32 v7, v197, v7\n"
    "v_mul_f32 v8, v197, v8\n"
    "v_mul_f32 v9, v197, v9\n"
    "v_mul_f32 v10, v197, v10\n"
    "v_mul_f32 v11, v197, v11\n"
    "v_mul_f32 v12, v197, v12\n"
    "v_mul_f32 v13, v197, v13\n"
    "v_mul_f32 v14, v197, v14\n"
    "v_mul_f32 v15, v197, v15\n"
    "v_mul_f32 v16, v197, v16\n"
    "v_mul_f32 v17, v197, v17\n"
    "v_mul_f32 v18, v197, v18\n"
    "v_mul_f32 v19, v197, v19\n"
    "v_mul_f32 v20, v197, v20\n"
    "v_mul_f32 v21, v197, v21\n"
    "v_mul_f32 v22, v197, v22\n"
    "v_mul_f32 v23, v197, v23\n"
    "v_mul_f32 v24, v197, v24\n"
    "v_mul_f32 v25, v197, v25\n"
    "v_mul_f32 v26, v197, v26\n"
    "v_mul_f32 v27, v197, v27\n"
    "v_mul_f32 v28, v197, v28\n"
    "v_mul_f32 v29, v197, v29\n"
    "v_mul_f32 v30, v197, v30\n"
    "v_mul_f32 v31, v197, v31\n"
    "v_accvgpr_read_b32 v198, a128\n"
    "v_mul_f32 v198, v197, v198\n"
    "v_accvgpr_write_b32 a128, v198\n"
    "v_accvgpr_read_b32 v198, a129\n"
    "v_mul_f32 v198, v197, v198\n"
    "v_accvgpr_write_b32 a129, v198\n"
    "v_accvgpr_read_b32 v198, a130\n"
    "v_mul_f32 v198, v197, v198\n"
    "v_accvgpr_write_b32 a130, v198\n"
    "v_accvgpr_read_b32 v198, a131\n"
    "v_mul_f32 v198, v197, v198\n"
    "v_accvgpr_write_b32 a131, v198\n"
    "s_nop 1\n"
    "s_branch B_BACK_G_0\n"
    "B_RARE_G_1:\n"
    "v_mov_b32 v197, v196\n"
    "s_nop 1\n"
    "v_permlane16_swap_b32 v196, v197\n"
    "s_nop 0\n"
    "v_max_f32 v196, v196, v197\n"
    "v_mov_b32 v197, v196\n"
    "s_nop 1\n"
    "v_permlane32_swap_b32 v196, v197\n"
    "s_nop 0\n"
    "v_max3_f32 v196, v196, v197, v185\n"
    "v_sub_f32 v197, v185, v196\n"
    "v_mul_f32 v197, s69, v197\n"
    "v_exp_f32 v197, v197\n"
    "v_mov_b32 v185, v196\n"
    "v_add_f32 v190, 0x423c35a6, v196\n"
    "v_mul_f32 v201, s70, v196\n"
    "v_mul_f32 v32, v197, v32\n"
    "v_mul_f32 v33, v197, v33\n"
    "v_mul_f32 v34, v197, v34\n"
    "v_mul_f32 v35, v197, v35\n"
    "v_mul_f32 v36, v197, v36\n"
    "v_mul_f32 v37, v197, v37\n"
    "v_mul_f32 v38, v197, v38\n"
    "v_mul_f32 v39, v197, v39\n"
    "v_mul_f32 v40, v197, v40\n"
    "v_mul_f32 v41, v197, v41\n"
    "v_mul_f32 v42, v197, v42\n"
    "v_mul_f32 v43, v197, v43\n"
    "v_mul_f32 v44, v197, v44\n"
    "v_mul_f32 v45, v197, v45\n"
    "v_mul_f32 v46, v197, v46\n"
    "v_mul_f32 v47, v197, v47\n"
    "v_mul_f32 v48, v197, v48\n"
    "v_mul_f32 v49, v197, v49\n"
    "v_mul_f32 v50, v197, v50\n"
    "v_mul_f32 v51, v197, v51\n"
    "v_mul_f32 v52, v197, v52\n"
    "v_mul_f32 v53, v197, v53\n"
    "v_mul_f32 v54, v197, v54\n"
    "v_mul_f32 v55, v197, v55\n"
    "v_mul_f32 v56, v197, v56\n"
    "v_mul_f32 v57, v197, v57\n"
    "v_mul_f32 v58, v197, v58\n"
    "v_mul_f32 v59, v197, v59\n"
    "v_mul_f32 v60, v197, v60\n"
    "v_mul_f32 v61, v197, v61\n"
    "v_mul_f32 v62, v197, v62\n"
    "v_mul_f32 v63, v197, v63\n"
    "v_accvgpr_read_b32 v198, a132\n"
    "v_mul_f32 v198, v197, v198\n"
    "v_accvgpr_write_b32 a132, v198\n"
    "v_accvgpr_read_b32 v198, a133\n"
    "v_mul_f32 v198, v197, v198\n"
    "v_accvgpr_write_b32 a133, v198\n"
    "v_accvgpr_read_b32 v198, a134\n"
    "v_mul_f32 v198, v197, v198\n"
    "v_accvgpr_write_b32 a134, v198\n"
    "v_accvgpr_read_b32 v198, a135\n"
    "v_mul_f32 v198, v197, v198\n"
    "v_accvgpr_write_b32 a135, v198\n"
    "s_nop 1\n"
    "s_branch B_BACK_G_1\n"
    "B_RARE_G_2:\n"
    "v_mov_b32 v197, v196\n"
    "s_nop 1\n"
    "v_permlane16_swap_b32 v196, v197\n"
    "s_nop 0\n"
    "v_max_f32 v196, v196, v197\n"
    "v_mov_b32 v197, v196\n"
    "s_nop 1\n"
    "v_permlane32_swap_b32 v196, v197\n"
    "s_nop 0\n"
    "v_max3_f32 v196, v196, v197, v186\n"
    "v_sub_f32 v197, v186, v196\n"
    "v_mul_f32 v197, s69, v197\n"
    "v_exp_f32 v197, v197\n"
    "v_mov_b32 v186, v196\n"
    "v_add_f32 v191, 0x423c35a6, v196\n"
    "v_mul_f32 v202, s70, v196\n"
    "v_mul_f32 v64, v197, v64\n"
    "v_mul_f32 v65, v197, v65\n"
    "v_mul_f32 v66, v197, v66\n"
    "v_mul_f32 v67, v197, v67\n"
    "v_mul_f32 v68, v197, v68\n"
    "v_mul_f32 v69, v197, v69\n"
    "v_mul_f32 v70, v197, v70\n"
    "v_mul_f32 v71, v197, v71\n"
    "v_mul_f32 v72, v197, v72\n"
    "v_mul_f32 v73, v197, v73\n"
    "v_mul_f32 v74, v197, v74\n"
    "v_mul_f32 v75, v197, v75\n"
    "v_mul_f32 v76, v197, v76\n"
    "v_mul_f32 v77, v197, v77\n"
    "v_mul_f32 v78, v197, v78\n"
    "v_mul_f32 v79, v197, v79\n"
    "v_mul_f32 v80, v197, v80\n"
    "v_mul_f32 v81, v197, v81\n"
    "v_mul_f32 v82, v197, v82\n"
    "v_mul_f32 v83, v197, v83\n"
    "v_mul_f32 v84, v197, v84\n"
    "v_mul_f32 v85, v197, v85\n"
    "v_mul_f32 v86, v197, v86\n"
    "v_mul_f32 v87, v197, v87\n"
    "v_mul_f32 v88, v197, v88\n"
    "v_mul_f32 v89, v197, v89\n"
    "v_mul_f32 v90, v197, v90\n"
    "v_mul_f32 v91, v197, v91\n"
    "v_mul_f32 v92, v197, v92\n"
    "v_mul_f32 v93, v197, v93\n"
    "v_mul_f32 v94, v197, v94\n"
    "v_mul_f32 v95, v197, v95\n"
    "v_accvgpr_read_b32 v198, a136\n"
    "v_mul_f32 v198, v197, v198\n"
    "v_accvgpr_write_b32 a136, v198\n"
    "v_accvgpr_read_b32 v198, a137\n"
    "v_mul_f32 v198, v197, v198\n"
    "v_accvgpr_write_b32 a137, v198\n"
    "v_accvgpr_read_b32 v198, a138\n"
    "v_mul_f32 v198, v197, v198\n"
    "v_accvgpr_write_b32 a138, v198\n"
    "v_accvgpr_read_b32 v198, a139\n"
    "v_mul_f32 v198, v197, v198\n"
    "v_accvgpr_write_b32 a139, v198\n"
    "s_nop 1\n"
    "s_branch B_BACK_G_2\n"
    "B_RARE_G_3:\n"
    "v_mov_b32 v197, v196\n"
    "s_nop 1\n"
    "v_permlane16_swap_b32 v196, v197\n"
    "s_nop 0\n"
    "v_max_f32 v196, v196, v197\n"
    "v_mov_b32 v197, v196\n"
    "s_nop 1\n"
    "v_permlane32_swap_b32 v196, v197\n"
    "s_nop 0\n"
    "v_max3_f32 v196, v196, v197, v187\n"
    "v_sub_f32 v197, v187, v196\n"
    "v_mul_f32 v197, s69, v197\n"
    "v_exp_f32 v197, v197\n"
    "v_mov_b32 v187, v196\n"
    "v_add_f32 v192, 0x423c35a6, v196\n"
    "v_mul_f32 v203, s70, v196\n"
    "v_mul_f32 v96, v197, v96\n"
    "v_mul_f32 v97, v197, v97\n"
    "v_mul_f32 v98, v197, v98\n"
    "v_mul_f32 v99, v197, v99\n"
    "v_mul_f32 v100, v197, v100\n"
    "v_mul_f32 v101, v197, v101\n"
    "v_mul_f32 v102, v197, v102\n"
    "v_mul_f32 v103, v197, v103\n"
    "v_mul_f32 v104, v197, v104\n"
    "v_mul_f32 v105, v197, v105\n"
    "v_mul_f32 v106, v197, v106\n"
    "v_mul_f32 v107, v197, v107\n"
    "v_mul_f32 v108, v197, v108\n"
    "v_mul_f32 v109, v197, v109\n"
    "v_mul_f32 v110, v197, v110\n"
    "v_mul_f32 v111, v197, v111\n"
    "v_mul_f32 v112, v197, v112\n"
    "v_mul_f32 v113, v197, v113\n"
    "v_mul_f32 v114, v197, v114\n"
    "v_mul_f32 v115, v197, v115\n"
    "v_mul_f32 v116, v197, v116\n"
    "v_mul_f32 v117, v197, v117\n"
    "v_mul_f32 v118, v197, v118\n"
    "v_mul_f32 v119, v197, v119\n"
    "v_mul_f32 v120, v197, v120\n"
    "v_mul_f32 v121, v197, v121\n"
    "v_mul_f32 v122, v197, v122\n"
    "v_mul_f32 v123, v197, v123\n"
    "v_mul_f32 v124, v197, v124\n"
    "v_mul_f32 v125, v197, v125\n"
    "v_mul_f32 v126, v197, v126\n"
    "v_mul_f32 v127, v197, v127\n"
    "v_accvgpr_read_b32 v198, a140\n"
    "v_mul_f32 v198, v197, v198\n"
    "v_accvgpr_write_b32 a140, v198\n"
    "v_accvgpr_read_b32 v198, a141\n"
    "v_mul_f32 v198, v197, v198\n"
    "v_accvgpr_write_b32 a141, v198\n"
    "v_accvgpr_read_b32 v198, a142\n"
    "v_mul_f32 v198, v197, v198\n"
    "v_accvgpr_write_b32 a142, v198\n"
    "v_accvgpr_read_b32 v198, a143\n"
    "v_mul_f32 v198, v197, v198\n"
    "v_accvgpr_write_b32 a143, v198\n"
    "s_nop 1\n"
    "s_branch B_BACK_G_3\n"
    "B_RARE_G_4:\n"
    "v_mov_b32 v197, v196\n"
    "s_nop 1\n"
    "v_permlane16_swap_b32 v196, v197\n"
    "s_nop 0\n"
    "v_max_f32 v196, v196, v197\n"
    "v_mov_b32 v197, v196\n"
    "s_nop 1\n"
    "v_permlane32_swap_b32 v196, v197\n"
    "s_nop 0\n"
    "v_max3_f32 v196, v196, v197, v188\n"
    "v_sub_f32 v197, v188, v196\n"
    "v_mul_f32 v197, s69, v197\n"
    "v_exp_f32 v197, v197\n"
    "v_mov_b32 v188, v196\n"
    "v_add_f32 v193, 0x423c35a6, v196\n"
    "v_mul_f32 v204, s70, v196\n"
    "v_mul_f32 v128, v197, v128\n"
    "v_mul_f32 v129, v197, v129\n"
    "v_mul_f32 v130, v197, v130\n"
    "v_mul_f32 v131, v197, v131\n"
    "v_mul_f32 v132, v197, v132\n"
    "v_mul_f32 v133, v197, v133\n"
    "v_mul_f32 v134, v197, v134\n"
    "v_mul_f32 v135, v197, v135\n"
    "v_mul_f32 v136, v197, v136\n"
    "v_mul_f32 v137, v197, v137\n"
    "v_mul_f32 v138, v197, v138\n"
    "v_mul_f32 v139, v197, v139\n"
    "v_mul_f32 v140, v197, v140\n"
    "v_mul_f32 v141, v197, v141\n"
    "v_mul_f32 v142, v197, v142\n"
    "v_mul_f32 v143, v197, v143\n"
    "v_mul_f32 v144, v197, v144\n"
    "v_mul_f32 v145, v197, v145\n"
    "v_mul_f32 v146, v197, v146\n"
    "v_mul_f32 v147, v197, v147\n"
    "v_mul_f32 v148, v197, v148\n"
    "v_mul_f32 v149, v197, v149\n"
    "v_mul_f32 v150, v197, v150\n"
    "v_mul_f32 v151, v197, v151\n"
    "v_mul_f32 v152, v197, v152\n"
    "v_mul_f32 v153, v197, v153\n"
    "v_mul_f32 v154, v197, v154\n"
    "v_mul_f32 v155, v197, v155\n"
    "v_mul_f32 v156, v197, v156\n"
    "v_mul_f32 v157, v197, v157\n"
    "v_mul_f32 v158, v197, v158\n"
    "v_mul_f32 v159, v197, v159\n"
    "v_accvgpr_read_b32 v198, a144\n"
    "v_mul_f32 v198, v197, v198\n"
    "v_accvgpr_write_b32 a144, v198\n"
    "v_accvgpr_read_b32 v198, a145\n"
    "v_mul_f32 v198, v197, v198\n"
    "v_accvgpr_write_b32 a145, v198\n"
    "v_accvgpr_read_b32 v198, a146\n"
    "v_mul_f32 v198, v197, v198\n"
    "v_accvgpr_write_b32 a146, v198\n"
    "v_accvgpr_read_b32 v198, a147\n"
    "v_mul_f32 v198, v197, v198\n"
    "v_accvgpr_write_b32 a147, v198\n"
    "s_nop 1\n"
    "s_branch B_BACK_G_4\n"
    "B_RARE_DD_0:\n"
    "v_mov_b32 v197, v196\n"
    "s_nop 1\n"
    "v_permlane16_swap_b32 v196, v197\n"
    "s_nop 0\n"
    "v_max_f32 v196, v196, v197\n"
    "v_mov_b32 v197, v196\n"
    "s_nop 1\n"
    "v_permlane32_swap_b32 v196, v197\n"
    "s_nop 0\n"
    "v_max3_f32 v196, v196, v197, v184\n"
    "v_sub_f32 v197, v184, v196\n"
    "v_mul_f32 v197, s69, v197\n"
    "v_exp_f32 v197, v197\n"
    "v_mov_b32 v184, v196\n"
    "v_add_f32 v189, 0x423c35a6, v196\n"
    "v_mul_f32 v200, s70, v196\n"
    "v_mul_f32 v0, v197, v0\n"
    "v_mul_f32 v1, v197, v1\n"
    "v_mul_f32 v2, v197, v2\n"
    "v_mul_f32 v3, v197, v3\n"
    "v_mul_f32 v4, v197, v4\n"
    "v_mul_f32 v5, v197, v5\n"
    "v_mul_f32 v6, v197, v6\n"
    "v_mul_f32 v7, v197, v7\n"
    "v_mul_f32 v8, v197, v8\n"
    "v_mul_f32 v9, v197, v9\n"
    "v_mul_f32 v10, v197, v10\n"
    "v_mul_f32 v11, v197, v11\n"
    "v_mul_f32 v12, v197, v12\n"
    "v_mul_f32 v13, v197, v13\n"
    "v_mul_f32 v14, v197, v14\n"
    "v_mul_f32 v15, v197, v15\n"
    "v_mul_f32 v16, v197, v16\n"
    "v_mul_f32 v17, v197, v17\n"
    "v_mul_f32 v18, v197, v18\n"
    "v_mul_f32 v19, v197, v19\n"
    "v_mul_f32 v20, v197, v20\n"
    "v_mul_f32 v21, v197, v21\n"
    "v_mul_f32 v22, v197, v22\n"
    "v_mul_f32 v23, v197, v23\n"
    "v_mul_f32 v24, v197, v24\n"
    "v_mul_f32 v25, v197, v25\n"
    "v_mul_f32 v26, v197, v26\n"
    "v_mul_f32 v27, v197, v27\n"
    "v_mul_f32 v28, v197, v28\n"
    "v_mul_f32 v29, v197, v29\n"
    "v_mul_f32 v30, v197, v30\n"
    "v_mul_f32 v31, v197, v31\n"
    "v_accvgpr_read_b32 v198, a128\n"
    "v_mul_f32 v198, v197, v198\n"
    "v_accvgpr_write_b32 a128, v198\n"
    "v_accvgpr_read_b32 v198, a129\n"
    "v_mul_f32 v198, v197, v198\n"
    "v_accvgpr_write_b32 a129, v198\n"
    "v_accvgpr_read_b32 v198, a130\n"
    "v_mul_f32 v198, v197, v198\n"
    "v_accvgpr_write_b32 a130, v198\n"
    "v_accvgpr_read_b32 v198, a131\n"
    "v_mul_f32 v198, v197, v198\n"
    "v_accvgpr_write_b32 a131, v198\n"
    "s_nop 1\n"
    "s_branch B_BACK_DD_0\n"
    "B_RARE_DD_1:\n"
    "v_mov_b32 v197, v196\n"
    "s_nop 1\n"
    "v_permlane16_swap_b32 v196, v197\n"
    "s_nop 0\n"
    "v_max_f32 v196, v196, v197\n"
    "v_mov_b32 v197, v196\n"
    "s_nop 1\n"
    "v_permlane32_swap_b32 v196, v197\n"
    "s_nop 0\n"
    "v_max3_f32 v196, v196, v197, v185\n"
    "v_sub_f32 v197, v185, v196\n"
    "v_mul_f32 v197, s69, v197\n"
    "v_exp_f32 v197, v197\n"
    "v_mov_b32 v185, v196\n"
    "v_add_f32 v190, 0x423c35a6, v196\n"
    "v_mul_f32 v201, s70, v196\n"
    "v_mul_f32 v32, v197, v32\n"
    "v_mul_f32 v33, v197, v33\n"
    "v_mul_f32 v34, v197, v34\n"
    "v_mul_f32 v35, v197, v35\n"
    "v_mul_f32 v36, v197, v36\n"
    "v_mul_f32 v37, v197, v37\n"
    "v_mul_f32 v38, v197, v38\n"
    "v_mul_f32 v39, v197, v39\n"
    "v_mul_f32 v40, v197, v40\n"
    "v_mul_f32 v41, v197, v41\n"
    "v_mul_f32 v42, v197, v42\n"
    "v_mul_f32 v43, v197, v43\n"
    "v_mul_f32 v44, v197, v44\n"
    "v_mul_f32 v45, v197, v45\n"
    "v_mul_f32 v46, v197, v46\n"
    "v_mul_f32 v47, v197, v47\n"
    "v_mul_f32 v48, v197, v48\n"
    "v_mul_f32 v49, v197, v49\n"
    "v_mul_f32 v50, v197, v50\n"
    "v_mul_f32 v51, v197, v51\n"
    "v_mul_f32 v52, v197, v52\n"
    "v_mul_f32 v53, v197, v53\n"
    "v_mul_f32 v54, v197, v54\n"
    "v_mul_f32 v55, v197, v55\n"
    "v_mul_f32 v56, v197, v56\n"
    "v_mul_f32 v57, v197, v57\n"
    "v_mul_f32 v58, v197, v58\n"
    "v_mul_f32 v59, v197, v59\n"
    "v_mul_f32 v60, v197, v60\n"
    "v_mul_f32 v61, v197, v61\n"
    "v_mul_f32 v62, v197, v62\n"
    "v_mul_f32 v63, v197, v63\n"
    "v_accvgpr_read_b32 v198, a132\n"
    "v_mul_f32 v198, v197, v198\n"
    "v_accvgpr_write_b32 a132, v198\n"
    "v_accvgpr_read_b32 v198, a133\n"
    "v_mul_f32 v198, v197, v198\n"
    "v_accvgpr_write_b32 a133, v198\n"
    "v_accvgpr_read_b32 v198, a134\n"
    "v_mul_f32 v198, v197, v198\n"
    "v_accvgpr_write_b32 a134, v198\n"
    "v_accvgpr_read_b32 v198, a135\n"
    "v_mul_f32 v198, v197, v198\n"
    "v_accvgpr_write_b32 a135, v198\n"
    "s_nop 1\n"
    "s_branch B_BACK_DD_1\n"
    "B_RARE_DD_2:\n"
    "v_mov_b32 v197, v196\n"
    "s_nop 1\n"
    "v_permlane16_swap_b32 v196, v197\n"
    "s_nop 0\n"
    "v_max_f32 v196, v196, v197\n"
    "v_mov_b32 v197, v196\n"
    "s_nop 1\n"
    "v_permlane32_swap_b32 v196, v197\n"
    "s_nop 0\n"
    "v_max3_f32 v196, v196, v197, v186\n"
    "v_sub_f32 v197, v186, v196\n"
    "v_mul_f32 v197, s69, v197\n"
    "v_exp_f32 v197, v197\n"
    "v_mov_b32 v186, v196\n"
    "v_add_f32 v191, 0x423c35a6, v196\n"
    "v_mul_f32 v202, s70, v196\n"
    "v_mul_f32 v64, v197, v64\n"
    "v_mul_f32 v65, v197, v65\n"
    "v_mul_f32 v66, v197, v66\n"
    "v_mul_f32 v67, v197, v67\n"
    "v_mul_f32 v68, v197, v68\n"
    "v_mul_f32 v69, v197, v69\n"
    "v_mul_f32 v70, v197, v70\n"
    "v_mul_f32 v71, v197, v71\n"
    "v_mul_f32 v72, v197, v72\n"
    "v_mul_f32 v73, v197, v73\n"
    "v_mul_f32 v74, v197, v74\n"
    "v_mul_f32 v75, v197, v75\n"
    "v_mul_f32 v76, v197, v76\n"
    "v_mul_f32 v77, v197, v77\n"
    "v_mul_f32 v78, v197, v78\n"
    "v_mul_f32 v79, v197, v79\n"
    "v_mul_f32 v80, v197, v80\n"
    "v_mul_f32 v81, v197, v81\n"
    "v_mul_f32 v82, v197, v82\n"
    "v_mul_f32 v83, v197, v83\n"
    "v_mul_f32 v84, v197, v84\n"
    "v_mul_f32 v85, v197, v85\n"
    "v_mul_f32 v86, v197, v86\n"
    "v_mul_f32 v87, v197, v87\n"
    "v_mul_f32 v88, v197, v88\n"
    "v_mul_f32 v89, v197, v89\n"
    "v_mul_f32 v90, v197, v90\n"
    "v_mul_f32 v91, v197, v91\n"
    "v_mul_f32 v92, v197, v92\n"
    "v_mul_f32 v93, v197, v93\n"
    "v_mul_f32 v94, v197, v94\n"
    "v_mul_f32 v95, v197, v95\n"
    "v_accvgpr_read_b32 v198, a136\n"
    "v_mul_f32 v198, v197, v198\n"
    "v_accvgpr_write_b32 a136, v198\n"
    "v_accvgpr_read_b32 v198, a137\n"
    "v_mul_f32 v198, v197, v198\n"
    "v_accvgpr_write_b32 a137, v198\n"
    "v_accvgpr_read_b32 v198, a138\n"
    "v_mul_f32 v198, v197, v198\n"
    "v_accvgpr_write_b32 a138, v198\n"
    "v_accvgpr_read_b32 v198, a139\n"
    "v_mul_f32 v198, v197, v198\n"
    "v_accvgpr_write_b32 a139, v198\n"
    "s_nop 1\n"
    "s_branch B_BACK_DD_2\n"
    "B_RARE_DD_3:\n"
    "v_mov_b32 v197, v196\n"
    "s_nop 1\n"
    "v_permlane16_swap_b32 v196, v197\n"
    "s_nop 0\n"
    "v_max_f32 v196, v196, v197\n"
    "v_mov_b32 v197, v196\n"
    "s_nop 1\n"
    "v_permlane32_swap_b32 v196, v197\n"
    "s_nop 0\n"
    "v_max3_f32 v196, v196, v197, v187\n"
    "v_sub_f32 v197, v187, v196\n"
    "v_mul_f32 v197, s69, v197\n"
    "v_exp_f32 v197, v197\n"
    "v_mov_b32 v187, v196\n"
    "v_add_f32 v192, 0x423c35a6, v196\n"
    "v_mul_f32 v203, s70, v196\n"
    "v_mul_f32 v96, v197, v96\n"
    "v_mul_f32 v97, v197, v97\n"
    "v_mul_f32 v98, v197, v98\n"
    "v_mul_f32 v99, v197, v99\n"
    "v_mul_f32 v100, v197, v100\n"
    "v_mul_f32 v101, v197, v101\n"
    "v_mul_f32 v102, v197, v102\n"
    "v_mul_f32 v103, v197, v103\n"
    "v_mul_f32 v104, v197, v104\n"
    "v_mul_f32 v105, v197, v105\n"
    "v_mul_f32 v106, v197, v106\n"
    "v_mul_f32 v107, v197, v107\n"
    "v_mul_f32 v108, v197, v108\n"
    "v_mul_f32 v109, v197, v109\n"
    "v_mul_f32 v110, v197, v110\n"
    "v_mul_f32 v111, v197, v111\n"
    "v_mul_f32 v112, v197, v112\n"
    "v_mul_f32 v113, v197, v113\n"
    "v_mul_f32 v114, v197, v114\n"
    "v_mul_f32 v115, v197, v115\n"
    "v_mul_f32 v116, v197, v116\n"
    "v_mul_f32 v117, v197, v117\n"
    "v_mul_f32 v118, v197, v118\n"
    "v_mul_f32 v119, v197, v119\n"
    "v_mul_f32 v120, v197, v120\n"
    "v_mul_f32 v121, v197, v121\n"
    "v_mul_f32 v122, v197, v122\n"
    "v_mul_f32 v123, v197, v123\n"
    "v_mul_f32 v124, v197, v124\n"
    "v_mul_f32 v125, v197, v125\n"
    "v_mul_f32 v126, v197, v126\n"
    "v_mul_f32 v127, v197, v127\n"
    "v_accvgpr_read_b32 v198, a140\n"
    "v_mul_f32 v198, v197, v198\n"
    "v_accvgpr_write_b32 a140, v198\n"
    "v_accvgpr_read_b32 v198, a141\n"
    "v_mul_f32 v198, v197, v198\n"
    "v_accvgpr_write_b32 a141, v198\n"
    "v_accvgpr_read_b32 v198, a142\n"
    "v_mul_f32 v198, v197, v198\n"
    "v_accvgpr_write_b32 a142, v198\n"
    "v_accvgpr_read_b32 v198, a143\n"
    "v_mul_f32 v198, v197, v198\n"
    "v_accvgpr_write_b32 a143, v198\n"
    "s_nop 1\n"
    "s_branch B_BACK_DD_3\n"
    "B_RARE_DD_4:\n"
    "v_mov_b32 v197, v196\n"
    "s_nop 1\n"
    "v_permlane16_swap_b32 v196, v197\n"
    "s_nop 0\n"
    "v_max_f32 v196, v196, v197\n"
    "v_mov_b32 v197, v196\n"
    "s_nop 1\n"
    "v_permlane32_swap_b32 v196, v197\n"
    "s_nop 0\n"
    "v_max3_f32 v196, v196, v197, v188\n"
    "v_sub_f32 v197, v188, v196\n"
    "v_mul_f32 v197, s69, v197\n"
    "v_exp_f32 v197, v197\n"
    "v_mov_b32 v188, v196\n"
    "v_add_f32 v193, 0x423c35a6, v196\n"
    "v_mul_f32 v204, s70, v196\n"
    "v_mul_f32 v128, v197, v128\n"
    "v_mul_f32 v129, v197, v129\n"
    "v_mul_f32 v130, v197, v130\n"
    "v_mul_f32 v131, v197, v131\n"
    "v_mul_f32 v132, v197, v132\n"
    "v_mul_f32 v133, v197, v133\n"
    "v_mul_f32 v134, v197, v134\n"
    "v_mul_f32 v135, v197, v135\n"
    "v_mul_f32 v136, v197, v136\n"
    "v_mul_f32 v137, v197, v137\n"
    "v_mul_f32 v138, v197, v138\n"
    "v_mul_f32 v139, v197, v139\n"
    "v_mul_f32 v140, v197, v140\n"
    "v_mul_f32 v141, v197, v141\n"
    "v_mul_f32 v142, v197, v142\n"
    "v_mul_f32 v143, v197, v143\n"
    "v_mul_f32 v144, v197, v144\n"
    "v_mul_f32 v145, v197, v145\n"
    "v_mul_f32 v146, v197, v146\n"
    "v_mul_f32 v147, v197, v147\n"
    "v_mul_f32 v148, v197, v148\n"
    "v_mul_f32 v149, v197, v149\n"
    "v_mul_f32 v150, v197, v150\n"
    "v_mul_f32 v151, v197, v151\n"
    "v_mul_f32 v152, v197, v152\n"
    "v_mul_f32 v153, v197, v153\n"
    "v_mul_f32 v154, v197, v154\n"
    "v_mul_f32 v155, v197, v155\n"
    "v_mul_f32 v156, v197, v156\n"
    "v_mul_f32 v157, v197, v157\n"
    "v_mul_f32 v158, v197, v158\n"
    "v_mul_f32 v159, v197, v159\n"
    "v_accvgpr_read_b32 v198, a144\n"
    "v_mul_f32 v198, v197, v198\n"
    "v_accvgpr_write_b32 a144, v198\n"
    "v_accvgpr_read_b32 v198, a145\n"
    "v_mul_f32 v198, v197, v198\n"
    "v_accvgpr_write_b32 a145, v198\n"
    "v_accvgpr_read_b32 v198, a146\n"
    "v_mul_f32 v198, v197, v198\n"
    "v_accvgpr_write_b32 a146, v198\n"
    "v_accvgpr_read_b32 v198, a147\n"
    "v_mul_f32 v198, v197, v198\n"
    "v_accvgpr_write_b32 a147, v198\n"
    "s_nop 1\n"
    "s_branch B_BACK_DD_4\n"
    "B_RARE_DP_0:\n"
    "v_mov_b32 v197, v196\n"
    "s_nop 1\n"
    "v_permlane16_swap_b32 v196, v197\n"
    "s_nop 0\n"
    "v_max_f32 v196, v196, v197\n"
    "v_mov_b32 v197, v196\n"
    "s_nop 1\n"
    "v_permlane32_swap_b32 v196, v197\n"
    "s_nop 0\n"
    "v_max3_f32 v196, v196, v197, v184\n"
    "v_sub_f32 v197, v184, v196\n"
    "v_mul_f32 v197, s69, v197\n"
    "v_exp_f32 v197, v197\n"
    "v_mov_b32 v184, v196\n"
    "v_add_f32 v189, 0x423c35a6, v196\n"
    "v_mul_f32 v200, s70, v196\n"
    "v_mul_f32 v0, v197, v0\n"
    "v_mul_f32 v1, v197, v1\n"
    "v_mul_f32 v2, v197, v2\n"
    "v_mul_f32 v3, v197, v3\n"
    "v_mul_f32 v4, v197, v4\n"
    "v_mul_f32 v5, v197, v5\n"
    "v_mul_f32 v6, v197, v6\n"
    "v_mul_f32 v7, v197, v7\n"
    "v_mul_f32 v8, v197, v8\n"
    "v_mul_f32 v9, v197, v9\n"
    "v_mul_f32 v10, v197, v10\n"
    "v_mul_f32 v11, v197, v11\n"
    "v_mul_f32 v12, v197, v12\n"
    "v_mul_f32 v13, v197, v13\n"
    "v_mul_f32 v14, v197, v14\n"
    "v_mul_f32 v15, v197, v15\n"
    "v_mul_f32 v16, v197, v16\n"
    "v_mul_f32 v17, v197, v17\n"
    "v_mul_f32 v18, v197, v18\n"
    "v_mul_f32 v19, v197, v19\n"
    "v_mul_f32 v20, v197, v20\n"
    "v_mul_f32 v21, v197, v21\n"
    "v_mul_f32 v22, v197, v22\n"
    "v_mul_f32 v23, v197, v23\n"
    "v_mul_f32 v24, v197, v24\n"
    "v_mul_f32 v25, v197, v25\n"
    "v_mul_f32 v26, v197, v26\n"
    "v_mul_f32 v27, v197, v27\n"
    "v_mul_f32 v28, v197, v28\n"
    "v_mul_f32 v29, v197, v29\n"
    "v_mul_f32 v30, v197, v30\n"
    "v_mul_f32 v31, v197, v31\n"
    "v_accvgpr_read_b32 v198, a128\n"
    "v_mul_f32 v198, v197, v198\n"
    "v_accvgpr_write_b32 a128, v198\n"
    "v_accvgpr_read_b32 v198, a129\n"
    "v_mul_f32 v198, v197, v198\n"
    "v_accvgpr_write_b32 a129, v198\n"
    "v_accvgpr_read_b32 v198, a130\n"
    "v_mul_f32 v198, v197, v198\n"
    "v_accvgpr_write_b32 a130, v198\n"
    "v_accvgpr_read_b32 v198, a131\n"
    "v_mul_f32 v198, v197, v198\n"
    "v_accvgpr_write_b32 a131, v198\n"
    "s_nop 1\n"
    "s_branch B_BACK_DP_0\n"
    "B_RARE_DP_1:\n"
    "v_mov_b32 v197, v196\n"
    "s_nop 1\n"
    "v_permlane16_swap_b32 v196, v197\n"
    "s_nop 0\n"
    "v_max_f32 v196, v196, v197\n"
    "v_mov_b32 v197, v196\n"
    "s_nop 1\n"
    "v_permlane32_swap_b32 v196, v197\n"
    "s_nop 0\n"
    "v_max3_f32 v196, v196, v197, v185\n"
    "v_sub_f32 v197, v185, v196\n"
    "v_mul_f32 v197, s69, v197\n"
    "v_exp_f32 v197, v197\n"
    "v_mov_b32 v185, v196\n"
    "v_add_f32 v190, 0x423c35a6, v196\n"
    "v_mul_f32 v201, s70, v196\n"
    "v_mul_f32 v32, v197, v32\n"
    "v_mul_f32 v33, v197, v33\n"
    "v_mul_f32 v34, v197, v34\n"
    "v_mul_f32 v35, v197, v35\n"
    "v_mul_f32 v36, v197, v36\n"
    "v_mul_f32 v37, v197, v37\n"
    "v_mul_f32 v38, v197, v38\n"
    "v_mul_f32 v39, v197, v39\n"
    "v_mul_f32 v40, v197, v40\n"
    "v_mul_f32 v41, v197, v41\n"
    "v_mul_f32 v42, v197, v42\n"
    "v_mul_f32 v43, v197, v43\n"
    "v_mul_f32 v44, v197, v44\n"
    "v_mul_f32 v45, v197, v45\n"
    "v_mul_f32 v46, v197, v46\n"
    "v_mul_f32 v47, v197, v47\n"
    "v_mul_f32 v48, v197, v48\n"
    "v_mul_f32 v49, v197, v49\n"
    "v_mul_f32 v50, v197, v50\n"
    "v_mul_f32 v51, v197, v51\n"
    "v_mul_f32 v52, v197, v52\n"
    "v_mul_f32 v53, v197, v53\n"
    "v_mul_f32 v54, v197, v54\n"
    "v_mul_f32 v55, v197, v55\n"
    "v_mul_f32 v56, v197, v56\n"
    "v_mul_f32 v57, v197, v57\n"
    "v_mul_f32 v58, v197, v58\n"
    "v_mul_f32 v59, v197, v59\n"
    "v_mul_f32 v60, v197, v60\n"
    "v_mul_f32 v61, v197, v61\n"
    "v_mul_f32 v62, v197, v62\n"
    "v_mul_f32 v63, v197, v63\n"
    "v_accvgpr_read_b32 v198, a132\n"
    "v_mul_f32 v198, v197, v198\n"
    "v_accvgpr_write_b32 a132, v198\n"
    "v_accvgpr_read_b32 v198, a133\n"
    "v_mul_f32 v198, v197, v198\n"
    "v_accvgpr_write_b32 a133, v198\n"
    "v_accvgpr_read_b32 v198, a134\n"
    "v_mul_f32 v198, v197, v198\n"
    "v_accvgpr_write_b32 a134, v198\n"
    "v_accvgpr_read_b32 v198, a135\n"
    "v_mul_f32 v198, v197, v198\n"
    "v_accvgpr_write_b32 a135, v198\n"
    "s_nop 1\n"
    "s_branch B_BACK_DP_1\n"
    "B_RARE_DP_2:\n"
    "v_mov_b32 v197, v196\n"
    "s_nop 1\n"
    "v_permlane16_swap_b32 v196, v197\n"
    "s_nop 0\n"
    "v_max_f32 v196, v196, v197\n"
    "v_mov_b32 v197, v196\n"
    "s_nop 1\n"
    "v_permlane32_swap_b32 v196, v197\n"
    "s_nop 0\n"
    "v_max3_f32 v196, v196, v197, v186\n"
    "v_sub_f32 v197, v186, v196\n"
    "v_mul_f32 v197, s69, v197\n"
    "v_exp_f32 v197, v197\n"
    "v_mov_b32 v186, v196\n"
    "v_add_f32 v191, 0x423c35a6, v196\n"
    "v_mul_f32 v202, s70, v196\n"
    "v_mul_f32 v64, v197, v64\n"
    "v_mul_f32 v65, v197, v65\n"
    "v_mul_f32 v66, v197, v66\n"
    "v_mul_f32 v67, v197, v67\n"
    "v_mul_f32 v68, v197, v68\n"
    "v_mul_f32 v69, v197, v69\n"
    "v_mul_f32 v70, v197, v70\n"
    "v_mul_f32 v71, v197, v71\n"
    "v_mul_f32 v72, v197, v72\n"
    "v_mul_f32 v73, v197, v73\n"
    "v_mul_f32 v74, v197, v74\n"
    "v_mul_f32 v75, v197, v75\n"
    "v_mul_f32 v76, v197, v76\n"
    "v_mul_f32 v77, v197, v77\n"
    "v_mul_f32 v78, v197, v78\n"
    "v_mul_f32 v79, v197, v79\n"
    "v_mul_f32 v80, v197, v80\n"
    "v_mul_f32 v81, v197, v81\n"
    "v_mul_f32 v82, v197, v82\n"
    "v_mul_f32 v83, v197, v83\n"
    "v_mul_f32 v84, v197, v84\n"
    "v_mul_f32 v85, v197, v85\n"
    "v_mul_f32 v86, v197, v86\n"
    "v_mul_f32 v87, v197, v87\n"
    "v_mul_f32 v88, v197, v88\n"
    "v_mul_f32 v89, v197, v89\n"
    "v_mul_f32 v90, v197, v90\n"
    "v_mul_f32 v91, v197, v91\n"
    "v_mul_f32 v92, v197, v92\n"
    "v_mul_f32 v93, v197, v93\n"
    "v_mul_f32 v94, v197, v94\n"
    "v_mul_f32 v95, v197, v95\n"
    "v_accvgpr_read_b32 v198, a136\n"
    "v_mul_f32 v198, v197, v198\n"
    "v_accvgpr_write_b32 a136, v198\n"
    "v_accvgpr_read_b32 v198, a137\n"
    "v_mul_f32 v198, v197, v198\n"
    "v_accvgpr_write_b32 a137, v198\n"
    "v_accvgpr_read_b32 v198, a138\n"
    "v_mul_f32 v198, v197, v198\n"
    "v_accvgpr_write_b32 a138, v198\n"
    "v_accvgpr_read_b32 v198, a139\n"
    "v_mul_f32 v198, v197, v198\n"
    "v_accvgpr_write_b32 a139, v198\n"
    "s_nop 1\n"
    "s_branch B_BACK_DP_2\n"
    "B_RARE_DP_3:\n"
    "v_mov_b32 v197, v196\n"
    "s_nop 1\n"
    "v_permlane16_swap_b32 v196, v197\n"
    "s_nop 0\n"
    "v_max_f32 v196, v196, v197\n"
    "v_mov_b32 v197, v196\n"
    "s_nop 1\n"
    "v_permlane32_swap_b32 v196, v197\n"
    "s_nop 0\n"
    "v_max3_f32 v196, v196, v197, v187\n"
    "v_sub_f32 v197, v187, v196\n"
    "v_mul_f32 v197, s69, v197\n"
    "v_exp_f32 v197, v197\n"
    "v_mov_b32 v187, v196\n"
    "v_add_f32 v192, 0x423c35a6, v196\n"
    "v_mul_f32 v203, s70, v196\n"
    "v_mul_f32 v96, v197, v96\n"
    "v_mul_f32 v97, v197, v97\n"
    "v_mul_f32 v98, v197, v98\n"
    "v_mul_f32 v99, v197, v99\n"
    "v_mul_f32 v100, v197, v100\n"
    "v_mul_f32 v101, v197, v101\n"
    "v_mul_f32 v102, v197, v102\n"
    "v_mul_f32 v103, v197, v103\n"
    "v_mul_f32 v104, v197, v104\n"
    "v_mul_f32 v105, v197, v105\n"
    "v_mul_f32 v106, v197, v106\n"
    "v_mul_f32 v107, v197, v107\n"
    "v_mul_f32 v108, v197, v108\n"
    "v_mul_f32 v109, v197, v109\n"
    "v_mul_f32 v110, v197, v110\n"
    "v_mul_f32 v111, v197, v111\n"
    "v_mul_f32 v112, v197, v112\n"
    "v_mul_f32 v113, v197, v113\n"
    "v_mul_f32 v114, v197, v114\n"
    "v_mul_f32 v115, v197, v115\n"
    "v_mul_f32 v116, v197, v116\n"
    "v_mul_f32 v117, v197, v117\n"
    "v_mul_f32 v118, v197, v118\n"
    "v_mul_f32 v119, v197, v119\n"
    "v_mul_f32 v120, v197, v120\n"
    "v_mul_f32 v121, v197, v121\n"
    "v_mul_f32 v122, v197, v122\n"
    "v_mul_f32 v123, v197, v123\n"
    "v_mul_f32 v124, v197, v124\n"
    "v_mul_f32 v125, v197, v125\n"
    "v_mul_f32 v126, v197, v126\n"
    "v_mul_f32 v127, v197, v127\n"
    "v_accvgpr_read_b32 v198, a140\n"
    "v_mul_f32 v198, v197, v198\n"
    "v_accvgpr_write_b32 a140, v198\n"
    "v_accvgpr_read_b32 v198, a141\n"
    "v_mul_f32 v198, v197, v198\n"
    "v_accvgpr_write_b32 a141, v198\n"
    "v_accvgpr_read_b32 v198, a142\n"
    "v_mul_f32 v198, v197, v198\n"
    "v_accvgpr_write_b32 a142, v198\n"
    "v_accvgpr_read_b32 v198, a143\n"
    "v_mul_f32 v198, v197, v198\n"
    "v_accvgpr_write_b32 a143, v198\n"
    "s_nop 1\n"
    "s_branch B_BACK_DP_3\n"
    "B_RARE_DP_4:\n"
    "v_mov_b32 v197, v196\n"
    "s_nop 1\n"
    "v_permlane16_swap_b32 v196, v197\n"
    "s_nop 0\n"
    "v_max_f32 v196, v196, v197\n"
    "v_mov_b32 v197, v196\n"
    "s_nop 1\n"
    "v_permlane32_swap_b32 v196, v197\n"
    "s_nop 0\n"
    "v_max3_f32 v196, v196, v197, v188\n"
    "v_sub_f32 v197, v188, v196\n"
    "v_mul_f32 v197, s69, v197\n"
    "v_exp_f32 v197, v197\n"
    "v_mov_b32 v188, v196\n"
    "v_add_f32 v193, 0x423c35a6, v196\n"
    "v_mul_f32 v204, s70, v196\n"
    "v_mul_f32 v128, v197, v128\n"
    "v_mul_f32 v129, v197, v129\n"
    "v_mul_f32 v130, v197, v130\n"
    "v_mul_f32 v131, v197, v131\n"
    "v_mul_f32 v132, v197, v132\n"
    "v_mul_f32 v133, v197, v133\n"
    "v_mul_f32 v134, v197, v134\n"
    "v_mul_f32 v135, v197, v135\n"
    "v_mul_f32 v136, v197, v136\n"
    "v_mul_f32 v137, v197, v137\n"
    "v_mul_f32 v138, v197, v138\n"
    "v_mul_f32 v139, v197, v139\n"
    "v_mul_f32 v140, v197, v140\n"
    "v_mul_f32 v141, v197, v141\n"
    "v_mul_f32 v142, v197, v142\n"
    "v_mul_f32 v143, v197, v143\n"
    "v_mul_f32 v144, v197, v144\n"
    "v_mul_f32 v145, v197, v145\n"
    "v_mul_f32 v146, v197, v146\n"
    "v_mul_f32 v147, v197, v147\n"
    "v_mul_f32 v148, v197, v148\n"
    "v_mul_f32 v149, v197, v149\n"
    "v_mul_f32 v150, v197, v150\n"
    "v_mul_f32 v151, v197, v151\n"
    "v_mul_f32 v152, v197, v152\n"
    "v_mul_f32 v153, v197, v153\n"
    "v_mul_f32 v154, v197, v154\n"
    "v_mul_f32 v155, v197, v155\n"
    "v_mul_f32 v156, v197, v156\n"
    "v_mul_f32 v157, v197, v157\n"
    "v_mul_f32 v158, v197, v158\n"
    "v_mul_f32 v159, v197, v159\n"
    "v_accvgpr_read_b32 v198, a144\n"
    "v_mul_f32 v198, v197, v198\n"
    "v_accvgpr_write_b32 a144, v198\n"
    "v_accvgpr_read_b32 v198, a145\n"
    "v_mul_f32 v198, v197, v198\n"
    "v_accvgpr_write_b32 a145, v198\n"
    "v_accvgpr_read_b32 v198, a146\n"
    "v_mul_f32 v198, v197, v198\n"
    "v_accvgpr_write_b32 a146, v198\n"
    "v_accvgpr_read_b32 v198, a147\n"
    "v_mul_f32 v198, v197, v198\n"
    "v_accvgpr_write_b32 a147, v198\n"
    "s_nop 1\n"
    "s_branch B_BACK_DP_4\n"
    "B_END:\n"
    ".p2align 12\n"
    "A_START:\n"
    "v_mbcnt_lo_u32_b32 v239, -1, 0\n"
    "v_mbcnt_hi_u32_b32 v239, -1, v239\n"
    "s_mov_b32 s72, %2\n"
    "s_mul_i32 s71, s72, 0xa000\n"
    "s_sub_u32 s79, s34, 9\n"
    "s_cselect_b32 s79, s34, s79\n"
    "s_lshl_b32 s76, s72, 2\n"
    "s_add_u32 s76, s76, 0\n"
    "s_min_u32 s76, s76, 14\n"
    "s_mul_i32 s77, s76, 52\n"
    "s_lshr_b32 s77, s77, 8\n"
    "s_mul_i32 s28, s77, 5\n"
    "s_sub_u32 s76, s76, s28\n"
    "s_mul_i32 s76, s76, 0x4800\n"
    "s_add_u32 s84, s76, 0x480\n"
    "s_add_u32 s77, s77, s79\n"
    "s_lshl_b32 s77, s77, 7\n"
    "s_add_u32 s28, s76, s77\n"
    "s_lshl_b32 s76, s72, 2\n"
    "s_add_u32 s76, s76, 1\n"
    "s_min_u32 s76, s76, 14\n"
    "s_mul_i32 s77, s76, 52\n"
    "s_lshr_b32 s77, s77, 8\n"
    "s_mul_i32 s29, s77, 5\n"
    "s_sub_u32 s76, s76, s29\n"
    "s_mul_i32 s76, s76, 0x4800\n"
    "s_add_u32 s85, s76, 0x480\n"
    "s_add_u32 s77, s77, s79\n"
    "s_lshl_b32 s77, s77, 7\n"
    "s_add_u32 s29, s76, s77\n"
    "s_lshl_b32 s76, s72, 2\n"
    "s_add_u32 s76, s76, 2\n"
    "s_min_u32 s76, s76, 14\n"
    "s_mul_i32 s77, s76, 52\n"
    "s_lshr_b32 s77, s77, 8\n"
    "s_mul_i32 s30, s77, 5\n"
    "s_sub_u32 s76, s76, s30\n"
    "s_mul_i32 s76, s76, 0x4800\n"
    "s_add_u32 s87, s76, 0x480\n"
    "s_add_u32 s77, s77, s79\n"
    "s_lshl_b32 s77, s77, 7\n"
    "s_add_u32 s30, s76, s77\n"
    "s_lshl_b32 s76, s72, 2\n"
    "s_add_u32 s76, s76, 3\n"
    "s_min_u32 s76, s76, 14\n"
    "s_mul_i32 s77, s76, 52\n"
    "s_lshr_b32 s77, s77, 8\n"
    "s_mul_i32 s31, s77, 5\n"
    "s_sub_u32 s76, s76, s31\n"
    "s_mul_i32 s76, s76, 0x4800\n"
    "s_add_u32 s88, s76, 0x480\n"
    "s_add_u32 s77, s77, s79\n"
    "s_lshl_b32 s77, s77, 7\n"
    "s_add_u32 s31, s76, s77\n"
    "s_lshl_b32 s73, s79, 19\n"
    "s_lshl_b32 s78, s35, 2\n"
    "s_add_u32 s78, s78, s72\n"
    "s_lshl_b32 s90, s78, 8\n"
    "s_mul_i32 s89, s34, 0x16800\n"
    "s_lshr_b32 s78, s78, 1\n"
    "s_lshl_b32 s76, s34, 4\n"
    "s_add_u32 s81, s78, s76\n"
    "s_memrealtime s[56:57]\n"
    "s_waitcnt lgkmcnt(0)\n"
    "s_mov_b32 s83, s56\n"
    "s_add_u32 s36, s20, s90\n"
    "s_addc_u32 s37, s21, 0\n"
    "s_and_b32 s37, s37, 0xffff\n"
    "s_mov_b32 s38, 0x47e100\n"
    "s_mov_b32 s39, 0x20000\n"
    "v_lshrrev_b32 v209, 1, v239\n"
    "v_lshlrev_b32 v209, 13, v209\n"
    "v_and_b32 v210, 1, v239\n"
    "v_lshl_add_u32 v209, v210, 7, v209\n"
    "s_mul_i32 s76, s34, 0x48000\n"
    "s_add_u32 s77, s76, 0x40000\n"
    "buffer_load_dword v0, v209, s[36:39], s76 offen\n"
    "v_cmp_gt_u32 vcc, 8, v239\n"
    "v_mov_b32 v213, 0x40000000\n"
    "s_nop 0\n"
    "v_cndmask_b32 v210, v213, v209, vcc\n"
    "buffer_load_dword v1, v210, s[36:39], s77 offen\n"
    "s_add_u32 s92, s14, s89\n"
    "s_addc_u32 s93, s15, 0\n"
    "s_and_b32 s93, s93, 0xffff\n"
    "s_mov_b32 s94, 0x16800\n"
    "s_mov_b32 s95, 0x20000\n"
    "s_mul_i32 s76, s72, 0x78\n"
    "s_add_u32 s77, s79, 3\n"
    "v_add_u32 v209, s76, v239\n"
    "v_mul_u32_u24 v210, 0x2aab, v209\n"
    "v_lshrrev_b32 v210, 16, v210\n"
    "v_mul_u32_u24 v211, 6, v210\n"
    "v_sub_u32 v209, v209, v211\n"
    "v_add_u32 v209, s77, v209\n"
    "v_subrev_u32 v211, 9, v209\n"
    "v_min_u32 v209, v209, v211\n"
    "v_lshlrev_b32 v209, 7, v209\n"
    "v_mul_u32_u24 v210, 0x480, v210\n"
    "v_add_u32 v209, v209, v210\n"
    "buffer_load_dword v2, v209, s[92:95], 0 offen\n"
    "s_add_u32 s76, s76, 64\n"
    "v_add_u32 v209, s76, v239\n"
    "v_mul_u32_u24 v210, 0x2aab, v209\n"
    "v_lshrrev_b32 v210, 16, v210\n"
    "v_mul_u32_u24 v211, 6, v210\n"
    "v_sub_u32 v209, v209, v211\n"
    "v_add_u32 v209, s77, v209\n"
    "v_subrev_u32 v211, 9, v209\n"
    "v_min_u32 v209, v209, v211\n"
    "v_lshlrev_b32 v209, 7, v209\n"
    "v_mul_u32_u24 v210, 0x480, v210\n"
    "v_add_u32 v209, v209, v210\n"
    "v_cmp_gt_u32 vcc, 56, v239\n"
    "v_mov_b32 v213, 0x40000000\n"
    "s_nop 0\n"
    "v_cndmask_b32 v209, v213, v209, vcc\n"
    "buffer_load_dword v3, v209, s[92:95], 0 offen\n"
    "s_add_u32 s76, s76, 64\n"
    "s_add_u32 s92, s4, s89\n"
    "s_addc_u32 s93, s5, 0\n"
    "s_and_b32 s93, s93, 0xffff\n"
    "s_mov_b32 s94, 0x16800\n"
    "s_mov_b32 s95, 0x20000\n"
    "v_lshrrev_b32 v209, 3, v239\n"
    "v_and_b32 v210, 7, v239\n"
    "v_lshrrev_b32 v211, 4, v239\n"
    "v_xor_b32 v210, v210, v211\n"
    "v_lshlrev_b32 v210, 4, v210\n"
    "v_mul_u32_u24 v209, 0x480, v209\n"
    "v_add_u32 v206, v209, v210\n"
    "v_xor_b32 v210, 64, v210\n"
    "v_add_u32 v209, 0x2000, v209\n"
    "v_add_u32 v207, v209, v210\n"
    "s_add_u32 s36, s10, s90\n"
    "s_addc_u32 s37, s11, 0\n"
    "s_and_b32 s37, s37, 0xffff\n"
    "s_mov_b32 s38, 0x47e100\n"
    "s_mov_b32 s39, 0x20000\n"
    "s_mov_b32 s60, 0x2000\n"
    "s_mov_b32 s61, 0x4000\n"
    "s_mov_b32 s62, 0x6000\n"
    "s_mov_b32 s63, 0x8000\n"
    "s_mov_b32 s64, 0xa000\n"
    "s_mov_b32 s65, 0xc000\n"
    "s_mov_b32 s66, 0xe000\n"
    "v_lshrrev_b32 v209, 4, v239\n"
    "v_and_b32 v210, 15, v239\n"
    "v_lshlrev_b32 v209, 16, v209\n"
    "v_lshl_add_u32 v200, v210, 4, v209\n"
    "s_getpc_b64 s[56:57]\n"
    "A_PC:\n"
    "s_and_b32 s57, s57, 0xffff\n"
    "s_mov_b32 s58, A_END-A_PC\n"
    "s_mov_b32 s59, 0x20000\n"
    "v_lshlrev_b32 v209, 13, v239\n"
    "s_lshl_b32 s76, s34, 2\n"
    "s_add_u32 s76, s76, s72\n"
    "s_lshl_b32 s76, s76, 7\n"
    "buffer_load_dword v252, v209, s[56:59], s76 offen\n"
    "s_lshl_b32 s76, s81, 17\n"
    "s_add_u32 s40, s6, s76\n"
    "s_addc_u32 s41, s7, 0\n"
    "s_and_b32 s41, s41, 0xffff\n"
    "s_mov_b32 s42, 0x20000\n"
    "s_mov_b32 s43, 0x20000\n"
    "s_add_u32 s44, s8, s76\n"
    "s_addc_u32 s45, s9, 0\n"
    "s_and_b32 s45, s45, 0xffff\n"
    "s_mov_b32 s46, 0x20000\n"
    "s_mov_b32 s47, 0x20000\n"
    "s_mul_i32 s82, s35, 0x90000\n"
    "s_add_u32 s52, s12, s82\n"
    "s_addc_u32 s53, s13, 0\n"
    "s_and_b32 s53, s53, 0xffff\n"
    "s_mov_b32 s54, 0x90000\n"
    "s_mov_b32 s55, 0x20000\n"
    "s_add_u32 s76, s34, 0\n"
    "s_mul_i32 s76, s76, 0xb4000\n"
    "s_add_u32 s48, s24, s76\n"
    "s_addc_u32 s49, s25, 0\n"
    "s_and_b32 s49, s49, 0xffff\n"
    "s_mov_b32 s50, 0xb4000\n"
    "s_mov_b32 s51, 0x20000\n"
    "s_add_u32 s74, s35, s34\n"
    "s_add_u32 s74, s74, 1\n"
    "s_sub_u32 s76, s74, 9\n"
    "s_cselect_b32 s74, s74, s76\n"
    "s_sub_u32 s76, s74, 9\n"
    "s_cselect_b32 s74, s74, s76\n"
    "s_lshl_b32 s74, s74, 7\n"
    "s_mul_i32 s75, s81, 5\n"
    "s_mul_i32 s76, s34, 1\n"
    "s_add_u32 s75, s75, s76\n"
    "s_mov_b32 s76, s75\n"
    "s_mov_b32 s75, 0\n"
    "s_bitcmp1_b32 s76, 0\n"
    "s_cselect_b32 s77, 0x2000, 0\n"
    "s_or_b32 s75, s75, s77\n"
    "s_bitcmp1_b32 s76, 1\n"
    "s_cselect_b32 s77, 0x4000, 0\n"
    "s_or_b32 s75, s75, s77\n"
    "s_bitcmp1_b32 s76, 2\n"
    "s_cselect_b32 s77, 0x8000, 0\n"
    "s_or_b32 s75, s75, s77\n"
    "s_bitcmp1_b32 s76, 3\n"
    "s_cselect_b32 s77, 0x10000, 0\n"
    "s_or_b32 s75, s75, s77\n"
    "s_and_b32 s76, s72, 1\n"
    "s_mul_i32 s78, s76, 0x1000\n"
    "s_mul_i32 s76, s76, 0xa000\n"
    "s_sub_u32 s79, s71, s76\n"
    "s_mov_b32 s67, 0x5040100\n"
    "s_mov_b32 s68, 0x7060302\n"
    "s_mov_b32 s69, 0x3e0293ee\n"
    "s_mov_b32 s70, 0xbe0293ee\n"
    "v_lshrrev_b32 v209, 4, v239\n"
    "v_and_b32 v210, 15, v239\n"
    "v_lshlrev_b32 v238, 4, v239\n"
    "v_and_b32 v248, 0x1ff, v238\n"
    "v_lshrrev_b32 v211, 5, v239\n"
    "v_lshl_or_b32 v248, v211, 3, v248\n"
    "v_lshrrev_b32 v211, 1, v210\n"
    "v_xor_b32 v211, v211, v209\n"
    "v_lshlrev_b32 v211, 4, v211\n"
    "v_lshl_add_u32 v211, v210, 7, v211\n"
    "v_add_u32 v202, 0x6000, v211\n"
    "v_xor_b32 v204, 64, v202\n"
    "v_add_u32 v203, 0x14000, v202\n"
    "v_add_u32 v205, 0x14000, v204\n"
    "v_lshlrev_b32 v211, 2, v209\n"
    "v_xor_b32 v211, v211, v210\n"
    "v_lshlrev_b32 v211, 4, v211\n"
    "v_lshl_add_u32 v226, v209, 8, v211\n"
    "v_xor_b32 v227, 32, v226\n"
    "v_xor_b32 v228, 16, v226\n"
    "v_xor_b32 v229, 48, v226\n"
    "v_add_u32 v226, s78, v226\n"
    "v_add_u32 v227, s78, v227\n"
    "v_add_u32 v228, s78, v228\n"
    "v_add_u32 v229, s78, v229\n"
    "v_mov_b32 v230, v226\n"
    "v_mov_b32 v231, v227\n"
    "v_mov_b32 v232, v228\n"
    "v_mov_b32 v233, v229\n"
    "v_and_b32 v211, 3, v210\n"
    "v_xor_b32 v211, v211, v209\n"
    "v_lshlrev_b32 v211, 2, v211\n"
    "v_bfe_u32 v212, v210, 3, 1\n"
    "v_or_b32 v211, v211, v212\n"
    "v_bfe_u32 v212, v210, 2, 1\n"
    "v_lshl_or_b32 v211, v212, 1, v211\n"
    "v_lshlrev_b32 v211, 4, v211\n"
    "v_lshl_add_u32 v211, v210, 8, v211\n"
    "v_add_u32 v214, s79, v211\n"
    "v_xor_b32 v215, 16, v214\n"
    "v_xor_b32 v216, 32, v214\n"
    "v_xor_b32 v217, 48, v214\n"
    "v_and_b32 v211, 12, v210\n"
    "v_bfe_u32 v212, v210, 1, 1\n"
    "v_or_b32 v211, v211, v212\n"
    "v_lshrrev_b32 v212, 1, v209\n"
    "v_xor_b32 v211, v211, v212\n"
    "v_and_b32 v212, 1, v209\n"
    "v_lshlrev_b32 v212, 1, v212\n"
    "v_xor_b32 v211, v211, v212\n"
    "v_lshlrev_b32 v211, 4, v211\n"
    "v_and_b32 v212, 1, v210\n"
    "v_lshl_or_b32 v211, v212, 3, v211\n"
    "v_lshrrev_b32 v212, 2, v239\n"
    "v_lshl_add_u32 v211, v212, 8, v211\n"
    "v_add_u32 v218, s79, v211\n"
    "v_xor_b32 v219, 32, v218\n"
    "v_xor_b32 v220, 64, v218\n"
    "v_xor_b32 v221, 0x60, v218\n"
    "v_xor_b32 v222, 0x80, v218\n"
    "v_xor_b32 v223, 0xa0, v218\n"
    "v_xor_b32 v224, 0xc0, v218\n"
    "v_xor_b32 v225, 0xe0, v218\n"
    "v_and_b32 v211, 7, v239\n"
    "v_lshlrev_b32 v212, 1, v209\n"
    "v_xor_b32 v211, v211, v212\n"
    "v_lshlrev_b32 v211, 4, v211\n"
    "v_lshrrev_b32 v212, 3, v239\n"
    "v_mul_u32_u24 v212, 0x480, v212\n"
    "v_add_u32 v234, v211, v212\n"
    "v_add_u32 v235, 0x2000, v234\n"
    "v_add_u32 v236, 0x4000, v234\n"
    "v_add_u32 v237, 0x6000, v234\n"
    "v_bfe_u32 v211, v239, 3, 2\n"
    "v_xor_b32 v211, s72, v211\n"
    "v_lshlrev_b32 v211, 5, v211\n"
    "v_and_b32 v212, 3, v239\n"
    "v_lshl_or_b32 v211, v212, 3, v211\n"
    "v_lshrrev_b32 v212, 2, v239\n"
    "v_lshl_add_u32 v211, v212, 7, v211\n"
    "s_add_u32 s76, s72, 0\n"
    "s_and_b32 s76, s76, 3\n"
    "s_mul_i32 s76, s76, 0xa000\n"
    "v_add_u32 v244, s76, v211\n"
    "v_add_u32 v240, s76, v238\n"
    "s_add_u32 s76, s72, 1\n"
    "s_and_b32 s76, s76, 3\n"
    "s_mul_i32 s76, s76, 0xa000\n"
    "v_add_u32 v245, s76, v211\n"
    "v_add_u32 v241, s76, v238\n"
    "s_add_u32 s76, s72, 2\n"
    "s_and_b32 s76, s76, 3\n"
    "s_mul_i32 s76, s76, 0xa000\n"
    "v_add_u32 v246, s76, v211\n"
    "v_add_u32 v242, s76, v238\n"
    "s_add_u32 s76, s72, 3\n"
    "s_and_b32 s76, s76, 3\n"
    "s_mul_i32 s76, s76, 0xa000\n"
    "v_add_u32 v247, s76, v211\n"
    "v_add_u32 v243, s76, v238\n"
    "s_lshl_b32 s76, s35, 9\n"
    "s_lshl_b32 s77, s72, 7\n"
    "s_add_u32 s76, s76, s77\n"
    "v_lshl_add_u32 v249, v239, 2, s76\n"
    "v_mov_b32 v212, 0x40000000\n"
    "v_cmp_gt_u32 vcc, 16, v239\n"
    "s_add_u32 s76, s35, 1\n"
    "s_nop 0\n"
    "v_cndmask_b32 v249, v212, v249, vcc\n"
    "v_add_u32 v211, s76, v239\n"
    "v_subrev_u32 v209, 9, v211\n"
    "v_min_u32 v211, v211, v209\n"
    "v_lshlrev_b32 v211, 9, v211\n"
    "v_lshl_add_u32 v211, v239, 2, v211\n"
    "s_sub_u32 s76, 15, s35\n"
    "s_lshl_b32 s76, s76, 2\n"
    "v_and_b32 v209, 7, v239\n"
    "v_lshl_add_u32 v209, v209, 9, s76\n"
    "v_cndmask_b32 v209, v212, v209, vcc\n"
    "v_cmp_gt_u32 vcc, 8, v239\n"
    "s_nop 1\n"
    "v_cndmask_b32 v211, v209, v211, vcc\n"
    "v_add_u32 v250, s77, v211\n"
    "s_mul_i32 s91, s34, 0x9000\n"
    "s_mul_i32 s76, s72, 0x2400\n"
    "s_add_u32 s91, s91, s76\n"
    "s_mul_i32 s86, s72, 0x24000\n"
    "s_mul_i32 s90, s72, 0xa00\n"
    "s_mov_b64 exec, 0\n"
    "s_cmp_eq_u32 s72, 0\n"
    "s_cbranch_scc1 A_TH_FIRST0\n"
    "s_cmp_eq_u32 s72, 1\n"
    "s_cbranch_scc1 A_TH_FIRST1\n"
    "s_cmp_eq_u32 s72, 2\n"
    "s_cbranch_scc1 A_TH_FIRST2\n"
    "s_cmp_eq_u32 s72, 3\n"
    "s_cbranch_scc1 A_TH_FIRST3\n"
    "A_TH_RET:\n"
    "s_mov_b64 exec, -1\n"
    "s_cmp_ge_u32 s28, s84\n"
    "s_cselect_b32 s76, 0x480, 0\n"
    "s_add_u32 m0, s71, 0x6000\n"
    "s_sub_u32 s28, s28, s76\n"
    "buffer_load_dwordx4 v206, s[92:95], s28 offen lds\n"
    "buffer_load_dwordx4 v207, s[92:95], s28 offen offset:1024 lds\n"
    "s_cmp_ge_u32 s29, s85\n"
    "s_cselect_b32 s76, 0x480, 0\n"
    "s_add_u32 m0, s71, 0x6800\n"
    "s_sub_u32 s29, s29, s76\n"
    "buffer_load_dwordx4 v206, s[92:95], s29 offen lds\n"
    "buffer_load_dwordx4 v207, s[92:95], s29 offen offset:1024 lds\n"
    "s_cmp_ge_u32 s30, s87\n"
    "s_cselect_b32 s76, 0x480, 0\n"
    "s_add_u32 m0, s71, 0x7000\n"
    "s_sub_u32 s30, s30, s76\n"
    "buffer_load_dwordx4 v206, s[92:95], s30 offen lds\n"
    "buffer_load_dwordx4 v207, s[92:95], s30 offen offset:1024 lds\n"
    "s_cmp_ge_u32 s31, s88\n"
    "s_cselect_b32 s76, 0x480, 0\n"
    "s_add_u32 m0, s71, 0x7800\n"
    "s_sub_u32 s31, s31, s76\n"
    "buffer_load_dwordx4 v206, s[92:95], s31 offen lds\n"
    "buffer_load_dwordx4 v207, s[92:95], s31 offen offset:1024 lds\n"
    "s_lshl_b32 s76, s34, 3\n"
    "s_add_u32 s76, s76, s35\n"
    "s_lshr_b32 s77, s72, 1\n"
    "s_add_u32 s77, s77, 18\n"
    "s_lshl_b32 s76, s76, s77\n"
    "s_lshl_b32 s77, 1, s77\n"
    "s_add_u32 s77, s77, s76\n"
    "s_sub_u32 s77, s77, 64\n"
    "s_mov_b64 s[56:57], s[6:7]\n"
    "s_cmp_eq_u32 s72, 1\n"
    "s_cselect_b64 s[56:57], s[8:9], s[56:57]\n"
    "s_cmp_eq_u32 s72, 2\n"
    "s_cselect_b64 s[56:57], s[16:17], s[56:57]\n"
    "s_cmp_eq_u32 s72, 3\n"
    "s_cselect_b64 s[56:57], s[18:19], s[56:57]\n"
    "s_load_dword s80, s[56:57], s76\n"
    "s_load_dword s80, s[56:57], s77\n"
    "s_add_u32 s28, s28, 0x180\n"
    "s_cmp_ge_u32 s28, s84\n"
    "s_cselect_b32 s76, 0x480, 0\n"
    "s_add_u32 m0, s71, 0x8000\n"
    "s_sub_u32 s28, s28, s76\n"
    "buffer_load_dwordx4 v206, s[92:95], s28 offen lds\n"
    "buffer_load_dwordx4 v207, s[92:95], s28 offen offset:1024 lds\n"
    "s_add_u32 s29, s29, 0x180\n"
    "s_cmp_ge_u32 s29, s85\n"
    "s_cselect_b32 s76, 0x480, 0\n"
    "s_add_u32 m0, s71, 0x8800\n"
    "s_sub_u32 s29, s29, s76\n"
    "buffer_load_dwordx4 v206, s[92:95], s29 offen lds\n"
    "buffer_load_dwordx4 v207, s[92:95], s29 offen offset:1024 lds\n"
    "s_add_u32 s30, s30, 0x180\n"
    "s_cmp_ge_u32 s30, s87\n"
    "s_cselect_b32 s76, 0x480, 0\n"
    "s_add_u32 m0, s71, 0x9000\n"
    "s_sub_u32 s30, s30, s76\n"
    "buffer_load_dwordx4 v206, s[92:95], s30 offen lds\n"
    "buffer_load_dwordx4 v207, s[92:95], s30 offen offset:1024 lds\n"
    "s_add_u32 s31, s31, 0x180\n"
    "s_cmp_ge_u32 s31, s88\n"
    "s_cselect_b32 s76, 0x480, 0\n"
    "s_add_u32 m0, s71, 0x9800\n"
    "s_sub_u32 s31, s31, s76\n"
    "buffer_load_dwordx4 v206, s[92:95], s31 offen lds\n"
    "buffer_load_dwordx4 v207, s[92:95], s31 offen offset:1024 lds\n"
    "s_add_u32 s28, s28, 0x180\n"
    "s_cmp_ge_u32 s28, s84\n"
    "s_cselect_b32 s76, 0x480, 0\n"
    "s_add_u32 m0, s71, 0x4000\n"
    "s_sub_u32 s28, s28, s76\n"
    "buffer_load_dwordx4 v206, s[92:95], s28 offen lds\n"
    "buffer_load_dwordx4 v207, s[92:95], s28 offen offset:1024 lds\n"
    "s_add_u32 s29, s29, 0x180\n"
    "s_cmp_ge_u32 s29, s85\n"
    "s_cselect_b32 s76, 0x480, 0\n"
    "s_add_u32 m0, s71, 0x4800\n"
    "s_sub_u32 s29, s29, s76\n"
    "buffer_load_dwordx4 v206, s[92:95], s29 offen lds\n"
    "buffer_load_dwordx4 v207, s[92:95], s29 offen offset:1024 lds\n"
    "s_add_u32 s30, s30, 0x180\n"
    "s_cmp_ge_u32 s30, s87\n"
    "s_cselect_b32 s76, 0x480, 0\n"
    "s_add_u32 m0, s71, 0x5000\n"
    "s_sub_u32 s30, s30, s76\n"
    "buffer_load_dwordx4 v206, s[92:95], s30 offen lds\n"
    "buffer_load_dwordx4 v207, s[92:95], s30 offen offset:1024 lds\n"
    "s_add_u32 s31, s31, 0x180\n"
    "s_cmp_ge_u32 s31, s88\n"
    "s_cselect_b32 s76, 0x480, 0\n"
    "s_add_u32 m0, s71, 0x5800\n"
    "s_sub_u32 s31, s31, s76\n"
    "buffer_load_dwordx4 v206, s[92:95], s31 offen lds\n"
    "buffer_load_dwordx4 v207, s[92:95], s31 offen offset:1024 lds\n"
    "v_lshrrev_b32 v209, 1, v239\n"
    "v_lshlrev_b32 v209, 13, v209\n"
    "v_and_b32 v210, 1, v239\n"
    "v_lshl_add_u32 v209, v210, 7, v209\n"
    "s_mul_i32 s76, s34, 0x48000\n"
    "s_add_u32 s77, s76, 0x40000\n"
    "buffer_load_dword v5, v209, s[36:39], s76 offen\n"
    "v_cmp_gt_u32 vcc, 8, v239\n"
    "v_mov_b32 v213, 0x40000000\n"
    "s_nop 0\n"
    "v_cndmask_b32 v210, v213, v209, vcc\n"
    "buffer_load_dword v6, v210, s[36:39], s77 offen\n"
    "s_add_u32 m0, s71, 0x2000\n"
    "s_add_u32 s81, s75, 0\n"
    "s_and_b32 s81, s81, 0x1e000\n"
    "buffer_load_dwordx4 v226, s[40:43], s81 offen nt lds\n"
    "buffer_load_dwordx4 v227, s[40:43], s81 offen offset:1024 nt lds\n"
    "buffer_load_dwordx4 v228, s[40:43], s81 offen offset:2048 nt lds\n"
    "buffer_load_dwordx4 v229, s[40:43], s81 offen offset:3072 nt lds\n"
    "s_add_u32 m0, s71, 0x1000\n"
    "s_add_u32 s81, s75, 0\n"
    "s_and_b32 s81, s81, 0x1e000\n"
    "buffer_load_dwordx4 v226, s[44:47], s81 offen nt lds\n"
    "buffer_load_dwordx4 v227, s[44:47], s81 offen offset:1024 nt lds\n"
    "buffer_load_dwordx4 v228, s[44:47], s81 offen offset:2048 nt lds\n"
    "buffer_load_dwordx4 v229, s[44:47], s81 offen offset:3072 nt lds\n"
    "s_add_u32 m0, s71, 0x3000\n"
    "s_add_u32 s81, s75, 0x2000\n"
    "s_and_b32 s81, s81, 0x1e000\n"
    "buffer_load_dwordx4 v226, s[40:43], s81 offen nt lds\n"
    "buffer_load_dwordx4 v227, s[40:43], s81 offen offset:1024 nt lds\n"
    "buffer_load_dwordx4 v228, s[40:43], s81 offen offset:2048 nt lds\n"
    "buffer_load_dwordx4 v229, s[40:43], s81 offen offset:3072 nt lds\n"
    "s_add_u32 m0, s71, 0\n"
    "s_add_u32 s81, s75, 0x2000\n"
    "s_and_b32 s81, s81, 0x1e000\n"
    "buffer_load_dwordx4 v226, s[44:47], s81 offen nt lds\n"
    "buffer_load_dwordx4 v227, s[44:47], s81 offen offset:1024 nt lds\n"
    "buffer_load_dwordx4 v228, s[44:47], s81 offen offset:2048 nt lds\n"
    "buffer_load_dwordx4 v229, s[44:47], s81 offen offset:3072 nt lds\n"
    "s_add_u32 s56, s22, s82\n"
    "s_addc_u32 s57, s23, 0\n"
    "s_and_b32 s57, s57, 0xffff\n"
    "s_mov_b32 s58, 0x90000\n"
    "s_mov_b32 s59, 0x20000\n"
    "v_lshlrev_b32 v209, 7, v239\n"
    "s_add_u32 s76, s91, 0x2000\n"
    "v_cmp_gt_u32 vcc, 8, v239\n"
    "v_mov_b32 v213, 0x40000000\n"
    "s_nop 0\n"
    "v_cndmask_b32 v210, v213, v209, vcc\n"
    "buffer_load_dword v7, v209, s[56:59], s91 offen\n"
    "buffer_load_dword v8, v210, s[56:59], s76 offen\n"
    "buffer_load_dword v9, v209, s[52:55], s91 offen\n"
    "buffer_load_dword v10, v210, s[52:55], s76 offen\n"
    "s_waitcnt vmcnt(22)\n"
    "v_add_u32 v201, s73, v200\n"
    "s_add_u32 s73, s73, 0x40000\n"
    "s_cmp_eq_u32 s73, 0x480000\n"
    "s_cselect_b32 s73, 0, s73\n"
    "buffer_load_dwordx4 v[128:131], v201, s[36:39], 0 offen\n"
    "buffer_load_dwordx4 v[132:135], v201, s[36:39], s60 offen\n"
    "buffer_load_dwordx4 v[136:139], v201, s[36:39], s61 offen\n"
    "buffer_load_dwordx4 v[140:143], v201, s[36:39], s62 offen\n"
    "buffer_load_dwordx4 v[144:147], v201, s[36:39], s63 offen\n"
    "buffer_load_dwordx4 v[148:151], v201, s[36:39], s64 offen\n"
    "buffer_load_dwordx4 v[152:155], v201, s[36:39], s65 offen\n"
    "buffer_load_dwordx4 v[156:159], v201, s[36:39], s66 offen\n"
    "v_add_u32 v208, s73, v200\n"
    "s_add_u32 s73, s73, 0x40000\n"
    "s_cmp_eq_u32 s73, 0x480000\n"
    "s_cselect_b32 s73, 0, s73\n"
    "buffer_load_dwordx4 v[160:163], v208, s[36:39], 0 offen\n"
    "buffer_load_dwordx4 v[164:167], v208, s[36:39], s60 offen\n"
    "buffer_load_dwordx4 v[168:171], v208, s[36:39], s61 offen\n"
    "buffer_load_dwordx4 v[172:175], v208, s[36:39], s62 offen\n"
    "buffer_load_dwordx4 v[176:179], v208, s[36:39], s63 offen\n"
    "buffer_load_dwordx4 v[180:183], v208, s[36:39], s64 offen\n"
    "buffer_load_dwordx4 v[184:187], v208, s[36:39], s65 offen\n"
    "buffer_load_dwordx4 v[188:191], v208, s[36:39], s66 offen\n"
    "v_add_u32 v201, s73, v200\n"
    "s_add_u32 s73, s73, 0x40000\n"
    "s_cmp_eq_u32 s73, 0x480000\n"
    "s_cselect_b32 s73, 0, s73\n"
    "buffer_load_dwordx4 v[64:67], v201, s[36:39], 0 offen\n"
    "buffer_load_dwordx4 v[68:71], v201, s[36:39], s60 offen\n"
    "buffer_load_dwordx4 v[72:75], v201, s[36:39], s61 offen\n"
    "buffer_load_dwordx4 v[76:79], v201, s[36:39], s62 offen\n"
    "buffer_load_dwordx4 v[80:83], v201, s[36:39], s63 offen\n"
    "buffer_load_dwordx4 v[84:87], v201, s[36:39], s64 offen\n"
    "buffer_load_dwordx4 v[88:91], v201, s[36:39], s65 offen\n"
    "buffer_load_dwordx4 v[92:95], v201, s[36:39], s66 offen\n"
    "v_add_u32 v208, s73, v200\n"
    "s_add_u32 s73, s73, 0x40000\n"
    "s_cmp_eq_u32 s73, 0x480000\n"
    "s_cselect_b32 s73, 0, s73\n"
    "A_TH_BEGIN:\n"
    "s_waitcnt vmcnt(16)\n"
    "s_barrier\n"
    "ds_read_b128 a[160:163], v202 offset:0\n"
    "ds_read_b128 a[164:167], v202 offset:2048\n"
    "ds_read_b128 a[168:171], v202 offset:4096\n"
    "ds_read_b128 a[172:175], v202 offset:6144\n"
    "ds_read_b128 a[176:179], v202 offset:40960\n"
    "v_perm_b32 v192, v132, v128, s67\n"
    "v_perm_b32 v193, v140, v136, s67\n"
    "v_perm_b32 v194, v148, v144, s67\n"
    "v_perm_b32 v195, v156, v152, s67\n"
    "s_waitcnt lgkmcnt(0)\n"
    "s_nop 0\n"
    "v_mfma_f32_16x16x32_bf16 a[0:3], v[192:195], a[160:163], 0\n"
    "v_perm_b32 v196, v132, v128, s68\n"
    "v_perm_b32 v197, v140, v136, s68\n"
    "v_mfma_f32_16x16x32_bf16 a[32:35], v[192:195], a[164:167], 0\n"
    "v_perm_b32 v198, v148, v144, s68\n"
    "v_perm_b32 v199, v156, v152, s68\n"
    "v_mfma_f32_16x16x32_bf16 a[64:67], v[192:195], a[168:171], 0\n"
    "buffer_load_dwordx4 v[96:99], v208, s[36:39], 0 offen\n"
    "v_mfma_f32_16x16x32_bf16 a[96:99], v[192:195], a[172:175], 0\n"
    "v_mfma_f32_16x16x32_bf16 a[128:131], v[192:195], a[176:179], 0\n"
    "v_mfma_f32_16x16x32_bf16 a[4:7], v[196:199], a[160:163], 0\n"
    "v_perm_b32 v192, v133, v129, s67\n"
    "v_perm_b32 v193, v141, v137, s67\n"
    "v_mfma_f32_16x16x32_bf16 a[36:39], v[196:199], a[164:167], 0\n"
    "v_perm_b32 v194, v149, v145, s67\n"
    "v_perm_b32 v195, v157, v153, s67\n"
    "v_mfma_f32_16x16x32_bf16 a[68:71], v[196:199], a[168:171], 0\n"
    "buffer_load_dwordx4 v[100:103], v208, s[36:39], s60 offen\n"
    "v_mfma_f32_16x16x32_bf16 a[100:103], v[196:199], a[172:175], 0\n"
    "ds_read_b128 a[180:183], v204 offset:0\n"
    "v_mfma_f32_16x16x32_bf16 a[132:135], v[196:199], a[176:179], 0\n"
    "v_mfma_f32_16x16x32_bf16 a[8:11], v[192:195], a[160:163], 0\n"
    "v_perm_b32 v196, v133, v129, s68\n"
    "v_perm_b32 v197, v141, v137, s68\n"
    "v_mfma_f32_16x16x32_bf16 a[40:43], v[192:195], a[164:167], 0\n"
    "v_perm_b32 v198, v149, v145, s68\n"
    "v_perm_b32 v199, v157, v153, s68\n"
    "v_mfma_f32_16x16x32_bf16 a[72:75], v[192:195], a[168:171], 0\n"
    "buffer_load_dwordx4 v[104:107], v208, s[36:39], s61 offen\n"
    "v_mfma_f32_16x16x32_bf16 a[104:107], v[192:195], a[172:175], 0\n"
    "ds_read_b128 a[184:187], v204 offset:2048\n"
    "v_mfma_f32_16x16x32_bf16 a[136:139], v[192:195], a[176:179], 0\n"
    "v_mfma_f32_16x16x32_bf16 a[12:15], v[196:199], a[160:163], 0\n"
    "v_perm_b32 v192, v134, v130, s67\n"
    "v_perm_b32 v193, v142, v138, s67\n"
    "v_mfma_f32_16x16x32_bf16 a[44:47], v[196:199], a[164:167], 0\n"
    "v_perm_b32 v194, v150, v146, s67\n"
    "v_perm_b32 v195, v158, v154, s67\n"
    "v_mfma_f32_16x16x32_bf16 a[76:79], v[196:199], a[168:171], 0\n"
    "buffer_load_dwordx4 v[108:111], v208, s[36:39], s62 offen\n"
    "v_mfma_f32_16x16x32_bf16 a[108:111], v[196:199], a[172:175], 0\n"
    "ds_read_b128 a[188:191], v204 offset:4096\n"
    "v_mfma_f32_16x16x32_bf16 a[140:143], v[196:199], a[176:179], 0\n"
    "v_mfma_f32_16x16x32_bf16 a[16:19], v[192:195], a[160:163], 0\n"
    "v_perm_b32 v196, v134, v130, s68\n"
    "v_perm_b32 v197, v142, v138, s68\n"
    "v_mfma_f32_16x16x32_bf16 a[48:51], v[192:195], a[164:167], 0\n"
    "v_perm_b32 v198, v150, v146, s68\n"
    "v_perm_b32 v199, v158, v154, s68\n"
    "v_mfma_f32_16x16x32_bf16 a[80:83], v[192:195], a[168:171], 0\n"
    "buffer_load_dwordx4 v[112:115], v208, s[36:39], s63 offen\n"
    "v_mfma_f32_16x16x32_bf16 a[112:115], v[192:195], a[172:175], 0\n"
    "ds_read_b128 a[192:195], v204 offset:6144\n"
    "v_mfma_f32_16x16x32_bf16 a[144:147], v[192:195], a[176:179], 0\n"
    "v_mfma_f32_16x16x32_bf16 a[20:23], v[196:199], a[160:163], 0\n"
    "v_perm_b32 v192, v135, v131, s67\n"
    "v_perm_b32 v193, v143, v139, s67\n"
    "v_mfma_f32_16x16x32_bf16 a[52:55], v[196:199], a[164:167], 0\n"
    "v_perm_b32 v194, v151, v147, s67\n"
    "v_perm_b32 v195, v159, v155, s67\n"
    "v_mfma_f32_16x16x32_bf16 a[84:87], v[196:199], a[168:171], 0\n"
    "buffer_load_dwordx4 v[116:119], v208, s[36:39], s64 offen\n"
    "v_mfma_f32_16x16x32_bf16 a[116:119], v[196:199], a[172:175], 0\n"
    "ds_read_b128 a[196:199], v204 offset:40960\n"
    "v_mfma_f32_16x16x32_bf16 a[148:151], v[196:199], a[176:179], 0\n"
    "v_mfma_f32_16x16x32_bf16 a[24:27], v[192:195], a[160:163], 0\n"
    "v_perm_b32 v196, v135, v131, s68\n"
    "v_perm_b32 v197, v143, v139, s68\n"
    "v_mfma_f32_16x16x32_bf16 a[56:59], v[192:195], a[164:167], 0\n"
    "v_perm_b32 v198, v151, v147, s68\n"
    "v_perm_b32 v199, v159, v155, s68\n"
    "v_mfma_f32_16x16x32_bf16 a[88:91], v[192:195], a[168:171], 0\n"
    "buffer_load_dwordx4 v[120:123], v208, s[36:39], s65 offen\n"
    "v_mfma_f32_16x16x32_bf16 a[120:123], v[192:195], a[172:175], 0\n"
    "s_memrealtime s[14:15]\n"
    "v_mfma_f32_16x16x32_bf16 a[152:155], v[192:195], a[176:179], 0\n"
    "s_waitcnt vmcnt(15)\n"
    "v_mfma_f32_16x16x32_bf16 a[28:31], v[196:199], a[160:163], 0\n"
    "v_perm_b32 v192, v164, v160, s67\n"
    "v_perm_b32 v193, v172, v168, s67\n"
    "v_mfma_f32_16x16x32_bf16 a[60:63], v[196:199], a[164:167], 0\n"
    "v_perm_b32 v194, v180, v176, s67\n"
    "v_perm_b32 v195, v188, v184, s67\n"
    "v_mfma_f32_16x16x32_bf16 a[92:95], v[196:199], a[168:171], 0\n"
    "buffer_load_dwordx4 v[124:127], v208, s[36:39], s66 offen\n"
    "v_mfma_f32_16x16x32_bf16 a[124:127], v[196:199], a[172:175], 0\n"
    "v_add_u32 v201, s73, v200\n"
    "v_mfma_f32_16x16x32_bf16 a[156:159], v[196:199], a[176:179], 0\n"
    "s_waitcnt lgkmcnt(0)\n"
    "s_add_u32 s73, s73, 0x40000\n"
    "s_cmp_eq_u32 s73, 0x480000\n"
    "s_cselect_b32 s73, 0, s73\n"
    "s_sub_u32 s78, s14, s83\n"
    "s_cmp_gt_u32 s78, 0x3f2\n"
    "s_cselect_b32 s77, 47, 0\n"
    "s_add_u32 s20, s14, s77\n"
    "v_mfma_f32_16x16x32_bf16 a[0:3], v[192:195], a[180:183], a[0:3]\n"
    "v_perm_b32 v196, v164, v160, s68\n"
    "v_perm_b32 v197, v172, v168, s68\n"
    "v_mfma_f32_16x16x32_bf16 a[32:35], v[192:195], a[184:187], a[32:35]\n"
    "v_perm_b32 v198, v180, v176, s68\n"
    "v_perm_b32 v199, v188, v184, s68\n"
    "v_mfma_f32_16x16x32_bf16 a[64:67], v[192:195], a[188:191], a[64:67]\n"
    "buffer_load_dwordx4 v[128:131], v201, s[36:39], 0 offen\n"
    "v_mfma_f32_16x16x32_bf16 a[96:99], v[192:195], a[192:195], a[96:99]\n"
    "v_mfma_f32_16x16x32_bf16 a[128:131], v[192:195], a[196:199], a[128:131]\n"
    "v_mfma_f32_16x16x32_bf16 a[4:7], v[196:199], a[180:183], a[4:7]\n"
    "v_perm_b32 v192, v165, v161, s67\n"
    "v_perm_b32 v193, v173, v169, s67\n"
    "v_mfma_f32_16x16x32_bf16 a[36:39], v[196:199], a[184:187], a[36:39]\n"
    "v_perm_b32 v194, v181, v177, s67\n"
    "v_perm_b32 v195, v189, v185, s67\n"
    "A_TH_FIRST0:\n"
    "A_TH_S0:\n"
    "s_cbranch_execz A_TH_S4\n"
    "v_mfma_f32_16x16x32_bf16 a[68:71], v[196:199], a[188:191], a[68:71]\n"
    "buffer_load_dwordx4 v[132:135], v201, s[36:39], s60 offen\n"
    "v_mfma_f32_16x16x32_bf16 a[100:103], v[196:199], a[192:195], a[100:103]\n"
    "ds_read_b128 a[160:163], v202 offset:43008\n"
    "v_mfma_f32_16x16x32_bf16 a[132:135], v[196:199], a[196:199], a[132:135]\n"
    "v_mfma_f32_16x16x32_bf16 a[8:11], v[192:195], a[180:183], a[8:11]\n"
    "v_perm_b32 v196, v165, v161, s68\n"
    "v_perm_b32 v197, v173, v169, s68\n"
    "v_mfma_f32_16x16x32_bf16 a[40:43], v[192:195], a[184:187], a[40:43]\n"
    "v_perm_b32 v198, v181, v177, s68\n"
    "v_perm_b32 v199, v189, v185, s68\n"
    "v_mfma_f32_16x16x32_bf16 a[72:75], v[192:195], a[188:191], a[72:75]\n"
    "buffer_load_dwordx4 v[136:139], v201, s[36:39], s61 offen\n"
    "v_mfma_f32_16x16x32_bf16 a[104:107], v[192:195], a[192:195], a[104:107]\n"
    "ds_read_b128 a[164:167], v202 offset:45056\n"
    "v_mfma_f32_16x16x32_bf16 a[136:139], v[192:195], a[196:199], a[136:139]\n"
    "v_mfma_f32_16x16x32_bf16 a[12:15], v[196:199], a[180:183], a[12:15]\n"
    "v_perm_b32 v192, v166, v162, s67\n"
    "v_perm_b32 v193, v174, v170, s67\n"
    "v_mfma_f32_16x16x32_bf16 a[44:47], v[196:199], a[184:187], a[44:47]\n"
    "v_perm_b32 v194, v182, v178, s67\n"
    "v_perm_b32 v195, v190, v186, s67\n"
    "v_mfma_f32_16x16x32_bf16 a[76:79], v[196:199], a[188:191], a[76:79]\n"
    "buffer_load_dwordx4 v[140:143], v201, s[36:39], s62 offen\n"
    "v_mfma_f32_16x16x32_bf16 a[108:111], v[196:199], a[192:195], a[108:111]\n"
    "ds_read_b128 a[168:171], v202 offset:47104\n"
    "v_mfma_f32_16x16x32_bf16 a[140:143], v[196:199], a[196:199], a[140:143]\n"
    "v_mfma_f32_16x16x32_bf16 a[16:19], v[192:195], a[180:183], a[16:19]\n"
    "v_perm_b32 v196, v166, v162, s68\n"
    "v_perm_b32 v197, v174, v170, s68\n"
    "v_mfma_f32_16x16x32_bf16 a[48:51], v[192:195], a[184:187], a[48:51]\n"
    "v_perm_b32 v198, v182, v178, s68\n"
    "v_perm_b32 v199, v190, v186, s68\n"
    "v_mfma_f32_16x16x32_bf16 a[80:83], v[192:195], a[188:191], a[80:83]\n"
    "buffer_load_dwordx4 v[144:147], v201, s[36:39], s63 offen\n"
    "v_mfma_f32_16x16x32_bf16 a[112:115], v[192:195], a[192:195], a[112:115]\n"
    "ds_read_b128 a[172:175], v203 offset:0\n"
    "v_mfma_f32_16x16x32_bf16 a[144:147], v[192:195], a[196:199], a[144:147]\n"
    "v_mfma_f32_16x16x32_bf16 a[20:23], v[196:199], a[180:183], a[20:23]\n"
    "v_perm_b32 v192, v167, v163, s67\n"
    "v_perm_b32 v193, v175, v171, s67\n"
    "v_mfma_f32_16x16x32_bf16 a[52:55], v[196:199], a[184:187], a[52:55]\n"
    "v_perm_b32 v194, v183, v179, s67\n"
    "v_perm_b32 v195, v191, v187, s67\n"
    "v_mfma_f32_16x16x32_bf16 a[84:87], v[196:199], a[188:191], a[84:87]\n"
    "buffer_load_dwordx4 v[148:151], v201, s[36:39], s64 offen\n"
    "v_mfma_f32_16x16x32_bf16 a[116:119], v[196:199], a[192:195], a[116:119]\n"
    "ds_read_b128 a[176:179], v203 offset:2048\n"
    "v_mfma_f32_16x16x32_bf16 a[148:151], v[196:199], a[196:199], a[148:151]\n"
    "v_mfma_f32_16x16x32_bf16 a[24:27], v[192:195], a[180:183], a[24:27]\n"
    "v_perm_b32 v196, v167, v163, s68\n"
    "v_perm_b32 v197, v175, v171, s68\n"
    "v_mfma_f32_16x16x32_bf16 a[56:59], v[192:195], a[184:187], a[56:59]\n"
    "v_perm_b32 v198, v183, v179, s68\n"
    "v_perm_b32 v199, v191, v187, s68\n"
    "v_mfma_f32_16x16x32_bf16 a[88:91], v[192:195], a[188:191], a[88:91]\n"
    "buffer_load_dwordx4 v[152:155], v201, s[36:39], s65 offen\n"
    "v_mfma_f32_16x16x32_bf16 a[120:123], v[192:195], a[192:195], a[120:123]\n"
    "s_memrealtime s[14:15]\n"
    "v_mfma_f32_16x16x32_bf16 a[152:155], v[192:195], a[196:199], a[152:155]\n"
    "s_waitcnt vmcnt(15)\n"
    "v_mfma_f32_16x16x32_bf16 a[28:31], v[196:199], a[180:183], a[28:31]\n"
    "v_perm_b32 v192, v68, v64, s67\n"
    "v_perm_b32 v193, v76, v72, s67\n"
    "v_mfma_f32_16x16x32_bf16 a[60:63], v[196:199], a[184:187], a[60:63]\n"
    "v_perm_b32 v194, v84, v80, s67\n"
    "v_perm_b32 v195, v92, v88, s67\n"
    "v_mfma_f32_16x16x32_bf16 a[92:95], v[196:199], a[188:191], a[92:95]\n"
    "buffer_load_dwordx4 v[156:159], v201, s[36:39], s66 offen\n"
    "v_mfma_f32_16x16x32_bf16 a[124:127], v[196:199], a[192:195], a[124:127]\n"
    "v_add_u32 v208, s73, v200\n"
    "v_mfma_f32_16x16x32_bf16 a[156:159], v[196:199], a[196:199], a[156:159]\n"
    "s_waitcnt lgkmcnt(0)\n"
    "s_add_u32 s73, s73, 0x40000\n"
    "s_cmp_eq_u32 s73, 0x480000\n"
    "s_cselect_b32 s73, 0, s73\n"
    "A_QCHK1:\n"
    "s_sub_u32 s77, s20, s14\n"
    "s_cmp_lt_u32 s77, 0xc8\n"
    "s_cbranch_scc0 A_QOK1\n"
    "s_memrealtime s[14:15]\n"
    "s_waitcnt lgkmcnt(0)\n"
    "s_branch A_QCHK1\n"
    "A_QOK1:\n"
    "s_sub_u32 s78, s14, s83\n"
    "s_cmp_gt_u32 s78, 0x3f2\n"
    "s_cselect_b32 s77, 47, 0\n"
    "s_add_u32 s20, s14, s77\n"
    "v_mfma_f32_16x16x32_bf16 a[0:3], v[192:195], a[160:163], a[0:3]\n"
    "v_perm_b32 v196, v68, v64, s68\n"
    "v_perm_b32 v197, v76, v72, s68\n"
    "v_mfma_f32_16x16x32_bf16 a[32:35], v[192:195], a[164:167], a[32:35]\n"
    "v_perm_b32 v198, v84, v80, s68\n"
    "v_perm_b32 v199, v92, v88, s68\n"
    "v_mfma_f32_16x16x32_bf16 a[64:67], v[192:195], a[168:171], a[64:67]\n"
    "buffer_load_dwordx4 v[160:163], v208, s[36:39], 0 offen\n"
    "v_mfma_f32_16x16x32_bf16 a[96:99], v[192:195], a[172:175], a[96:99]\n"
    "v_mfma_f32_16x16x32_bf16 a[128:131], v[192:195], a[176:179], a[128:131]\n"
    "v_mfma_f32_16x16x32_bf16 a[4:7], v[196:199], a[160:163], a[4:7]\n"
    "v_perm_b32 v192, v69, v65, s67\n"
    "v_perm_b32 v193, v77, v73, s67\n"
    "v_mfma_f32_16x16x32_bf16 a[36:39], v[196:199], a[164:167], a[36:39]\n"
    "v_perm_b32 v194, v85, v81, s67\n"
    "v_perm_b32 v195, v93, v89, s67\n"
    "v_mfma_f32_16x16x32_bf16 a[68:71], v[196:199], a[168:171], a[68:71]\n"
    "buffer_load_dwordx4 v[164:167], v208, s[36:39], s60 offen\n"
    "v_mfma_f32_16x16x32_bf16 a[100:103], v[196:199], a[172:175], a[100:103]\n"
    "ds_read_b128 a[180:183], v204 offset:43008\n"
    "v_mfma_f32_16x16x32_bf16 a[132:135], v[196:199], a[176:179], a[132:135]\n"
    "v_mfma_f32_16x16x32_bf16 a[8:11], v[192:195], a[160:163], a[8:11]\n"
    "v_perm_b32 v196, v69, v65, s68\n"
    "v_perm_b32 v197, v77, v73, s68\n"
    "v_mfma_f32_16x16x32_bf16 a[40:43], v[192:195], a[164:167], a[40:43]\n"
    "v_perm_b32 v198, v85, v81, s68\n"
    "v_perm_b32 v199, v93, v89, s68\n"
    "v_mfma_f32_16x16x32_bf16 a[72:75], v[192:195], a[168:171], a[72:75]\n"
    "buffer_load_dwordx4 v[168:171], v208, s[36:39], s61 offen\n"
    "v_mfma_f32_16x16x32_bf16 a[104:107], v[192:195], a[172:175], a[104:107]\n"
    "ds_read_b128 a[184:187], v204 offset:45056\n"
    "v_mfma_f32_16x16x32_bf16 a[136:139], v[192:195], a[176:179], a[136:139]\n"
    "v_mfma_f32_16x16x32_bf16 a[12:15], v[196:199], a[160:163], a[12:15]\n"
    "v_perm_b32 v192, v70, v66, s67\n"
    "v_perm_b32 v193, v78, v74, s67\n"
    "v_mfma_f32_16x16x32_bf16 a[44:47], v[196:199], a[164:167], a[44:47]\n"
    "v_perm_b32 v194, v86, v82, s67\n"
    "v_perm_b32 v195, v94, v90, s67\n"
    "v_mfma_f32_16x16x32_bf16 a[76:79], v[196:199], a[168:171], a[76:79]\n"
    "buffer_load_dwordx4 v[172:175], v208, s[36:39], s62 offen\n"
    "v_mfma_f32_16x16x32_bf16 a[108:111], v[196:199], a[172:175], a[108:111]\n"
    "ds_read_b128 a[188:191], v204 offset:47104\n"
    "v_mfma_f32_16x16x32_bf16 a[140:143], v[196:199], a[176:179], a[140:143]\n"
    "v_mfma_f32_16x16x32_bf16 a[16:19], v[192:195], a[160:163], a[16:19]\n"
    "v_perm_b32 v196, v70, v66, s68\n"
    "v_perm_b32 v197, v78, v74, s68\n"
    "v_mfma_f32_16x16x32_bf16 a[48:51], v[192:195], a[164:167], a[48:51]\n"
    "v_perm_b32 v198, v86, v82, s68\n"
    "v_perm_b32 v199, v94, v90, s68\n"
    "v_mfma_f32_16x16x32_bf16 a[80:83], v[192:195], a[168:171], a[80:83]\n"
    "buffer_load_dwordx4 v[176:179], v208, s[36:39], s63 offen\n"
    "v_mfma_f32_16x16x32_bf16 a[112:115], v[192:195], a[172:175], a[112:115]\n"
    "ds_read_b128 a[192:195], v205 offset:0\n"
    "v_mfma_f32_16x16x32_bf16 a[144:147], v[192:195], a[176:179], a[144:147]\n"
    "v_mfma_f32_16x16x32_bf16 a[20:23], v[196:199], a[160:163], a[20:23]\n"
    "v_perm_b32 v192, v71, v67, s67\n"
    "v_perm_b32 v193, v79, v75, s67\n"
    "v_mfma_f32_16x16x32_bf16 a[52:55], v[196:199], a[164:167], a[52:55]\n"
    "v_perm_b32 v194, v87, v83, s67\n"
    "v_perm_b32 v195, v95, v91, s67\n"
    "v_mfma_f32_16x16x32_bf16 a[84:87], v[196:199], a[168:171], a[84:87]\n"
    "buffer_load_dwordx4 v[180:183], v208, s[36:39], s64 offen\n"
    "v_mfma_f32_16x16x32_bf16 a[116:119], v[196:199], a[172:175], a[116:119]\n"
    "ds_read_b128 a[196:199], v205 offset:2048\n"
    "v_mfma_f32_16x16x32_bf16 a[148:151], v[196:199], a[176:179], a[148:151]\n"
    "v_mfma_f32_16x16x32_bf16 a[24:27], v[192:195], a[160:163], a[24:27]\n"
    "v_perm_b32 v196, v71, v67, s68\n"
    "v_perm_b32 v197, v79, v75, s68\n"
    "v_mfma_f32_16x16x32_bf16 a[56:59], v[192:195], a[164:167], a[56:59]\n"
    "v_perm_b32 v198, v87, v83, s68\n"
    "v_perm_b32 v199, v95, v91, s68\n"
    "v_mfma_f32_16x16x32_bf16 a[88:91], v[192:195], a[168:171], a[88:91]\n"
    "buffer_load_dwordx4 v[184:187], v208, s[36:39], s65 offen\n"
    "v_mfma_f32_16x16x32_bf16 a[120:123], v[192:195], a[172:175], a[120:123]\n"
    "s_memrealtime s[14:15]\n"
    "v_mfma_f32_16x16x32_bf16 a[152:155], v[192:195], a[176:179], a[152:155]\n"
    "s_waitcnt vmcnt(15)\n"
    "v_mfma_f32_16x16x32_bf16 a[28:31], v[196:199], a[160:163], a[28:31]\n"
    "v_perm_b32 v192, v100, v96, s67\n"
    "v_perm_b32 v193, v108, v104, s67\n"
    "v_mfma_f32_16x16x32_bf16 a[60:63], v[196:199], a[164:167], a[60:63]\n"
    "v_perm_b32 v194, v116, v112, s67\n"
    "v_perm_b32 v195, v124, v120, s67\n"
    "v_mfma_f32_16x16x32_bf16 a[92:95], v[196:199], a[168:171], a[92:95]\n"
    "buffer_load_dwordx4 v[188:191], v208, s[36:39], s66 offen\n"
    "v_mfma_f32_16x16x32_bf16 a[124:127], v[196:199], a[172:175], a[124:127]\n"
    "v_add_u32 v201, s73, v200\n"
    "v_mfma_f32_16x16x32_bf16 a[156:159], v[196:199], a[176:179], a[156:159]\n"
    "s_waitcnt lgkmcnt(0)\n"
    "s_add_u32 s73, s73, 0x40000\n"
    "s_cmp_eq_u32 s73, 0x480000\n"
    "s_cselect_b32 s73, 0, s73\n"
    "A_QCHK2:\n"
    "s_sub_u32 s77, s20, s14\n"
    "s_cmp_lt_u32 s77, 0xc8\n"
    "s_cbranch_scc0 A_QOK2\n"
    "s_memrealtime s[14:15]\n"
    "s_waitcnt lgkmcnt(0)\n"
    "s_branch A_QCHK2\n"
    "A_QOK2:\n"
    "s_sub_u32 s78, s14, s83\n"
    "s_cmp_gt_u32 s78, 0x3f2\n"
    "s_cselect_b32 s77, 47, 0\n"
    "s_add_u32 s20, s14, s77\n"
    "v_mfma_f32_16x16x32_bf16 a[0:3], v[192:195], a[180:183], a[0:3]\n"
    "v_perm_b32 v196, v100, v96, s68\n"
    "v_perm_b32 v197, v108, v104, s68\n"
    "v_mfma_f32_16x16x32_bf16 a[32:35], v[192:195], a[184:187], a[32:35]\n"
    "v_perm_b32 v198, v116, v112, s68\n"
    "v_perm_b32 v199, v124, v120, s68\n"
    "v_mfma_f32_16x16x32_bf16 a[64:67], v[192:195], a[188:191], a[64:67]\n"
    "buffer_load_dwordx4 v[64:67], v201, s[36:39], 0 offen\n"
    "v_mfma_f32_16x16x32_bf16 a[96:99], v[192:195], a[192:195], a[96:99]\n"
    "v_mfma_f32_16x16x32_bf16 a[128:131], v[192:195], a[196:199], a[128:131]\n"
    "v_mfma_f32_16x16x32_bf16 a[4:7], v[196:199], a[180:183], a[4:7]\n"
    "v_perm_b32 v192, v101, v97, s67\n"
    "v_perm_b32 v193, v109, v105, s67\n"
    "v_mfma_f32_16x16x32_bf16 a[36:39], v[196:199], a[184:187], a[36:39]\n"
    "v_perm_b32 v194, v117, v113, s67\n"
    "v_perm_b32 v195, v125, v121, s67\n"
    "v_mfma_f32_16x16x32_bf16 a[68:71], v[196:199], a[188:191], a[68:71]\n"
    "buffer_load_dwordx4 v[68:71], v201, s[36:39], s60 offen\n"
    "v_mfma_f32_16x16x32_bf16 a[100:103], v[196:199], a[192:195], a[100:103]\n"
    "ds_read_b128 a[160:163], v203 offset:4096\n"
    "v_mfma_f32_16x16x32_bf16 a[132:135], v[196:199], a[196:199], a[132:135]\n"
    "v_mfma_f32_16x16x32_bf16 a[8:11], v[192:195], a[180:183], a[8:11]\n"
    "v_perm_b32 v196, v101, v97, s68\n"
    "v_perm_b32 v197, v109, v105, s68\n"
    "v_mfma_f32_16x16x32_bf16 a[40:43], v[192:195], a[184:187], a[40:43]\n"
    "v_perm_b32 v198, v117, v113, s68\n"
    "v_perm_b32 v199, v125, v121, s68\n"
    "v_mfma_f32_16x16x32_bf16 a[72:75], v[192:195], a[188:191], a[72:75]\n"
    "buffer_load_dwordx4 v[72:75], v201, s[36:39], s61 offen\n"
    "v_mfma_f32_16x16x32_bf16 a[104:107], v[192:195], a[192:195], a[104:107]\n"
    "ds_read_b128 a[164:167], v203 offset:6144\n"
    "v_mfma_f32_16x16x32_bf16 a[136:139], v[192:195], a[196:199], a[136:139]\n"
    "v_mfma_f32_16x16x32_bf16 a[12:15], v[196:199], a[180:183], a[12:15]\n"
    "v_perm_b32 v192, v102, v98, s67\n"
    "v_perm_b32 v193, v110, v106, s67\n"
    "v_mfma_f32_16x16x32_bf16 a[44:47], v[196:199], a[184:187], a[44:47]\n"
    "v_perm_b32 v194, v118, v114, s67\n"
    "v_perm_b32 v195, v126, v122, s67\n"
    "v_mfma_f32_16x16x32_bf16 a[76:79], v[196:199], a[188:191], a[76:79]\n"
    "buffer_load_dwordx4 v[76:79], v201, s[36:39], s62 offen\n"
    "v_mfma_f32_16x16x32_bf16 a[108:111], v[196:199], a[192:195], a[108:111]\n"
    "ds_read_b128 a[168:171], v203 offset:40960\n"
    "v_mfma_f32_16x16x32_bf16 a[140:143], v[196:199], a[196:199], a[140:143]\n"
    "v_mfma_f32_16x16x32_bf16 a[16:19], v[192:195], a[180:183], a[16:19]\n"
    "v_perm_b32 v196, v102, v98, s68\n"
    "v_perm_b32 v197, v110, v106, s68\n"
    "v_mfma_f32_16x16x32_bf16 a[48:51], v[192:195], a[184:187], a[48:51]\n"
    "v_perm_b32 v198, v118, v114, s68\n"
    "v_perm_b32 v199, v126, v122, s68\n"
    "v_mfma_f32_16x16x32_bf16 a[80:83], v[192:195], a[188:191], a[80:83]\n"
    "buffer_load_dwordx4 v[80:83], v201, s[36:39], s63 offen\n"
    "v_mfma_f32_16x16x32_bf16 a[112:115], v[192:195], a[192:195], a[112:115]\n"
    "ds_read_b128 a[172:175], v203 offset:43008\n"
    "v_mfma_f32_16x16x32_bf16 a[144:147], v[192:195], a[196:199], a[144:147]\n"
    "v_mfma_f32_16x16x32_bf16 a[20:23], v[196:199], a[180:183], a[20:23]\n"
    "v_perm_b32 v192, v103, v99, s67\n"
    "v_perm_b32 v193, v111, v107, s67\n"
    "v_mfma_f32_16x16x32_bf16 a[52:55], v[196:199], a[184:187], a[52:55]\n"
    "v_perm_b32 v194, v119, v115, s67\n"
    "v_perm_b32 v195, v127, v123, s67\n"
    "v_mfma_f32_16x16x32_bf16 a[84:87], v[196:199], a[188:191], a[84:87]\n"
    "buffer_load_dwordx4 v[84:87], v201, s[36:39], s64 offen\n"
    "v_mfma_f32_16x16x32_bf16 a[116:119], v[196:199], a[192:195], a[116:119]\n"
    "ds_read_b128 a[176:179], v203 offset:45056\n"
    "v_mfma_f32_16x16x32_bf16 a[148:151], v[196:199], a[196:199], a[148:151]\n"
    "v_mfma_f32_16x16x32_bf16 a[24:27], v[192:195], a[180:183], a[24:27]\n"
    "v_perm_b32 v196, v103, v99, s68\n"
    "v_perm_b32 v197, v111, v107, s68\n"
    "v_mfma_f32_16x16x32_bf16 a[56:59], v[192:195], a[184:187], a[56:59]\n"
    "v_perm_b32 v198, v119, v115, s68\n"
    "v_perm_b32 v199, v127, v123, s68\n"
    "v_mfma_f32_16x16x32_bf16 a[88:91], v[192:195], a[188:191], a[88:91]\n"
    "buffer_load_dwordx4 v[88:91], v201, s[36:39], s65 offen\n"
    "v_mfma_f32_16x16x32_bf16 a[120:123], v[192:195], a[192:195], a[120:123]\n"
    "s_memrealtime s[14:15]\n"
    "v_mfma_f32_16x16x32_bf16 a[152:155], v[192:195], a[196:199], a[152:155]\n"
    "s_waitcnt vmcnt(15)\n"
    "v_mfma_f32_16x16x32_bf16 a[28:31], v[196:199], a[180:183], a[28:31]\n"
    "v_perm_b32 v192, v132, v128, s67\n"
    "v_perm_b32 v193, v140, v136, s67\n"
    "v_mfma_f32_16x16x32_bf16 a[60:63], v[196:199], a[184:187], a[60:63]\n"
    "v_perm_b32 v194, v148, v144, s67\n"
    "v_perm_b32 v195, v156, v152, s67\n"
    "v_mfma_f32_16x16x32_bf16 a[92:95], v[196:199], a[188:191], a[92:95]\n"
    "buffer_load_dwordx4 v[92:95], v201, s[36:39], s66 offen\n"
    "v_mfma_f32_16x16x32_bf16 a[124:127], v[196:199], a[192:195], a[124:127]\n"
    "v_add_u32 v208, s73, v200\n"
    "v_mfma_f32_16x16x32_bf16 a[156:159], v[196:199], a[196:199], a[156:159]\n"
    "s_waitcnt lgkmcnt(0)\n"
    "s_add_u32 s73, s73, 0x40000\n"
    "s_cmp_eq_u32 s73, 0x480000\n"
    "s_cselect_b32 s73, 0, s73\n"
    "A_QCHK3:\n"
    "s_sub_u32 s77, s20, s14\n"
    "s_cmp_lt_u32 s77, 0xc8\n"
    "s_cbranch_scc0 A_QOK3\n"
    "s_memrealtime s[14:15]\n"
    "s_waitcnt lgkmcnt(0)\n"
    "s_branch A_QCHK3\n"
    "A_QOK3:\n"
    "s_sub_u32 s78, s14, s83\n"
    "s_cmp_gt_u32 s78, 0x3f2\n"
    "s_cselect_b32 s77, 47, 0\n"
    "s_add_u32 s20, s14, s77\n"
    "v_mfma_f32_16x16x32_bf16 a[0:3], v[192:195], a[160:163], a[0:3]\n"
    "v_perm_b32 v196, v132, v128, s68\n"
    "v_perm_b32 v197, v140, v136, s68\n"
    "v_mfma_f32_16x16x32_bf16 a[32:35], v[192:195], a[164:167], a[32:35]\n"
    "v_perm_b32 v198, v148, v144, s68\n"
    "v_perm_b32 v199, v156, v152, s68\n"
    "v_mfma_f32_16x16x32_bf16 a[64:67], v[192:195], a[168:171], a[64:67]\n"
    "buffer_load_dwordx4 v[96:99], v208, s[36:39], 0 offen\n"
    "v_mfma_f32_16x16x32_bf16 a[96:99], v[192:195], a[172:175], a[96:99]\n"
    "v_mfma_f32_16x16x32_bf16 a[128:131], v[192:195], a[176:179], a[128:131]\n"
    "v_mfma_f32_16x16x32_bf16 a[4:7], v[196:199], a[160:163], a[4:7]\n"
    "v_perm_b32 v192, v133, v129, s67\n"
    "v_perm_b32 v193, v141, v137, s67\n"
    "v_mfma_f32_16x16x32_bf16 a[36:39], v[196:199], a[164:167], a[36:39]\n"
    "v_perm_b32 v194, v149, v145, s67\n"
    "v_perm_b32 v195, v157, v153, s67\n"
    "v_mfma_f32_16x16x32_bf16 a[68:71], v[196:199], a[168:171], a[68:71]\n"
    "buffer_load_dwordx4 v[100:103], v208, s[36:39], s60 offen\n"
    "v_mfma_f32_16x16x32_bf16 a[100:103], v[196:199], a[172:175], a[100:103]\n"
    "ds_read_b128 a[180:183], v205 offset:4096\n"
    "v_mfma_f32_16x16x32_bf16 a[132:135], v[196:199], a[176:179], a[132:135]\n"
    "v_mfma_f32_16x16x32_bf16 a[8:11], v[192:195], a[160:163], a[8:11]\n"
    "v_perm_b32 v196, v133, v129, s68\n"
    "v_perm_b32 v197, v141, v137, s68\n"
    "v_mfma_f32_16x16x32_bf16 a[40:43], v[192:195], a[164:167], a[40:43]\n"
    "v_perm_b32 v198, v149, v145, s68\n"
    "v_perm_b32 v199, v157, v153, s68\n"
    "v_mfma_f32_16x16x32_bf16 a[72:75], v[192:195], a[168:171], a[72:75]\n"
    "buffer_load_dwordx4 v[104:107], v208, s[36:39], s61 offen\n"
    "v_mfma_f32_16x16x32_bf16 a[104:107], v[192:195], a[172:175], a[104:107]\n"
    "ds_read_b128 a[184:187], v205 offset:6144\n"
    "v_mfma_f32_16x16x32_bf16 a[136:139], v[192:195], a[176:179], a[136:139]\n"
    "v_mfma_f32_16x16x32_bf16 a[12:15], v[196:199], a[160:163], a[12:15]\n"
    "v_perm_b32 v192, v134, v130, s67\n"
    "v_perm_b32 v193, v142, v138, s67\n"
    "v_mfma_f32_16x16x32_bf16 a[44:47], v[196:199], a[164:167], a[44:47]\n"
    "v_perm_b32 v194, v150, v146, s67\n"
    "v_perm_b32 v195, v158, v154, s67\n"
    "v_mfma_f32_16x16x32_bf16 a[76:79], v[196:199], a[168:171], a[76:79]\n"
    "buffer_load_dwordx4 v[108:111], v208, s[36:39], s62 offen\n"
    "v_mfma_f32_16x16x32_bf16 a[108:111], v[196:199], a[172:175], a[108:111]\n"
    "ds_read_b128 a[188:191], v205 offset:40960\n"
    "v_mfma_f32_16x16x32_bf16 a[140:143], v[196:199], a[176:179], a[140:143]\n"
    "v_mfma_f32_16x16x32_bf16 a[16:19], v[192:195], a[160:163], a[16:19]\n"
    "v_perm_b32 v196, v134, v130, s68\n"
    "v_perm_b32 v197, v142, v138, s68\n"
    "v_mfma_f32_16x16x32_bf16 a[48:51], v[192:195], a[164:167], a[48:51]\n"
    "v_perm_b32 v198, v150, v146, s68\n"
    "v_perm_b32 v199, v158, v154, s68\n"
    "v_mfma_f32_16x16x32_bf16 a[80:83], v[192:195], a[168:171], a[80:83]\n"
    "buffer_load_dwordx4 v[112:115], v208, s[36:39], s63 offen\n"
    "v_mfma_f32_16x16x32_bf16 a[112:115], v[192:195], a[172:175], a[112:115]\n"
    "ds_read_b128 a[192:195], v205 offset:43008\n"
    "v_mfma_f32_16x16x32_bf16 a[144:147], v[192:195], a[176:179], a[144:147]\n"
    "v_mfma_f32_16x16x32_bf16 a[20:23], v[196:199], a[160:163], a[20:23]\n"
    "v_perm_b32 v192, v135, v131, s67\n"
    "v_perm_b32 v193, v143, v139, s67\n"
    "v_mfma_f32_16x16x32_bf16 a[52:55], v[196:199], a[164:167], a[52:55]\n"
    "v_perm_b32 v194, v151, v147, s67\n"
    "v_perm_b32 v195, v159, v155, s67\n"
    "v_mfma_f32_16x16x32_bf16 a[84:87], v[196:199], a[168:171], a[84:87]\n"
    "buffer_load_dwordx4 v[116:119], v208, s[36:39], s64 offen\n"
    "v_mfma_f32_16x16x32_bf16 a[116:119], v[196:199], a[172:175], a[116:119]\n"
    "ds_read_b128 a[196:199], v205 offset:45056\n"
    "v_mfma_f32_16x16x32_bf16 a[148:151], v[196:199], a[176:179], a[148:151]\n"
    "v_mfma_f32_16x16x32_bf16 a[24:27], v[192:195], a[160:163], a[24:27]\n"
    "v_perm_b32 v196, v135, v131, s68\n"
    "v_perm_b32 v197, v143, v139, s68\n"
    "v_mfma_f32_16x16x32_bf16 a[56:59], v[192:195], a[164:167], a[56:59]\n"
    "v_perm_b32 v198, v151, v147, s68\n"
    "v_perm_b32 v199, v159, v155, s68\n"
    "v_mfma_f32_16x16x32_bf16 a[88:91], v[192:195], a[168:171], a[88:91]\n"
    "buffer_load_dwordx4 v[120:123], v208, s[36:39], s65 offen\n"
    "v_mfma_f32_16x16x32_bf16 a[120:123], v[192:195], a[172:175], a[120:123]\n"
    "s_memrealtime s[14:15]\n"
    "v_mfma_f32_16x16x32_bf16 a[152:155], v[192:195], a[176:179], a[152:155]\n"
    "s_waitcnt vmcnt(15)\n"
    "v_mfma_f32_16x16x32_bf16 a[28:31], v[196:199], a[160:163], a[28:31]\n"
    "v_perm_b32 v192, v164, v160, s67\n"
    "v_perm_b32 v193, v172, v168, s67\n"
    "v_mfma_f32_16x16x32_bf16 a[60:63], v[196:199], a[164:167], a[60:63]\n"
    "v_perm_b32 v194, v180, v176, s67\n"
    "v_perm_b32 v195, v188, v184, s67\n"
    "v_mfma_f32_16x16x32_bf16 a[92:95], v[196:199], a[168:171], a[92:95]\n"
    "buffer_load_dwordx4 v[124:127], v208, s[36:39], s66 offen\n"
    "v_mfma_f32_16x16x32_bf16 a[124:127], v[196:199], a[172:175], a[124:127]\n"
    "v_add_u32 v201, s73, v200\n"
    "v_mfma_f32_16x16x32_bf16 a[156:159], v[196:199], a[176:179], a[156:159]\n"
    "s_waitcnt lgkmcnt(0)\n"
    "s_add_u32 s73, s73, 0x40000\n"
    "s_cmp_eq_u32 s73, 0x480000\n"
    "s_cselect_b32 s73, 0, s73\n"
    "A_QCHK4:\n"
    "s_sub_u32 s77, s20, s14\n"
    "s_cmp_lt_u32 s77, 0xc8\n"
    "s_cbranch_scc0 A_QOK4\n"
    "s_memrealtime s[14:15]\n"
    "s_waitcnt lgkmcnt(0)\n"
    "s_branch A_QCHK4\n"
    "A_QOK4:\n"
    "s_sub_u32 s78, s14, s83\n"
    "s_cmp_gt_u32 s78, 0x3f2\n"
    "s_cselect_b32 s77, 47, 0\n"
    "s_add_u32 s20, s14, s77\n"
    "v_mfma_f32_16x16x32_bf16 a[0:3], v[192:195], a[180:183], a[0:3]\n"
    "v_perm_b32 v196, v164, v160, s68\n"
    "v_perm_b32 v197, v172, v168, s68\n"
    "v_mfma_f32_16x16x32_bf16 a[32:35], v[192:195], a[184:187], a[32:35]\n"
    "v_perm_b32 v198, v180, v176, s68\n"
    "v_perm_b32 v199, v188, v184, s68\n"
    "v_mfma_f32_16x16x32_bf16 a[64:67], v[192:195], a[188:191], a[64:67]\n"
    "buffer_load_dwordx4 v[128:131], v201, s[36:39], 0 offen\n"
    "s_waitcnt vmcnt(63)\n"
    "s_barrier\n"
    "v_mfma_f32_16x16x32_bf16 a[96:99], v[192:195], a[192:195], a[96:99]\n"
    "v_add_u32 v202, 0x2000, v202\n"
    "v_add_u32 v203, 0x2000, v203\n"
    "v_mfma_f32_16x16x32_bf16 a[128:131], v[192:195], a[196:199], a[128:131]\n"
    "v_add_u32 v204, 0x2000, v204\n"
    "v_add_u32 v205, 0x2000, v205\n"
    "v_mfma_f32_16x16x32_bf16 a[4:7], v[196:199], a[180:183], a[4:7]\n"
    "v_perm_b32 v192, v165, v161, s67\n"
    "v_perm_b32 v193, v173, v169, s67\n"
    "v_mfma_f32_16x16x32_bf16 a[36:39], v[196:199], a[184:187], a[36:39]\n"
    "v_perm_b32 v194, v181, v177, s67\n"
    "v_perm_b32 v195, v189, v185, s67\n"
    "v_mfma_f32_16x16x32_bf16 a[68:71], v[196:199], a[188:191], a[68:71]\n"
    "buffer_load_dwordx4 v[132:135], v201, s[36:39], s60 offen\n"
    "v_mfma_f32_16x16x32_bf16 a[100:103], v[196:199], a[192:195], a[100:103]\n"
    "ds_read_b128 a[160:163], v202 offset:0\n"
    "v_mfma_f32_16x16x32_bf16 a[132:135], v[196:199], a[196:199], a[132:135]\n"
    "v_mfma_f32_16x16x32_bf16 a[8:11], v[192:195], a[180:183], a[8:11]\n"
    "v_perm_b32 v196, v165, v161, s68\n"
    "v_perm_b32 v197, v173, v169, s68\n"
    "v_mfma_f32_16x16x32_bf16 a[40:43], v[192:195], a[184:187], a[40:43]\n"
    "v_perm_b32 v198, v181, v177, s68\n"
    "v_perm_b32 v199, v189, v185, s68\n"
    "v_mfma_f32_16x16x32_bf16 a[72:75], v[192:195], a[188:191], a[72:75]\n"
    "buffer_load_dwordx4 v[136:139], v201, s[36:39], s61 offen\n"
    "v_mfma_f32_16x16x32_bf16 a[104:107], v[192:195], a[192:195], a[104:107]\n"
    "ds_read_b128 a[164:167], v202 offset:2048\n"
    "v_mfma_f32_16x16x32_bf16 a[136:139], v[192:195], a[196:199], a[136:139]\n"
    "v_mfma_f32_16x16x32_bf16 a[12:15], v[196:199], a[180:183], a[12:15]\n"
    "v_perm_b32 v192, v166, v162, s67\n"
    "v_perm_b32 v193, v174, v170, s67\n"
    "v_mfma_f32_16x16x32_bf16 a[44:47], v[196:199], a[184:187], a[44:47]\n"
    "v_perm_b32 v194, v182, v178, s67\n"
    "v_perm_b32 v195, v190, v186, s67\n"
    "v_mfma_f32_16x16x32_bf16 a[76:79], v[196:199], a[188:191], a[76:79]\n"
    "buffer_load_dwordx4 v[140:143], v201, s[36:39], s62 offen\n"
    "v_mfma_f32_16x16x32_bf16 a[108:111], v[196:199], a[192:195], a[108:111]\n"
    "ds_read_b128 a[168:171], v202 offset:4096\n"
    "v_mfma_f32_16x16x32_bf16 a[140:143], v[196:199], a[196:199], a[140:143]\n"
    "v_mfma_f32_16x16x32_bf16 a[16:19], v[192:195], a[180:183], a[16:19]\n"
    "v_perm_b32 v196, v166, v162, s68\n"
    "v_perm_b32 v197, v174, v170, s68\n"
    "v_mfma_f32_16x16x32_bf16 a[48:51], v[192:195], a[184:187], a[48:51]\n"
    "v_perm_b32 v198, v182, v178, s68\n"
    "v_perm_b32 v199, v190, v186, s68\n"
    "v_mfma_f32_16x16x32_bf16 a[80:83], v[192:195], a[188:191], a[80:83]\n"
    "buffer_load_dwordx4 v[144:147], v201, s[36:39], s63 offen\n"
    "v_mfma_f32_16x16x32_bf16 a[112:115], v[192:195], a[192:195], a[112:115]\n"
    "ds_read_b128 a[172:175], v202 offset:6144\n"
    "v_mfma_f32_16x16x32_bf16 a[144:147], v[192:195], a[196:199], a[144:147]\n"
    "v_mfma_f32_16x16x32_bf16 a[20:23], v[196:199], a[180:183], a[20:23]\n"
    "v_perm_b32 v192, v167, v163, s67\n"
    "v_perm_b32 v193, v175, v171, s67\n"
    "v_mfma_f32_16x16x32_bf16 a[52:55], v[196:199], a[184:187], a[52:55]\n"
    "v_perm_b32 v194, v183, v179, s67\n"
    "v_perm_b32 v195, v191, v187, s67\n"
    "v_mfma_f32_16x16x32_bf16 a[84:87], v[196:199], a[188:191], a[84:87]\n"
    "buffer_load_dwordx4 v[148:151], v201, s[36:39], s64 offen\n"
    "v_mfma_f32_16x16x32_bf16 a[116:119], v[196:199], a[192:195], a[116:119]\n"
    "ds_read_b128 a[176:179], v202 offset:40960\n"
    "v_mfma_f32_16x16x32_bf16 a[148:151], v[196:199], a[196:199], a[148:151]\n"
    "v_mfma_f32_16x16x32_bf16 a[24:27], v[192:195], a[180:183], a[24:27]\n"
    "v_perm_b32 v196, v167, v163, s68\n"
    "v_perm_b32 v197, v175, v171, s68\n"
    "v_mfma_f32_16x16x32_bf16 a[56:59], v[192:195], a[184:187], a[56:59]\n"
    "v_perm_b32 v198, v183, v179, s68\n"
    "v_perm_b32 v199, v191, v187, s68\n"
    "v_mfma_f32_16x16x32_bf16 a[88:91], v[192:195], a[188:191], a[88:91]\n"
    "buffer_load_dwordx4 v[152:155], v201, s[36:39], s65 offen\n"
    "v_mfma_f32_16x16x32_bf16 a[120:123], v[192:195], a[192:195], a[120:123]\n"
    "s_memrealtime s[14:15]\n"
    "v_mfma_f32_16x16x32_bf16 a[152:155], v[192:195], a[196:199], a[152:155]\n"
    "s_waitcnt vmcnt(15)\n"
    "v_mfma_f32_16x16x32_bf16 a[28:31], v[196:199], a[180:183], a[28:31]\n"
    "v_perm_b32 v192, v68, v64, s67\n"
    "v_perm_b32 v193, v76, v72, s67\n"
    "v_mfma_f32_16x16x32_bf16 a[60:63], v[196:199], a[184:187], a[60:63]\n"
    "v_perm_b32 v194, v84, v80, s67\n"
    "v_perm_b32 v195, v92, v88, s67\n"
    "v_mfma_f32_16x16x32_bf16 a[92:95], v[196:199], a[188:191], a[92:95]\n"
    "buffer_load_dwordx4 v[156:159], v201, s[36:39], s66 offen\n"
    "v_mfma_f32_16x16x32_bf16 a[124:127], v[196:199], a[192:195], a[124:127]\n"
    "v_add_u32 v208, s73, v200\n"
    "v_mfma_f32_16x16x32_bf16 a[156:159], v[196:199], a[196:199], a[156:159]\n"
    "s_waitcnt lgkmcnt(0)\n"
    "s_add_u32 s73, s73, 0x40000\n"
    "s_cmp_eq_u32 s73, 0x480000\n"
    "s_cselect_b32 s73, 0, s73\n"
    "A_QCHK5:\n"
    "s_sub_u32 s77, s20, s14\n"
    "s_cmp_lt_u32 s77, 0xc8\n"
    "s_cbranch_scc0 A_QOK5\n"
    "s_memrealtime s[14:15]\n"
    "s_waitcnt lgkmcnt(0)\n"
    "s_branch A_QCHK5\n"
    "A_QOK5:\n"
    "s_sub_u32 s78, s14, s83\n"
    "s_cmp_gt_u32 s78, 0x3f2\n"
    "s_cselect_b32 s77, 47, 0\n"
    "s_add_u32 s20, s14, s77\n"
    "v_mfma_f32_16x16x32_bf16 a[0:3], v[192:195], a[160:163], a[0:3]\n"
    "v_perm_b32 v196, v68, v64, s68\n"
    "v_perm_b32 v197, v76, v72, s68\n"
    "v_mfma_f32_16x16x32_bf16 a[32:35], v[192:195], a[164:167], a[32:35]\n"
    "v_perm_b32 v198, v84, v80, s68\n"
    "v_perm_b32 v199, v92, v88, s68\n"
    "v_mfma_f32_16x16x32_bf16 a[64:67], v[192:195], a[168:171], a[64:67]\n"
    "buffer_load_dwordx4 v[160:163], v208, s[36:39], 0 offen\n"
    "v_mov_b32 v9, 0\n"
    "v_mfma_f32_16x16x32_bf16 a[96:99], v[192:195], a[172:175], a[96:99]\n"
    "v_mov_b32 v0, 0\n"
    "v_mov_b32 v10, 0\n"
    "v_mfma_f32_16x16x32_bf16 a[128:131], v[192:195], a[176:179], a[128:131]\n"
    "v_mov_b32 v1, 0\n"
    "v_mov_b32 v11, 0\n"
    "v_mfma_f32_16x16x32_bf16 a[4:7], v[196:199], a[160:163], a[4:7]\n"
    "v_perm_b32 v192, v69, v65, s67\n"
    "v_perm_b32 v193, v77, v73, s67\n"
    "v_mfma_f32_16x16x32_bf16 a[36:39], v[196:199], a[164:167], a[36:39]\n"
    "v_perm_b32 v194, v85, v81, s67\n"
    "v_perm_b32 v195, v93, v89, s67\n"
    "v_mfma_f32_16x16x32_bf16 a[68:71], v[196:199], a[168:171], a[68:71]\n"
    "buffer_load_dwordx4 v[164:167], v208, s[36:39], s60 offen\n"
    "v_mov_b32 v12, 0\n"
    "v_mfma_f32_16x16x32_bf16 a[100:103], v[196:199], a[172:175], a[100:103]\n"
    "ds_read_b128 a[180:183], v204 offset:0\n"
    "v_mov_b32 v13, 0\n"
    "v_mfma_f32_16x16x32_bf16 a[132:135], v[196:199], a[176:179], a[132:135]\n"
    "v_mov_b32 v2, 0\n"
    "v_mov_b32 v14, 0\n"
    "v_mfma_f32_16x16x32_bf16 a[8:11], v[192:195], a[160:163], a[8:11]\n"
    "v_perm_b32 v196, v69, v65, s68\n"
    "v_perm_b32 v197, v77, v73, s68\n"
    "v_mfma_f32_16x16x32_bf16 a[40:43], v[192:195], a[164:167], a[40:43]\n"
    "v_perm_b32 v198, v85, v81, s68\n"
    "v_perm_b32 v199, v93, v89, s68\n"
    "v_mfma_f32_16x16x32_bf16 a[72:75], v[192:195], a[168:171], a[72:75]\n"
    "buffer_load_dwordx4 v[168:171], v208, s[36:39], s61 offen\n"
    "v_mov_b32 v15, 0\n"
    "v_mfma_f32_16x16x32_bf16 a[104:107], v[192:195], a[172:175], a[104:107]\n"
    "ds_read_b128 a[184:187], v204 offset:2048\n"
    "v_mfma_f32_16x16x32_bf16 a[136:139], v[192:195], a[176:179], a[136:139]\n"
    "v_mov_b32 v3, 0\n"
    "v_mfma_f32_16x16x32_bf16 a[12:15], v[196:199], a[160:163], a[12:15]\n"
    "v_perm_b32 v192, v70, v66, s67\n"
    "A_TH_FIRST1:\n"
    "A_TH_S1:\n"
    "s_cbranch_execz A_TH_S5\n"
    "v_perm_b32 v193, v78, v74, s67\n"
    "v_mfma_f32_16x16x32_bf16 a[44:47], v[196:199], a[164:167], a[44:47]\n"
    "v_perm_b32 v194, v86, v82, s67\n"
    "v_perm_b32 v195, v94, v90, s67\n"
    "v_mfma_f32_16x16x32_bf16 a[76:79], v[196:199], a[168:171], a[76:79]\n"
    "buffer_load_dwordx4 v[172:175], v208, s[36:39], s62 offen\n"
    "v_mfma_f32_16x16x32_bf16 a[108:111], v[196:199], a[172:175], a[108:111]\n"
    "ds_read_b128 a[188:191], v204 offset:4096\n"
    "v_mfma_f32_16x16x32_bf16 a[140:143], v[196:199], a[176:179], a[140:143]\n"
    "v_mov_b32 v4, 0\n"
    "v_mfma_f32_16x16x32_bf16 a[16:19], v[192:195], a[160:163], a[16:19]\n"
    "v_perm_b32 v196, v70, v66, s68\n"
    "v_perm_b32 v197, v78, v74, s68\n"
    "v_mfma_f32_16x16x32_bf16 a[48:51], v[192:195], a[164:167], a[48:51]\n"
    "v_perm_b32 v198, v86, v82, s68\n"
    "v_perm_b32 v199, v94, v90, s68\n"
    "v_mfma_f32_16x16x32_bf16 a[80:83], v[192:195], a[168:171], a[80:83]\n"
    "buffer_load_dwordx4 v[176:179], v208, s[36:39], s63 offen\n"
    "v_mfma_f32_16x16x32_bf16 a[112:115], v[192:195], a[172:175], a[112:115]\n"
    "ds_read_b128 a[192:195], v204 offset:6144\n"
    "v_mfma_f32_16x16x32_bf16 a[144:147], v[192:195], a[176:179], a[144:147]\n"
    "v_mov_b32 v5, 0\n"
    "v_mfma_f32_16x16x32_bf16 a[20:23], v[196:199], a[160:163], a[20:23]\n"
    "v_perm_b32 v192, v71, v67, s67\n"
    "v_perm_b32 v193, v79, v75, s67\n"
    "v_mfma_f32_16x16x32_bf16 a[52:55], v[196:199], a[164:167], a[52:55]\n"
    "v_perm_b32 v194, v87, v83, s67\n"
    "v_perm_b32 v195, v95, v91, s67\n"
    "v_mfma_f32_16x16x32_bf16 a[84:87], v[196:199], a[168:171], a[84:87]\n"
    "buffer_load_dwordx4 v[180:183], v208, s[36:39], s64 offen\n"
    "v_mfma_f32_16x16x32_bf16 a[116:119], v[196:199], a[172:175], a[116:119]\n"
    "ds_read_b128 a[196:199], v204 offset:40960\n"
    "v_mfma_f32_16x16x32_bf16 a[148:151], v[196:199], a[176:179], a[148:151]\n"
    "v_mov_b32 v6, 0\n"
    "v_mfma_f32_16x16x32_bf16 a[24:27], v[192:195], a[160:163], a[24:27]\n"
    "v_perm_b32 v196, v71, v67, s68\n"
    "v_perm_b32 v197, v79, v75, s68\n"
    "v_mfma_f32_16x16x32_bf16 a[56:59], v[192:195], a[164:167], a[56:59]\n"
    "v_perm_b32 v198, v87, v83, s68\n"
    "v_perm_b32 v199, v95, v91, s68\n"
    "v_mfma_f32_16x16x32_bf16 a[88:91], v[192:195], a[168:171], a[88:91]\n"
    "buffer_load_dwordx4 v[184:187], v208, s[36:39], s65 offen\n"
    "v_mfma_f32_16x16x32_bf16 a[120:123], v[192:195], a[172:175], a[120:123]\n"
    "v_mov_b32 v7, 0\n"
    "s_memrealtime s[14:15]\n"
    "v_mfma_f32_16x16x32_bf16 a[152:155], v[192:195], a[176:179], a[152:155]\n"
    "s_waitcnt vmcnt(15)\n"
    "v_mov_b32 v8, 0\n"
    "v_mfma_f32_16x16x32_bf16 a[28:31], v[196:199], a[160:163], a[28:31]\n"
    "v_perm_b32 v192, v100, v96, s67\n"
    "v_perm_b32 v193, v108, v104, s67\n"
    "v_mfma_f32_16x16x32_bf16 a[60:63], v[196:199], a[164:167], a[60:63]\n"
    "v_perm_b32 v194, v116, v112, s67\n"
    "v_perm_b32 v195, v124, v120, s67\n"
    "v_mfma_f32_16x16x32_bf16 a[92:95], v[196:199], a[168:171], a[92:95]\n"
    "buffer_load_dwordx4 v[188:191], v208, s[36:39], s66 offen\n"
    "v_mfma_f32_16x16x32_bf16 a[124:127], v[196:199], a[172:175], a[124:127]\n"
    "v_add_u32 v201, s73, v200\n"
    "v_mfma_f32_16x16x32_bf16 a[156:159], v[196:199], a[176:179], a[156:159]\n"
    "s_waitcnt lgkmcnt(0)\n"
    "s_add_u32 s73, s73, 0x40000\n"
    "s_cmp_eq_u32 s73, 0x480000\n"
    "s_cselect_b32 s73, 0, s73\n"
    "A_QCHK6:\n"
    "s_sub_u32 s77, s20, s14\n"
    "s_cmp_lt_u32 s77, 0xc8\n"
    "s_cbranch_scc0 A_QOK6\n"
    "s_memrealtime s[14:15]\n"
    "s_waitcnt lgkmcnt(0)\n"
    "s_branch A_QCHK6\n"
    "A_QOK6:\n"
    "s_sub_u32 s78, s14, s83\n"
    "s_cmp_gt_u32 s78, 0x3f2\n"
    "s_cselect_b32 s77, 47, 0\n"
    "s_add_u32 s20, s14, s77\n"
    "v_mfma_f32_16x16x32_bf16 a[0:3], v[192:195], a[180:183], a[0:3]\n"
    "v_perm_b32 v196, v100, v96, s68\n"
    "v_perm_b32 v197, v108, v104, s68\n"
    "v_mfma_f32_16x16x32_bf16 a[32:35], v[192:195], a[184:187], a[32:35]\n"
    "v_perm_b32 v198, v116, v112, s68\n"
    "v_perm_b32 v199, v124, v120, s68\n"
    "v_mfma_f32_16x16x32_bf16 a[64:67], v[192:195], a[188:191], a[64:67]\n"
    "buffer_load_dwordx4 v[64:67], v201, s[36:39], 0 offen\n"
    "v_mov_b32 v25, 0\n"
    "v_mfma_f32_16x16x32_bf16 a[96:99], v[192:195], a[192:195], a[96:99]\n"
    "v_mov_b32 v16, 0\n"
    "v_mov_b32 v26, 0\n"
    "v_mfma_f32_16x16x32_bf16 a[128:131], v[192:195], a[196:199], a[128:131]\n"
    "v_mov_b32 v17, 0\n"
    "v_mov_b32 v27, 0\n"
    "v_mfma_f32_16x16x32_bf16 a[4:7], v[196:199], a[180:183], a[4:7]\n"
    "v_perm_b32 v192, v101, v97, s67\n"
    "v_perm_b32 v193, v109, v105, s67\n"
    "v_mfma_f32_16x16x32_bf16 a[36:39], v[196:199], a[184:187], a[36:39]\n"
    "v_perm_b32 v194, v117, v113, s67\n"
    "v_perm_b32 v195, v125, v121, s67\n"
    "v_mfma_f32_16x16x32_bf16 a[68:71], v[196:199], a[188:191], a[68:71]\n"
    "buffer_load_dwordx4 v[68:71], v201, s[36:39], s60 offen\n"
    "v_mov_b32 v28, 0\n"
    "v_mfma_f32_16x16x32_bf16 a[100:103], v[196:199], a[192:195], a[100:103]\n"
    "ds_read_b128 a[160:163], v202 offset:43008\n"
    "v_mov_b32 v29, 0\n"
    "v_mfma_f32_16x16x32_bf16 a[132:135], v[196:199], a[196:199], a[132:135]\n"
    "v_mov_b32 v18, 0\n"
    "v_mov_b32 v30, 0\n"
    "v_mfma_f32_16x16x32_bf16 a[8:11], v[192:195], a[180:183], a[8:11]\n"
    "v_perm_b32 v196, v101, v97, s68\n"
    "v_perm_b32 v197, v109, v105, s68\n"
    "v_mfma_f32_16x16x32_bf16 a[40:43], v[192:195], a[184:187], a[40:43]\n"
    "v_perm_b32 v198, v117, v113, s68\n"
    "v_perm_b32 v199, v125, v121, s68\n"
    "v_mfma_f32_16x16x32_bf16 a[72:75], v[192:195], a[188:191], a[72:75]\n"
    "buffer_load_dwordx4 v[72:75], v201, s[36:39], s61 offen\n"
    "v_mov_b32 v31, 0\n"
    "v_mfma_f32_16x16x32_bf16 a[104:107], v[192:195], a[192:195], a[104:107]\n"
    "ds_read_b128 a[164:167], v202 offset:45056\n"
    "v_mfma_f32_16x16x32_bf16 a[136:139], v[192:195], a[196:199], a[136:139]\n"
    "v_mov_b32 v19, 0\n"
    "v_mfma_f32_16x16x32_bf16 a[12:15], v[196:199], a[180:183], a[12:15]\n"
    "v_perm_b32 v192, v102, v98, s67\n"
    "v_perm_b32 v193, v110, v106, s67\n"
    "v_mfma_f32_16x16x32_bf16 a[44:47], v[196:199], a[184:187], a[44:47]\n"
    "v_perm_b32 v194, v118, v114, s67\n"
    "v_perm_b32 v195, v126, v122, s67\n"
    "v_mfma_f32_16x16x32_bf16 a[76:79], v[196:199], a[188:191], a[76:79]\n"
    "buffer_load_dwordx4 v[76:79], v201, s[36:39], s62 offen\n"
    "v_mfma_f32_16x16x32_bf16 a[108:111], v[196:199], a[192:195], a[108:111]\n"
    "ds_read_b128 a[168:171], v202 offset:47104\n"
    "v_mfma_f32_16x16x32_bf16 a[140:143], v[196:199], a[196:199], a[140:143]\n"
    "v_mov_b32 v20, 0\n"
    "v_mfma_f32_16x16x32_bf16 a[16:19], v[192:195], a[180:183], a[16:19]\n"
    "v_perm_b32 v196, v102, v98, s68\n"
    "v_perm_b32 v197, v110, v106, s68\n"
    "v_mfma_f32_16x16x32_bf16 a[48:51], v[192:195], a[184:187], a[48:51]\n"
    "v_perm_b32 v198, v118, v114, s68\n"
    "v_perm_b32 v199, v126, v122, s68\n"
    "v_mfma_f32_16x16x32_bf16 a[80:83], v[192:195], a[188:191], a[80:83]\n"
    "buffer_load_dwordx4 v[80:83], v201, s[36:39], s63 offen\n"
    "v_mfma_f32_16x16x32_bf16 a[112:115], v[192:195], a[192:195], a[112:115]\n"
    "ds_read_b128 a[172:175], v203 offset:0\n"
    "v_mfma_f32_16x16x32_bf16 a[144:147], v[192:195], a[196:199], a[144:147]\n"
    "v_mov_b32 v21, 0\n"
    "v_mfma_f32_16x16x32_bf16 a[20:23], v[196:199], a[180:183], a[20:23]\n"
    "v_perm_b32 v192, v103, v99, s67\n"
    "v_perm_b32 v193, v111, v107, s67\n"
    "v_mfma_f32_16x16x32_bf16 a[52:55], v[196:199], a[184:187], a[52:55]\n"
    "v_perm_b32 v194, v119, v115, s67\n"
    "v_perm_b32 v195, v127, v123, s67\n"
    "v_mfma_f32_16x16x32_bf16 a[84:87], v[196:199], a[188:191], a[84:87]\n"
    "buffer_load_dwordx4 v[84:87], v201, s[36:39], s64 offen\n"
    "v_mfma_f32_16x16x32_bf16 a[116:119], v[196:199], a[192:195], a[116:119]\n"
    "ds_read_b128 a[176:179], v203 offset:2048\n"
    "v_mfma_f32_16x16x32_bf16 a[148:151], v[196:199], a[196:199], a[148:151]\n"
    "v_mov_b32 v22, 0\n"
    "v_mfma_f32_16x16x32_bf16 a[24:27], v[192:195], a[180:183], a[24:27]\n"
    "v_perm_b32 v196, v103, v99, s68\n"
    "v_perm_b32 v197, v111, v107, s68\n"
    "v_mfma_f32_16x16x32_bf16 a[56:59], v[192:195], a[184:187], a[56:59]\n"
    "v_perm_b32 v198, v119, v115, s68\n"
    "v_perm_b32 v199, v127, v123, s68\n"
    "v_mfma_f32_16x16x32_bf16 a[88:91], v[192:195], a[188:191], a[88:91]\n"
    "buffer_load_dwordx4 v[88:91], v201, s[36:39], s65 offen\n"
    "v_mfma_f32_16x16x32_bf16 a[120:123], v[192:195], a[192:195], a[120:123]\n"
    "v_mov_b32 v23, 0\n"
    "s_memrealtime s[14:15]\n"
    "v_mfma_f32_16x16x32_bf16 a[152:155], v[192:195], a[196:199], a[152:155]\n"
    "s_waitcnt vmcnt(15)\n"
    "v_mov_b32 v24, 0\n"
    "v_mfma_f32_16x16x32_bf16 a[28:31], v[196:199], a[180:183], a[28:31]\n"
    "v_perm_b32 v192, v132, v128, s67\n"
    "v_perm_b32 v193, v140, v136, s67\n"
    "v_mfma_f32_16x16x32_bf16 a[60:63], v[196:199], a[184:187], a[60:63]\n"
    "v_perm_b32 v194, v148, v144, s67\n"
    "v_perm_b32 v195, v156, v152, s67\n"
    "v_mfma_f32_16x16x32_bf16 a[92:95], v[196:199], a[188:191], a[92:95]\n"
    "buffer_load_dwordx4 v[92:95], v201, s[36:39], s66 offen\n"
    "v_mfma_f32_16x16x32_bf16 a[124:127], v[196:199], a[192:195], a[124:127]\n"
    "v_add_u32 v208, s73, v200\n"
    "v_mfma_f32_16x16x32_bf16 a[156:159], v[196:199], a[196:199], a[156:159]\n"
    "s_waitcnt lgkmcnt(0)\n"
    "s_add_u32 s73, s73, 0x40000\n"
    "s_cmp_eq_u32 s73, 0x480000\n"
    "s_cselect_b32 s73, 0, s73\n"
    "A_QCHK7:\n"
    "s_sub_u32 s77, s20, s14\n"
    "s_cmp_lt_u32 s77, 0xc8\n"
    "s_cbranch_scc0 A_QOK7\n"
    "s_memrealtime s[14:15]\n"
    "s_waitcnt lgkmcnt(0)\n"
    "s_branch A_QCHK7\n"
    "A_QOK7:\n"
    "s_sub_u32 s78, s14, s83\n"
    "s_cmp_gt_u32 s78, 0x3f2\n"
    "s_cselect_b32 s77, 47, 0\n"
    "s_add_u32 s20, s14, s77\n"
    "v_mfma_f32_16x16x32_bf16 a[0:3], v[192:195], a[160:163], a[0:3]\n"
    "v_perm_b32 v196, v132, v128, s68\n"
    "v_perm_b32 v197, v140, v136, s68\n"
    "v_mfma_f32_16x16x32_bf16 a[32:35], v[192:195], a[164:167], a[32:35]\n"
    "v_perm_b32 v198, v148, v144, s68\n"
    "v_perm_b32 v199, v156, v152, s68\n"
    "v_mfma_f32_16x16x32_bf16 a[64:67], v[192:195], a[168:171], a[64:67]\n"
    "buffer_load_dwordx4 v[96:99], v208, s[36:39], 0 offen\n"
    "v_mov_b32 v41, 0\n"
    "v_mfma_f32_16x16x32_bf16 a[96:99], v[192:195], a[172:175], a[96:99]\n"
    "v_mov_b32 v32, 0\n"
    "v_mov_b32 v42, 0\n"
    "v_mfma_f32_16x16x32_bf16 a[128:131], v[192:195], a[176:179], a[128:131]\n"
    "v_mov_b32 v33, 0\n"
    "v_mov_b32 v43, 0\n"
    "v_mfma_f32_16x16x32_bf16 a[4:7], v[196:199], a[160:163], a[4:7]\n"
    "v_perm_b32 v192, v133, v129, s67\n"
    "v_perm_b32 v193, v141, v137, s67\n"
    "v_mfma_f32_16x16x32_bf16 a[36:39], v[196:199], a[164:167], a[36:39]\n"
    "v_perm_b32 v194, v149, v145, s67\n"
    "v_perm_b32 v195, v157, v153, s67\n"
    "v_mfma_f32_16x16x32_bf16 a[68:71], v[196:199], a[168:171], a[68:71]\n"
    "buffer_load_dwordx4 v[100:103], v208, s[36:39], s60 offen\n"
    "v_mov_b32 v44, 0\n"
    "v_mfma_f32_16x16x32_bf16 a[100:103], v[196:199], a[172:175], a[100:103]\n"
    "ds_read_b128 a[180:183], v204 offset:43008\n"
    "v_mov_b32 v45, 0\n"
    "v_mfma_f32_16x16x32_bf16 a[132:135], v[196:199], a[176:179], a[132:135]\n"
    "v_mov_b32 v34, 0\n"
    "v_mov_b32 v46, 0\n"
    "v_mfma_f32_16x16x32_bf16 a[8:11], v[192:195], a[160:163], a[8:11]\n"
    "v_perm_b32 v196, v133, v129, s68\n"
    "v_perm_b32 v197, v141, v137, s68\n"
    "v_mfma_f32_16x16x32_bf16 a[40:43], v[192:195], a[164:167], a[40:43]\n"
    "v_perm_b32 v198, v149, v145, s68\n"
    "v_perm_b32 v199, v157, v153, s68\n"
    "v_mfma_f32_16x16x32_bf16 a[72:75], v[192:195], a[168:171], a[72:75]\n"
    "buffer_load_dwordx4 v[104:107], v208, s[36:39], s61 offen\n"
    "v_mov_b32 v47, 0\n"
    "v_mfma_f32_16x16x32_bf16 a[104:107], v[192:195], a[172:175], a[104:107]\n"
    "ds_read_b128 a[184:187], v204 offset:45056\n"
    "v_mfma_f32_16x16x32_bf16 a[136:139], v[192:195], a[176:179], a[136:139]\n"
    "v_mov_b32 v35, 0\n"
    "v_mfma_f32_16x16x32_bf16 a[12:15], v[196:199], a[160:163], a[12:15]\n"
    "v_perm_b32 v192, v134, v130, s67\n"
    "v_perm_b32 v193, v142, v138, s67\n"
    "v_mfma_f32_16x16x32_bf16 a[44:47], v[196:199], a[164:167], a[44:47]\n"
    "v_perm_b32 v194, v150, v146, s67\n"
    "v_perm_b32 v195, v158, v154, s67\n"
    "v_mfma_f32_16x16x32_bf16 a[76:79], v[196:199], a[168:171], a[76:79]\n"
    "buffer_load_dwordx4 v[108:111], v208, s[36:39], s62 offen\n"
    "v_mfma_f32_16x16x32_bf16 a[108:111], v[196:199], a[172:175], a[108:111]\n"
    "ds_read_b128 a[188:191], v204 offset:47104\n"
    "v_mfma_f32_16x16x32_bf16 a[140:143], v[196:199], a[176:179], a[140:143]\n"
    "v_mov_b32 v36, 0\n"
    "v_mfma_f32_16x16x32_bf16 a[16:19], v[192:195], a[160:163], a[16:19]\n"
    "v_perm_b32 v196, v134, v130, s68\n"
    "v_perm_b32 v197, v142, v138, s68\n"
    "v_mfma_f32_16x16x32_bf16 a[48:51], v[192:195], a[164:167], a[48:51]\n"
    "v_perm_b32 v198, v150, v146, s68\n"
    "v_perm_b32 v199, v158, v154, s68\n"
    "v_mfma_f32_16x16x32_bf16 a[80:83], v[192:195], a[168:171], a[80:83]\n"
    "buffer_load_dwordx4 v[112:115], v208, s[36:39], s63 offen\n"
    "v_mfma_f32_16x16x32_bf16 a[112:115], v[192:195], a[172:175], a[112:115]\n"
    "ds_read_b128 a[192:195], v205 offset:0\n"
    "v_mfma_f32_16x16x32_bf16 a[144:147], v[192:195], a[176:179], a[144:147]\n"
    "v_mov_b32 v37, 0\n"
    "v_mfma_f32_16x16x32_bf16 a[20:23], v[196:199], a[160:163], a[20:23]\n"
    "v_perm_b32 v192, v135, v131, s67\n"
    "v_perm_b32 v193, v143, v139, s67\n"
    "v_mfma_f32_16x16x32_bf16 a[52:55], v[196:199], a[164:167], a[52:55]\n"
    "v_perm_b32 v194, v151, v147, s67\n"
    "v_perm_b32 v195, v159, v155, s67\n"
    "v_mfma_f32_16x16x32_bf16 a[84:87], v[196:199], a[168:171], a[84:87]\n"
    "buffer_load_dwordx4 v[116:119], v208, s[36:39], s64 offen\n"
    "v_mfma_f32_16x16x32_bf16 a[116:119], v[196:199], a[172:175], a[116:119]\n"
    "ds_read_b128 a[196:199], v205 offset:2048\n"
    "v_mfma_f32_16x16x32_bf16 a[148:151], v[196:199], a[176:179], a[148:151]\n"
    "v_mov_b32 v38, 0\n"
    "v_mfma_f32_16x16x32_bf16 a[24:27], v[192:195], a[160:163], a[24:27]\n"
    "v_perm_b32 v196, v135, v131, s68\n"
    "v_perm_b32 v197, v143, v139, s68\n"
    "v_mfma_f32_16x16x32_bf16 a[56:59], v[192:195], a[164:167], a[56:59]\n"
    "v_perm_b32 v198, v151, v147, s68\n"
    "v_perm_b32 v199, v159, v155, s68\n"
    "v_mfma_f32_16x16x32_bf16 a[88:91], v[192:195], a[168:171], a[88:91]\n"
    "buffer_load_dwordx4 v[120:123], v208, s[36:39], s65 offen\n"
    "v_mfma_f32_16x16x32_bf16 a[120:123], v[192:195], a[172:175], a[120:123]\n"
    "v_mov_b32 v39, 0\n"
    "s_memrealtime s[14:15]\n"
    "v_mfma_f32_16x16x32_bf16 a[152:155], v[192:195], a[176:179], a[152:155]\n"
    "s_waitcnt vmcnt(15)\n"
    "v_mov_b32 v40, 0\n"
    "v_mfma_f32_16x16x32_bf16 a[28:31], v[196:199], a[160:163], a[28:31]\n"
    "v_perm_b32 v192, v164, v160, s67\n"
    "v_perm_b32 v193, v172, v168, s67\n"
    "v_mfma_f32_16x16x32_bf16 a[60:63], v[196:199], a[164:167], a[60:63]\n"
    "v_perm_b32 v194, v180, v176, s67\n"
    "v_perm_b32 v195, v188, v184, s67\n"
    "v_mfma_f32_16x16x32_bf16 a[92:95], v[196:199], a[168:171], a[92:95]\n"
    "buffer_load_dwordx4 v[124:127], v208, s[36:39], s66 offen\n"
    "v_mfma_f32_16x16x32_bf16 a[124:127], v[196:199], a[172:175], a[124:127]\n"
    "v_add_u32 v201, s73, v200\n"
    "v_mfma_f32_16x16x32_bf16 a[156:159], v[196:199], a[176:179], a[156:159]\n"
    "s_waitcnt lgkmcnt(0)\n"
    "s_add_u32 s73, s73, 0x40000\n"
    "s_cmp_eq_u32 s73, 0x480000\n"
    "s_cselect_b32 s73, 0, s73\n"
    "A_QCHK8:\n"
    "s_sub_u32 s77, s20, s14\n"
    "s_cmp_lt_u32 s77, 0xc8\n"
    "s_cbranch_scc0 A_QOK8\n"
    "s_memrealtime s[14:15]\n"
    "s_waitcnt lgkmcnt(0)\n"
    "s_branch A_QCHK8\n"
    "A_QOK8:\n"
    "s_sub_u32 s78, s14, s83\n"
    "s_cmp_gt_u32 s78, 0x3f2\n"
    "s_cselect_b32 s77, 47, 0\n"
    "s_add_u32 s20, s14, s77\n"
    "v_mfma_f32_16x16x32_bf16 a[0:3], v[192:195], a[180:183], a[0:3]\n"
    "v_perm_b32 v196, v164, v160, s68\n"
    "v_perm_b32 v197, v172, v168, s68\n"
    "v_mfma_f32_16x16x32_bf16 a[32:35], v[192:195], a[184:187], a[32:35]\n"
    "v_perm_b32 v198, v180, v176, s68\n"
    "v_perm_b32 v199, v188, v184, s68\n"
    "v_mfma_f32_16x16x32_bf16 a[64:67], v[192:195], a[188:191], a[64:67]\n"
    "buffer_load_dwordx4 v[128:131], v201, s[36:39], 0 offen\n"
    "v_mov_b32 v57, 0\n"
    "v_mfma_f32_16x16x32_bf16 a[96:99], v[192:195], a[192:195], a[96:99]\n"
    "v_mov_b32 v48, 0\n"
    "v_mov_b32 v58, 0\n"
    "v_mfma_f32_16x16x32_bf16 a[128:131], v[192:195], a[196:199], a[128:131]\n"
    "v_mov_b32 v49, 0\n"
    "v_mov_b32 v59, 0\n"
    "v_mfma_f32_16x16x32_bf16 a[4:7], v[196:199], a[180:183], a[4:7]\n"
    "v_perm_b32 v192, v165, v161, s67\n"
    "v_perm_b32 v193, v173, v169, s67\n"
    "v_mfma_f32_16x16x32_bf16 a[36:39], v[196:199], a[184:187], a[36:39]\n"
    "v_perm_b32 v194, v181, v177, s67\n"
    "v_perm_b32 v195, v189, v185, s67\n"
    "v_mfma_f32_16x16x32_bf16 a[68:71], v[196:199], a[188:191], a[68:71]\n"
    "buffer_load_dwordx4 v[132:135], v201, s[36:39], s60 offen\n"
    "v_mov_b32 v60, 0\n"
    "v_mfma_f32_16x16x32_bf16 a[100:103], v[196:199], a[192:195], a[100:103]\n"
    "ds_read_b128 a[160:163], v203 offset:4096\n"
    "v_mov_b32 v61, 0\n"
    "v_mfma_f32_16x16x32_bf16 a[132:135], v[196:199], a[196:199], a[132:135]\n"
    "v_mov_b32 v50, 0\n"
    "v_mov_b32 v62, 0\n"
    "v_mfma_f32_16x16x32_bf16 a[8:11], v[192:195], a[180:183], a[8:11]\n"
    "v_perm_b32 v196, v165, v161, s68\n"
    "v_perm_b32 v197, v173, v169, s68\n"
    "v_mfma_f32_16x16x32_bf16 a[40:43], v[192:195], a[184:187], a[40:43]\n"
    "v_perm_b32 v198, v181, v177, s68\n"
    "v_perm_b32 v199, v189, v185, s68\n"
    "v_mfma_f32_16x16x32_bf16 a[72:75], v[192:195], a[188:191], a[72:75]\n"
    "buffer_load_dwordx4 v[136:139], v201, s[36:39], s61 offen\n"
    "v_mov_b32 v63, 0\n"
    "v_mfma_f32_16x16x32_bf16 a[104:107], v[192:195], a[192:195], a[104:107]\n"
    "ds_read_b128 a[164:167], v203 offset:6144\n"
    "v_mfma_f32_16x16x32_bf16 a[136:139], v[192:195], a[196:199], a[136:139]\n"
    "v_mov_b32 v51, 0\n"
    "v_mfma_f32_16x16x32_bf16 a[12:15], v[196:199], a[180:183], a[12:15]\n"
    "v_perm_b32 v192, v166, v162, s67\n"
    "v_perm_b32 v193, v174, v170, s67\n"
    "v_mfma_f32_16x16x32_bf16 a[44:47], v[196:199], a[184:187], a[44:47]\n"
    "v_perm_b32 v194, v182, v178, s67\n"
    "v_perm_b32 v195, v190, v186, s67\n"
    "v_mfma_f32_16x16x32_bf16 a[76:79], v[196:199], a[188:191], a[76:79]\n"
    "buffer_load_dwordx4 v[140:143], v201, s[36:39], s62 offen\n"
    "v_mfma_f32_16x16x32_bf16 a[108:111], v[196:199], a[192:195], a[108:111]\n"
    "ds_read_b128 a[168:171], v203 offset:40960\n"
    "v_mfma_f32_16x16x32_bf16 a[140:143], v[196:199], a[196:199], a[140:143]\n"
    "v_mov_b32 v52, 0\n"
    "v_mfma_f32_16x16x32_bf16 a[16:19], v[192:195], a[180:183], a[16:19]\n"
    "v_perm_b32 v196, v166, v162, s68\n"
    "v_perm_b32 v197, v174, v170, s68\n"
    "v_mfma_f32_16x16x32_bf16 a[48:51], v[192:195], a[184:187], a[48:51]\n"
    "v_perm_b32 v198, v182, v178, s68\n"
    "v_perm_b32 v199, v190, v186, s68\n"
    "v_mfma_f32_16x16x32_bf16 a[80:83], v[192:195], a[188:191], a[80:83]\n"
    "buffer_load_dwordx4 v[144:147], v201, s[36:39], s63 offen\n"
    "v_mfma_f32_16x16x32_bf16 a[112:115], v[192:195], a[192:195], a[112:115]\n"
    "ds_read_b128 a[172:175], v203 offset:43008\n"
    "v_mfma_f32_16x16x32_bf16 a[144:147], v[192:195], a[196:199], a[144:147]\n"
    "v_mov_b32 v53, 0\n"
    "v_mfma_f32_16x16x32_bf16 a[20:23], v[196:199], a[180:183], a[20:23]\n"
    "v_perm_b32 v192, v167, v163, s67\n"
    "v_perm_b32 v193, v175, v171, s67\n"
    "v_mfma_f32_16x16x32_bf16 a[52:55], v[196:199], a[184:187], a[52:55]\n"
    "v_perm_b32 v194, v183, v179, s67\n"
    "v_perm_b32 v195, v191, v187, s67\n"
    "v_mfma_f32_16x16x32_bf16 a[84:87], v[196:199], a[188:191], a[84:87]\n"
    "buffer_load_dwordx4 v[148:151], v201, s[36:39], s64 offen\n"
    "v_mfma_f32_16x16x32_bf16 a[116:119], v[196:199], a[192:195], a[116:119]\n"
    "ds_read_b128 a[176:179], v203 offset:45056\n"
    "v_mfma_f32_16x16x32_bf16 a[148:151], v[196:199], a[196:199], a[148:151]\n"
    "v_mov_b32 v54, 0\n"
    "v_mfma_f32_16x16x32_bf16 a[24:27], v[192:195], a[180:183], a[24:27]\n"
    "v_perm_b32 v196, v167, v163, s68\n"
    "v_perm_b32 v197, v175, v171, s68\n"
    "v_mfma_f32_16x16x32_bf16 a[56:59], v[192:195], a[184:187], a[56:59]\n"
    "v_perm_b32 v198, v183, v179, s68\n"
    "v_perm_b32 v199, v191, v187, s68\n"
    "v_mfma_f32_16x16x32_bf16 a[88:91], v[192:195], a[188:191], a[88:91]\n"
    "buffer_load_dwordx4 v[152:155], v201, s[36:39], s65 offen\n"
    "v_mfma_f32_16x16x32_bf16 a[120:123], v[192:195], a[192:195], a[120:123]\n"
    "v_mov_b32 v55, 0\n"
    "s_memrealtime s[14:15]\n"
    "v_mfma_f32_16x16x32_bf16 a[152:155], v[192:195], a[196:199], a[152:155]\n"
    "s_waitcnt vmcnt(15)\n"
    "v_mov_b32 v56, 0\n"
    "v_mfma_f32_16x16x32_bf16 a[28:31], v[196:199], a[180:183], a[28:31]\n"
    "v_perm_b32 v192, v68, v64, s67\n"
    "v_perm_b32 v193, v76, v72, s67\n"
    "v_mfma_f32_16x16x32_bf16 a[60:63], v[196:199], a[184:187], a[60:63]\n"
    "v_perm_b32 v194, v84, v80, s67\n"
    "v_perm_b32 v195, v92, v88, s67\n"
    "v_mfma_f32_16x16x32_bf16 a[92:95], v[196:199], a[188:191], a[92:95]\n"
    "buffer_load_dwordx4 v[156:159], v201, s[36:39], s66 offen\n"
    "v_mfma_f32_16x16x32_bf16 a[124:127], v[196:199], a[192:195], a[124:127]\n"
    "v_add_u32 v208, s73, v200\n"
    "v_mfma_f32_16x16x32_bf16 a[156:159], v[196:199], a[196:199], a[156:159]\n"
    "s_waitcnt lgkmcnt(0)\n"
    "s_add_u32 s73, s73, 0x40000\n"
    "s_cmp_eq_u32 s73, 0x480000\n"
    "s_cselect_b32 s73, 0, s73\n"
    "A_QCHK9:\n"
    "s_sub_u32 s77, s20, s14\n"
    "s_cmp_lt_u32 s77, 0xc8\n"
    "s_cbranch_scc0 A_QOK9\n"
    "s_memrealtime s[14:15]\n"
    "s_waitcnt lgkmcnt(0)\n"
    "s_branch A_QCHK9\n"
    "A_QOK9:\n"
    "s_sub_u32 s78, s14, s83\n"
    "s_cmp_gt_u32 s78, 0x3f2\n"
    "s_cselect_b32 s77, 47, 0\n"
    "s_add_u32 s20, s14, s77\n"
    "v_mfma_f32_16x16x32_bf16 a[0:3], v[192:195], a[160:163], a[0:3]\n"
    "v_perm_b32 v196, v68, v64, s68\n"
    "v_perm_b32 v197, v76, v72, s68\n"
    "v_mfma_f32_16x16x32_bf16 a[32:35], v[192:195], a[164:167], a[32:35]\n"
    "v_perm_b32 v198, v84, v80, s68\n"
    "v_perm_b32 v199, v92, v88, s68\n"
    "v_mfma_f32_16x16x32_bf16 a[64:67], v[192:195], a[168:171], a[64:67]\n"
    "buffer_load_dwordx4 v[160:163], v208, s[36:39], 0 offen\n"
    "v_mfma_f32_16x16x32_bf16 a[96:99], v[192:195], a[172:175], a[96:99]\n"
    "v_mfma_f32_16x16x32_bf16 a[128:131], v[192:195], a[176:179], a[128:131]\n"
    "v_mfma_f32_16x16x32_bf16 a[4:7], v[196:199], a[160:163], a[4:7]\n"
    "v_perm_b32 v192, v69, v65, s67\n"
    "v_perm_b32 v193, v77, v73, s67\n"
    "v_mfma_f32_16x16x32_bf16 a[36:39], v[196:199], a[164:167], a[36:39]\n"
    "v_perm_b32 v194, v85, v81, s67\n"
    "v_perm_b32 v195, v93, v89, s67\n"
    "v_mfma_f32_16x16x32_bf16 a[68:71], v[196:199], a[168:171], a[68:71]\n"
    "buffer_load_dwordx4 v[164:167], v208, s[36:39], s60 offen\n"
    "v_mfma_f32_16x16x32_bf16 a[100:103], v[196:199], a[172:175], a[100:103]\n"
    "ds_read_b128 a[180:183], v205 offset:4096\n"
    "v_mfma_f32_16x16x32_bf16 a[132:135], v[196:199], a[176:179], a[132:135]\n"
    "v_mfma_f32_16x16x32_bf16 a[8:11], v[192:195], a[160:163], a[8:11]\n"
    "v_perm_b32 v196, v69, v65, s68\n"
    "v_perm_b32 v197, v77, v73, s68\n"
    "v_mfma_f32_16x16x32_bf16 a[40:43], v[192:195], a[164:167], a[40:43]\n"
    "v_perm_b32 v198, v85, v81, s68\n"
    "v_perm_b32 v199, v93, v89, s68\n"
    "v_mfma_f32_16x16x32_bf16 a[72:75], v[192:195], a[168:171], a[72:75]\n"
    "buffer_load_dwordx4 v[168:171], v208, s[36:39], s61 offen\n"
    "v_mfma_f32_16x16x32_bf16 a[104:107], v[192:195], a[172:175], a[104:107]\n"
    "ds_read_b128 a[184:187], v205 offset:6144\n"
    "v_mfma_f32_16x16x32_bf16 a[136:139], v[192:195], a[176:179], a[136:139]\n"
    "v_mfma_f32_16x16x32_bf16 a[12:15], v[196:199], a[160:163], a[12:15]\n"
    "v_perm_b32 v192, v70, v66, s67\n"
    "v_perm_b32 v193, v78, v74, s67\n"
    "v_mfma_f32_16x16x32_bf16 a[44:47], v[196:199], a[164:167], a[44:47]\n"
    "v_perm_b32 v194, v86, v82, s67\n"
    "v_perm_b32 v195, v94, v90, s67\n"
    "v_mfma_f32_16x16x32_bf16 a[76:79], v[196:199], a[168:171], a[76:79]\n"
    "buffer_load_dwordx4 v[172:175], v208, s[36:39], s62 offen\n"
    "v_mfma_f32_16x16x32_bf16 a[108:111], v[196:199], a[172:175], a[108:111]\n"
    "ds_read_b128 a[188:191], v205 offset:40960\n"
    "v_mfma_f32_16x16x32_bf16 a[140:143], v[196:199], a[176:179], a[140:143]\n"
    "v_mfma_f32_16x16x32_bf16 a[16:19], v[192:195], a[160:163], a[16:19]\n"
    "v_perm_b32 v196, v70, v66, s68\n"
    "v_perm_b32 v197, v78, v74, s68\n"
    "v_mfma_f32_16x16x32_bf16 a[48:51], v[192:195], a[164:167], a[48:51]\n"
    "v_perm_b32 v198, v86, v82, s68\n"
    "v_perm_b32 v199, v94, v90, s68\n"
    "v_mfma_f32_16x16x32_bf16 a[80:83], v[192:195], a[168:171], a[80:83]\n"
    "buffer_load_dwordx4 v[176:179], v208, s[36:39], s63 offen\n"
    "v_mfma_f32_16x16x32_bf16 a[112:115], v[192:195], a[172:175], a[112:115]\n"
    "ds_read_b128 a[192:195], v205 offset:43008\n"
    "v_mfma_f32_16x16x32_bf16 a[144:147], v[192:195], a[176:179], a[144:147]\n"
    "v_mfma_f32_16x16x32_bf16 a[20:23], v[196:199], a[160:163], a[20:23]\n"
    "v_perm_b32 v192, v71, v67, s67\n"
    "v_perm_b32 v193, v79, v75, s67\n"
    "v_mfma_f32_16x16x32_bf16 a[52:55], v[196:199], a[164:167], a[52:55]\n"
    "v_perm_b32 v194, v87, v83, s67\n"
    "v_perm_b32 v195, v95, v91, s67\n"
    "v_mfma_f32_16x16x32_bf16 a[84:87], v[196:199], a[168:171], a[84:87]\n"
    "buffer_load_dwordx4 v[180:183], v208, s[36:39], s64 offen\n"
    "v_mfma_f32_16x16x32_bf16 a[116:119], v[196:199], a[172:175], a[116:119]\n"
    "ds_read_b128 a[196:199], v205 offset:45056\n"
    "v_mfma_f32_16x16x32_bf16 a[148:151], v[196:199], a[176:179], a[148:151]\n"
    "v_mfma_f32_16x16x32_bf16 a[24:27], v[192:195], a[160:163], a[24:27]\n"
    "v_perm_b32 v196, v71, v67, s68\n"
    "v_perm_b32 v197, v79, v75, s68\n"
    "v_mfma_f32_16x16x32_bf16 a[56:59], v[192:195], a[164:167], a[56:59]\n"
    "v_perm_b32 v198, v87, v83, s68\n"
    "v_perm_b32 v199, v95, v91, s68\n"
    "v_mfma_f32_16x16x32_bf16 a[88:91], v[192:195], a[168:171], a[88:91]\n"
    "buffer_load_dwordx4 v[184:187], v208, s[36:39], s65 offen\n"
    "v_mfma_f32_16x16x32_bf16 a[120:123], v[192:195], a[172:175], a[120:123]\n"
    "s_memrealtime s[14:15]\n"
    "v_mfma_f32_16x16x32_bf16 a[152:155], v[192:195], a[176:179], a[152:155]\n"
    "s_waitcnt vmcnt(15)\n"
    "v_mfma_f32_16x16x32_bf16 a[28:31], v[196:199], a[160:163], a[28:31]\n"
    "v_perm_b32 v192, v100, v96, s67\n"
    "v_perm_b32 v193, v108, v104, s67\n"
    "v_mfma_f32_16x16x32_bf16 a[60:63], v[196:199], a[164:167], a[60:63]\n"
    "v_perm_b32 v194, v116, v112, s67\n"
    "v_perm_b32 v195, v124, v120, s67\n"
    "v_mfma_f32_16x16x32_bf16 a[92:95], v[196:199], a[168:171], a[92:95]\n"
    "buffer_load_dwordx4 v[188:191], v208, s[36:39], s66 offen\n"
    "v_mfma_f32_16x16x32_bf16 a[124:127], v[196:199], a[172:175], a[124:127]\n"
    "v_add_u32 v201, s73, v200\n"
    "v_mfma_f32_16x16x32_bf16 a[156:159], v[196:199], a[176:179], a[156:159]\n"
    "s_waitcnt lgkmcnt(0)\n"
    "s_add_u32 s73, s73, 0x40000\n"
    "s_cmp_eq_u32 s73, 0x480000\n"
    "s_cselect_b32 s73, 0, s73\n"
    "A_QCHK10:\n"
    "s_sub_u32 s77, s20, s14\n"
    "s_cmp_lt_u32 s77, 0xc8\n"
    "s_cbranch_scc0 A_QOK10\n"
    "s_memrealtime s[14:15]\n"
    "s_waitcnt lgkmcnt(0)\n"
    "s_branch A_QCHK10\n"
    "A_QOK10:\n"
    "s_sub_u32 s78, s14, s83\n"
    "s_cmp_gt_u32 s78, 0x3f2\n"
    "s_cselect_b32 s77, 47, 0\n"
    "s_add_u32 s20, s14, s77\n"
    "v_mfma_f32_16x16x32_bf16 a[0:3], v[192:195], a[180:183], a[0:3]\n"
    "v_perm_b32 v196, v100, v96, s68\n"
    "v_perm_b32 v197, v108, v104, s68\n"
    "v_mfma_f32_16x16x32_bf16 a[32:35], v[192:195], a[184:187], a[32:35]\n"
    "v_perm_b32 v198, v116, v112, s68\n"
    "v_perm_b32 v199, v124, v120, s68\n"
    "v_mfma_f32_16x16x32_bf16 a[64:67], v[192:195], a[188:191], a[64:67]\n"
    "buffer_load_dwordx4 v[64:67], v201, s[36:39], 0 offen\n"
    "s_waitcnt vmcnt(63)\n"
    "s_barrier\n"
    "v_mfma_f32_16x16x32_bf16 a[96:99], v[192:195], a[192:195], a[96:99]\n"
    "v_add_u32 v202, 0xffffc000, v202\n"
    "v_add_u32 v203, 0xffffc000, v203\n"
    "v_mfma_f32_16x16x32_bf16 a[128:131], v[192:195], a[196:199], a[128:131]\n"
    "v_add_u32 v204, 0xffffc000, v204\n"
    "v_add_u32 v205, 0xffffc000, v205\n"
    "v_mfma_f32_16x16x32_bf16 a[4:7], v[196:199], a[180:183], a[4:7]\n"
    "v_perm_b32 v192, v101, v97, s67\n"
    "v_perm_b32 v193, v109, v105, s67\n"
    "v_mfma_f32_16x16x32_bf16 a[36:39], v[196:199], a[184:187], a[36:39]\n"
    "v_perm_b32 v194, v117, v113, s67\n"
    "v_perm_b32 v195, v125, v121, s67\n"
    "v_mfma_f32_16x16x32_bf16 a[68:71], v[196:199], a[188:191], a[68:71]\n"
    "buffer_load_dwordx4 v[68:71], v201, s[36:39], s60 offen\n"
    "v_mfma_f32_16x16x32_bf16 a[100:103], v[196:199], a[192:195], a[100:103]\n"
    "ds_read_b128 a[160:163], v202 offset:0\n"
    "v_mfma_f32_16x16x32_bf16 a[132:135], v[196:199], a[196:199], a[132:135]\n"
    "v_mfma_f32_16x16x32_bf16 a[8:11], v[192:195], a[180:183], a[8:11]\n"
    "v_perm_b32 v196, v101, v97, s68\n"
    "v_perm_b32 v197, v109, v105, s68\n"
    "v_mfma_f32_16x16x32_bf16 a[40:43], v[192:195], a[184:187], a[40:43]\n"
    "v_perm_b32 v198, v117, v113, s68\n"
    "v_perm_b32 v199, v125, v121, s68\n"
    "v_mfma_f32_16x16x32_bf16 a[72:75], v[192:195], a[188:191], a[72:75]\n"
    "buffer_load_dwordx4 v[72:75], v201, s[36:39], s61 offen\n"
    "v_mfma_f32_16x16x32_bf16 a[104:107], v[192:195], a[192:195], a[104:107]\n"
    "ds_read_b128 a[164:167], v202 offset:2048\n"
    "v_mfma_f32_16x16x32_bf16 a[136:139], v[192:195], a[196:199], a[136:139]\n"
    "A_TH_FIRST2:\n"
    "A_TH_S2:\n"
    "s_cbranch_execz A_TH_RET\n"
    "v_mfma_f32_16x16x32_bf16 a[12:15], v[196:199], a[180:183], a[12:15]\n"
    "v_perm_b32 v192, v102, v98, s67\n"
    "v_perm_b32 v193, v110, v106, s67\n"
    "v_mfma_f32_16x16x32_bf16 a[44:47], v[196:199], a[184:187], a[44:47]\n"
    "v_perm_b32 v194, v118, v114, s67\n"
    "v_perm_b32 v195, v126, v122, s67\n"
    "v_mfma_f32_16x16x32_bf16 a[76:79], v[196:199], a[188:191], a[76:79]\n"
    "buffer_load_dwordx4 v[76:79], v201, s[36:39], s62 offen\n"
    "v_mfma_f32_16x16x32_bf16 a[108:111], v[196:199], a[192:195], a[108:111]\n"
    "ds_read_b128 a[168:171], v202 offset:4096\n"
    "v_mfma_f32_16x16x32_bf16 a[140:143], v[196:199], a[196:199], a[140:143]\n"
    "v_mfma_f32_16x16x32_bf16 a[16:19], v[192:195], a[180:183], a[16:19]\n"
    "v_perm_b32 v196, v102, v98, s68\n"
    "v_perm_b32 v197, v110, v106, s68\n"
    "v_mfma_f32_16x16x32_bf16 a[48:51], v[192:195], a[184:187], a[48:51]\n"
    "v_perm_b32 v198, v118, v114, s68\n"
    "v_perm_b32 v199, v126, v122, s68\n"
    "v_mfma_f32_16x16x32_bf16 a[80:83], v[192:195], a[188:191], a[80:83]\n"
    "buffer_load_dwordx4 v[80:83], v201, s[36:39], s63 offen\n"
    "v_mfma_f32_16x16x32_bf16 a[112:115], v[192:195], a[192:195], a[112:115]\n"
    "ds_read_b128 a[172:175], v202 offset:6144\n"
    "v_mfma_f32_16x16x32_bf16 a[144:147], v[192:195], a[196:199], a[144:147]\n"
    "v_mfma_f32_16x16x32_bf16 a[20:23], v[196:199], a[180:183], a[20:23]\n"
    "v_perm_b32 v192, v103, v99, s67\n"
    "v_perm_b32 v193, v111, v107, s67\n"
    "v_mfma_f32_16x16x32_bf16 a[52:55], v[196:199], a[184:187], a[52:55]\n"
    "v_perm_b32 v194, v119, v115, s67\n"
    "v_perm_b32 v195, v127, v123, s67\n"
    "v_mfma_f32_16x16x32_bf16 a[84:87], v[196:199], a[188:191], a[84:87]\n"
    "buffer_load_dwordx4 v[84:87], v201, s[36:39], s64 offen\n"
    "v_mfma_f32_16x16x32_bf16 a[116:119], v[196:199], a[192:195], a[116:119]\n"
    "ds_read_b128 a[176:179], v202 offset:40960\n"
    "v_mfma_f32_16x16x32_bf16 a[148:151], v[196:199], a[196:199], a[148:151]\n"
    "v_mfma_f32_16x16x32_bf16 a[24:27], v[192:195], a[180:183], a[24:27]\n"
    "v_perm_b32 v196, v103, v99, s68\n"
    "v_perm_b32 v197, v111, v107, s68\n"
    "v_mfma_f32_16x16x32_bf16 a[56:59], v[192:195], a[184:187], a[56:59]\n"
    "v_perm_b32 v198, v119, v115, s68\n"
    "v_perm_b32 v199, v127, v123, s68\n"
    "v_mfma_f32_16x16x32_bf16 a[88:91], v[192:195], a[188:191], a[88:91]\n"
    "buffer_load_dwordx4 v[88:91], v201, s[36:39], s65 offen\n"
    "v_mfma_f32_16x16x32_bf16 a[120:123], v[192:195], a[192:195], a[120:123]\n"
    "s_memrealtime s[14:15]\n"
    "v_mfma_f32_16x16x32_bf16 a[152:155], v[192:195], a[196:199], a[152:155]\n"
    "s_waitcnt vmcnt(15)\n"
    "v_mfma_f32_16x16x32_bf16 a[28:31], v[196:199], a[180:183], a[28:31]\n"
    "v_perm_b32 v192, v132, v128, s67\n"
    "v_perm_b32 v193, v140, v136, s67\n"
    "v_mfma_f32_16x16x32_bf16 a[60:63], v[196:199], a[184:187], a[60:63]\n"
    "v_perm_b32 v194, v148, v144, s67\n"
    "v_perm_b32 v195, v156, v152, s67\n"
    "v_mfma_f32_16x16x32_bf16 a[92:95], v[196:199], a[188:191], a[92:95]\n"
    "buffer_load_dwordx4 v[92:95], v201, s[36:39], s66 offen\n"
    "v_mfma_f32_16x16x32_bf16 a[124:127], v[196:199], a[192:195], a[124:127]\n"
    "v_add_u32 v208, s73, v200\n"
    "v_mfma_f32_16x16x32_bf16 a[156:159], v[196:199], a[196:199], a[156:159]\n"
    "s_waitcnt lgkmcnt(0)\n"
    "s_add_u32 s73, s73, 0x40000\n"
    "s_cmp_eq_u32 s73, 0x480000\n"
    "s_cselect_b32 s73, 0, s73\n"
    "A_QCHK11:\n"
    "s_sub_u32 s77, s20, s14\n"
    "s_cmp_lt_u32 s77, 0xc8\n"
    "s_cbranch_scc0 A_QOK11\n"
    "s_memrealtime s[14:15]\n"
    "s_waitcnt lgkmcnt(0)\n"
    "s_branch A_QCHK11\n"
    "A_QOK11:\n"
    "s_sub_u32 s78, s14, s83\n"
    "s_cmp_gt_u32 s78, 0x3f2\n"
    "s_cselect_b32 s77, 47, 0\n"
    "s_add_u32 s20, s14, s77\n"
    "v_mfma_f32_16x16x32_bf16 a[0:3], v[192:195], a[160:163], a[0:3]\n"
    "v_perm_b32 v196, v132, v128, s68\n"
    "v_perm_b32 v197, v140, v136, s68\n"
    "v_mfma_f32_16x16x32_bf16 a[32:35], v[192:195], a[164:167], a[32:35]\n"
    "v_perm_b32 v198, v148, v144, s68\n"
    "v_perm_b32 v199, v156, v152, s68\n"
    "v_mfma_f32_16x16x32_bf16 a[64:67], v[192:195], a[168:171], a[64:67]\n"
    "buffer_load_dwordx4 v[96:99], v208, s[36:39], 0 offen\n"
    "v_mfma_f32_16x16x32_bf16 a[96:99], v[192:195], a[172:175], a[96:99]\n"
    "v_mfma_f32_16x16x32_bf16 a[128:131], v[192:195], a[176:179], a[128:131]\n"
    "v_mfma_f32_16x16x32_bf16 a[4:7], v[196:199], a[160:163], a[4:7]\n"
    "v_perm_b32 v192, v133, v129, s67\n"
    "v_perm_b32 v193, v141, v137, s67\n"
    "v_mfma_f32_16x16x32_bf16 a[36:39], v[196:199], a[164:167], a[36:39]\n"
    "v_perm_b32 v194, v149, v145, s67\n"
    "v_perm_b32 v195, v157, v153, s67\n"
    "v_mfma_f32_16x16x32_bf16 a[68:71], v[196:199], a[168:171], a[68:71]\n"
    "buffer_load_dwordx4 v[100:103], v208, s[36:39], s60 offen\n"
    "v_mfma_f32_16x16x32_bf16 a[100:103], v[196:199], a[172:175], a[100:103]\n"
    "ds_read_b128 a[180:183], v204 offset:0\n"
    "v_mfma_f32_16x16x32_bf16 a[132:135], v[196:199], a[176:179], a[132:135]\n"
    "v_mfma_f32_16x16x32_bf16 a[8:11], v[192:195], a[160:163], a[8:11]\n"
    "v_perm_b32 v196, v133, v129, s68\n"
    "v_perm_b32 v197, v141, v137, s68\n"
    "v_mfma_f32_16x16x32_bf16 a[40:43], v[192:195], a[164:167], a[40:43]\n"
    "v_perm_b32 v198, v149, v145, s68\n"
    "v_perm_b32 v199, v157, v153, s68\n"
    "v_mfma_f32_16x16x32_bf16 a[72:75], v[192:195], a[168:171], a[72:75]\n"
    "buffer_load_dwordx4 v[104:107], v208, s[36:39], s61 offen\n"
    "v_mfma_f32_16x16x32_bf16 a[104:107], v[192:195], a[172:175], a[104:107]\n"
    "ds_read_b128 a[184:187], v204 offset:2048\n"
    "v_mfma_f32_16x16x32_bf16 a[136:139], v[192:195], a[176:179], a[136:139]\n"
    "v_mfma_f32_16x16x32_bf16 a[12:15], v[196:199], a[160:163], a[12:15]\n"
    "v_perm_b32 v192, v134, v130, s67\n"
    "v_perm_b32 v193, v142, v138, s67\n"
    "v_mfma_f32_16x16x32_bf16 a[44:47], v[196:199], a[164:167], a[44:47]\n"
    "v_perm_b32 v194, v150, v146, s67\n"
    "v_perm_b32 v195, v158, v154, s67\n"
    "v_mfma_f32_16x16x32_bf16 a[76:79], v[196:199], a[168:171], a[76:79]\n"
    "buffer_load_dwordx4 v[108:111], v208, s[36:39], s62 offen\n"
    "v_mfma_f32_16x16x32_bf16 a[108:111], v[196:199], a[172:175], a[108:111]\n"
    "ds_read_b128 a[188:191], v204 offset:4096\n"
    "v_mfma_f32_16x16x32_bf16 a[140:143], v[196:199], a[176:179], a[140:143]\n"
    "v_mfma_f32_16x16x32_bf16 a[16:19], v[192:195], a[160:163], a[16:19]\n"
    "v_perm_b32 v196, v134, v130, s68\n"
    "v_perm_b32 v197, v142, v138, s68\n"
    "v_mfma_f32_16x16x32_bf16 a[48:51], v[192:195], a[164:167], a[48:51]\n"
    "v_perm_b32 v198, v150, v146, s68\n"
    "v_perm_b32 v199, v158, v154, s68\n"
    "v_mfma_f32_16x16x32_bf16 a[80:83], v[192:195], a[168:171], a[80:83]\n"
    "buffer_load_dwordx4 v[112:115], v208, s[36:39], s63 offen\n"
    "v_mfma_f32_16x16x32_bf16 a[112:115], v[192:195], a[172:175], a[112:115]\n"
    "ds_read_b128 a[192:195], v204 offset:6144\n"
    "v_mfma_f32_16x16x32_bf16 a[144:147], v[192:195], a[176:179], a[144:147]\n"
    "v_mfma_f32_16x16x32_bf16 a[20:23], v[196:199], a[160:163], a[20:23]\n"
    "v_perm_b32 v192, v135, v131, s67\n"
    "v_perm_b32 v193, v143, v139, s67\n"
    "v_mfma_f32_16x16x32_bf16 a[52:55], v[196:199], a[164:167], a[52:55]\n"
    "v_perm_b32 v194, v151, v147, s67\n"
    "v_perm_b32 v195, v159, v155, s67\n"
    "v_mfma_f32_16x16x32_bf16 a[84:87], v[196:199], a[168:171], a[84:87]\n"
    "buffer_load_dwordx4 v[116:119], v208, s[36:39], s64 offen\n"
    "v_mfma_f32_16x16x32_bf16 a[116:119], v[196:199], a[172:175], a[116:119]\n"
    "ds_read_b128 a[196:199], v204 offset:40960\n"
    "v_mfma_f32_16x16x32_bf16 a[148:151], v[196:199], a[176:179], a[148:151]\n"
    "v_mfma_f32_16x16x32_bf16 a[24:27], v[192:195], a[160:163], a[24:27]\n"
    "v_perm_b32 v196, v135, v131, s68\n"
    "v_perm_b32 v197, v143, v139, s68\n"
    "v_mfma_f32_16x16x32_bf16 a[56:59], v[192:195], a[164:167], a[56:59]\n"
    "v_perm_b32 v198, v151, v147, s68\n"
    "v_perm_b32 v199, v159, v155, s68\n"
    "v_mfma_f32_16x16x32_bf16 a[88:91], v[192:195], a[168:171], a[88:91]\n"
    "buffer_load_dwordx4 v[120:123], v208, s[36:39], s65 offen\n"
    "v_mfma_f32_16x16x32_bf16 a[120:123], v[192:195], a[172:175], a[120:123]\n"
    "s_memrealtime s[14:15]\n"
    "v_mfma_f32_16x16x32_bf16 a[152:155], v[192:195], a[176:179], a[152:155]\n"
    "s_waitcnt vmcnt(15)\n"
    "v_mfma_f32_16x16x32_bf16 a[28:31], v[196:199], a[160:163], a[28:31]\n"
    "v_perm_b32 v192, v164, v160, s67\n"
    "v_perm_b32 v193, v172, v168, s67\n"
    "v_mfma_f32_16x16x32_bf16 a[60:63], v[196:199], a[164:167], a[60:63]\n"
    "v_perm_b32 v194, v180, v176, s67\n"
    "v_perm_b32 v195, v188, v184, s67\n"
    "v_mfma_f32_16x16x32_bf16 a[92:95], v[196:199], a[168:171], a[92:95]\n"
    "buffer_load_dwordx4 v[124:127], v208, s[36:39], s66 offen\n"
    "v_mfma_f32_16x16x32_bf16 a[124:127], v[196:199], a[172:175], a[124:127]\n"
    "v_add_u32 v201, s73, v200\n"
    "v_mfma_f32_16x16x32_bf16 a[156:159], v[196:199], a[176:179], a[156:159]\n"
    "s_waitcnt lgkmcnt(0)\n"
    "s_add_u32 s73, s73, 0x40000\n"
    "s_cmp_eq_u32 s73, 0x480000\n"
    "s_cselect_b32 s73, 0, s73\n"
    "A_QCHK12:\n"
    "s_sub_u32 s77, s20, s14\n"
    "s_cmp_lt_u32 s77, 0xc8\n"
    "s_cbranch_scc0 A_QOK12\n"
    "s_memrealtime s[14:15]\n"
    "s_waitcnt lgkmcnt(0)\n"
    "s_branch A_QCHK12\n"
    "A_QOK12:\n"
    "s_sub_u32 s78, s14, s83\n"
    "s_cmp_gt_u32 s78, 0x3f2\n"
    "s_cselect_b32 s77, 47, 0\n"
    "s_add_u32 s20, s14, s77\n"
    "v_mfma_f32_16x16x32_bf16 a[0:3], v[192:195], a[180:183], a[0:3]\n"
    "v_perm_b32 v196, v164, v160, s68\n"
    "v_perm_b32 v197, v172, v168, s68\n"
    "v_mfma_f32_16x16x32_bf16 a[32:35], v[192:195], a[184:187], a[32:35]\n"
    "v_perm_b32 v198, v180, v176, s68\n"
    "v_perm_b32 v199, v188, v184, s68\n"
    "v_mfma_f32_16x16x32_bf16 a[64:67], v[192:195], a[188:191], a[64:67]\n"
    "buffer_load_dwordx4 v[128:131], v201, s[36:39], 0 offen\n"
    "v_mfma_f32_16x16x32_bf16 a[96:99], v[192:195], a[192:195], a[96:99]\n"
    "v_mfma_f32_16x16x32_bf16 a[128:131], v[192:195], a[196:199], a[128:131]\n"
    "v_mfma_f32_16x16x32_bf16 a[4:7], v[196:199], a[180:183], a[4:7]\n"
    "v_perm_b32 v192, v165, v161, s67\n"
    "v_perm_b32 v193, v173, v169, s67\n"
    "v_mfma_f32_16x16x32_bf16 a[36:39], v[196:199], a[184:187], a[36:39]\n"
    "v_perm_b32 v194, v181, v177, s67\n"
    "v_perm_b32 v195, v189, v185, s67\n"
    "v_mfma_f32_16x16x32_bf16 a[68:71], v[196:199], a[188:191], a[68:71]\n"
    "buffer_load_dwordx4 v[132:135], v201, s[36:39], s60 offen\n"
    "v_mfma_f32_16x16x32_bf16 a[100:103], v[196:199], a[192:195], a[100:103]\n"
    "ds_read_b128 a[160:163], v202 offset:43008\n"
    "v_mfma_f32_16x16x32_bf16 a[132:135], v[196:199], a[196:199], a[132:135]\n"
    "v_mfma_f32_16x16x32_bf16 a[8:11], v[192:195], a[180:183], a[8:11]\n"
    "v_perm_b32 v196, v165, v161, s68\n"
    "v_perm_b32 v197, v173, v169, s68\n"
    "v_mfma_f32_16x16x32_bf16 a[40:43], v[192:195], a[184:187], a[40:43]\n"
    "v_perm_b32 v198, v181, v177, s68\n"
    "v_perm_b32 v199, v189, v185, s68\n"
    "v_mfma_f32_16x16x32_bf16 a[72:75], v[192:195], a[188:191], a[72:75]\n"
    "buffer_load_dwordx4 v[136:139], v201, s[36:39], s61 offen\n"
    "v_mfma_f32_16x16x32_bf16 a[104:107], v[192:195], a[192:195], a[104:107]\n"
    "ds_read_b128 a[164:167], v202 offset:45056\n"
    "v_mfma_f32_16x16x32_bf16 a[136:139], v[192:195], a[196:199], a[136:139]\n"
    "v_mfma_f32_16x16x32_bf16 a[12:15], v[196:199], a[180:183], a[12:15]\n"
    "v_perm_b32 v192, v166, v162, s67\n"
    "v_perm_b32 v193, v174, v170, s67\n"
    "v_mfma_f32_16x16x32_bf16 a[44:47], v[196:199], a[184:187], a[44:47]\n"
    "v_perm_b32 v194, v182, v178, s67\n"
    "v_perm_b32 v195, v190, v186, s67\n"
    "v_mfma_f32_16x16x32_bf16 a[76:79], v[196:199], a[188:191], a[76:79]\n"
    "buffer_load_dwordx4 v[140:143], v201, s[36:39], s62 offen\n"
    "v_mfma_f32_16x16x32_bf16 a[108:111], v[196:199], a[192:195], a[108:111]\n"
    "ds_read_b128 a[168:171], v202 offset:47104\n"
    "v_mfma_f32_16x16x32_bf16 a[140:143], v[196:199], a[196:199], a[140:143]\n"
    "v_mfma_f32_16x16x32_bf16 a[16:19], v[192:195], a[180:183], a[16:19]\n"
    "v_perm_b32 v196, v166, v162, s68\n"
    "v_perm_b32 v197, v174, v170, s68\n"
    "v_mfma_f32_16x16x32_bf16 a[48:51], v[192:195], a[184:187], a[48:51]\n"
    "v_perm_b32 v198, v182, v178, s68\n"
    "v_perm_b32 v199, v190, v186, s68\n"
    "v_mfma_f32_16x16x32_bf16 a[80:83], v[192:195], a[188:191], a[80:83]\n"
    "buffer_load_dwordx4 v[144:147], v201, s[36:39], s63 offen\n"
    "v_mfma_f32_16x16x32_bf16 a[112:115], v[192:195], a[192:195], a[112:115]\n"
    "ds_read_b128 a[172:175], v203 offset:0\n"
    "v_mfma_f32_16x16x32_bf16 a[144:147], v[192:195], a[196:199], a[144:147]\n"
    "v_mfma_f32_16x16x32_bf16 a[20:23], v[196:199], a[180:183], a[20:23]\n"
    "v_perm_b32 v192, v167, v163, s67\n"
    "v_perm_b32 v193, v175, v171, s67\n"
    "v_mfma_f32_16x16x32_bf16 a[52:55], v[196:199], a[184:187], a[52:55]\n"
    "v_perm_b32 v194, v183, v179, s67\n"
    "v_perm_b32 v195, v191, v187, s67\n"
    "v_mfma_f32_16x16x32_bf16 a[84:87], v[196:199], a[188:191], a[84:87]\n"
    "buffer_load_dwordx4 v[148:151], v201, s[36:39], s64 offen\n"
    "v_mfma_f32_16x16x32_bf16 a[116:119], v[196:199], a[192:195], a[116:119]\n"
    "ds_read_b128 a[176:179], v203 offset:2048\n"
    "v_mfma_f32_16x16x32_bf16 a[148:151], v[196:199], a[196:199], a[148:151]\n"
    "v_mfma_f32_16x16x32_bf16 a[24:27], v[192:195], a[180:183], a[24:27]\n"
    "v_perm_b32 v196, v167, v163, s68\n"
    "v_perm_b32 v197, v175, v171, s68\n"
    "v_mfma_f32_16x16x32_bf16 a[56:59], v[192:195], a[184:187], a[56:59]\n"
    "v_perm_b32 v198, v183, v179, s68\n"
    "v_perm_b32 v199, v191, v187, s68\n"
    "v_mfma_f32_16x16x32_bf16 a[88:91], v[192:195], a[188:191], a[88:91]\n"
    "buffer_load_dwordx4 v[152:155], v201, s[36:39], s65 offen\n"
    "v_mfma_f32_16x16x32_bf16 a[120:123], v[192:195], a[192:195], a[120:123]\n"
    "s_memrealtime s[14:15]\n"
    "v_mfma_f32_16x16x32_bf16 a[152:155], v[192:195], a[196:199], a[152:155]\n"
    "s_waitcnt vmcnt(15)\n"
    "v_mfma_f32_16x16x32_bf16 a[28:31], v[196:199], a[180:183], a[28:31]\n"
    "v_perm_b32 v192, v68, v64, s67\n"
    "v_perm_b32 v193, v76, v72, s67\n"
    "v_mfma_f32_16x16x32_bf16 a[60:63], v[196:199], a[184:187], a[60:63]\n"
    "v_perm_b32 v194, v84, v80, s67\n"
    "v_perm_b32 v195, v92, v88, s67\n"
    "v_mfma_f32_16x16x32_bf16 a[92:95], v[196:199], a[188:191], a[92:95]\n"
    "buffer_load_dwordx4 v[156:159], v201, s[36:39], s66 offen\n"
    "v_mfma_f32_16x16x32_bf16 a[124:127], v[196:199], a[192:195], a[124:127]\n"
    "v_add_u32 v208, s73, v200\n"
    "v_mfma_f32_16x16x32_bf16 a[156:159], v[196:199], a[196:199], a[156:159]\n"
    "s_waitcnt lgkmcnt(0)\n"
    "s_add_u32 s73, s73, 0x40000\n"
    "s_cmp_eq_u32 s73, 0x480000\n"
    "s_cselect_b32 s73, 0, s73\n"
    "A_QCHK13:\n"
    "s_sub_u32 s77, s20, s14\n"
    "s_cmp_lt_u32 s77, 0xc8\n"
    "s_cbranch_scc0 A_QOK13\n"
    "s_memrealtime s[14:15]\n"
    "s_waitcnt lgkmcnt(0)\n"
    "s_branch A_QCHK13\n"
    "A_QOK13:\n"
    "s_sub_u32 s78, s14, s83\n"
    "s_cmp_gt_u32 s78, 0x3f2\n"
    "s_cselect_b32 s77, 47, 0\n"
    "s_add_u32 s20, s14, s77\n"
    "v_mfma_f32_16x16x32_bf16 a[0:3], v[192:195], a[160:163], a[0:3]\n"
    "v_perm_b32 v196, v68, v64, s68\n"
    "v_perm_b32 v197, v76, v72, s68\n"
    "v_mfma_f32_16x16x32_bf16 a[32:35], v[192:195], a[164:167], a[32:35]\n"
    "v_perm_b32 v198, v84, v80, s68\n"
    "v_perm_b32 v199, v92, v88, s68\n"
    "v_mfma_f32_16x16x32_bf16 a[64:67], v[192:195], a[168:171], a[64:67]\n"
    "buffer_load_dwordx4 v[160:163], v208, s[36:39], 0 offen\n"
    "v_mfma_f32_16x16x32_bf16 a[96:99], v[192:195], a[172:175], a[96:99]\n"
    "v_mfma_f32_16x16x32_bf16 a[128:131], v[192:195], a[176:179], a[128:131]\n"
    "v_mfma_f32_16x16x32_bf16 a[4:7], v[196:199], a[160:163], a[4:7]\n"
    "v_perm_b32 v192, v69, v65, s67\n"
    "v_perm_b32 v193, v77, v73, s67\n"
    "v_mfma_f32_16x16x32_bf16 a[36:39], v[196:199], a[164:167], a[36:39]\n"
    "v_perm_b32 v194, v85, v81, s67\n"
    "v_perm_b32 v195, v93, v89, s67\n"
    "v_mfma_f32_16x16x32_bf16 a[68:71], v[196:199], a[168:171], a[68:71]\n"
    "buffer_load_dwordx4 v[164:167], v208, s[36:39], s60 offen\n"
    "v_mfma_f32_16x16x32_bf16 a[100:103], v[196:199], a[172:175], a[100:103]\n"
    "ds_read_b128 a[180:183], v204 offset:43008\n"
    "v_mfma_f32_16x16x32_bf16 a[132:135], v[196:199], a[176:179], a[132:135]\n"
    "v_mfma_f32_16x16x32_bf16 a[8:11], v[192:195], a[160:163], a[8:11]\n"
    "v_perm_b32 v196, v69, v65, s68\n"
    "v_perm_b32 v197, v77, v73, s68\n"
    "v_mfma_f32_16x16x32_bf16 a[40:43], v[192:195], a[164:167], a[40:43]\n"
    "v_perm_b32 v198, v85, v81, s68\n"
    "v_perm_b32 v199, v93, v89, s68\n"
    "v_mfma_f32_16x16x32_bf16 a[72:75], v[192:195], a[168:171], a[72:75]\n"
    "buffer_load_dwordx4 v[168:171], v208, s[36:39], s61 offen\n"
    "v_mfma_f32_16x16x32_bf16 a[104:107], v[192:195], a[172:175], a[104:107]\n"
    "ds_read_b128 a[184:187], v204 offset:45056\n"
    "v_mfma_f32_16x16x32_bf16 a[136:139], v[192:195], a[176:179], a[136:139]\n"
    "v_mfma_f32_16x16x32_bf16 a[12:15], v[196:199], a[160:163], a[12:15]\n"
    "v_perm_b32 v192, v70, v66, s67\n"
    "v_perm_b32 v193, v78, v74, s67\n"
    "v_mfma_f32_16x16x32_bf16 a[44:47], v[196:199], a[164:167], a[44:47]\n"
    "v_perm_b32 v194, v86, v82, s67\n"
    "v_perm_b32 v195, v94, v90, s67\n"
    "v_mfma_f32_16x16x32_bf16 a[76:79], v[196:199], a[168:171], a[76:79]\n"
    "buffer_load_dwordx4 v[172:175], v208, s[36:39], s62 offen\n"
    "v_mfma_f32_16x16x32_bf16 a[108:111], v[196:199], a[172:175], a[108:111]\n"
    "ds_read_b128 a[188:191], v204 offset:47104\n"
    "v_mfma_f32_16x16x32_bf16 a[140:143], v[196:199], a[176:179], a[140:143]\n"
    "v_mfma_f32_16x16x32_bf16 a[16:19], v[192:195], a[160:163], a[16:19]\n"
    "v_perm_b32 v196, v70, v66, s68\n"
    "v_perm_b32 v197, v78, v74, s68\n"
    "v_mfma_f32_16x16x32_bf16 a[48:51], v[192:195], a[164:167], a[48:51]\n"
    "v_perm_b32 v198, v86, v82, s68\n"
    "v_perm_b32 v199, v94, v90, s68\n"
    "v_mfma_f32_16x16x32_bf16 a[80:83], v[192:195], a[168:171], a[80:83]\n"
    "buffer_load_dwordx4 v[176:179], v208, s[36:39], s63 offen\n"
    "v_mfma_f32_16x16x32_bf16 a[112:115], v[192:195], a[172:175], a[112:115]\n"
    "ds_read_b128 a[192:195], v205 offset:0\n"
    "v_mfma_f32_16x16x32_bf16 a[144:147], v[192:195], a[176:179], a[144:147]\n"
    "v_mfma_f32_16x16x32_bf16 a[20:23], v[196:199], a[160:163], a[20:23]\n"
    "v_perm_b32 v192, v71, v67, s67\n"
    "v_perm_b32 v193, v79, v75, s67\n"
    "v_mfma_f32_16x16x32_bf16 a[52:55], v[196:199], a[164:167], a[52:55]\n"
    "v_perm_b32 v194, v87, v83, s67\n"
    "v_perm_b32 v195, v95, v91, s67\n"
    "v_mfma_f32_16x16x32_bf16 a[84:87], v[196:199], a[168:171], a[84:87]\n"
    "buffer_load_dwordx4 v[180:183], v208, s[36:39], s64 offen\n"
    "v_mfma_f32_16x16x32_bf16 a[116:119], v[196:199], a[172:175], a[116:119]\n"
    "ds_read_b128 a[196:199], v205 offset:2048\n"
    "v_mfma_f32_16x16x32_bf16 a[148:151], v[196:199], a[176:179], a[148:151]\n"
    "v_mfma_f32_16x16x32_bf16 a[24:27], v[192:195], a[160:163], a[24:27]\n"
    "v_perm_b32 v196, v71, v67, s68\n"
    "v_perm_b32 v197, v79, v75, s68\n"
    "v_mfma_f32_16x16x32_bf16 a[56:59], v[192:195], a[164:167], a[56:59]\n"
    "v_perm_b32 v198, v87, v83, s68\n"
    "v_perm_b32 v199, v95, v91, s68\n"
    "v_mfma_f32_16x16x32_bf16 a[88:91], v[192:195], a[168:171], a[88:91]\n"
    "buffer_load_dwordx4 v[184:187], v208, s[36:39], s65 offen\n"
    "v_mfma_f32_16x16x32_bf16 a[120:123], v[192:195], a[172:175], a[120:123]\n"
    "s_memrealtime s[14:15]\n"
    "v_mfma_f32_16x16x32_bf16 a[152:155], v[192:195], a[176:179], a[152:155]\n"
    "s_waitcnt vmcnt(15)\n"
    "v_mfma_f32_16x16x32_bf16 a[28:31], v[196:199], a[160:163], a[28:31]\n"
    "v_perm_b32 v192, v100, v96, s67\n"
    "v_perm_b32 v193, v108, v104, s67\n"
    "v_mfma_f32_16x16x32_bf16 a[60:63], v[196:199], a[164:167], a[60:63]\n"
    "v_perm_b32 v194, v116, v112, s67\n"
    "v_perm_b32 v195, v124, v120, s67\n"
    "v_mfma_f32_16x16x32_bf16 a[92:95], v[196:199], a[168:171], a[92:95]\n"
    "buffer_load_dwordx4 v[188:191], v208, s[36:39], s66 offen\n"
    "v_mfma_f32_16x16x32_bf16 a[124:127], v[196:199], a[172:175], a[124:127]\n"
    "v_mfma_f32_16x16x32_bf16 a[156:159], v[196:199], a[176:179], a[156:159]\n"
    "s_waitcnt lgkmcnt(0)\n"
    "A_QCHK14:\n"
    "s_sub_u32 s77, s20, s14\n"
    "s_cmp_lt_u32 s77, 0xc8\n"
    "s_cbranch_scc0 A_QOK14\n"
    "s_memrealtime s[14:15]\n"
    "s_waitcnt lgkmcnt(0)\n"
    "s_branch A_QCHK14\n"
    "A_QOK14:\n"
    "s_sub_u32 s78, s14, s83\n"
    "s_cmp_gt_u32 s78, 0x3f2\n"
    "s_cselect_b32 s77, 47, 0\n"
    "s_add_u32 s20, s14, s77\n"
    "v_mfma_f32_16x16x32_bf16 a[0:3], v[192:195], a[180:183], a[0:3]\n"
    "v_perm_b32 v196, v100, v96, s68\n"
    "v_perm_b32 v197, v108, v104, s68\n"
    "v_mfma_f32_16x16x32_bf16 a[32:35], v[192:195], a[184:187], a[32:35]\n"
    "v_perm_b32 v198, v116, v112, s68\n"
    "v_perm_b32 v199, v124, v120, s68\n"
    "v_mfma_f32_16x16x32_bf16 a[64:67], v[192:195], a[188:191], a[64:67]\n"
    "v_mov_b32 v64, 0\n"
    "v_mov_b32 v83, 0\n"
    "v_mfma_f32_16x16x32_bf16 a[96:99], v[192:195], a[192:195], a[96:99]\n"
    "v_mov_b32 v65, 0\n"
    "v_mov_b32 v84, 0\n"
    "v_mfma_f32_16x16x32_bf16 a[128:131], v[192:195], a[196:199], a[128:131]\n"
    "v_mov_b32 v66, 0\n"
    "v_mov_b32 v85, 0\n"
    "v_mfma_f32_16x16x32_bf16 a[4:7], v[196:199], a[180:183], a[4:7]\n"
    "v_perm_b32 v192, v101, v97, s67\n"
    "v_perm_b32 v193, v109, v105, s67\n"
    "v_mfma_f32_16x16x32_bf16 a[36:39], v[196:199], a[184:187], a[36:39]\n"
    "v_perm_b32 v194, v117, v113, s67\n"
    "v_perm_b32 v195, v125, v121, s67\n"
    "v_mfma_f32_16x16x32_bf16 a[68:71], v[196:199], a[188:191], a[68:71]\n"
    "v_mov_b32 v67, 0\n"
    "v_mov_b32 v86, 0\n"
    "v_mfma_f32_16x16x32_bf16 a[100:103], v[196:199], a[192:195], a[100:103]\n"
    "ds_read_b128 a[160:163], v203 offset:4096\n"
    "v_mov_b32 v87, 0\n"
    "v_mfma_f32_16x16x32_bf16 a[132:135], v[196:199], a[196:199], a[132:135]\n"
    "v_mov_b32 v68, 0\n"
    "v_mov_b32 v88, 0\n"
    "v_mfma_f32_16x16x32_bf16 a[8:11], v[192:195], a[180:183], a[8:11]\n"
    "v_perm_b32 v196, v101, v97, s68\n"
    "v_perm_b32 v197, v109, v105, s68\n"
    "v_mfma_f32_16x16x32_bf16 a[40:43], v[192:195], a[184:187], a[40:43]\n"
    "v_perm_b32 v198, v117, v113, s68\n"
    "v_perm_b32 v199, v125, v121, s68\n"
    "v_mfma_f32_16x16x32_bf16 a[72:75], v[192:195], a[188:191], a[72:75]\n"
    "v_mov_b32 v69, 0\n"
    "v_mov_b32 v89, 0\n"
    "v_mfma_f32_16x16x32_bf16 a[104:107], v[192:195], a[192:195], a[104:107]\n"
    "ds_read_b128 a[164:167], v203 offset:6144\n"
    "v_mov_b32 v90, 0\n"
    "v_mfma_f32_16x16x32_bf16 a[136:139], v[192:195], a[196:199], a[136:139]\n"
    "v_mov_b32 v70, 0\n"
    "v_mov_b32 v91, 0\n"
    "v_mfma_f32_16x16x32_bf16 a[12:15], v[196:199], a[180:183], a[12:15]\n"
    "v_perm_b32 v192, v102, v98, s67\n"
    "v_perm_b32 v193, v110, v106, s67\n"
    "v_mfma_f32_16x16x32_bf16 a[44:47], v[196:199], a[184:187], a[44:47]\n"
    "v_perm_b32 v194, v118, v114, s67\n"
    "v_perm_b32 v195, v126, v122, s67\n"
    "v_mfma_f32_16x16x32_bf16 a[76:79], v[196:199], a[188:191], a[76:79]\n"
    "v_mov_b32 v71, 0\n"
    "v_mov_b32 v92, 0\n"
    "v_mfma_f32_16x16x32_bf16 a[108:111], v[196:199], a[192:195], a[108:111]\n"
    "ds_read_b128 a[168:171], v203 offset:40960\n"
    "v_mov_b32 v93, 0\n"
    "v_mfma_f32_16x16x32_bf16 a[140:143], v[196:199], a[196:199], a[140:143]\n"
    "v_mov_b32 v72, 0\n"
    "v_mov_b32 v94, 0\n"
    "v_mfma_f32_16x16x32_bf16 a[16:19], v[192:195], a[180:183], a[16:19]\n"
    "v_perm_b32 v196, v102, v98, s68\n"
    "v_perm_b32 v197, v110, v106, s68\n"
    "v_mfma_f32_16x16x32_bf16 a[48:51], v[192:195], a[184:187], a[48:51]\n"
    "v_perm_b32 v198, v118, v114, s68\n"
    "v_perm_b32 v199, v126, v122, s68\n"
    "v_mfma_f32_16x16x32_bf16 a[80:83], v[192:195], a[188:191], a[80:83]\n"
    "v_mov_b32 v73, 0\n"
    "v_mov_b32 v95, 0\n"
    "v_mfma_f32_16x16x32_bf16 a[112:115], v[192:195], a[192:195], a[112:115]\n"
    "ds_read_b128 a[172:175], v203 offset:43008\n"
    "v_mfma_f32_16x16x32_bf16 a[144:147], v[192:195], a[196:199], a[144:147]\n"
    "v_mov_b32 v74, 0\n"
    "v_mfma_f32_16x16x32_bf16 a[20:23], v[196:199], a[180:183], a[20:23]\n"
    "v_perm_b32 v192, v103, v99, s67\n"
    "v_perm_b32 v193, v111, v107, s67\n"
    "v_mfma_f32_16x16x32_bf16 a[52:55], v[196:199], a[184:187], a[52:55]\n"
    "v_perm_b32 v194, v119, v115, s67\n"
    "v_perm_b32 v195, v127, v123, s67\n"
    "v_mfma_f32_16x16x32_bf16 a[84:87], v[196:199], a[188:191], a[84:87]\n"
    "v_mov_b32 v75, 0\n"
    "v_mfma_f32_16x16x32_bf16 a[116:119], v[196:199], a[192:195], a[116:119]\n"
    "ds_read_b128 a[176:179], v203 offset:45056\n"
    "v_mfma_f32_16x16x32_bf16 a[148:151], v[196:199], a[196:199], a[148:151]\n"
    "v_mov_b32 v76, 0\n"
    "v_mfma_f32_16x16x32_bf16 a[24:27], v[192:195], a[180:183], a[24:27]\n"
    "v_perm_b32 v196, v103, v99, s68\n"
    "v_perm_b32 v197, v111, v107, s68\n"
    "v_mfma_f32_16x16x32_bf16 a[56:59], v[192:195], a[184:187], a[56:59]\n"
    "v_perm_b32 v198, v119, v115, s68\n"
    "v_perm_b32 v199, v127, v123, s68\n"
    "v_mfma_f32_16x16x32_bf16 a[88:91], v[192:195], a[188:191], a[88:91]\n"
    "v_mov_b32 v77, 0\n"
    "v_mfma_f32_16x16x32_bf16 a[120:123], v[192:195], a[192:195], a[120:123]\n"
    "v_mov_b32 v78, 0\n"
    "s_memrealtime s[14:15]\n"
    "v_mfma_f32_16x16x32_bf16 a[152:155], v[192:195], a[196:199], a[152:155]\n"
    "s_waitcnt vmcnt(8)\n"
    "v_mov_b32 v81, 0\n"
    "v_mfma_f32_16x16x32_bf16 a[28:31], v[196:199], a[180:183], a[28:31]\n"
    "v_perm_b32 v192, v132, v128, s67\n"
    "v_perm_b32 v193, v140, v136, s67\n"
    "v_mfma_f32_16x16x32_bf16 a[60:63], v[196:199], a[184:187], a[60:63]\n"
    "v_perm_b32 v194, v148, v144, s67\n"
    "v_perm_b32 v195, v156, v152, s67\n"
    "v_mfma_f32_16x16x32_bf16 a[92:95], v[196:199], a[188:191], a[92:95]\n"
    "v_mov_b32 v79, 0\n"
    "v_mfma_f32_16x16x32_bf16 a[124:127], v[196:199], a[192:195], a[124:127]\n"
    "v_mov_b32 v80, 0\n"
    "v_mfma_f32_16x16x32_bf16 a[156:159], v[196:199], a[196:199], a[156:159]\n"
    "s_waitcnt lgkmcnt(0)\n"
    "v_mov_b32 v82, 0\n"
    "A_QCHK15:\n"
    "s_sub_u32 s77, s20, s14\n"
    "s_cmp_lt_u32 s77, 0xc8\n"
    "s_cbranch_scc0 A_QOK15\n"
    "s_memrealtime s[14:15]\n"
    "s_waitcnt lgkmcnt(0)\n"
    "s_branch A_QCHK15\n"
    "A_QOK15:\n"
    "s_sub_u32 s78, s14, s83\n"
    "s_cmp_gt_u32 s78, 0x3f2\n"
    "s_cselect_b32 s77, 47, 0\n"
    "s_add_u32 s20, s14, s77\n"
    "v_mfma_f32_16x16x32_bf16 a[0:3], v[192:195], a[160:163], a[0:3]\n"
    "v_perm_b32 v196, v132, v128, s68\n"
    "v_perm_b32 v197, v140, v136, s68\n"
    "v_mfma_f32_16x16x32_bf16 a[32:35], v[192:195], a[164:167], a[32:35]\n"
    "v_perm_b32 v198, v148, v144, s68\n"
    "v_perm_b32 v199, v156, v152, s68\n"
    "v_mfma_f32_16x16x32_bf16 a[64:67], v[192:195], a[168:171], a[64:67]\n"
    "v_mov_b32 v96, 0\n"
    "v_mov_b32 v115, 0\n"
    "v_mfma_f32_16x16x32_bf16 a[96:99], v[192:195], a[172:175], a[96:99]\n"
    "v_mov_b32 v97, 0\n"
    "v_mov_b32 v116, 0\n"
    "v_mfma_f32_16x16x32_bf16 a[128:131], v[192:195], a[176:179], a[128:131]\n"
    "v_mov_b32 v98, 0\n"
    "v_mov_b32 v117, 0\n"
    "v_mfma_f32_16x16x32_bf16 a[4:7], v[196:199], a[160:163], a[4:7]\n"
    "v_perm_b32 v192, v133, v129, s67\n"
    "v_perm_b32 v193, v141, v137, s67\n"
    "v_mfma_f32_16x16x32_bf16 a[36:39], v[196:199], a[164:167], a[36:39]\n"
    "v_perm_b32 v194, v149, v145, s67\n"
    "v_perm_b32 v195, v157, v153, s67\n"
    "v_mfma_f32_16x16x32_bf16 a[68:71], v[196:199], a[168:171], a[68:71]\n"
    "v_mov_b32 v99, 0\n"
    "v_mov_b32 v118, 0\n"
    "v_mfma_f32_16x16x32_bf16 a[100:103], v[196:199], a[172:175], a[100:103]\n"
    "ds_read_b128 a[180:183], v205 offset:4096\n"
    "v_mov_b32 v119, 0\n"
    "v_mfma_f32_16x16x32_bf16 a[132:135], v[196:199], a[176:179], a[132:135]\n"
    "v_mov_b32 v100, 0\n"
    "v_mov_b32 v120, 0\n"
    "v_mfma_f32_16x16x32_bf16 a[8:11], v[192:195], a[160:163], a[8:11]\n"
    "v_perm_b32 v196, v133, v129, s68\n"
    "v_perm_b32 v197, v141, v137, s68\n"
    "v_mfma_f32_16x16x32_bf16 a[40:43], v[192:195], a[164:167], a[40:43]\n"
    "v_perm_b32 v198, v149, v145, s68\n"
    "v_perm_b32 v199, v157, v153, s68\n"
    "v_mfma_f32_16x16x32_bf16 a[72:75], v[192:195], a[168:171], a[72:75]\n"
    "v_mov_b32 v101, 0\n"
    "v_mov_b32 v121, 0\n"
    "v_mfma_f32_16x16x32_bf16 a[104:107], v[192:195], a[172:175], a[104:107]\n"
    "ds_read_b128 a[184:187], v205 offset:6144\n"
    "v_mov_b32 v122, 0\n"
    "v_mfma_f32_16x16x32_bf16 a[136:139], v[192:195], a[176:179], a[136:139]\n"
    "v_mov_b32 v102, 0\n"
    "v_mov_b32 v123, 0\n"
    "v_mfma_f32_16x16x32_bf16 a[12:15], v[196:199], a[160:163], a[12:15]\n"
    "v_perm_b32 v192, v134, v130, s67\n"
    "v_perm_b32 v193, v142, v138, s67\n"
    "v_mfma_f32_16x16x32_bf16 a[44:47], v[196:199], a[164:167], a[44:47]\n"
    "v_perm_b32 v194, v150, v146, s67\n"
    "v_perm_b32 v195, v158, v154, s67\n"
    "v_mfma_f32_16x16x32_bf16 a[76:79], v[196:199], a[168:171], a[76:79]\n"
    "v_mov_b32 v103, 0\n"
    "v_mov_b32 v124, 0\n"
    "v_mfma_f32_16x16x32_bf16 a[108:111], v[196:199], a[172:175], a[108:111]\n"
    "ds_read_b128 a[188:191], v205 offset:40960\n"
    "v_mov_b32 v125, 0\n"
    "v_mfma_f32_16x16x32_bf16 a[140:143], v[196:199], a[176:179], a[140:143]\n"
    "v_mov_b32 v104, 0\n"
    "v_mov_b32 v126, 0\n"
    "v_mfma_f32_16x16x32_bf16 a[16:19], v[192:195], a[160:163], a[16:19]\n"
    "v_perm_b32 v196, v134, v130, s68\n"
    "v_perm_b32 v197, v142, v138, s68\n"
    "v_mfma_f32_16x16x32_bf16 a[48:51], v[192:195], a[164:167], a[48:51]\n"
    "v_perm_b32 v198, v150, v146, s68\n"
    "v_perm_b32 v199, v158, v154, s68\n"
    "v_mfma_f32_16x16x32_bf16 a[80:83], v[192:195], a[168:171], a[80:83]\n"
    "v_mov_b32 v105, 0\n"
    "v_mov_b32 v127, 0\n"
    "v_mfma_f32_16x16x32_bf16 a[112:115], v[192:195], a[172:175], a[112:115]\n"
    "A_TH_FIRST3:\n"
    "A_TH_S3:\n"
    "s_cbranch_execz A_TH_RET\n"
    "ds_read_b128 a[192:195], v205 offset:43008\n"
    "v_mfma_f32_16x16x32_bf16 a[144:147], v[192:195], a[176:179], a[144:147]\n"
    "v_mov_b32 v106, 0\n"
    "v_mfma_f32_16x16x32_bf16 a[20:23], v[196:199], a[160:163], a[20:23]\n"
    "v_perm_b32 v192, v135, v131, s67\n"
    "v_perm_b32 v193, v143, v139, s67\n"
    "v_mfma_f32_16x16x32_bf16 a[52:55], v[196:199], a[164:167], a[52:55]\n"
    "v_perm_b32 v194, v151, v147, s67\n"
    "v_perm_b32 v195, v159, v155, s67\n"
    "v_mfma_f32_16x16x32_bf16 a[84:87], v[196:199], a[168:171], a[84:87]\n"
    "v_mov_b32 v107, 0\n"
    "v_mfma_f32_16x16x32_bf16 a[116:119], v[196:199], a[172:175], a[116:119]\n"
    "ds_read_b128 a[196:199], v205 offset:45056\n"
    "v_mfma_f32_16x16x32_bf16 a[148:151], v[196:199], a[176:179], a[148:151]\n"
    "v_mov_b32 v108, 0\n"
    "v_mfma_f32_16x16x32_bf16 a[24:27], v[192:195], a[160:163], a[24:27]\n"
    "v_perm_b32 v196, v135, v131, s68\n"
    "v_perm_b32 v197, v143, v139, s68\n"
    "v_mfma_f32_16x16x32_bf16 a[56:59], v[192:195], a[164:167], a[56:59]\n"
    "v_perm_b32 v198, v151, v147, s68\n"
    "v_perm_b32 v199, v159, v155, s68\n"
    "v_mfma_f32_16x16x32_bf16 a[88:91], v[192:195], a[168:171], a[88:91]\n"
    "v_mov_b32 v109, 0\n"
    "v_mfma_f32_16x16x32_bf16 a[120:123], v[192:195], a[172:175], a[120:123]\n"
    "v_mov_b32 v110, 0\n"
    "s_memrealtime s[14:15]\n"
    "v_mfma_f32_16x16x32_bf16 a[152:155], v[192:195], a[176:179], a[152:155]\n"
    "s_waitcnt vmcnt(0)\n"
    "v_mov_b32 v113, 0\n"
    "v_mfma_f32_16x16x32_bf16 a[28:31], v[196:199], a[160:163], a[28:31]\n"
    "v_perm_b32 v192, v164, v160, s67\n"
    "v_perm_b32 v193, v172, v168, s67\n"
    "v_mfma_f32_16x16x32_bf16 a[60:63], v[196:199], a[164:167], a[60:63]\n"
    "v_perm_b32 v194, v180, v176, s67\n"
    "v_perm_b32 v195, v188, v184, s67\n"
    "v_mfma_f32_16x16x32_bf16 a[92:95], v[196:199], a[168:171], a[92:95]\n"
    "v_mov_b32 v111, 0\n"
    "v_mfma_f32_16x16x32_bf16 a[124:127], v[196:199], a[172:175], a[124:127]\n"
    "v_mov_b32 v112, 0\n"
    "v_mfma_f32_16x16x32_bf16 a[156:159], v[196:199], a[176:179], a[156:159]\n"
    "s_waitcnt lgkmcnt(0)\n"
    "v_mov_b32 v114, 0\n"
    "A_QCHK16:\n"
    "s_sub_u32 s77, s20, s14\n"
    "s_cmp_lt_u32 s77, 0xc8\n"
    "s_cbranch_scc0 A_QOK16\n"
    "s_memrealtime s[14:15]\n"
    "s_waitcnt lgkmcnt(0)\n"
    "s_branch A_QCHK16\n"
    "A_QOK16:\n"
    "s_sub_u32 s78, s14, s83\n"
    "s_cmp_gt_u32 s78, 0x3f2\n"
    "s_cselect_b32 s77, 47, 0\n"
    "s_add_u32 s20, s14, s77\n"
    "v_mfma_f32_16x16x32_bf16 a[0:3], v[192:195], a[180:183], a[0:3]\n"
    "v_perm_b32 v196, v164, v160, s68\n"
    "v_perm_b32 v197, v172, v168, s68\n"
    "v_mfma_f32_16x16x32_bf16 a[32:35], v[192:195], a[184:187], a[32:35]\n"
    "v_perm_b32 v198, v180, v176, s68\n"
    "v_perm_b32 v199, v188, v184, s68\n"
    "v_mfma_f32_16x16x32_bf16 a[64:67], v[192:195], a[188:191], a[64:67]\n"
    "s_barrier\n"
    "v_mov_b32 v153, 0\n"
    "v_mfma_f32_16x16x32_bf16 a[96:99], v[192:195], a[192:195], a[96:99]\n"
    "v_mov_b32 v128, 0\n"
    "v_mov_b32 v154, 0\n"
    "v_mfma_f32_16x16x32_bf16 a[128:131], v[192:195], a[196:199], a[128:131]\n"
    "v_mov_b32 v129, 0\n"
    "v_mov_b32 v155, 0\n"
    "v_mfma_f32_16x16x32_bf16 a[4:7], v[196:199], a[180:183], a[4:7]\n"
    "v_perm_b32 v192, v165, v161, s67\n"
    "v_perm_b32 v193, v173, v169, s67\n"
    "v_mfma_f32_16x16x32_bf16 a[36:39], v[196:199], a[184:187], a[36:39]\n"
    "v_perm_b32 v194, v181, v177, s67\n"
    "v_perm_b32 v195, v189, v185, s67\n"
    "v_mfma_f32_16x16x32_bf16 a[68:71], v[196:199], a[188:191], a[68:71]\n"
    "v_mov_b32 v130, 0\n"
    "v_mov_b32 v156, 0\n"
    "v_mfma_f32_16x16x32_bf16 a[100:103], v[196:199], a[192:195], a[100:103]\n"
    "v_mov_b32 v131, 0\n"
    "v_mov_b32 v157, 0\n"
    "v_mfma_f32_16x16x32_bf16 a[132:135], v[196:199], a[196:199], a[132:135]\n"
    "v_mov_b32 v132, 0\n"
    "v_mov_b32 v158, 0\n"
    "v_mfma_f32_16x16x32_bf16 a[8:11], v[192:195], a[180:183], a[8:11]\n"
    "v_perm_b32 v196, v165, v161, s68\n"
    "v_perm_b32 v197, v173, v169, s68\n"
    "v_mfma_f32_16x16x32_bf16 a[40:43], v[192:195], a[184:187], a[40:43]\n"
    "v_perm_b32 v198, v181, v177, s68\n"
    "v_perm_b32 v199, v189, v185, s68\n"
    "v_mfma_f32_16x16x32_bf16 a[72:75], v[192:195], a[188:191], a[72:75]\n"
    "v_mov_b32 v133, 0\n"
    "v_mov_b32 v159, 0\n"
    "v_mfma_f32_16x16x32_bf16 a[104:107], v[192:195], a[192:195], a[104:107]\n"
    "v_mov_b32 v134, 0\n"
    "v_mfma_f32_16x16x32_bf16 a[136:139], v[192:195], a[196:199], a[136:139]\n"
    "v_mov_b32 v135, 0\n"
    "v_mfma_f32_16x16x32_bf16 a[12:15], v[196:199], a[180:183], a[12:15]\n"
    "v_perm_b32 v192, v166, v162, s67\n"
    "v_perm_b32 v193, v174, v170, s67\n"
    "v_mfma_f32_16x16x32_bf16 a[44:47], v[196:199], a[184:187], a[44:47]\n"
    "v_perm_b32 v194, v182, v178, s67\n"
    "v_perm_b32 v195, v190, v186, s67\n"
    "v_mfma_f32_16x16x32_bf16 a[76:79], v[196:199], a[188:191], a[76:79]\n"
    "v_mov_b32 v136, 0\n"
    "v_mfma_f32_16x16x32_bf16 a[108:111], v[196:199], a[192:195], a[108:111]\n"
    "v_mov_b32 v137, 0\n"
    "v_mfma_f32_16x16x32_bf16 a[140:143], v[196:199], a[196:199], a[140:143]\n"
    "v_mov_b32 v138, 0\n"
    "v_mfma_f32_16x16x32_bf16 a[16:19], v[192:195], a[180:183], a[16:19]\n"
    "v_perm_b32 v196, v166, v162, s68\n"
    "v_perm_b32 v197, v174, v170, s68\n"
    "v_mfma_f32_16x16x32_bf16 a[48:51], v[192:195], a[184:187], a[48:51]\n"
    "v_perm_b32 v198, v182, v178, s68\n"
    "v_perm_b32 v199, v190, v186, s68\n"
    "v_mfma_f32_16x16x32_bf16 a[80:83], v[192:195], a[188:191], a[80:83]\n"
    "v_mov_b32 v139, 0\n"
    "v_mfma_f32_16x16x32_bf16 a[112:115], v[192:195], a[192:195], a[112:115]\n"
    "v_mov_b32 v140, 0\n"
    "v_mfma_f32_16x16x32_bf16 a[144:147], v[192:195], a[196:199], a[144:147]\n"
    "v_mov_b32 v141, 0\n"
    "v_mfma_f32_16x16x32_bf16 a[20:23], v[196:199], a[180:183], a[20:23]\n"
    "v_perm_b32 v192, v167, v163, s67\n"
    "v_perm_b32 v193, v175, v171, s67\n"
    "v_mfma_f32_16x16x32_bf16 a[52:55], v[196:199], a[184:187], a[52:55]\n"
    "v_perm_b32 v194, v183, v179, s67\n"
    "v_perm_b32 v195, v191, v187, s67\n"
    "v_mfma_f32_16x16x32_bf16 a[84:87], v[196:199], a[188:191], a[84:87]\n"
    "v_mov_b32 v142, 0\n"
    "v_mfma_f32_16x16x32_bf16 a[116:119], v[196:199], a[192:195], a[116:119]\n"
    "v_mov_b32 v143, 0\n"
    "v_mfma_f32_16x16x32_bf16 a[148:151], v[196:199], a[196:199], a[148:151]\n"
    "v_mov_b32 v144, 0\n"
    "v_mfma_f32_16x16x32_bf16 a[24:27], v[192:195], a[180:183], a[24:27]\n"
    "v_perm_b32 v196, v167, v163, s68\n"
    "v_perm_b32 v197, v175, v171, s68\n"
    "v_mfma_f32_16x16x32_bf16 a[56:59], v[192:195], a[184:187], a[56:59]\n"
    "v_perm_b32 v198, v183, v179, s68\n"
    "v_perm_b32 v199, v191, v187, s68\n"
    "v_mfma_f32_16x16x32_bf16 a[88:91], v[192:195], a[188:191], a[88:91]\n"
    "v_mov_b32 v145, 0\n"
    "v_mfma_f32_16x16x32_bf16 a[120:123], v[192:195], a[192:195], a[120:123]\n"
    "v_mov_b32 v146, 0\n"
    "v_mfma_f32_16x16x32_bf16 a[152:155], v[192:195], a[196:199], a[152:155]\n"
    "v_mov_b32 v147, 0\n"
    "v_mfma_f32_16x16x32_bf16 a[28:31], v[196:199], a[180:183], a[28:31]\n"
    "v_mov_b32 v148, 0\n"
    "v_mfma_f32_16x16x32_bf16 a[60:63], v[196:199], a[184:187], a[60:63]\n"
    "v_mov_b32 v149, 0\n"
    "v_mfma_f32_16x16x32_bf16 a[92:95], v[196:199], a[188:191], a[92:95]\n"
    "v_mov_b32 v150, 0\n"
    "v_mfma_f32_16x16x32_bf16 a[124:127], v[196:199], a[192:195], a[124:127]\n"
    "v_mov_b32 v151, 0\n"
    "v_mfma_f32_16x16x32_bf16 a[156:159], v[196:199], a[196:199], a[156:159]\n"
    "v_mov_b32 v152, 0\n"
    "s_nop 7\n"
    "v_accvgpr_read_b32 v160, a0\n"
    "v_accvgpr_read_b32 v161, a4\n"
    "v_accvgpr_read_b32 v162, a8\n"
    "v_accvgpr_read_b32 v163, a12\n"
    "v_accvgpr_read_b32 v164, a16\n"
    "v_accvgpr_read_b32 v165, a20\n"
    "v_accvgpr_read_b32 v166, a24\n"
    "v_accvgpr_read_b32 v167, a28\n"
    "v_cvt_pk_bf16_f32 v168, v160, v161\n"
    "v_cvt_pk_bf16_f32 v169, v162, v163\n"
    "v_cvt_pk_bf16_f32 v170, v164, v165\n"
    "v_cvt_pk_bf16_f32 v171, v166, v167\n"
    "v_accvgpr_write_b32 a160, v168\n"
    "v_accvgpr_write_b32 a161, v169\n"
    "v_accvgpr_write_b32 a162, v170\n"
    "v_accvgpr_write_b32 a163, v171\n"
    "v_accvgpr_read_b32 v172, a1\n"
    "v_accvgpr_read_b32 v173, a5\n"
    "v_accvgpr_read_b32 v174, a9\n"
    "v_accvgpr_read_b32 v175, a13\n"
    "v_accvgpr_read_b32 v176, a17\n"
    "v_accvgpr_read_b32 v177, a21\n"
    "v_accvgpr_read_b32 v178, a25\n"
    "v_accvgpr_read_b32 v179, a29\n"
    "v_cvt_pk_bf16_f32 v180, v172, v173\n"
    "v_cvt_pk_bf16_f32 v181, v174, v175\n"
    "v_cvt_pk_bf16_f32 v182, v176, v177\n"
    "v_cvt_pk_bf16_f32 v183, v178, v179\n"
    "v_accvgpr_write_b32 a164, v180\n"
    "v_accvgpr_write_b32 a165, v181\n"
    "v_accvgpr_write_b32 a166, v182\n"
    "v_accvgpr_write_b32 a167, v183\n"
    "v_accvgpr_read_b32 v160, a2\n"
    "v_accvgpr_read_b32 v161, a6\n"
    "v_accvgpr_read_b32 v162, a10\n"
    "v_accvgpr_read_b32 v163, a14\n"
    "v_accvgpr_read_b32 v164, a18\n"
    "v_accvgpr_read_b32 v165, a22\n"
    "v_accvgpr_read_b32 v166, a26\n"
    "v_accvgpr_read_b32 v167, a30\n"
    "v_cvt_pk_bf16_f32 v168, v160, v161\n"
    "v_cvt_pk_bf16_f32 v169, v162, v163\n"
    "v_cvt_pk_bf16_f32 v170, v164, v165\n"
    "v_cvt_pk_bf16_f32 v171, v166, v167\n"
    "v_accvgpr_write_b32 a168, v168\n"
    "v_accvgpr_write_b32 a169, v169\n"
    "v_accvgpr_write_b32 a170, v170\n"
    "v_accvgpr_write_b32 a171, v171\n"
    "v_accvgpr_read_b32 v172, a3\n"
    "v_accvgpr_read_b32 v173, a7\n"
    "v_accvgpr_read_b32 v174, a11\n"
    "v_accvgpr_read_b32 v175, a15\n"
    "v_accvgpr_read_b32 v176, a19\n"
    "v_accvgpr_read_b32 v177, a23\n"
    "v_accvgpr_read_b32 v178, a27\n"
    "v_accvgpr_read_b32 v179, a31\n"
    "v_cvt_pk_bf16_f32 v180, v172, v173\n"
    "v_cvt_pk_bf16_f32 v181, v174, v175\n"
    "v_cvt_pk_bf16_f32 v182, v176, v177\n"
    "v_cvt_pk_bf16_f32 v183, v178, v179\n"
    "v_accvgpr_write_b32 a172, v180\n"
    "v_accvgpr_write_b32 a173, v181\n"
    "v_accvgpr_write_b32 a174, v182\n"
    "v_accvgpr_write_b32 a175, v183\n"
    "s_waitcnt vmcnt(63)\n"
    "ds_read_b128 a[0:3], v214 offset:8192\n"
    "ds_read_b128 a[4:7], v215 offset:8192\n"
    "ds_read_b128 a[8:11], v216 offset:8192\n"
    "ds_read_b128 a[12:15], v217 offset:8192\n"
    "ds_read_b128 a[16:19], v214 offset:49152\n"
    "ds_read_b128 a[20:23], v215 offset:49152\n"
    "ds_read_b128 a[24:27], v216 offset:49152\n"
    "ds_read_b128 a[28:31], v217 offset:49152\n"
    "v_accvgpr_read_b32 v160, a32\n"
    "v_accvgpr_read_b32 v161, a36\n"
    "v_accvgpr_read_b32 v162, a40\n"
    "v_accvgpr_read_b32 v163, a44\n"
    "v_accvgpr_read_b32 v164, a48\n"
    "v_accvgpr_read_b32 v165, a52\n"
    "v_accvgpr_read_b32 v166, a56\n"
    "v_accvgpr_read_b32 v167, a60\n"
    "v_cvt_pk_bf16_f32 v168, v160, v161\n"
    "v_cvt_pk_bf16_f32 v169, v162, v163\n"
    "v_cvt_pk_bf16_f32 v170, v164, v165\n"
    "v_cvt_pk_bf16_f32 v171, v166, v167\n"
    "v_accvgpr_write_b32 a176, v168\n"
    "v_accvgpr_write_b32 a177, v169\n"
    "v_accvgpr_write_b32 a178, v170\n"
    "v_accvgpr_write_b32 a179, v171\n"
    "v_accvgpr_read_b32 v172, a33\n"
    "v_accvgpr_read_b32 v173, a37\n"
    "v_accvgpr_read_b32 v174, a41\n"
    "v_accvgpr_read_b32 v175, a45\n"
    "v_accvgpr_read_b32 v176, a49\n"
    "v_accvgpr_read_b32 v177, a53\n"
    "v_accvgpr_read_b32 v178, a57\n"
    "v_accvgpr_read_b32 v179, a61\n"
    "v_cvt_pk_bf16_f32 v180, v172, v173\n"
    "v_cvt_pk_bf16_f32 v181, v174, v175\n"
    "v_cvt_pk_bf16_f32 v182, v176, v177\n"
    "v_cvt_pk_bf16_f32 v183, v178, v179\n"
    "v_accvgpr_write_b32 a180, v180\n"
    "v_accvgpr_write_b32 a181, v181\n"
    "v_accvgpr_write_b32 a182, v182\n"
    "v_accvgpr_write_b32 a183, v183\n"
    "s_waitcnt lgkmcnt(0)\n"
    "s_barrier\n"
    "s_add_u32 s78, s75, 0x4000\n"
    "s_and_b32 s78, s78, 0x1e000\n"
    "s_add_u32 s79, s78, 0x1000\n"
    "v_accvgpr_read_b32 v160, a34\n"
    "v_accvgpr_read_b32 v161, a38\n"
    "v_accvgpr_read_b32 v162, a42\n"
    "v_accvgpr_read_b32 v163, a46\n"
    "v_accvgpr_read_b32 v164, a50\n"
    "v_accvgpr_read_b32 v165, a54\n"
    "v_accvgpr_read_b32 v166, a58\n"
    "v_accvgpr_read_b32 v167, a62\n"
    "v_cvt_pk_bf16_f32 v168, v160, v161\n"
    "v_cvt_pk_bf16_f32 v169, v162, v163\n"
    "v_cvt_pk_bf16_f32 v170, v164, v165\n"
    "v_cvt_pk_bf16_f32 v171, v166, v167\n"
    "v_accvgpr_write_b32 a184, v168\n"
    "v_accvgpr_write_b32 a185, v169\n"
    "v_accvgpr_write_b32 a186, v170\n"
    "v_accvgpr_write_b32 a187, v171\n"
    "s_add_u32 m0, s71, 0x2000\n"
    "v_accvgpr_read_b32 v172, a35\n"
    "v_accvgpr_read_b32 v173, a39\n"
    "v_accvgpr_read_b32 v174, a43\n"
    "v_accvgpr_read_b32 v175, a47\n"
    "v_accvgpr_read_b32 v176, a51\n"
    "v_accvgpr_read_b32 v177, a55\n"
    "v_accvgpr_read_b32 v178, a59\n"
    "v_accvgpr_read_b32 v179, a63\n"
    "v_cvt_pk_bf16_f32 v180, v172, v173\n"
    "v_cvt_pk_bf16_f32 v181, v174, v175\n"
    "v_cvt_pk_bf16_f32 v182, v176, v177\n"
    "v_cvt_pk_bf16_f32 v183, v178, v179\n"
    "v_accvgpr_write_b32 a188, v180\n"
    "v_accvgpr_write_b32 a189, v181\n"
    "v_accvgpr_write_b32 a190, v182\n"
    "v_accvgpr_write_b32 a191, v183\n"
    "buffer_load_dwordx4 v226, s[40:43], s78 offen nt lds\n"
    "v_accvgpr_read_b32 v160, a64\n"
    "v_accvgpr_read_b32 v161, a68\n"
    "v_accvgpr_read_b32 v162, a72\n"
    "v_accvgpr_read_b32 v163, a76\n"
    "v_accvgpr_read_b32 v164, a80\n"
    "v_accvgpr_read_b32 v165, a84\n"
    "v_accvgpr_read_b32 v166, a88\n"
    "v_accvgpr_read_b32 v167, a92\n"
    "v_cvt_pk_bf16_f32 v168, v160, v161\n"
    "v_cvt_pk_bf16_f32 v169, v162, v163\n"
    "v_cvt_pk_bf16_f32 v170, v164, v165\n"
    "v_cvt_pk_bf16_f32 v171, v166, v167\n"
    "v_accvgpr_write_b32 a192, v168\n"
    "v_accvgpr_write_b32 a193, v169\n"
    "v_accvgpr_write_b32 a194, v170\n"
    "v_accvgpr_write_b32 a195, v171\n"
    "buffer_load_dwordx4 v227, s[40:43], s78 offen offset:1024 nt lds\n"
    "v_accvgpr_read_b32 v172, a65\n"
    "v_accvgpr_read_b32 v173, a69\n"
    "v_accvgpr_read_b32 v174, a73\n"
    "v_accvgpr_read_b32 v175, a77\n"
    "v_accvgpr_read_b32 v176, a81\n"
    "v_accvgpr_read_b32 v177, a85\n"
    "v_accvgpr_read_b32 v178, a89\n"
    "v_accvgpr_read_b32 v179, a93\n"
    "v_cvt_pk_bf16_f32 v180, v172, v173\n"
    "v_cvt_pk_bf16_f32 v181, v174, v175\n"
    "v_cvt_pk_bf16_f32 v182, v176, v177\n"
    "v_cvt_pk_bf16_f32 v183, v178, v179\n"
    "v_accvgpr_write_b32 a196, v180\n"
    "v_accvgpr_write_b32 a197, v181\n"
    "v_accvgpr_write_b32 a198, v182\n"
    "v_accvgpr_write_b32 a199, v183\n"
    "buffer_load_dwordx4 v228, s[40:43], s78 offen offset:2048 nt lds\n"
    "v_accvgpr_read_b32 v160, a66\n"
    "v_accvgpr_read_b32 v161, a70\n"
    "v_accvgpr_read_b32 v162, a74\n"
    "v_accvgpr_read_b32 v163, a78\n"
    "v_accvgpr_read_b32 v164, a82\n"
    "v_accvgpr_read_b32 v165, a86\n"
    "v_accvgpr_read_b32 v166, a90\n"
    "v_accvgpr_read_b32 v167, a94\n"
    "v_cvt_pk_bf16_f32 v168, v160, v161\n"
    "v_cvt_pk_bf16_f32 v169, v162, v163\n"
    "v_cvt_pk_bf16_f32 v170, v164, v165\n"
    "v_cvt_pk_bf16_f32 v171, v166, v167\n"
    "v_accvgpr_write_b32 a200, v168\n"
    "v_accvgpr_write_b32 a201, v169\n"
    "v_accvgpr_write_b32 a202, v170\n"
    "v_accvgpr_write_b32 a203, v171\n"
    "buffer_load_dwordx4 v229, s[40:43], s78 offen offset:3072 nt lds\n"
    "v_accvgpr_read_b32 v172, a67\n"
    "v_accvgpr_read_b32 v173, a71\n"
    "v_accvgpr_read_b32 v174, a75\n"
    "v_accvgpr_read_b32 v175, a79\n"
    "v_accvgpr_read_b32 v176, a83\n"
    "v_accvgpr_read_b32 v177, a87\n"
    "v_accvgpr_read_b32 v178, a91\n"
    "v_accvgpr_read_b32 v179, a95\n"
    "v_cvt_pk_bf16_f32 v180, v172, v173\n"
    "v_cvt_pk_bf16_f32 v181, v174, v175\n"
    "v_cvt_pk_bf16_f32 v182, v176, v177\n"
    "v_cvt_pk_bf16_f32 v183, v178, v179\n"
    "v_accvgpr_write_b32 a204, v180\n"
    "v_accvgpr_write_b32 a205, v181\n"
    "v_accvgpr_write_b32 a206, v182\n"
    "v_accvgpr_write_b32 a207, v183\n"
    "v_accvgpr_read_b32 v160, a96\n"
    "v_accvgpr_read_b32 v161, a100\n"
    "v_accvgpr_read_b32 v162, a104\n"
    "v_accvgpr_read_b32 v163, a108\n"
    "v_accvgpr_read_b32 v164, a112\n"
    "v_accvgpr_read_b32 v165, a116\n"
    "v_accvgpr_read_b32 v166, a120\n"
    "v_accvgpr_read_b32 v167, a124\n"
    "v_cvt_pk_bf16_f32 v168, v160, v161\n"
    "v_cvt_pk_bf16_f32 v169, v162, v163\n"
    "v_cvt_pk_bf16_f32 v170, v164, v165\n"
    "v_cvt_pk_bf16_f32 v171, v166, v167\n"
    "v_accvgpr_write_b32 a208, v168\n"
    "v_accvgpr_write_b32 a209, v169\n"
    "v_accvgpr_write_b32 a210, v170\n"
    "v_accvgpr_write_b32 a211, v171\n"
    "v_accvgpr_read_b32 v172, a97\n"
    "v_accvgpr_read_b32 v173, a101\n"
    "v_accvgpr_read_b32 v174, a105\n"
    "v_accvgpr_read_b32 v175, a109\n"
    "v_accvgpr_read_b32 v176, a113\n"
    "v_accvgpr_read_b32 v177, a117\n"
    "v_accvgpr_read_b32 v178, a121\n"
    "v_accvgpr_read_b32 v179, a125\n"
    "v_cvt_pk_bf16_f32 v180, v172, v173\n"
    "v_cvt_pk_bf16_f32 v181, v174, v175\n"
    "v_cvt_pk_bf16_f32 v182, v176, v177\n"
    "v_cvt_pk_bf16_f32 v183, v178, v179\n"
    "v_accvgpr_write_b32 a212, v180\n"
    "v_accvgpr_write_b32 a213, v181\n"
    "v_accvgpr_write_b32 a214, v182\n"
    "v_accvgpr_write_b32 a215, v183\n"
    "v_accvgpr_read_b32 v160, a98\n"
    "v_accvgpr_read_b32 v161, a102\n"
    "v_accvgpr_read_b32 v162, a106\n"
    "v_accvgpr_read_b32 v163, a110\n"
    "v_accvgpr_read_b32 v164, a114\n"
    "v_accvgpr_read_b32 v165, a118\n"
    "v_accvgpr_read_b32 v166, a122\n"
    "v_accvgpr_read_b32 v167, a126\n"
    "v_cvt_pk_bf16_f32 v168, v160, v161\n"
    "v_cvt_pk_bf16_f32 v169, v162, v163\n"
    "v_cvt_pk_bf16_f32 v170, v164, v165\n"
    "v_cvt_pk_bf16_f32 v171, v166, v167\n"
    "v_accvgpr_write_b32 a216, v168\n"
    "v_accvgpr_write_b32 a217, v169\n"
    "v_accvgpr_write_b32 a218, v170\n"
    "v_accvgpr_write_b32 a219, v171\n"
    "v_accvgpr_read_b32 v172, a99\n"
    "v_accvgpr_read_b32 v173, a103\n"
    "v_accvgpr_read_b32 v174, a107\n"
    "v_accvgpr_read_b32 v175, a111\n"
    "v_accvgpr_read_b32 v176, a115\n"
    "v_accvgpr_read_b32 v177, a119\n"
    "v_accvgpr_read_b32 v178, a123\n"
    "v_accvgpr_read_b32 v179, a127\n"
    "v_cvt_pk_bf16_f32 v180, v172, v173\n"
    "v_cvt_pk_bf16_f32 v181, v174, v175\n"
    "v_cvt_pk_bf16_f32 v182, v176, v177\n"
    "v_cvt_pk_bf16_f32 v183, v178, v179\n"
    "v_accvgpr_write_b32 a220, v180\n"
    "v_accvgpr_write_b32 a221, v181\n"
    "v_accvgpr_write_b32 a222, v182\n"
    "v_accvgpr_write_b32 a223, v183\n"
    "v_accvgpr_read_b32 v160, a128\n"
    "v_accvgpr_read_b32 v161, a132\n"
    "v_accvgpr_read_b32 v162, a136\n"
    "v_accvgpr_read_b32 v163, a140\n"
    "v_accvgpr_read_b32 v164, a144\n"
    "v_accvgpr_read_b32 v165, a148\n"
    "v_accvgpr_read_b32 v166, a152\n"
    "v_accvgpr_read_b32 v167, a156\n"
    "v_cvt_pk_bf16_f32 v168, v160, v161\n"
    "v_cvt_pk_bf16_f32 v169, v162, v163\n"
    "v_cvt_pk_bf16_f32 v170, v164, v165\n"
    "v_cvt_pk_bf16_f32 v171, v166, v167\n"
    "v_accvgpr_write_b32 a224, v168\n"
    "v_accvgpr_write_b32 a225, v169\n"
    "v_accvgpr_write_b32 a226, v170\n"
    "v_accvgpr_write_b32 a227, v171\n"
    "v_accvgpr_read_b32 v172, a129\n"
    "v_accvgpr_read_b32 v173, a133\n"
    "v_accvgpr_read_b32 v174, a137\n"
    "v_accvgpr_read_b32 v175, a141\n"
    "v_accvgpr_read_b32 v176, a145\n"
    "v_accvgpr_read_b32 v177, a149\n"
    "v_accvgpr_read_b32 v178, a153\n"
    "v_accvgpr_read_b32 v179, a157\n"
    "v_cvt_pk_bf16_f32 v180, v172, v173\n"
    "v_cvt_pk_bf16_f32 v181, v174, v175\n"
    "v_cvt_pk_bf16_f32 v182, v176, v177\n"
    "v_cvt_pk_bf16_f32 v183, v178, v179\n"
    "v_accvgpr_write_b32 a228, v180\n"
    "v_accvgpr_write_b32 a229, v181\n"
    "v_accvgpr_write_b32 a230, v182\n"
    "v_accvgpr_write_b32 a231, v183\n"
    "v_accvgpr_read_b32 v160, a130\n"
    "v_accvgpr_read_b32 v161, a134\n"
    "v_accvgpr_read_b32 v162, a138\n"
    "v_accvgpr_read_b32 v163, a142\n"
    "v_accvgpr_read_b32 v164, a146\n"
    "v_accvgpr_read_b32 v165, a150\n"
    "v_accvgpr_read_b32 v166, a154\n"
    "v_accvgpr_read_b32 v167, a158\n"
    "v_cvt_pk_bf16_f32 v168, v160, v161\n"
    "v_cvt_pk_bf16_f32 v169, v162, v163\n"
    "v_cvt_pk_bf16_f32 v170, v164, v165\n"
    "v_cvt_pk_bf16_f32 v171, v166, v167\n"
    "v_accvgpr_write_b32 a232, v168\n"
    "v_accvgpr_write_b32 a233, v169\n"
    "v_accvgpr_write_b32 a234, v170\n"
    "v_accvgpr_write_b32 a235, v171\n"
    "v_accvgpr_read_b32 v172, a131\n"
    "v_accvgpr_read_b32 v173, a135\n"
    "v_accvgpr_read_b32 v174, a139\n"
    "v_accvgpr_read_b32 v175, a143\n"
    "v_accvgpr_read_b32 v176, a147\n"
    "v_accvgpr_read_b32 v177, a151\n"
    "v_accvgpr_read_b32 v178, a155\n"
    "v_accvgpr_read_b32 v179, a159\n"
    "v_cvt_pk_bf16_f32 v180, v172, v173\n"
    "v_cvt_pk_bf16_f32 v181, v174, v175\n"
    "v_cvt_pk_bf16_f32 v182, v176, v177\n"
    "v_cvt_pk_bf16_f32 v183, v178, v179\n"
    "v_accvgpr_write_b32 a236, v180\n"
    "v_accvgpr_write_b32 a237, v181\n"
    "v_accvgpr_write_b32 a238, v182\n"
    "v_accvgpr_write_b32 a239, v183\n"
    "s_waitcnt vmcnt(63)\n"
    "ds_read_b128 a[96:99], v214 offset:4096\n"
    "ds_read_b128 a[100:103], v215 offset:4096\n"
    "ds_read_b128 a[104:107], v216 offset:4096\n"
    "ds_read_b128 a[108:111], v217 offset:4096\n"
    "ds_read_b128 a[112:115], v214 offset:45056\n"
    "ds_read_b128 a[116:119], v215 offset:45056\n"
    "ds_read_b128 a[120:123], v216 offset:45056\n"
    "ds_read_b128 a[124:127], v217 offset:45056\n"
    "s_mov_b32 s80, 0\n"
    "s_mov_b64 s[56:57], s[40:41]\n"
    "s_mov_b32 s58, 0x20000\n"
    "s_mov_b32 s59, 0x20000\n"
    "s_add_u32 s81, s75, 0x4000\n"
    "s_and_b32 s81, s81, 0x1e000\n"
    "v_mfma_f32_16x16x32_bf16 v[160:163], a[0:3], a[160:163], 0\n"
    "v_accvgpr_write_b32 a128, 0\n"
    "v_accvgpr_write_b32 a129, 0\n"
    "v_mfma_f32_16x16x32_bf16 v[160:163], a[4:7], a[164:167], v[160:163]\n"
    "v_accvgpr_write_b32 a130, 0\n"
    "v_accvgpr_write_b32 a131, 0\n"
    "v_mfma_f32_16x16x32_bf16 v[160:163], a[8:11], a[168:171], v[160:163]\n"
    "v_accvgpr_write_b32 a132, 0\n"
    "v_accvgpr_write_b32 a133, 0\n"
    "v_mfma_f32_16x16x32_bf16 v[160:163], a[12:15], a[172:175], v[160:163]\n"
    "v_accvgpr_write_b32 a134, 0\n"
    "v_accvgpr_write_b32 a135, 0\n"
    "v_mfma_f32_16x16x32_bf16 v[164:167], a[16:19], a[160:163], 0\n"
    "v_accvgpr_write_b32 a136, 0\n"
    "v_accvgpr_write_b32 a137, 0\n"
    "v_mfma_f32_16x16x32_bf16 v[164:167], a[20:23], a[164:167], v[164:167]\n"
    "v_accvgpr_write_b32 a138, 0\n"
    "v_accvgpr_write_b32 a139, 0\n"
    "v_mfma_f32_16x16x32_bf16 v[164:167], a[24:27], a[168:171], v[164:167]\n"
    "v_accvgpr_write_b32 a140, 0\n"
    "v_accvgpr_write_b32 a141, 0\n"
    "v_mfma_f32_16x16x32_bf16 v[164:167], a[28:31], a[172:175], v[164:167]\n"
    "v_accvgpr_write_b32 a142, 0\n"
    "v_accvgpr_write_b32 a143, 0\n"
    "v_accvgpr_write_b32 a144, 0\n"
    "v_accvgpr_write_b32 a145, 0\n"
    "v_accvgpr_write_b32 a146, 0\n"
    "v_accvgpr_write_b32 a147, 0\n"
    "v_mov_b32 v197, 0x3f803f80\n"
    "v_accvgpr_write_b32 a148, v197\n"
    "v_accvgpr_write_b32 a149, v197\n"
    "v_accvgpr_write_b32 a150, v197\n"
    "v_accvgpr_write_b32 a151, v197\n"
    "A_TH_S4:\n"
    "s_cbranch_execz A_TH_RET\n"
    "v_mov_b32 v180, 0\n"
    "v_mov_b32 v181, 0\n"
    "v_mov_b32 v182, 0\n"
    "v_mov_b32 v183, 0\n"
    "v_mov_b32 v184, 0xff800000\n"
    "v_mov_b32 v185, 0xff800000\n"
    "v_mov_b32 v186, 0xff800000\n"
    "v_mov_b32 v187, 0xff800000\n"
    "v_mov_b32 v188, 0xff800000\n"
    "v_mov_b32 v189, 0xff800000\n"
    "v_mov_b32 v190, 0xff800000\n"
    "v_mov_b32 v191, 0xff800000\n"
    "v_mov_b32 v192, 0xff800000\n"
    "v_mov_b32 v193, 0xff800000\n"
    "s_waitcnt lgkmcnt(0)\n"
    "A_ATT_LOOP:\n"
    "s_add_u32 s82, s81, 0x4000\n"
    "s_and_b32 s82, s82, 0x1e000\n"
    "s_cmp_lg_u32 s80, 6\n"
    "s_cbranch_scc1 A_ATT_NOSW\n"
    "s_mov_b64 s[56:57], s[52:53]\n"
    "s_mov_b32 s58, 0x90000\n"
    "s_add_u32 s85, s74, s86\n"
    "s_mov_b32 s82, s85\n"
    "v_mov_b32 v230, v234\n"
    "v_mov_b32 v231, v235\n"
    "v_mov_b32 v232, v236\n"
    "v_mov_b32 v233, v237\n"
    "A_ATT_NOSW:\n"
    "s_cmp_lg_u32 s80, 1\n"
    "s_cbranch_scc1 A_HS0\n"
    "s_mul_i32 s76, s72, 0xa00\n"
    "s_mul_i32 s62, s35, 0x14000\n"
    "s_add_u32 s62, s62, s76\n"
    "s_lshl_b32 s77, s35, 9\n"
    "s_add_u32 s77, s77, s76\n"
    "s_add_u32 s66, s77, 0xa0000\n"
    "s_lshl_b32 s60, 1, s35\n"
    "s_or_b32 s77, s60, 0xff00\n"
    "s_cmp_lt_u32 s35, 5\n"
    "s_cselect_b32 s66, s66, 0x40000000\n"
    "s_cselect_b32 s60, s60, s77\n"
    "s_mov_b32 s28, -1\n"
    "s_mov_b32 s29, 0\n"
    "s_sub_u32 s30, s74, 0x80\n"
    "s_cselect_b32 s30, 0x400, s30\n"
    "s_add_u32 s31, s34, 8\n"
    "s_sub_u32 s77, s31, 9\n"
    "s_cselect_b32 s31, s31, s77\n"
    "s_sub_u32 s77, s31, 9\n"
    "s_cselect_b32 s31, s31, s77\n"
    "s_lshl_b32 s31, s31, 7\n"
    "s_mul_i32 s77, s35, 0x9000\n"
    "s_add_u32 s31, s31, s77\n"
    "s_cmp_lt_u32 s35, 5\n"
    "s_cselect_b32 s31, s31, 0x40000000\n"
    "s_mul_i32 s77, s34, 0x2d000\n"
    "s_lshl_b32 s76, s72, 5\n"
    "s_add_u32 s77, s77, 0\n"
    "s_add_u32 s77, s77, 0\n"
    "s_add_u32 s92, s26, s77\n"
    "s_addc_u32 s93, s27, 0\n"
    "s_and_b32 s93, s93, 0xffff\n"
    "s_mov_b32 s94, 0x2cb80\n"
    "s_mov_b32 s95, 0x20000\n"
    "v_and_b32 v209, 1, v239\n"
    "v_lshlrev_b32 v209, 4, v209\n"
    "v_mov_b32 v210, 0x3f80\n"
    "v_lshlrev_b32 v209, v209, v210\n"
    "v_lshrrev_b32 v208, 4, v239\n"
    "v_bfe_u32 v210, v239, 2, 2\n"
    "v_xor_b32 v210, v208, v210\n"
    "v_bfe_u32 v211, v239, 1, 1\n"
    "v_lshl_or_b32 v210, v210, 1, v211\n"
    "v_cmp_eq_u32 vcc, 0, v210\n"
    "s_nop 1\n"
    "v_cndmask_b32 v212, 0, v209, vcc\n"
    "v_accvgpr_write_b32 a240, v212\n"
    "v_accvgpr_write_b32 a242, v212\n"
    "v_cmp_eq_u32 vcc, 1, v210\n"
    "s_nop 1\n"
    "v_cndmask_b32 v212, 0, v209, vcc\n"
    "v_accvgpr_write_b32 a241, v212\n"
    "v_accvgpr_write_b32 a243, v212\n"
    "v_bfe_u32 v210, v239, 2, 1\n"
    "v_xor_b32 v208, v208, v210\n"
    "v_lshl_or_b32 v208, v208, 2, v211\n"
    "v_bfe_u32 v210, v239, 3, 1\n"
    "v_lshl_or_b32 v208, v210, 1, v208\n"
    "v_cmp_eq_u32 vcc, 0, v208\n"
    "s_nop 1\n"
    "v_cndmask_b32 v212, 0, v209, vcc\n"
    "v_accvgpr_write_b32 a244, v212\n"
    "v_cmp_eq_u32 vcc, 8, v208\n"
    "s_nop 1\n"
    "v_cndmask_b32 v212, 0, v209, vcc\n"
    "v_accvgpr_write_b32 a248, v212\n"
    "v_cmp_eq_u32 vcc, 1, v208\n"
    "s_nop 1\n"
    "v_cndmask_b32 v212, 0, v209, vcc\n"
    "v_accvgpr_write_b32 a245, v212\n"
    "v_cmp_eq_u32 vcc, 9, v208\n"
    "s_nop 1\n"
    "v_cndmask_b32 v212, 0, v209, vcc\n"
    "v_accvgpr_write_b32 a249, v212\n"
    "v_cmp_eq_u32 vcc, 2, v208\n"
    "s_nop 1\n"
    "v_cndmask_b32 v212, 0, v209, vcc\n"
    "v_accvgpr_write_b32 a246, v212\n"
    "v_cmp_eq_u32 vcc, 10, v208\n"
    "s_nop 1\n"
    "v_cndmask_b32 v212, 0, v209, vcc\n"
    "v_accvgpr_write_b32 a250, v212\n"
    "v_cmp_eq_u32 vcc, 3, v208\n"
    "s_nop 1\n"
    "v_cndmask_b32 v212, 0, v209, vcc\n"
    "v_accvgpr_write_b32 a247, v212\n"
    "v_cmp_eq_u32 vcc, 11, v208\n"
    "s_nop 1\n"
    "v_cndmask_b32 v212, 0, v209, vcc\n"
    "v_accvgpr_write_b32 a251, v212\n"
    "s_add_u32 s76, s34, 0\n"
    "s_lshl_b32 s76, s76, 12\n"
    "s_add_u32 s76, s76, 0x2000000\n"
    "s_add_u32 s36, s24, s76\n"
    "s_addc_u32 s37, s25, 0\n"
    "s_and_b32 s37, s37, 0xffff\n"
    "s_mov_b32 s38, 0x101000\n"
    "s_mov_b32 s39, 0x20000\n"
    "s_mov_b32 s67, 0x100000\n"
    "buffer_load_dword v251, v249, s[36:39], 0 offen sc1\n"
    "A_HS0:\n"
    "s_cmp_lg_u32 s80, 2\n"
    "s_cbranch_scc1 A_HS1\n"
    "v_add_u32 v251, 0x9e3779b9, v251\n"
    "buffer_store_dword v251, v249, s[36:39], s67 offen sc1\n"
    "A_HS1:\n"
    "s_cmp_lg_u32 s80, 5\n"
    "s_cbranch_scc1 A_HS2\n"
    "buffer_load_dword v252, v250, s[36:39], 0 offen sc1\n"
    "buffer_load_dword v253, v250, s[36:39], s67 offen sc1\n"
    "A_HS2:\n"
    "s_cmp_lg_u32 s80, 4\n"
    "s_cbranch_scc1 A_TT\n"
    "s_mov_b64 exec, 0\n"
    "s_cmp_eq_u32 s72, 0\n"
    "s_cbranch_scc1 A_TT_FIRST0\n"
    "s_cmp_eq_u32 s72, 1\n"
    "s_cbranch_scc1 A_TT_FIRST1\n"
    "s_cmp_eq_u32 s72, 2\n"
    "s_cbranch_scc1 A_TT_FIRST2\n"
    "s_cmp_eq_u32 s72, 3\n"
    "s_cbranch_scc1 A_TT_FIRST3\n"
    "A_TT_RET:\n"
    "A_TT_FIRST3:\n"
    "s_mov_b64 exec, -1\n"
    "A_TT:\n"
    "v_mfma_f32_16x16x32_bf16 v[168:171], a[0:3], a[176:179], 0\n"
    "s_waitcnt vmcnt(12)\n"
    "s_barrier\n"
    "v_max3_f32 v196, v160, v161, v162\n"
    "v_max3_f32 v197, v163, v164, v165\n"
    "v_mfma_f32_16x16x32_bf16 v[168:171], a[4:7], a[180:183], v[168:171]\n"
    "ds_read_b64_tr_b16 a[64:65], v218 offset:4096\n"
    "v_max3_f32 v196, v196, v166, v167\n"
    "v_max_f32 v196, v196, v197\n"
    "v_mfma_f32_16x16x32_bf16 v[168:171], a[8:11], a[184:187], v[168:171]\n"
    "ds_read_b64_tr_b16 a[66:67], v218 offset:45056\n"
    "ds_read_b64_tr_b16 a[68:69], v219 offset:4096\n"
    "v_cmp_gt_f32 vcc, v196, v189\n"
    "v_mfma_f32_16x16x32_bf16 v[168:171], a[12:15], a[188:191], v[168:171]\n"
    "s_cbranch_vccnz A_RARE_EP_0\n"
    "A_BACK_EP_0:\n"
    "v_fma_f32 v160, v160, s69, v200\n"
    "v_fma_f32 v161, v161, s69, v200\n"
    "v_mfma_f32_16x16x32_bf16 v[172:175], a[16:19], a[176:179], 0\n"
    "ds_read_b64_tr_b16 a[70:71], v219 offset:45056\n"
    "v_exp_f32 v160, v160\n"
    "v_mfma_f32_16x16x32_bf16 v[172:175], a[20:23], a[180:183], v[172:175]\n"
    "ds_read_b64_tr_b16 a[72:73], v220 offset:4096\n"
    "v_exp_f32 v161, v161\n"
    "v_mfma_f32_16x16x32_bf16 v[172:175], a[24:27], a[184:187], v[172:175]\n"
    "ds_read_b64_tr_b16 a[74:75], v220 offset:45056\n"
    "v_fma_f32 v162, v162, s69, v200\n"
    "v_fma_f32 v163, v163, s69, v200\n"
    "v_mfma_f32_16x16x32_bf16 v[172:175], a[28:31], a[188:191], v[172:175]\n"
    "ds_read_b64_tr_b16 a[76:77], v221 offset:4096\n"
    "v_exp_f32 v162, v162\n"
    "v_mfma_f32_16x16x32_bf16 a[144:147], a[148:151], v[180:183], a[144:147]\n"
    "ds_read_b64_tr_b16 a[78:79], v221 offset:45056\n"
    "v_exp_f32 v163, v163\n"
    "v_mfma_f32_16x16x32_bf16 v[128:131], a[96:99], v[180:183], v[128:131]\n"
    "ds_read_b64_tr_b16 a[80:81], v222 offset:4096\n"
    "v_fma_f32 v164, v164, s69, v200\n"
    "v_fma_f32 v165, v165, s69, v200\n"
    "v_mfma_f32_16x16x32_bf16 v[132:135], a[100:103], v[180:183], v[132:135]\n"
    "ds_read_b64_tr_b16 a[82:83], v222 offset:45056\n"
    "v_cvt_pk_bf16_f32 v176, v160, v161\n"
    "v_cvt_pk_bf16_f32 v177, v162, v163\n"
    "v_mfma_f32_16x16x32_bf16 v[136:139], a[104:107], v[180:183], v[136:139]\n"
    "ds_read_b64_tr_b16 a[84:85], v223 offset:4096\n"
    "v_exp_f32 v164, v164\n"
    "v_mfma_f32_16x16x32_bf16 v[140:143], a[108:111], v[180:183], v[140:143]\n"
    "ds_read_b64_tr_b16 a[86:87], v223 offset:45056\n"
    "v_exp_f32 v165, v165\n"
    "v_mfma_f32_16x16x32_bf16 v[144:147], a[112:115], v[180:183], v[144:147]\n"
    "ds_read_b64_tr_b16 a[88:89], v224 offset:4096\n"
    "v_fma_f32 v166, v166, s69, v200\n"
    "v_fma_f32 v167, v167, s69, v200\n"
    "v_mfma_f32_16x16x32_bf16 v[148:151], a[116:119], v[180:183], v[148:151]\n"
    "ds_read_b64_tr_b16 a[90:91], v224 offset:45056\n"
    "v_exp_f32 v166, v166\n"
    "v_mfma_f32_16x16x32_bf16 v[152:155], a[120:123], v[180:183], v[152:155]\n"
    "ds_read_b64_tr_b16 a[92:93], v225 offset:4096\n"
    "v_exp_f32 v167, v167\n"
    "v_mfma_f32_16x16x32_bf16 v[156:159], a[124:127], v[180:183], v[156:159]\n"
    "ds_read_b64_tr_b16 a[94:95], v225 offset:45056\n"
    "v_cvt_pk_bf16_f32 v178, v164, v165\n"
    "v_cvt_pk_bf16_f32 v179, v166, v167\n"
    "v_mfma_f32_16x16x32_bf16 v[160:163], a[0:3], a[192:195], 0\n"
    "v_max3_f32 v196, v168, v169, v170\n"
    "v_max3_f32 v197, v171, v172, v173\n"
    "v_mfma_f32_16x16x32_bf16 v[160:163], a[4:7], a[196:199], v[160:163]\n"
    "s_add_u32 s78, s81, 0\n"
    "v_max3_f32 v196, v196, v174, v175\n"
    "v_max_f32 v196, v196, v197\n"
    "v_mfma_f32_16x16x32_bf16 v[160:163], a[8:11], a[200:203], v[160:163]\n"
    "s_and_b32 s78, s78, 0x1e000\n"
    "v_cmp_gt_f32 vcc, v196, v190\n"
    "v_mfma_f32_16x16x32_bf16 v[160:163], a[12:15], a[204:207], v[160:163]\n"
    "s_add_u32 m0, s71, 0x1000\n"
    "s_waitcnt lgkmcnt(0)\n"
    "s_barrier\n"
    "s_cbranch_vccnz A_RARE_EP_1\n"
    "A_BACK_EP_1:\n"
    "v_fma_f32 v168, v168, s69, v201\n"
    "v_fma_f32 v169, v169, s69, v201\n"
    "v_mfma_f32_16x16x32_bf16 v[164:167], a[16:19], a[192:195], 0\n"
    "buffer_load_dwordx4 v226, s[44:47], s78 offen nt lds\n"
    "v_exp_f32 v168, v168\n"
    "v_mfma_f32_16x16x32_bf16 v[164:167], a[20:23], a[196:199], v[164:167]\n"
    "buffer_load_dwordx4 v227, s[44:47], s78 offen offset:1024 nt lds\n"
    "v_exp_f32 v169, v169\n"
    "v_mfma_f32_16x16x32_bf16 v[164:167], a[24:27], a[200:203], v[164:167]\n"
    "buffer_load_dwordx4 v228, s[44:47], s78 offen offset:2048 nt lds\n"
    "v_fma_f32 v170, v170, s69, v201\n"
    "v_fma_f32 v171, v171, s69, v201\n"
    "v_mfma_f32_16x16x32_bf16 v[164:167], a[28:31], a[204:207], v[164:167]\n"
    "buffer_load_dwordx4 v229, s[44:47], s78 offen offset:3072 nt lds\n"
    "v_exp_f32 v170, v170\n"
    "v_mfma_f32_16x16x32_bf16 a[128:131], a[148:151], v[176:179], a[128:131]\n"
    "v_exp_f32 v171, v171\n"
    "v_mfma_f32_16x16x32_bf16 v[0:3], a[64:67], v[176:179], v[0:3]\n"
    "v_fma_f32 v172, v172, s69, v201\n"
    "v_fma_f32 v173, v173, s69, v201\n"
    "v_mfma_f32_16x16x32_bf16 v[4:7], a[68:71], v[176:179], v[4:7]\n"
    "v_cvt_pk_bf16_f32 v180, v168, v169\n"
    "v_cvt_pk_bf16_f32 v181, v170, v171\n"
    "v_mfma_f32_16x16x32_bf16 v[8:11], a[72:75], v[176:179], v[8:11]\n"
    "v_exp_f32 v172, v172\n"
    "v_mfma_f32_16x16x32_bf16 v[12:15], a[76:79], v[176:179], v[12:15]\n"
    "v_exp_f32 v173, v173\n"
    "v_mfma_f32_16x16x32_bf16 v[16:19], a[80:83], v[176:179], v[16:19]\n"
    "v_fma_f32 v174, v174, s69, v201\n"
    "v_fma_f32 v175, v175, s69, v201\n"
    "v_mfma_f32_16x16x32_bf16 v[20:23], a[84:87], v[176:179], v[20:23]\n"
    "v_exp_f32 v174, v174\n"
    "v_mfma_f32_16x16x32_bf16 v[24:27], a[88:91], v[176:179], v[24:27]\n"
    "v_exp_f32 v175, v175\n"
    "v_mfma_f32_16x16x32_bf16 v[28:31], a[92:95], v[176:179], v[28:31]\n"
    "v_cvt_pk_bf16_f32 v182, v172, v173\n"
    "v_cvt_pk_bf16_f32 v183, v174, v175\n"
    "v_mfma_f32_16x16x32_bf16 v[168:171], a[0:3], a[208:211], 0\n"
    "v_max3_f32 v196, v160, v161, v162\n"
    "v_max3_f32 v197, v163, v164, v165\n"
    "v_mfma_f32_16x16x32_bf16 v[168:171], a[4:7], a[212:215], v[168:171]\n"
    "v_max3_f32 v196, v196, v166, v167\n"
    "v_max_f32 v196, v196, v197\n"
    "v_mfma_f32_16x16x32_bf16 v[168:171], a[8:11], a[216:219], v[168:171]\n"
    "v_cmp_gt_f32 vcc, v196, v191\n"
    "v_mfma_f32_16x16x32_bf16 v[168:171], a[12:15], a[220:223], v[168:171]\n"
    "s_cbranch_vccnz A_RARE_EP_2\n"
    "A_BACK_EP_2:\n"
    "v_fma_f32 v160, v160, s69, v202\n"
    "v_fma_f32 v161, v161, s69, v202\n"
    "v_mfma_f32_16x16x32_bf16 v[172:175], a[16:19], a[208:211], 0\n"
    "v_exp_f32 v160, v160\n"
    "v_mfma_f32_16x16x32_bf16 v[172:175], a[20:23], a[212:215], v[172:175]\n"
    "v_exp_f32 v161, v161\n"
    "v_mfma_f32_16x16x32_bf16 v[172:175], a[24:27], a[216:219], v[172:175]\n"
    "v_fma_f32 v162, v162, s69, v202\n"
    "v_fma_f32 v163, v163, s69, v202\n"
    "v_mfma_f32_16x16x32_bf16 v[172:175], a[28:31], a[220:223], v[172:175]\n"
    "v_exp_f32 v162, v162\n"
    "v_mfma_f32_16x16x32_bf16 a[132:135], a[148:151], v[180:183], a[132:135]\n"
    "s_waitcnt vmcnt(12)\n"
    "s_barrier\n"
    "v_exp_f32 v163, v163\n"
    "v_mfma_f32_16x16x32_bf16 v[32:35], a[64:67], v[180:183], v[32:35]\n"
    "ds_read_b128 a[32:35], v214 offset:12288\n"
    "v_fma_f32 v164, v164, s69, v202\n"
    "v_fma_f32 v165, v165, s69, v202\n"
    "v_mfma_f32_16x16x32_bf16 v[36:39], a[68:71], v[180:183], v[36:39]\n"
    "ds_read_b128 a[36:39], v215 offset:12288\n"
    "v_cvt_pk_bf16_f32 v176, v160, v161\n"
    "v_cvt_pk_bf16_f32 v177, v162, v163\n"
    "v_mfma_f32_16x16x32_bf16 v[40:43], a[72:75], v[180:183], v[40:43]\n"
    "ds_read_b128 a[40:43], v216 offset:12288\n"
    "v_exp_f32 v164, v164\n"
    "v_mfma_f32_16x16x32_bf16 v[44:47], a[76:79], v[180:183], v[44:47]\n"
    "ds_read_b128 a[44:47], v217 offset:12288\n"
    "v_exp_f32 v165, v165\n"
    "v_mfma_f32_16x16x32_bf16 v[48:51], a[80:83], v[180:183], v[48:51]\n"
    "ds_read_b128 a[48:51], v214 offset:53248\n"
    "v_fma_f32 v166, v166, s69, v202\n"
    "v_fma_f32 v167, v167, s69, v202\n"
    "v_mfma_f32_16x16x32_bf16 v[52:55], a[84:87], v[180:183], v[52:55]\n"
    "ds_read_b128 a[52:55], v215 offset:53248\n"
    "v_exp_f32 v166, v166\n"
    "v_mfma_f32_16x16x32_bf16 v[56:59], a[88:91], v[180:183], v[56:59]\n"
    "ds_read_b128 a[56:59], v216 offset:53248\n"
    "v_exp_f32 v167, v167\n"
    "v_mfma_f32_16x16x32_bf16 v[60:63], a[92:95], v[180:183], v[60:63]\n"
    "ds_read_b128 a[60:63], v217 offset:53248\n"
    "v_cvt_pk_bf16_f32 v178, v164, v165\n"
    "v_cvt_pk_bf16_f32 v179, v166, v167\n"
    "v_mfma_f32_16x16x32_bf16 v[160:163], a[0:3], a[224:227], 0\n"
    "v_max3_f32 v196, v168, v169, v170\n"
    "v_max3_f32 v197, v171, v172, v173\n"
    "v_mfma_f32_16x16x32_bf16 v[160:163], a[4:7], a[228:231], v[160:163]\n"
    "s_add_u32 s79, s81, 0x2000\n"
    "v_max3_f32 v196, v196, v174, v175\n"
    "v_max_f32 v196, v196, v197\n"
    "v_mfma_f32_16x16x32_bf16 v[160:163], a[8:11], a[232:235], v[160:163]\n"
    "s_and_b32 s79, s79, 0x1e000\n"
    "v_cmp_gt_f32 vcc, v196, v192\n"
    "v_mfma_f32_16x16x32_bf16 v[160:163], a[12:15], a[236:239], v[160:163]\n"
    "s_add_u32 m0, s71, 0x3000\n"
    "s_waitcnt lgkmcnt(0)\n"
    "s_barrier\n"
    "s_cbranch_vccnz A_RARE_EP_3\n"
    "A_BACK_EP_3:\n"
    "v_fma_f32 v168, v168, s69, v203\n"
    "v_fma_f32 v169, v169, s69, v203\n"
    "v_mfma_f32_16x16x32_bf16 v[164:167], a[16:19], a[224:227], 0\n"
    "buffer_load_dwordx4 v226, s[40:43], s79 offen nt lds\n"
    "v_exp_f32 v168, v168\n"
    "v_mfma_f32_16x16x32_bf16 v[164:167], a[20:23], a[228:231], v[164:167]\n"
    "buffer_load_dwordx4 v227, s[40:43], s79 offen offset:1024 nt lds\n"
    "v_exp_f32 v169, v169\n"
    "v_mfma_f32_16x16x32_bf16 v[164:167], a[24:27], a[232:235], v[164:167]\n"
    "buffer_load_dwordx4 v228, s[40:43], s79 offen offset:2048 nt lds\n"
    "v_fma_f32 v170, v170, s69, v203\n"
    "v_fma_f32 v171, v171, s69, v203\n"
    "v_mfma_f32_16x16x32_bf16 v[164:167], a[28:31], a[236:239], v[164:167]\n"
    "buffer_load_dwordx4 v229, s[40:43], s79 offen offset:3072 nt lds\n"
    "v_exp_f32 v170, v170\n"
    "v_mfma_f32_16x16x32_bf16 a[136:139], a[148:151], v[176:179], a[136:139]\n"
    "v_exp_f32 v171, v171\n"
    "v_mfma_f32_16x16x32_bf16 v[64:67], a[64:67], v[176:179], v[64:67]\n"
    "v_fma_f32 v172, v172, s69, v203\n"
    "v_fma_f32 v173, v173, s69, v203\n"
    "v_mfma_f32_16x16x32_bf16 v[68:71], a[68:71], v[176:179], v[68:71]\n"
    "v_cvt_pk_bf16_f32 v180, v168, v169\n"
    "v_cvt_pk_bf16_f32 v181, v170, v171\n"
    "v_mfma_f32_16x16x32_bf16 v[72:75], a[72:75], v[176:179], v[72:75]\n"
    "v_exp_f32 v172, v172\n"
    "v_mfma_f32_16x16x32_bf16 v[76:79], a[76:79], v[176:179], v[76:79]\n"
    "v_exp_f32 v173, v173\n"
    "v_mfma_f32_16x16x32_bf16 v[80:83], a[80:83], v[176:179], v[80:83]\n"
    "v_fma_f32 v174, v174, s69, v203\n"
    "v_fma_f32 v175, v175, s69, v203\n"
    "v_mfma_f32_16x16x32_bf16 v[84:87], a[84:87], v[176:179], v[84:87]\n"
    "v_exp_f32 v174, v174\n"
    "v_mfma_f32_16x16x32_bf16 v[88:91], a[88:91], v[176:179], v[88:91]\n"
    "v_exp_f32 v175, v175\n"
    "v_mfma_f32_16x16x32_bf16 v[92:95], a[92:95], v[176:179], v[92:95]\n"
    "v_cvt_pk_bf16_f32 v182, v172, v173\n"
    "v_cvt_pk_bf16_f32 v183, v174, v175\n"
    "v_mfma_f32_16x16x32_bf16 v[168:171], a[32:35], a[160:163], 0\n"
    "v_max3_f32 v196, v160, v161, v162\n"
    "v_max3_f32 v197, v163, v164, v165\n"
    "v_mfma_f32_16x16x32_bf16 v[168:171], a[36:39], a[164:167], v[168:171]\n"
    "v_max3_f32 v196, v196, v166, v167\n"
    "v_max_f32 v196, v196, v197\n"
    "v_mfma_f32_16x16x32_bf16 v[168:171], a[40:43], a[168:171], v[168:171]\n"
    "v_cmp_gt_f32 vcc, v196, v193\n"
    "v_mfma_f32_16x16x32_bf16 v[168:171], a[44:47], a[172:175], v[168:171]\n"
    "s_cbranch_vccnz A_RARE_EP_4\n"
    "A_BACK_EP_4:\n"
    "v_fma_f32 v160, v160, s69, v204\n"
    "v_fma_f32 v161, v161, s69, v204\n"
    "v_mfma_f32_16x16x32_bf16 v[172:175], a[48:51], a[160:163], 0\n"
    "v_exp_f32 v160, v160\n"
    "v_mfma_f32_16x16x32_bf16 v[172:175], a[52:55], a[164:167], v[172:175]\n"
    "v_exp_f32 v161, v161\n"
    "v_mfma_f32_16x16x32_bf16 v[172:175], a[56:59], a[168:171], v[172:175]\n"
    "v_fma_f32 v162, v162, s69, v204\n"
    "v_fma_f32 v163, v163, s69, v204\n"
    "v_mfma_f32_16x16x32_bf16 v[172:175], a[60:63], a[172:175], v[172:175]\n"
    "v_exp_f32 v162, v162\n"
    "v_mfma_f32_16x16x32_bf16 a[140:143], a[148:151], v[180:183], a[140:143]\n"
    "v_exp_f32 v163, v163\n"
    "v_mfma_f32_16x16x32_bf16 v[96:99], a[64:67], v[180:183], v[96:99]\n"
    "v_fma_f32 v164, v164, s69, v204\n"
    "v_fma_f32 v165, v165, s69, v204\n"
    "v_mfma_f32_16x16x32_bf16 v[100:103], a[68:71], v[180:183], v[100:103]\n"
    "v_cvt_pk_bf16_f32 v176, v160, v161\n"
    "v_cvt_pk_bf16_f32 v177, v162, v163\n"
    "v_mfma_f32_16x16x32_bf16 v[104:107], a[72:75], v[180:183], v[104:107]\n"
    "v_exp_f32 v164, v164\n"
    "v_mfma_f32_16x16x32_bf16 v[108:111], a[76:79], v[180:183], v[108:111]\n"
    "v_exp_f32 v165, v165\n"
    "v_mfma_f32_16x16x32_bf16 v[112:115], a[80:83], v[180:183], v[112:115]\n"
    "v_fma_f32 v166, v166, s69, v204\n"
    "v_fma_f32 v167, v167, s69, v204\n"
    "v_mfma_f32_16x16x32_bf16 v[116:119], a[84:87], v[180:183], v[116:119]\n"
    "v_exp_f32 v166, v166\n"
    "v_mfma_f32_16x16x32_bf16 v[120:123], a[88:91], v[180:183], v[120:123]\n"
    "v_exp_f32 v167, v167\n"
    "v_mfma_f32_16x16x32_bf16 v[124:127], a[92:95], v[180:183], v[124:127]\n"
    "v_cvt_pk_bf16_f32 v178, v164, v165\n"
    "v_cvt_pk_bf16_f32 v179, v166, v167\n"
    "v_mfma_f32_16x16x32_bf16 v[160:163], a[32:35], a[176:179], 0\n"
    "s_waitcnt vmcnt(12)\n"
    "s_barrier\n"
    "v_max3_f32 v196, v168, v169, v170\n"
    "v_max3_f32 v197, v171, v172, v173\n"
    "v_mfma_f32_16x16x32_bf16 v[160:163], a[36:39], a[180:183], v[160:163]\n"
    "ds_read_b64_tr_b16 a[96:97], v218 offset:0\n"
    "v_max3_f32 v196, v196, v174, v175\n"
    "v_max_f32 v196, v196, v197\n"
    "v_mfma_f32_16x16x32_bf16 v[160:163], a[40:43], a[184:187], v[160:163]\n"
    "ds_read_b64_tr_b16 a[98:99], v218 offset:40960\n"
    "ds_read_b64_tr_b16 a[100:101], v219 offset:0\n"
    "v_cmp_gt_f32 vcc, v196, v189\n"
    "v_mfma_f32_16x16x32_bf16 v[160:163], a[44:47], a[188:191], v[160:163]\n"
    "s_cbranch_vccnz A_RARE_DP_0\n"
    "A_BACK_DP_0:\n"
    "v_fma_f32 v168, v168, s69, v200\n"
    "v_fma_f32 v169, v169, s69, v200\n"
    "v_mfma_f32_16x16x32_bf16 v[164:167], a[48:51], a[176:179], 0\n"
    "ds_read_b64_tr_b16 a[102:103], v219 offset:40960\n"
    "v_exp_f32 v168, v168\n"
    "v_mfma_f32_16x16x32_bf16 v[164:167], a[52:55], a[180:183], v[164:167]\n"
    "ds_read_b64_tr_b16 a[104:105], v220 offset:0\n"
    "v_exp_f32 v169, v169\n"
    "v_mfma_f32_16x16x32_bf16 v[164:167], a[56:59], a[184:187], v[164:167]\n"
    "ds_read_b64_tr_b16 a[106:107], v220 offset:40960\n"
    "v_fma_f32 v170, v170, s69, v200\n"
    "v_fma_f32 v171, v171, s69, v200\n"
    "v_mfma_f32_16x16x32_bf16 v[164:167], a[60:63], a[188:191], v[164:167]\n"
    "ds_read_b64_tr_b16 a[108:109], v221 offset:0\n"
    "v_exp_f32 v170, v170\n"
    "v_mfma_f32_16x16x32_bf16 a[144:147], a[148:151], v[176:179], a[144:147]\n"
    "ds_read_b64_tr_b16 a[110:111], v221 offset:40960\n"
    "v_exp_f32 v171, v171\n"
    "v_mfma_f32_16x16x32_bf16 v[128:131], a[64:67], v[176:179], v[128:131]\n"
    "ds_read_b64_tr_b16 a[112:113], v222 offset:0\n"
    "v_fma_f32 v172, v172, s69, v200\n"
    "v_fma_f32 v173, v173, s69, v200\n"
    "v_mfma_f32_16x16x32_bf16 v[132:135], a[68:71], v[176:179], v[132:135]\n"
    "ds_read_b64_tr_b16 a[114:115], v222 offset:40960\n"
    "v_cvt_pk_bf16_f32 v180, v168, v169\n"
    "v_cvt_pk_bf16_f32 v181, v170, v171\n"
    "v_mfma_f32_16x16x32_bf16 v[136:139], a[72:75], v[176:179], v[136:139]\n"
    "ds_read_b64_tr_b16 a[116:117], v223 offset:0\n"
    "v_exp_f32 v172, v172\n"
    "v_mfma_f32_16x16x32_bf16 v[140:143], a[76:79], v[176:179], v[140:143]\n"
    "ds_read_b64_tr_b16 a[118:119], v223 offset:40960\n"
    "v_exp_f32 v173, v173\n"
    "v_mfma_f32_16x16x32_bf16 v[144:147], a[80:83], v[176:179], v[144:147]\n"
    "ds_read_b64_tr_b16 a[120:121], v224 offset:0\n"
    "v_fma_f32 v174, v174, s69, v200\n"
    "v_fma_f32 v175, v175, s69, v200\n"
    "v_mfma_f32_16x16x32_bf16 v[148:151], a[84:87], v[176:179], v[148:151]\n"
    "ds_read_b64_tr_b16 a[122:123], v224 offset:40960\n"
    "v_exp_f32 v174, v174\n"
    "v_mfma_f32_16x16x32_bf16 v[152:155], a[88:91], v[176:179], v[152:155]\n"
    "ds_read_b64_tr_b16 a[124:125], v225 offset:0\n"
    "v_exp_f32 v175, v175\n"
    "v_mfma_f32_16x16x32_bf16 v[156:159], a[92:95], v[176:179], v[156:159]\n"
    "ds_read_b64_tr_b16 a[126:127], v225 offset:40960\n"
    "v_cvt_pk_bf16_f32 v182, v172, v173\n"
    "v_cvt_pk_bf16_f32 v183, v174, v175\n"
    "v_mfma_f32_16x16x32_bf16 v[168:171], a[32:35], a[192:195], 0\n"
    "v_max3_f32 v196, v160, v161, v162\n"
    "v_max3_f32 v197, v163, v164, v165\n"
    "v_mfma_f32_16x16x32_bf16 v[168:171], a[36:39], a[196:199], v[168:171]\n"
    "s_add_u32 s78, s81, 0x2000\n"
    "v_max3_f32 v196, v196, v166, v167\n"
    "v_max_f32 v196, v196, v197\n"
    "v_mfma_f32_16x16x32_bf16 v[168:171], a[40:43], a[200:203], v[168:171]\n"
    "s_and_b32 s78, s78, 0x1e000\n"
    "v_cmp_gt_f32 vcc, v196, v190\n"
    "v_mfma_f32_16x16x32_bf16 v[168:171], a[44:47], a[204:207], v[168:171]\n"
    "s_add_u32 m0, s71, 0\n"
    "s_waitcnt lgkmcnt(0)\n"
    "s_barrier\n"
    "s_cbranch_vccnz A_RARE_DP_1\n"
    "A_BACK_DP_1:\n"
    "v_fma_f32 v160, v160, s69, v201\n"
    "v_fma_f32 v161, v161, s69, v201\n"
    "v_mfma_f32_16x16x32_bf16 v[172:175], a[48:51], a[192:195], 0\n"
    "buffer_load_dwordx4 v226, s[44:47], s78 offen nt lds\n"
    "v_exp_f32 v160, v160\n"
    "v_mfma_f32_16x16x32_bf16 v[172:175], a[52:55], a[196:199], v[172:175]\n"
    "buffer_load_dwordx4 v227, s[44:47], s78 offen offset:1024 nt lds\n"
    "v_exp_f32 v161, v161\n"
    "v_mfma_f32_16x16x32_bf16 v[172:175], a[56:59], a[200:203], v[172:175]\n"
    "buffer_load_dwordx4 v228, s[44:47], s78 offen offset:2048 nt lds\n"
    "v_fma_f32 v162, v162, s69, v201\n"
    "v_fma_f32 v163, v163, s69, v201\n"
    "v_mfma_f32_16x16x32_bf16 v[172:175], a[60:63], a[204:207], v[172:175]\n"
    "buffer_load_dwordx4 v229, s[44:47], s78 offen offset:3072 nt lds\n"
    "v_exp_f32 v162, v162\n"
    "v_mfma_f32_16x16x32_bf16 a[128:131], a[148:151], v[180:183], a[128:131]\n"
    "v_exp_f32 v163, v163\n"
    "v_mfma_f32_16x16x32_bf16 v[0:3], a[96:99], v[180:183], v[0:3]\n"
    "v_fma_f32 v164, v164, s69, v201\n"
    "v_fma_f32 v165, v165, s69, v201\n"
    "v_mfma_f32_16x16x32_bf16 v[4:7], a[100:103], v[180:183], v[4:7]\n"
    "v_cvt_pk_bf16_f32 v176, v160, v161\n"
    "v_cvt_pk_bf16_f32 v177, v162, v163\n"
    "v_mfma_f32_16x16x32_bf16 v[8:11], a[104:107], v[180:183], v[8:11]\n"
    "v_exp_f32 v164, v164\n"
    "v_mfma_f32_16x16x32_bf16 v[12:15], a[108:111], v[180:183], v[12:15]\n"
    "v_exp_f32 v165, v165\n"
    "v_mfma_f32_16x16x32_bf16 v[16:19], a[112:115], v[180:183], v[16:19]\n"
    "v_fma_f32 v166, v166, s69, v201\n"
    "v_fma_f32 v167, v167, s69, v201\n"
    "v_mfma_f32_16x16x32_bf16 v[20:23], a[116:119], v[180:183], v[20:23]\n"
    "v_exp_f32 v166, v166\n"
    "v_mfma_f32_16x16x32_bf16 v[24:27], a[120:123], v[180:183], v[24:27]\n"
    "v_exp_f32 v167, v167\n"
    "v_mfma_f32_16x16x32_bf16 v[28:31], a[124:127], v[180:183], v[28:31]\n"
    "v_cvt_pk_bf16_f32 v178, v164, v165\n"
    "v_cvt_pk_bf16_f32 v179, v166, v167\n"
    "v_mfma_f32_16x16x32_bf16 v[160:163], a[32:35], a[208:211], 0\n"
    "v_max3_f32 v196, v168, v169, v170\n"
    "v_max3_f32 v197, v171, v172, v173\n"
    "v_mfma_f32_16x16x32_bf16 v[160:163], a[36:39], a[212:215], v[160:163]\n"
    "v_max3_f32 v196, v196, v174, v175\n"
    "v_max_f32 v196, v196, v197\n"
    "v_mfma_f32_16x16x32_bf16 v[160:163], a[40:43], a[216:219], v[160:163]\n"
    "v_cmp_gt_f32 vcc, v196, v191\n"
    "v_mfma_f32_16x16x32_bf16 v[160:163], a[44:47], a[220:223], v[160:163]\n"
    "s_cbranch_vccnz A_RARE_DP_2\n"
    "A_BACK_DP_2:\n"
    "v_fma_f32 v168, v168, s69, v202\n"
    "v_fma_f32 v169, v169, s69, v202\n"
    "v_mfma_f32_16x16x32_bf16 v[164:167], a[48:51], a[208:211], 0\n"
    "v_exp_f32 v168, v168\n"
    "v_mfma_f32_16x16x32_bf16 v[164:167], a[52:55], a[212:215], v[164:167]\n"
    "v_exp_f32 v169, v169\n"
    "v_mfma_f32_16x16x32_bf16 v[164:167], a[56:59], a[216:219], v[164:167]\n"
    "v_fma_f32 v170, v170, s69, v202\n"
    "v_fma_f32 v171, v171, s69, v202\n"
    "v_mfma_f32_16x16x32_bf16 v[164:167], a[60:63], a[220:223], v[164:167]\n"
    "v_exp_f32 v170, v170\n"
    "v_mfma_f32_16x16x32_bf16 a[132:135], a[148:151], v[176:179], a[132:135]\n"
    "s_waitcnt vmcnt(12)\n"
    "s_barrier\n"
    "v_exp_f32 v171, v171\n"
    "v_mfma_f32_16x16x32_bf16 v[32:35], a[96:99], v[176:179], v[32:35]\n"
    "ds_read_b128 a[0:3], v214 offset:8192\n"
    "v_fma_f32 v172, v172, s69, v202\n"
    "v_fma_f32 v173, v173, s69, v202\n"
    "v_mfma_f32_16x16x32_bf16 v[36:39], a[100:103], v[176:179], v[36:39]\n"
    "ds_read_b128 a[4:7], v215 offset:8192\n"
    "v_cvt_pk_bf16_f32 v180, v168, v169\n"
    "v_cvt_pk_bf16_f32 v181, v170, v171\n"
    "v_mfma_f32_16x16x32_bf16 v[40:43], a[104:107], v[176:179], v[40:43]\n"
    "ds_read_b128 a[8:11], v216 offset:8192\n"
    "v_exp_f32 v172, v172\n"
    "v_mfma_f32_16x16x32_bf16 v[44:47], a[108:111], v[176:179], v[44:47]\n"
    "ds_read_b128 a[12:15], v217 offset:8192\n"
    "v_exp_f32 v173, v173\n"
    "v_mfma_f32_16x16x32_bf16 v[48:51], a[112:115], v[176:179], v[48:51]\n"
    "ds_read_b128 a[16:19], v214 offset:49152\n"
    "v_fma_f32 v174, v174, s69, v202\n"
    "v_fma_f32 v175, v175, s69, v202\n"
    "v_mfma_f32_16x16x32_bf16 v[52:55], a[116:119], v[176:179], v[52:55]\n"
    "ds_read_b128 a[20:23], v215 offset:49152\n"
    "v_exp_f32 v174, v174\n"
    "v_mfma_f32_16x16x32_bf16 v[56:59], a[120:123], v[176:179], v[56:59]\n"
    "ds_read_b128 a[24:27], v216 offset:49152\n"
    "v_exp_f32 v175, v175\n"
    "v_mfma_f32_16x16x32_bf16 v[60:63], a[124:127], v[176:179], v[60:63]\n"
    "ds_read_b128 a[28:31], v217 offset:49152\n"
    "v_cvt_pk_bf16_f32 v182, v172, v173\n"
    "v_cvt_pk_bf16_f32 v183, v174, v175\n"
    "v_mfma_f32_16x16x32_bf16 v[168:171], a[32:35], a[224:227], 0\n"
    "v_max3_f32 v196, v160, v161, v162\n"
    "v_max3_f32 v197, v163, v164, v165\n"
    "v_mfma_f32_16x16x32_bf16 v[168:171], a[36:39], a[228:231], v[168:171]\n"
    "v_max3_f32 v196, v196, v166, v167\n"
    "v_max_f32 v196, v196, v197\n"
    "v_mfma_f32_16x16x32_bf16 v[168:171], a[40:43], a[232:235], v[168:171]\n"
    "v_cmp_gt_f32 vcc, v196, v192\n"
    "v_mfma_f32_16x16x32_bf16 v[168:171], a[44:47], a[236:239], v[168:171]\n"
    "s_add_u32 m0, s71, 0x2000\n"
    "s_waitcnt lgkmcnt(0)\n"
    "s_barrier\n"
    "s_cbranch_vccnz A_RARE_DP_3\n"
    "A_BACK_DP_3:\n"
    "v_fma_f32 v160, v160, s69, v203\n"
    "v_fma_f32 v161, v161, s69, v203\n"
    "v_mfma_f32_16x16x32_bf16 v[172:175], a[48:51], a[224:227], 0\n"
    "buffer_load_dwordx4 v230, s[56:59], s82 offen nt lds\n"
    "v_exp_f32 v160, v160\n"
    "v_mfma_f32_16x16x32_bf16 v[172:175], a[52:55], a[228:231], v[172:175]\n"
    "buffer_load_dwordx4 v231, s[56:59], s82 offen offset:1024 nt lds\n"
    "v_exp_f32 v161, v161\n"
    "v_mfma_f32_16x16x32_bf16 v[172:175], a[56:59], a[232:235], v[172:175]\n"
    "buffer_load_dwordx4 v232, s[56:59], s82 offen offset:2048 nt lds\n"
    "v_fma_f32 v162, v162, s69, v203\n"
    "v_fma_f32 v163, v163, s69, v203\n"
    "v_mfma_f32_16x16x32_bf16 v[172:175], a[60:63], a[236:239], v[172:175]\n"
    "buffer_load_dwordx4 v233, s[56:59], s82 offen offset:3072 nt lds\n"
    "v_exp_f32 v162, v162\n"
    "v_mfma_f32_16x16x32_bf16 a[136:139], a[148:151], v[180:183], a[136:139]\n"
    "v_exp_f32 v163, v163\n"
    "v_mfma_f32_16x16x32_bf16 v[64:67], a[96:99], v[180:183], v[64:67]\n"
    "v_fma_f32 v164, v164, s69, v203\n"
    "v_fma_f32 v165, v165, s69, v203\n"
    "v_mfma_f32_16x16x32_bf16 v[68:71], a[100:103], v[180:183], v[68:71]\n"
    "v_cvt_pk_bf16_f32 v176, v160, v161\n"
    "v_cvt_pk_bf16_f32 v177, v162, v163\n"
    "v_mfma_f32_16x16x32_bf16 v[72:75], a[104:107], v[180:183], v[72:75]\n"
    "v_exp_f32 v164, v164\n"
    "v_mfma_f32_16x16x32_bf16 v[76:79], a[108:111], v[180:183], v[76:79]\n"
    "v_exp_f32 v165, v165\n"
    "A_TH_S5:\n"
    "s_cbranch_execz A_TH_RET\n"
    "v_mfma_f32_16x16x32_bf16 v[80:83], a[112:115], v[180:183], v[80:83]\n"
    "v_fma_f32 v166, v166, s69, v203\n"
    "v_fma_f32 v167, v167, s69, v203\n"
    "v_mfma_f32_16x16x32_bf16 v[84:87], a[116:119], v[180:183], v[84:87]\n"
    "v_exp_f32 v166, v166\n"
    "v_mfma_f32_16x16x32_bf16 v[88:91], a[120:123], v[180:183], v[88:91]\n"
    "v_exp_f32 v167, v167\n"
    "v_mfma_f32_16x16x32_bf16 v[92:95], a[124:127], v[180:183], v[92:95]\n"
    "v_cvt_pk_bf16_f32 v178, v164, v165\n"
    "v_cvt_pk_bf16_f32 v179, v166, v167\n"
    "v_mfma_f32_16x16x32_bf16 v[160:163], a[0:3], a[160:163], 0\n"
    "v_max3_f32 v196, v168, v169, v170\n"
    "v_max3_f32 v197, v171, v172, v173\n"
    "v_mfma_f32_16x16x32_bf16 v[160:163], a[4:7], a[164:167], v[160:163]\n"
    "v_max3_f32 v196, v196, v174, v175\n"
    "v_max_f32 v196, v196, v197\n"
    "v_mfma_f32_16x16x32_bf16 v[160:163], a[8:11], a[168:171], v[160:163]\n"
    "v_cmp_gt_f32 vcc, v196, v193\n"
    "v_mfma_f32_16x16x32_bf16 v[160:163], a[12:15], a[172:175], v[160:163]\n"
    "s_cbranch_vccnz A_RARE_DP_4\n"
    "A_BACK_DP_4:\n"
    "v_fma_f32 v168, v168, s69, v204\n"
    "v_fma_f32 v169, v169, s69, v204\n"
    "v_mfma_f32_16x16x32_bf16 v[164:167], a[16:19], a[160:163], 0\n"
    "v_exp_f32 v168, v168\n"
    "v_mfma_f32_16x16x32_bf16 v[164:167], a[20:23], a[164:167], v[164:167]\n"
    "v_exp_f32 v169, v169\n"
    "v_mfma_f32_16x16x32_bf16 v[164:167], a[24:27], a[168:171], v[164:167]\n"
    "v_fma_f32 v170, v170, s69, v204\n"
    "v_fma_f32 v171, v171, s69, v204\n"
    "v_mfma_f32_16x16x32_bf16 v[164:167], a[28:31], a[172:175], v[164:167]\n"
    "v_exp_f32 v170, v170\n"
    "v_mfma_f32_16x16x32_bf16 a[140:143], a[148:151], v[176:179], a[140:143]\n"
    "v_exp_f32 v171, v171\n"
    "v_mfma_f32_16x16x32_bf16 v[96:99], a[96:99], v[176:179], v[96:99]\n"
    "v_fma_f32 v172, v172, s69, v204\n"
    "v_fma_f32 v173, v173, s69, v204\n"
    "v_mfma_f32_16x16x32_bf16 v[100:103], a[100:103], v[176:179], v[100:103]\n"
    "v_cvt_pk_bf16_f32 v180, v168, v169\n"
    "v_cvt_pk_bf16_f32 v181, v170, v171\n"
    "v_mfma_f32_16x16x32_bf16 v[104:107], a[104:107], v[176:179], v[104:107]\n"
    "v_exp_f32 v172, v172\n"
    "v_mfma_f32_16x16x32_bf16 v[108:111], a[108:111], v[176:179], v[108:111]\n"
    "v_exp_f32 v173, v173\n"
    "v_mfma_f32_16x16x32_bf16 v[112:115], a[112:115], v[176:179], v[112:115]\n"
    "v_fma_f32 v174, v174, s69, v204\n"
    "v_fma_f32 v175, v175, s69, v204\n"
    "v_mfma_f32_16x16x32_bf16 v[116:119], a[116:119], v[176:179], v[116:119]\n"
    "v_exp_f32 v174, v174\n"
    "v_mfma_f32_16x16x32_bf16 v[120:123], a[120:123], v[176:179], v[120:123]\n"
    "v_exp_f32 v175, v175\n"
    "v_mfma_f32_16x16x32_bf16 v[124:127], a[124:127], v[176:179], v[124:127]\n"
    "v_cvt_pk_bf16_f32 v182, v172, v173\n"
    "v_cvt_pk_bf16_f32 v183, v174, v175\n"
    "s_add_u32 s81, s81, 0x4000\n"
    "s_and_b32 s81, s81, 0x1e000\n"
    "s_add_u32 s80, s80, 1\n"
    "s_cmp_lt_u32 s80, 7\n"
    "s_cbranch_scc1 A_ATT_LOOP\n"
    "A_TT_BEGIN:\n"
    "v_add_u32 v252, 0x9e3779b9, v252\n"
    "s_sub_u32 s61, 7, s35\n"
    "s_lshl_b32 s76, 1, s61\n"
    "s_andn2_b32 s76, 0xffff, s76\n"
    "s_mov_b32 s65, 0\n"
    "A_HS_CHK:\n"
    "v_cmp_ne_u32 vcc, v252, v253\n"
    "s_and_b32 s77, vcc_lo, s76\n"
    "s_cbranch_scc1 A_HS_SLOW\n"
    "A_HS_OK:\n"
    "v_mfma_f32_16x16x32_bf16 v[168:171], a[0:3], a[176:179], 0\n"
    "s_waitcnt vmcnt(12)\n"
    "s_barrier\n"
    "v_max3_f32 v196, v160, v161, v162\n"
    "v_max3_f32 v197, v163, v164, v165\n"
    "v_mfma_f32_16x16x32_bf16 v[168:171], a[4:7], a[180:183], v[168:171]\n"
    "ds_read_b64_tr_b16 a[64:65], v218 offset:4096\n"
    "v_max3_f32 v196, v196, v166, v167\n"
    "v_max_f32 v196, v196, v197\n"
    "v_mfma_f32_16x16x32_bf16 v[168:171], a[8:11], a[184:187], v[168:171]\n"
    "ds_read_b64_tr_b16 a[66:67], v218 offset:45056\n"
    "ds_read_b64_tr_b16 a[68:69], v219 offset:4096\n"
    "v_cmp_gt_f32 vcc, v196, v189\n"
    "v_mfma_f32_16x16x32_bf16 v[168:171], a[12:15], a[188:191], v[168:171]\n"
    "s_cbranch_vccnz A_RARE_F_0\n"
    "A_BACK_F_0:\n"
    "v_fma_f32 v160, v160, s69, v200\n"
    "v_fma_f32 v161, v161, s69, v200\n"
    "v_mfma_f32_16x16x32_bf16 v[172:175], a[16:19], a[176:179], 0\n"
    "ds_read_b64_tr_b16 a[70:71], v219 offset:45056\n"
    "v_exp_f32 v160, v160\n"
    "v_mfma_f32_16x16x32_bf16 v[172:175], a[20:23], a[180:183], v[172:175]\n"
    "ds_read_b64_tr_b16 a[72:73], v220 offset:4096\n"
    "v_exp_f32 v161, v161\n"
    "v_mfma_f32_16x16x32_bf16 v[172:175], a[24:27], a[184:187], v[172:175]\n"
    "s_add_u32 s85, s74, s86\n"
    "ds_read_b64_tr_b16 a[74:75], v220 offset:45056\n"
    "v_fma_f32 v162, v162, s69, v200\n"
    "v_fma_f32 v163, v163, s69, v200\n"
    "v_mfma_f32_16x16x32_bf16 v[172:175], a[28:31], a[188:191], v[172:175]\n"
    "s_add_u32 s87, s74, 0x80\n"
    "s_cmp_eq_u32 s87, 0x480\n"
    "s_cselect_b32 s87, 0, s87\n"
    "ds_read_b64_tr_b16 a[76:77], v221 offset:4096\n"
    "v_exp_f32 v162, v162\n"
    "v_mfma_f32_16x16x32_bf16 a[144:147], a[148:151], v[180:183], a[144:147]\n"
    "ds_read_b64_tr_b16 a[78:79], v221 offset:45056\n"
    "v_exp_f32 v163, v163\n"
    "v_mfma_f32_16x16x32_bf16 v[128:131], a[96:99], v[180:183], v[128:131]\n"
    "ds_read_b64_tr_b16 a[80:81], v222 offset:4096\n"
    "v_fma_f32 v164, v164, s69, v200\n"
    "v_fma_f32 v165, v165, s69, v200\n"
    "v_mfma_f32_16x16x32_bf16 v[132:135], a[100:103], v[180:183], v[132:135]\n"
    "ds_read_b64_tr_b16 a[82:83], v222 offset:45056\n"
    "v_cvt_pk_bf16_f32 v176, v160, v161\n"
    "v_cvt_pk_bf16_f32 v177, v162, v163\n"
    "v_mfma_f32_16x16x32_bf16 v[136:139], a[104:107], v[180:183], v[136:139]\n"
    "ds_read_b64_tr_b16 a[84:85], v223 offset:4096\n"
    "v_exp_f32 v164, v164\n"
    "v_mfma_f32_16x16x32_bf16 v[140:143], a[108:111], v[180:183], v[140:143]\n"
    "ds_read_b64_tr_b16 a[86:87], v223 offset:45056\n"
    "v_exp_f32 v165, v165\n"
    "v_mfma_f32_16x16x32_bf16 v[144:147], a[112:115], v[180:183], v[144:147]\n"
    "ds_read_b64_tr_b16 a[88:89], v224 offset:4096\n"
    "v_fma_f32 v166, v166, s69, v200\n"
    "v_fma_f32 v167, v167, s69, v200\n"
    "v_mfma_f32_16x16x32_bf16 v[148:151], a[116:119], v[180:183], v[148:151]\n"
    "ds_read_b64_tr_b16 a[90:91], v224 offset:45056\n"
    "v_exp_f32 v166, v166\n"
    "v_mfma_f32_16x16x32_bf16 v[152:155], a[120:123], v[180:183], v[152:155]\n"
    "ds_read_b64_tr_b16 a[92:93], v225 offset:4096\n"
    "v_exp_f32 v167, v167\n"
    "v_mfma_f32_16x16x32_bf16 v[156:159], a[124:127], v[180:183], v[156:159]\n"
    "ds_read_b64_tr_b16 a[94:95], v225 offset:45056\n"
    "v_cvt_pk_bf16_f32 v178, v164, v165\n"
    "v_cvt_pk_bf16_f32 v179, v166, v167\n"
    "v_mfma_f32_16x16x32_bf16 v[160:163], a[0:3], a[192:195], 0\n"
    "v_max3_f32 v196, v168, v169, v170\n"
    "v_max3_f32 v197, v171, v172, v173\n"
    "v_mfma_f32_16x16x32_bf16 v[160:163], a[4:7], a[196:199], v[160:163]\n"
    "v_max3_f32 v196, v196, v174, v175\n"
    "v_max_f32 v196, v196, v197\n"
    "v_mfma_f32_16x16x32_bf16 v[160:163], a[8:11], a[200:203], v[160:163]\n"
    "v_cmp_gt_f32 vcc, v196, v190\n"
    "v_mfma_f32_16x16x32_bf16 v[160:163], a[12:15], a[204:207], v[160:163]\n"
    "s_waitcnt lgkmcnt(0)\n"
    "s_barrier\n"
    "s_cbranch_vccnz A_RARE_F_1\n"
    "A_BACK_F_1:\n"
    "v_fma_f32 v168, v168, s69, v201\n"
    "v_fma_f32 v169, v169, s69, v201\n"
    "v_mfma_f32_16x16x32_bf16 v[164:167], a[16:19], a[192:195], 0\n"
    "v_exp_f32 v168, v168\n"
    "v_mfma_f32_16x16x32_bf16 v[164:167], a[20:23], a[196:199], v[164:167]\n"
    "v_exp_f32 v169, v169\n"
    "v_mfma_f32_16x16x32_bf16 v[164:167], a[24:27], a[200:203], v[164:167]\n"
    "v_fma_f32 v170, v170, s69, v201\n"
    "v_fma_f32 v171, v171, s69, v201\n"
    "v_mfma_f32_16x16x32_bf16 v[164:167], a[28:31], a[204:207], v[164:167]\n"
    "s_add_u32 s78, s85, 0x12000\n"
    "v_exp_f32 v170, v170\n"
    "v_mfma_f32_16x16x32_bf16 a[128:131], a[148:151], v[176:179], a[128:131]\n"
    "s_add_u32 m0, s71, 0x4000\n"
    "v_exp_f32 v171, v171\n"
    "v_mfma_f32_16x16x32_bf16 v[0:3], a[64:67], v[176:179], v[0:3]\n"
    "buffer_load_dwordx4 v234, s[52:55], s78 offen lds\n"
    "v_fma_f32 v172, v172, s69, v201\n"
    "v_fma_f32 v173, v173, s69, v201\n"
    "v_mfma_f32_16x16x32_bf16 v[4:7], a[68:71], v[176:179], v[4:7]\n"
    "buffer_load_dwordx4 v235, s[52:55], s78 offen offset:1024 lds\n"
    "v_cvt_pk_bf16_f32 v180, v168, v169\n"
    "v_cvt_pk_bf16_f32 v181, v170, v171\n"
    "v_mfma_f32_16x16x32_bf16 v[8:11], a[72:75], v[176:179], v[8:11]\n"
    "buffer_load_dwordx4 v236, s[52:55], s78 offen offset:2048 lds\n"
    "v_exp_f32 v172, v172\n"
    "v_mfma_f32_16x16x32_bf16 v[12:15], a[76:79], v[176:179], v[12:15]\n"
    "v_exp_f32 v173, v173\n"
    "v_mfma_f32_16x16x32_bf16 v[16:19], a[80:83], v[176:179], v[16:19]\n"
    "v_fma_f32 v174, v174, s69, v201\n"
    "v_fma_f32 v175, v175, s69, v201\n"
    "v_mfma_f32_16x16x32_bf16 v[20:23], a[84:87], v[176:179], v[20:23]\n"
    "v_exp_f32 v174, v174\n"
    "v_mfma_f32_16x16x32_bf16 v[24:27], a[88:91], v[176:179], v[24:27]\n"
    "v_exp_f32 v175, v175\n"
    "v_mfma_f32_16x16x32_bf16 v[28:31], a[92:95], v[176:179], v[28:31]\n"
    "v_cvt_pk_bf16_f32 v182, v172, v173\n"
    "v_cvt_pk_bf16_f32 v183, v174, v175\n"
    "v_mfma_f32_16x16x32_bf16 v[168:171], a[0:3], a[208:211], 0\n"
    "v_max3_f32 v196, v160, v161, v162\n"
    "v_max3_f32 v197, v163, v164, v165\n"
    "v_mfma_f32_16x16x32_bf16 v[168:171], a[4:7], a[212:215], v[168:171]\n"
    "v_max3_f32 v196, v196, v166, v167\n"
    "v_max_f32 v196, v196, v197\n"
    "v_mfma_f32_16x16x32_bf16 v[168:171], a[8:11], a[216:219], v[168:171]\n"
    "buffer_load_dwordx4 v237, s[52:55], s78 offen offset:3072 lds\n"
    "v_cmp_gt_f32 vcc, v196, v191\n"
    "v_mfma_f32_16x16x32_bf16 v[168:171], a[12:15], a[220:223], v[168:171]\n"
    "s_cbranch_vccnz A_RARE_F_2\n"
    "A_BACK_F_2:\n"
    "v_fma_f32 v160, v160, s69, v202\n"
    "v_fma_f32 v161, v161, s69, v202\n"
    "v_mfma_f32_16x16x32_bf16 v[172:175], a[16:19], a[208:211], 0\n"
    "v_exp_f32 v160, v160\n"
    "v_mfma_f32_16x16x32_bf16 v[172:175], a[20:23], a[212:215], v[172:175]\n"
    "v_exp_f32 v161, v161\n"
    "v_mfma_f32_16x16x32_bf16 v[172:175], a[24:27], a[216:219], v[172:175]\n"
    "v_fma_f32 v162, v162, s69, v202\n"
    "v_fma_f32 v163, v163, s69, v202\n"
    "v_mfma_f32_16x16x32_bf16 v[172:175], a[28:31], a[220:223], v[172:175]\n"
    "v_exp_f32 v162, v162\n"
    "v_mfma_f32_16x16x32_bf16 a[132:135], a[148:151], v[180:183], a[132:135]\n"
    "s_waitcnt vmcnt(12)\n"
    "s_barrier\n"
    "v_exp_f32 v163, v163\n"
    "v_mfma_f32_16x16x32_bf16 v[32:35], a[64:67], v[180:183], v[32:35]\n"
    "ds_read_b128 a[32:35], v214 offset:12288\n"
    "v_fma_f32 v164, v164, s69, v202\n"
    "v_fma_f32 v165, v165, s69, v202\n"
    "v_mfma_f32_16x16x32_bf16 v[36:39], a[68:71], v[180:183], v[36:39]\n"
    "ds_read_b128 a[36:39], v215 offset:12288\n"
    "v_cvt_pk_bf16_f32 v176, v160, v161\n"
    "v_cvt_pk_bf16_f32 v177, v162, v163\n"
    "v_mfma_f32_16x16x32_bf16 v[40:43], a[72:75], v[180:183], v[40:43]\n"
    "ds_read_b128 a[40:43], v216 offset:12288\n"
    "v_exp_f32 v164, v164\n"
    "v_mfma_f32_16x16x32_bf16 v[44:47], a[76:79], v[180:183], v[44:47]\n"
    "ds_read_b128 a[44:47], v217 offset:12288\n"
    "v_exp_f32 v165, v165\n"
    "v_mfma_f32_16x16x32_bf16 v[48:51], a[80:83], v[180:183], v[48:51]\n"
    "ds_read_b128 a[48:51], v214 offset:53248\n"
    "v_fma_f32 v166, v166, s69, v202\n"
    "v_fma_f32 v167, v167, s69, v202\n"
    "v_mfma_f32_16x16x32_bf16 v[52:55], a[84:87], v[180:183], v[52:55]\n"
    "ds_read_b128 a[52:55], v215 offset:53248\n"
    "v_exp_f32 v166, v166\n"
    "v_mfma_f32_16x16x32_bf16 v[56:59], a[88:91], v[180:183], v[56:59]\n"
    "ds_read_b128 a[56:59], v216 offset:53248\n"
    "v_exp_f32 v167, v167\n"
    "v_mfma_f32_16x16x32_bf16 v[60:63], a[92:95], v[180:183], v[60:63]\n"
    "ds_read_b128 a[60:63], v217 offset:53248\n"
    "v_cvt_pk_bf16_f32 v178, v164, v165\n"
    "v_cvt_pk_bf16_f32 v179, v166, v167\n"
    "v_mfma_f32_16x16x32_bf16 v[160:163], a[0:3], a[224:227], 0\n"
    "v_max3_f32 v196, v168, v169, v170\n"
    "v_max3_f32 v197, v171, v172, v173\n"
    "v_mfma_f32_16x16x32_bf16 v[160:163], a[4:7], a[228:231], v[160:163]\n"
    "v_max3_f32 v196, v196, v174, v175\n"
    "v_max_f32 v196, v196, v197\n"
    "v_mfma_f32_16x16x32_bf16 v[160:163], a[8:11], a[232:235], v[160:163]\n"
    "s_add_u32 s79, s85, 0x9000\n"
    "v_cmp_gt_f32 vcc, v196, v192\n"
    "v_mfma_f32_16x16x32_bf16 v[160:163], a[12:15], a[236:239], v[160:163]\n"
    "s_add_u32 m0, s71, 0x3000\n"
    "s_waitcnt lgkmcnt(0)\n"
    "s_barrier\n"
    "s_cbranch_vccnz A_RARE_F_3\n"
    "A_BACK_F_3:\n"
    "v_fma_f32 v168, v168, s69, v203\n"
    "v_fma_f32 v169, v169, s69, v203\n"
    "v_mfma_f32_16x16x32_bf16 v[164:167], a[16:19], a[224:227], 0\n"
    "buffer_load_dwordx4 v234, s[52:55], s79 offen lds\n"
    "v_exp_f32 v168, v168\n"
    "v_mfma_f32_16x16x32_bf16 v[164:167], a[20:23], a[228:231], v[164:167]\n"
    "buffer_load_dwordx4 v235, s[52:55], s79 offen offset:1024 lds\n"
    "v_exp_f32 v169, v169\n"
    "v_mfma_f32_16x16x32_bf16 v[164:167], a[24:27], a[232:235], v[164:167]\n"
    "buffer_load_dwordx4 v236, s[52:55], s79 offen offset:2048 lds\n"
    "v_fma_f32 v170, v170, s69, v203\n"
    "v_fma_f32 v171, v171, s69, v203\n"
    "v_mfma_f32_16x16x32_bf16 v[164:167], a[28:31], a[236:239], v[164:167]\n"
    "buffer_load_dwordx4 v237, s[52:55], s79 offen offset:3072 lds\n"
    "v_exp_f32 v170, v170\n"
    "v_mfma_f32_16x16x32_bf16 a[136:139], a[148:151], v[176:179], a[136:139]\n"
    "v_exp_f32 v171, v171\n"
    "v_mfma_f32_16x16x32_bf16 v[64:67], a[64:67], v[176:179], v[64:67]\n"
    "v_fma_f32 v172, v172, s69, v203\n"
    "v_fma_f32 v173, v173, s69, v203\n"
    "v_mfma_f32_16x16x32_bf16 v[68:71], a[68:71], v[176:179], v[68:71]\n"
    "v_cvt_pk_bf16_f32 v180, v168, v169\n"
    "v_cvt_pk_bf16_f32 v181, v170, v171\n"
    "v_mfma_f32_16x16x32_bf16 v[72:75], a[72:75], v[176:179], v[72:75]\n"
    "v_exp_f32 v172, v172\n"
    "v_mfma_f32_16x16x32_bf16 v[76:79], a[76:79], v[176:179], v[76:79]\n"
    "s_add_u32 s78, s85, 0x1b000\n"
    "v_exp_f32 v173, v173\n"
    "v_mfma_f32_16x16x32_bf16 v[80:83], a[80:83], v[176:179], v[80:83]\n"
    "s_add_u32 m0, s71, 0x5000\n"
    "v_fma_f32 v174, v174, s69, v203\n"
    "v_fma_f32 v175, v175, s69, v203\n"
    "v_mfma_f32_16x16x32_bf16 v[84:87], a[84:87], v[176:179], v[84:87]\n"
    "buffer_load_dwordx4 v234, s[52:55], s78 offen lds\n"
    "v_exp_f32 v174, v174\n"
    "v_mfma_f32_16x16x32_bf16 v[88:91], a[88:91], v[176:179], v[88:91]\n"
    "buffer_load_dwordx4 v235, s[52:55], s78 offen offset:1024 lds\n"
    "v_exp_f32 v175, v175\n"
    "v_mfma_f32_16x16x32_bf16 v[92:95], a[92:95], v[176:179], v[92:95]\n"
    "buffer_load_dwordx4 v236, s[52:55], s78 offen offset:2048 lds\n"
    "v_cvt_pk_bf16_f32 v182, v172, v173\n"
    "v_cvt_pk_bf16_f32 v183, v174, v175\n"
    "v_mfma_f32_16x16x32_bf16 v[168:171], a[32:35], a[160:163], 0\n"
    "v_max3_f32 v196, v160, v161, v162\n"
    "v_max3_f32 v197, v163, v164, v165\n"
    "v_mfma_f32_16x16x32_bf16 v[168:171], a[36:39], a[164:167], v[168:171]\n"
    "v_max3_f32 v196, v196, v166, v167\n"
    "v_max_f32 v196, v196, v197\n"
    "v_mfma_f32_16x16x32_bf16 v[168:171], a[40:43], a[168:171], v[168:171]\n"
    "buffer_load_dwordx4 v237, s[52:55], s78 offen offset:3072 lds\n"
    "v_cmp_gt_f32 vcc, v196, v193\n"
    "v_mfma_f32_16x16x32_bf16 v[168:171], a[44:47], a[172:175], v[168:171]\n"
    "s_cbranch_vccnz A_RARE_F_4\n"
    "A_BACK_F_4:\n"
    "v_fma_f32 v160, v160, s69, v204\n"
    "v_fma_f32 v161, v161, s69, v204\n"
    "v_mfma_f32_16x16x32_bf16 v[172:175], a[48:51], a[160:163], 0\n"
    "v_exp_f32 v160, v160\n"
    "v_mfma_f32_16x16x32_bf16 v[172:175], a[52:55], a[164:167], v[172:175]\n"
    "s_add_u32 s79, s87, s86\n"
    "v_exp_f32 v161, v161\n"
    "v_mfma_f32_16x16x32_bf16 v[172:175], a[56:59], a[168:171], v[172:175]\n"
    "s_add_u32 m0, s71, 0x1000\n"
    "v_fma_f32 v162, v162, s69, v204\n"
    "v_fma_f32 v163, v163, s69, v204\n"
    "v_mfma_f32_16x16x32_bf16 v[172:175], a[60:63], a[172:175], v[172:175]\n"
    "buffer_load_dwordx4 v234, s[52:55], s79 offen lds\n"
    "v_exp_f32 v162, v162\n"
    "v_mfma_f32_16x16x32_bf16 a[140:143], a[148:151], v[180:183], a[140:143]\n"
    "buffer_load_dwordx4 v235, s[52:55], s79 offen offset:1024 lds\n"
    "v_exp_f32 v163, v163\n"
    "v_mfma_f32_16x16x32_bf16 v[96:99], a[64:67], v[180:183], v[96:99]\n"
    "buffer_load_dwordx4 v236, s[52:55], s79 offen offset:2048 lds\n"
    "v_fma_f32 v164, v164, s69, v204\n"
    "v_fma_f32 v165, v165, s69, v204\n"
    "v_mfma_f32_16x16x32_bf16 v[100:103], a[68:71], v[180:183], v[100:103]\n"
    "buffer_load_dwordx4 v237, s[52:55], s79 offen offset:3072 lds\n"
    "v_cvt_pk_bf16_f32 v176, v160, v161\n"
    "v_cvt_pk_bf16_f32 v177, v162, v163\n"
    "v_mfma_f32_16x16x32_bf16 v[104:107], a[72:75], v[180:183], v[104:107]\n"
    "v_exp_f32 v164, v164\n"
    "v_mfma_f32_16x16x32_bf16 v[108:111], a[76:79], v[180:183], v[108:111]\n"
    "v_exp_f32 v165, v165\n"
    "v_mfma_f32_16x16x32_bf16 v[112:115], a[80:83], v[180:183], v[112:115]\n"
    "v_fma_f32 v166, v166, s69, v204\n"
    "v_fma_f32 v167, v167, s69, v204\n"
    "v_mfma_f32_16x16x32_bf16 v[116:119], a[84:87], v[180:183], v[116:119]\n"
    "v_exp_f32 v166, v166\n"
    "v_mfma_f32_16x16x32_bf16 v[120:123], a[88:91], v[180:183], v[120:123]\n"
    "v_exp_f32 v167, v167\n"
    "v_mfma_f32_16x16x32_bf16 v[124:127], a[92:95], v[180:183], v[124:127]\n"
    "v_cvt_pk_bf16_f32 v178, v164, v165\n"
    "v_cvt_pk_bf16_f32 v179, v166, v167\n"
    "v_mfma_f32_16x16x32_bf16 v[160:163], a[32:35], a[176:179], 0\n"
    "v_max3_f32 v196, v168, v169, v170\n"
    "v_max3_f32 v197, v171, v172, v173\n"
    "v_mfma_f32_16x16x32_bf16 v[160:163], a[36:39], a[180:183], v[160:163]\n"
    "v_max3_f32 v196, v196, v174, v175\n"
    "v_max_f32 v196, v196, v197\n"
    "v_mfma_f32_16x16x32_bf16 v[160:163], a[40:43], a[184:187], v[160:163]\n"
    "v_cmp_gt_f32 vcc, v196, v189\n"
    "v_mfma_f32_16x16x32_bf16 v[160:163], a[44:47], a[188:191], v[160:163]\n"
    "s_cbranch_vccnz A_RARE_G_0\n"
    "A_BACK_G_0:\n"
    "v_fma_f32 v168, v168, s69, v200\n"
    "v_fma_f32 v169, v169, s69, v200\n"
    "v_mfma_f32_16x16x32_bf16 v[164:167], a[48:51], a[176:179], 0\n"
    "v_exp_f32 v168, v168\n"
    "v_mfma_f32_16x16x32_bf16 v[164:167], a[52:55], a[180:183], v[164:167]\n"
    "v_exp_f32 v169, v169\n"
    "v_mfma_f32_16x16x32_bf16 v[164:167], a[56:59], a[184:187], v[164:167]\n"
    "v_fma_f32 v170, v170, s69, v200\n"
    "v_fma_f32 v171, v171, s69, v200\n"
    "v_mfma_f32_16x16x32_bf16 v[164:167], a[60:63], a[188:191], v[164:167]\n"
    "v_exp_f32 v170, v170\n"
    "v_mfma_f32_16x16x32_bf16 a[144:147], a[148:151], v[176:179], a[144:147]\n"
    "v_exp_f32 v171, v171\n"
    "v_mfma_f32_16x16x32_bf16 v[128:131], a[64:67], v[176:179], v[128:131]\n"
    "v_fma_f32 v172, v172, s69, v200\n"
    "v_fma_f32 v173, v173, s69, v200\n"
    "v_mfma_f32_16x16x32_bf16 v[132:135], a[68:71], v[176:179], v[132:135]\n"
    "v_cvt_pk_bf16_f32 v180, v168, v169\n"
    "v_cvt_pk_bf16_f32 v181, v170, v171\n"
    "v_mfma_f32_16x16x32_bf16 v[136:139], a[72:75], v[176:179], v[136:139]\n"
    "v_exp_f32 v172, v172\n"
    "v_mfma_f32_16x16x32_bf16 v[140:143], a[76:79], v[176:179], v[140:143]\n"
    "v_exp_f32 v173, v173\n"
    "v_mfma_f32_16x16x32_bf16 v[144:147], a[80:83], v[176:179], v[144:147]\n"
    "v_fma_f32 v174, v174, s69, v200\n"
    "v_fma_f32 v175, v175, s69, v200\n"
    "v_mfma_f32_16x16x32_bf16 v[148:151], a[84:87], v[176:179], v[148:151]\n"
    "v_exp_f32 v174, v174\n"
    "v_mfma_f32_16x16x32_bf16 v[152:155], a[88:91], v[176:179], v[152:155]\n"
    "v_exp_f32 v175, v175\n"
    "v_mfma_f32_16x16x32_bf16 v[156:159], a[92:95], v[176:179], v[156:159]\n"
    "v_cvt_pk_bf16_f32 v182, v172, v173\n"
    "v_cvt_pk_bf16_f32 v183, v174, v175\n"
    "v_mfma_f32_16x16x32_bf16 v[168:171], a[32:35], a[192:195], 0\n"
    "v_max3_f32 v196, v160, v161, v162\n"
    "v_max3_f32 v197, v163, v164, v165\n"
    "v_mfma_f32_16x16x32_bf16 v[168:171], a[36:39], a[196:199], v[168:171]\n"
    "v_max3_f32 v196, v196, v166, v167\n"
    "v_max_f32 v196, v196, v197\n"
    "v_mfma_f32_16x16x32_bf16 v[168:171], a[40:43], a[200:203], v[168:171]\n"
    "v_cmp_gt_f32 vcc, v196, v190\n"
    "v_mfma_f32_16x16x32_bf16 v[168:171], a[44:47], a[204:207], v[168:171]\n"
    "s_cbranch_vccnz A_RARE_G_1\n"
    "A_BACK_G_1:\n"
    "v_fma_f32 v160, v160, s69, v201\n"
    "v_fma_f32 v161, v161, s69, v201\n"
    "v_mfma_f32_16x16x32_bf16 v[172:175], a[48:51], a[192:195], 0\n"
    "v_exp_f32 v160, v160\n"
    "v_mfma_f32_16x16x32_bf16 v[172:175], a[52:55], a[196:199], v[172:175]\n"
    "v_exp_f32 v161, v161\n"
    "v_mfma_f32_16x16x32_bf16 v[172:175], a[56:59], a[200:203], v[172:175]\n"
    "v_fma_f32 v162, v162, s69, v201\n"
    "v_fma_f32 v163, v163, s69, v201\n"
    "v_mfma_f32_16x16x32_bf16 v[172:175], a[60:63], a[204:207], v[172:175]\n"
    "v_exp_f32 v162, v162\n"
    "v_exp_f32 v163, v163\n"
    "v_fma_f32 v164, v164, s69, v201\n"
    "v_fma_f32 v165, v165, s69, v201\n"
    "v_cvt_pk_bf16_f32 v214, v160, v161\n"
    "v_cvt_pk_bf16_f32 v215, v162, v163\n"
    "v_exp_f32 v164, v164\n"
    "v_exp_f32 v165, v165\n"
    "v_fma_f32 v166, v166, s69, v201\n"
    "v_fma_f32 v167, v167, s69, v201\n"
    "v_exp_f32 v166, v166\n"
    "v_exp_f32 v167, v167\n"
    "v_cvt_pk_bf16_f32 v216, v164, v165\n"
    "v_cvt_pk_bf16_f32 v217, v166, v167\n"
    "v_mfma_f32_16x16x32_bf16 v[160:163], a[32:35], a[208:211], 0\n"
    "v_max3_f32 v196, v168, v169, v170\n"
    "v_max3_f32 v197, v171, v172, v173\n"
    "v_mfma_f32_16x16x32_bf16 v[160:163], a[36:39], a[212:215], v[160:163]\n"
    "v_max3_f32 v196, v196, v174, v175\n"
    "v_max_f32 v196, v196, v197\n"
    "v_mfma_f32_16x16x32_bf16 v[160:163], a[40:43], a[216:219], v[160:163]\n"
    "v_cmp_gt_f32 vcc, v196, v191\n"
    "v_mfma_f32_16x16x32_bf16 v[160:163], a[44:47], a[220:223], v[160:163]\n"
    "s_cbranch_vccnz A_RARE_G_2\n"
    "A_BACK_G_2:\n"
    "v_fma_f32 v168, v168, s69, v202\n"
    "v_fma_f32 v169, v169, s69, v202\n"
    "v_mfma_f32_16x16x32_bf16 v[164:167], a[48:51], a[208:211], 0\n"
    "v_exp_f32 v168, v168\n"
    "v_mfma_f32_16x16x32_bf16 v[164:167], a[52:55], a[212:215], v[164:167]\n"
    "v_exp_f32 v169, v169\n"
    "v_mfma_f32_16x16x32_bf16 v[164:167], a[56:59], a[216:219], v[164:167]\n"
    "v_fma_f32 v170, v170, s69, v202\n"
    "v_fma_f32 v171, v171, s69, v202\n"
    "v_mfma_f32_16x16x32_bf16 v[164:167], a[60:63], a[220:223], v[164:167]\n"
    "v_exp_f32 v170, v170\n"
    "v_exp_f32 v171, v171\n"
    "v_fma_f32 v172, v172, s69, v202\n"
    "v_fma_f32 v173, v173, s69, v202\n"
    "v_cvt_pk_bf16_f32 v226, v168, v169\n"
    "v_cvt_pk_bf16_f32 v227, v170, v171\n"
    "v_exp_f32 v172, v172\n"
    "v_exp_f32 v173, v173\n"
    "v_fma_f32 v174, v174, s69, v202\n"
    "v_fma_f32 v175, v175, s69, v202\n"
    "v_exp_f32 v174, v174\n"
    "v_exp_f32 v175, v175\n"
    "v_cvt_pk_bf16_f32 v228, v172, v173\n"
    "v_cvt_pk_bf16_f32 v229, v174, v175\n"
    "s_waitcnt vmcnt(20)\n"
    "s_barrier\n"
    "v_mfma_f32_16x16x32_bf16 v[168:171], a[32:35], a[224:227], 0\n"
    "ds_read_b64_tr_b16 a[96:97], v218 offset:0\n"
    "ds_read_b64_tr_b16 a[98:99], v218 offset:40960\n"
    "v_max3_f32 v196, v160, v161, v162\n"
    "v_max3_f32 v197, v163, v164, v165\n"
    "v_mfma_f32_16x16x32_bf16 v[168:171], a[36:39], a[228:231], v[168:171]\n"
    "ds_read_b64_tr_b16 a[100:101], v219 offset:0\n"
    "ds_read_b64_tr_b16 a[102:103], v219 offset:40960\n"
    "v_max3_f32 v196, v196, v166, v167\n"
    "v_max_f32 v196, v196, v197\n"
    "v_mfma_f32_16x16x32_bf16 v[168:171], a[40:43], a[232:235], v[168:171]\n"
    "ds_read_b64_tr_b16 a[104:105], v220 offset:0\n"
    "ds_read_b64_tr_b16 a[106:107], v220 offset:40960\n"
    "v_cmp_gt_f32 vcc, v196, v192\n"
    "v_mfma_f32_16x16x32_bf16 v[168:171], a[44:47], a[236:239], v[168:171]\n"
    "ds_read_b64_tr_b16 a[108:109], v221 offset:0\n"
    "ds_read_b64_tr_b16 a[110:111], v221 offset:40960\n"
    "s_cbranch_vccnz A_RARE_G_3\n"
    "A_BACK_G_3:\n"
    "v_fma_f32 v160, v160, s69, v203\n"
    "v_fma_f32 v161, v161, s69, v203\n"
    "v_mfma_f32_16x16x32_bf16 v[172:175], a[48:51], a[224:227], 0\n"
    "ds_read_b64_tr_b16 a[112:113], v222 offset:0\n"
    "ds_read_b64_tr_b16 a[114:115], v222 offset:40960\n"
    "v_exp_f32 v160, v160\n"
    "v_mfma_f32_16x16x32_bf16 v[172:175], a[52:55], a[228:231], v[172:175]\n"
    "ds_read_b64_tr_b16 a[116:117], v223 offset:0\n"
    "ds_read_b64_tr_b16 a[118:119], v223 offset:40960\n"
    "v_exp_f32 v161, v161\n"
    "v_mfma_f32_16x16x32_bf16 v[172:175], a[56:59], a[232:235], v[172:175]\n"
    "ds_read_b64_tr_b16 a[120:121], v224 offset:0\n"
    "ds_read_b64_tr_b16 a[122:123], v224 offset:40960\n"
    "v_fma_f32 v162, v162, s69, v203\n"
    "v_fma_f32 v163, v163, s69, v203\n"
    "v_mfma_f32_16x16x32_bf16 v[172:175], a[60:63], a[236:239], v[172:175]\n"
    "ds_read_b64_tr_b16 a[124:125], v225 offset:0\n"
    "ds_read_b64_tr_b16 a[126:127], v225 offset:40960\n"
    "v_exp_f32 v162, v162\n"
    "v_exp_f32 v163, v163\n"
    "v_fma_f32 v164, v164, s69, v203\n"
    "v_fma_f32 v165, v165, s69, v203\n"
    "v_cvt_pk_bf16_f32 v230, v160, v161\n"
    "v_cvt_pk_bf16_f32 v231, v162, v163\n"
    "v_exp_f32 v164, v164\n"
    "v_exp_f32 v165, v165\n"
    "v_fma_f32 v166, v166, s69, v203\n"
    "v_fma_f32 v167, v167, s69, v203\n"
    "v_exp_f32 v166, v166\n"
    "v_exp_f32 v167, v167\n"
    "v_cvt_pk_bf16_f32 v232, v164, v165\n"
    "v_cvt_pk_bf16_f32 v233, v166, v167\n"
    "s_waitcnt lgkmcnt(0)\n"
    "s_barrier\n"
    "v_mfma_f32_16x16x32_bf16 a[128:131], a[148:151], v[180:183], a[128:131]\n"
    "v_max3_f32 v196, v168, v169, v170\n"
    "v_max3_f32 v197, v171, v172, v173\n"
    "v_mfma_f32_16x16x32_bf16 v[0:3], a[96:99], v[180:183], v[0:3]\n"
    "v_max3_f32 v196, v196, v174, v175\n"
    "v_max_f32 v196, v196, v197\n"
    "v_mfma_f32_16x16x32_bf16 v[4:7], a[100:103], v[180:183], v[4:7]\n"
    "v_cmp_gt_f32 vcc, v196, v193\n"
    "v_mfma_f32_16x16x32_bf16 v[8:11], a[104:107], v[180:183], v[8:11]\n"
    "s_cbranch_vccnz A_RARE_G_4\n"
    "A_BACK_G_4:\n"
    "v_fma_f32 v168, v168, s69, v204\n"
    "v_fma_f32 v169, v169, s69, v204\n"
    "v_mfma_f32_16x16x32_bf16 v[12:15], a[108:111], v[180:183], v[12:15]\n"
    "v_exp_f32 v168, v168\n"
    "v_exp_f32 v169, v169\n"
    "v_mfma_f32_16x16x32_bf16 v[16:19], a[112:115], v[180:183], v[16:19]\n"
    "v_fma_f32 v170, v170, s69, v204\n"
    "v_fma_f32 v171, v171, s69, v204\n"
    "v_exp_f32 v170, v170\n"
    "v_exp_f32 v171, v171\n"
    "v_mfma_f32_16x16x32_bf16 v[20:23], a[116:119], v[180:183], v[20:23]\n"
    "v_fma_f32 v172, v172, s69, v204\n"
    "v_fma_f32 v173, v173, s69, v204\n"
    "v_cvt_pk_bf16_f32 v176, v168, v169\n"
    "v_cvt_pk_bf16_f32 v177, v170, v171\n"
    "v_exp_f32 v172, v172\n"
    "v_mfma_f32_16x16x32_bf16 v[24:27], a[120:123], v[180:183], v[24:27]\n"
    "v_exp_f32 v173, v173\n"
    "v_fma_f32 v174, v174, s69, v204\n"
    "v_fma_f32 v175, v175, s69, v204\n"
    "v_exp_f32 v174, v174\n"
    "v_mfma_f32_16x16x32_bf16 v[28:31], a[124:127], v[180:183], v[28:31]\n"
    "v_exp_f32 v175, v175\n"
    "v_cvt_pk_bf16_f32 v178, v172, v173\n"
    "v_cvt_pk_bf16_f32 v179, v174, v175\n"
    "v_mfma_f32_16x16x32_bf16 a[132:135], a[148:151], v[214:217], a[132:135]\n"
    "v_accvgpr_read_b32 v254, a128\n"
    "v_rcp_f32 v254, v254\n"
    "s_nop 0\n"
    "v_mul_f32 v0, v254, v0\n"
    "v_mul_f32 v1, v254, v1\n"
    "v_mul_f32 v2, v254, v2\n"
    "v_mul_f32 v3, v254, v3\n"
    "v_mfma_f32_16x16x32_bf16 v[32:35], a[96:99], v[214:217], v[32:35]\n"
    "v_mul_f32 v4, v254, v4\n"
    "v_mul_f32 v5, v254, v5\n"
    "v_mul_f32 v6, v254, v6\n"
    "v_mul_f32 v7, v254, v7\n"
    "v_cvt_pk_bf16_f32 v0, v0, v1\n"
    "v_cvt_pk_bf16_f32 v1, v2, v3\n"
    "v_cvt_pk_bf16_f32 v2, v4, v5\n"
    "v_cvt_pk_bf16_f32 v3, v6, v7\n"
    "v_mfma_f32_16x16x32_bf16 v[36:39], a[100:103], v[214:217], v[36:39]\n"
    "ds_write_b128 v240, v[0:3] offset:24576\n"
    "v_mul_f32 v8, v254, v8\n"
    "v_mul_f32 v9, v254, v9\n"
    "v_mul_f32 v10, v254, v10\n"
    "v_mul_f32 v11, v254, v11\n"
    "v_mfma_f32_16x16x32_bf16 v[40:43], a[104:107], v[214:217], v[40:43]\n"
    "v_mul_f32 v12, v254, v12\n"
    "v_mul_f32 v13, v254, v13\n"
    "v_mul_f32 v14, v254, v14\n"
    "v_mul_f32 v15, v254, v15\n"
    "v_cvt_pk_bf16_f32 v8, v8, v9\n"
    "v_cvt_pk_bf16_f32 v9, v10, v11\n"
    "v_cvt_pk_bf16_f32 v10, v12, v13\n"
    "v_cvt_pk_bf16_f32 v11, v14, v15\n"
    "v_mfma_f32_16x16x32_bf16 v[44:47], a[108:111], v[214:217], v[44:47]\n"
    "ds_write_b128 v240, v[8:11] offset:29696\n"
    "v_mul_f32 v16, v254, v16\n"
    "v_mul_f32 v17, v254, v17\n"
    "v_mul_f32 v18, v254, v18\n"
    "v_mul_f32 v19, v254, v19\n"
    "v_mfma_f32_16x16x32_bf16 v[48:51], a[112:115], v[214:217], v[48:51]\n"
    "v_mul_f32 v20, v254, v20\n"
    "v_mul_f32 v21, v254, v21\n"
    "v_mul_f32 v22, v254, v22\n"
    "v_mul_f32 v23, v254, v23\n"
    "v_cvt_pk_bf16_f32 v16, v16, v17\n"
    "v_cvt_pk_bf16_f32 v17, v18, v19\n"
    "v_cvt_pk_bf16_f32 v18, v20, v21\n"
    "v_cvt_pk_bf16_f32 v19, v22, v23\n"
    "v_mfma_f32_16x16x32_bf16 v[52:55], a[116:119], v[214:217], v[52:55]\n"
    "ds_write_b128 v240, v[16:19] offset:34816\n"
    "v_mul_f32 v24, v254, v24\n"
    "v_mul_f32 v25, v254, v25\n"
    "v_mul_f32 v26, v254, v26\n"
    "v_mul_f32 v27, v254, v27\n"
    "v_mfma_f32_16x16x32_bf16 v[56:59], a[120:123], v[214:217], v[56:59]\n"
    "v_mul_f32 v28, v254, v28\n"
    "v_mul_f32 v29, v254, v29\n"
    "v_mul_f32 v30, v254, v30\n"
    "v_mul_f32 v31, v254, v31\n"
    "v_cvt_pk_bf16_f32 v24, v24, v25\n"
    "v_cvt_pk_bf16_f32 v25, v26, v27\n"
    "v_cvt_pk_bf16_f32 v26, v28, v29\n"
    "A_TT_FIRST0:\n"
    "A_TT_S0:\n"
    "s_cbranch_execz A_TT_RET\n"
    "v_cvt_pk_bf16_f32 v27, v30, v31\n"
    "v_mfma_f32_16x16x32_bf16 v[60:63], a[124:127], v[214:217], v[60:63]\n"
    "ds_write_b128 v240, v[24:27] offset:39936\n"
    "v_mfma_f32_16x16x32_bf16 a[136:139], a[148:151], v[226:229], a[136:139]\n"
    "v_accvgpr_read_b32 v254, a132\n"
    "v_rcp_f32 v254, v254\n"
    "s_nop 0\n"
    "v_mul_f32 v32, v254, v32\n"
    "v_mul_f32 v33, v254, v33\n"
    "v_mul_f32 v34, v254, v34\n"
    "v_mul_f32 v35, v254, v35\n"
    "v_mfma_f32_16x16x32_bf16 v[64:67], a[96:99], v[226:229], v[64:67]\n"
    "v_mul_f32 v36, v254, v36\n"
    "v_mul_f32 v37, v254, v37\n"
    "v_mul_f32 v38, v254, v38\n"
    "v_mul_f32 v39, v254, v39\n"
    "v_cvt_pk_bf16_f32 v32, v32, v33\n"
    "v_cvt_pk_bf16_f32 v33, v34, v35\n"
    "v_cvt_pk_bf16_f32 v34, v36, v37\n"
    "v_cvt_pk_bf16_f32 v35, v38, v39\n"
    "v_mfma_f32_16x16x32_bf16 v[68:71], a[100:103], v[226:229], v[68:71]\n"
    "ds_write_b128 v240, v[32:35] offset:25600\n"
    "v_mul_f32 v40, v254, v40\n"
    "v_mul_f32 v41, v254, v41\n"
    "v_mul_f32 v42, v254, v42\n"
    "v_mul_f32 v43, v254, v43\n"
    "v_mfma_f32_16x16x32_bf16 v[72:75], a[104:107], v[226:229], v[72:75]\n"
    "v_mul_f32 v44, v254, v44\n"
    "v_mul_f32 v45, v254, v45\n"
    "v_mul_f32 v46, v254, v46\n"
    "v_mul_f32 v47, v254, v47\n"
    "v_cvt_pk_bf16_f32 v40, v40, v41\n"
    "v_cvt_pk_bf16_f32 v41, v42, v43\n"
    "v_cvt_pk_bf16_f32 v42, v44, v45\n"
    "v_cvt_pk_bf16_f32 v43, v46, v47\n"
    "v_mfma_f32_16x16x32_bf16 v[76:79], a[108:111], v[226:229], v[76:79]\n"
    "ds_write_b128 v240, v[40:43] offset:30720\n"
    "v_mul_f32 v48, v254, v48\n"
    "v_mul_f32 v49, v254, v49\n"
    "v_mul_f32 v50, v254, v50\n"
    "v_mul_f32 v51, v254, v51\n"
    "v_mfma_f32_16x16x32_bf16 v[80:83], a[112:115], v[226:229], v[80:83]\n"
    "v_mul_f32 v52, v254, v52\n"
    "v_mul_f32 v53, v254, v53\n"
    "v_mul_f32 v54, v254, v54\n"
    "v_mul_f32 v55, v254, v55\n"
    "v_cvt_pk_bf16_f32 v48, v48, v49\n"
    "v_cvt_pk_bf16_f32 v49, v50, v51\n"
    "v_cvt_pk_bf16_f32 v50, v52, v53\n"
    "v_cvt_pk_bf16_f32 v51, v54, v55\n"
    "v_mfma_f32_16x16x32_bf16 v[84:87], a[116:119], v[226:229], v[84:87]\n"
    "ds_write_b128 v240, v[48:51] offset:35840\n"
    "v_mul_f32 v56, v254, v56\n"
    "v_mul_f32 v57, v254, v57\n"
    "v_mul_f32 v58, v254, v58\n"
    "v_mul_f32 v59, v254, v59\n"
    "v_mfma_f32_16x16x32_bf16 v[88:91], a[120:123], v[226:229], v[88:91]\n"
    "v_mul_f32 v60, v254, v60\n"
    "v_mul_f32 v61, v254, v61\n"
    "v_mul_f32 v62, v254, v62\n"
    "v_mul_f32 v63, v254, v63\n"
    "v_cvt_pk_bf16_f32 v56, v56, v57\n"
    "v_cvt_pk_bf16_f32 v57, v58, v59\n"
    "v_cvt_pk_bf16_f32 v58, v60, v61\n"
    "v_cvt_pk_bf16_f32 v59, v62, v63\n"
    "v_mfma_f32_16x16x32_bf16 v[92:95], a[124:127], v[226:229], v[92:95]\n"
    "ds_write_b128 v240, v[56:59] offset:0\n"
    "v_mfma_f32_16x16x32_bf16 a[140:143], a[148:151], v[230:233], a[140:143]\n"
    "v_accvgpr_read_b32 v254, a136\n"
    "v_rcp_f32 v254, v254\n"
    "s_nop 0\n"
    "v_mul_f32 v64, v254, v64\n"
    "v_mul_f32 v65, v254, v65\n"
    "v_mul_f32 v66, v254, v66\n"
    "v_mul_f32 v67, v254, v67\n"
    "v_mfma_f32_16x16x32_bf16 v[96:99], a[96:99], v[230:233], v[96:99]\n"
    "v_mul_f32 v68, v254, v68\n"
    "v_mul_f32 v69, v254, v69\n"
    "v_mul_f32 v70, v254, v70\n"
    "v_mul_f32 v71, v254, v71\n"
    "v_cvt_pk_bf16_f32 v64, v64, v65\n"
    "v_cvt_pk_bf16_f32 v65, v66, v67\n"
    "v_cvt_pk_bf16_f32 v66, v68, v69\n"
    "v_cvt_pk_bf16_f32 v67, v70, v71\n"
    "v_mfma_f32_16x16x32_bf16 v[100:103], a[100:103], v[230:233], v[100:103]\n"
    "ds_write_b128 v240, v[64:67] offset:26624\n"
    "v_mul_f32 v72, v254, v72\n"
    "v_mul_f32 v73, v254, v73\n"
    "v_mul_f32 v74, v254, v74\n"
    "v_mul_f32 v75, v254, v75\n"
    "v_mfma_f32_16x16x32_bf16 v[104:107], a[104:107], v[230:233], v[104:107]\n"
    "v_mul_f32 v76, v254, v76\n"
    "v_mul_f32 v77, v254, v77\n"
    "v_mul_f32 v78, v254, v78\n"
    "v_mul_f32 v79, v254, v79\n"
    "v_cvt_pk_bf16_f32 v72, v72, v73\n"
    "v_cvt_pk_bf16_f32 v73, v74, v75\n"
    "v_cvt_pk_bf16_f32 v74, v76, v77\n"
    "v_cvt_pk_bf16_f32 v75, v78, v79\n"
    "v_mfma_f32_16x16x32_bf16 v[108:111], a[108:111], v[230:233], v[108:111]\n"
    "ds_write_b128 v240, v[72:75] offset:31744\n"
    "v_mul_f32 v80, v254, v80\n"
    "v_mul_f32 v81, v254, v81\n"
    "v_mul_f32 v82, v254, v82\n"
    "v_mul_f32 v83, v254, v83\n"
    "v_mfma_f32_16x16x32_bf16 v[112:115], a[112:115], v[230:233], v[112:115]\n"
    "v_mul_f32 v84, v254, v84\n"
    "v_mul_f32 v85, v254, v85\n"
    "v_mul_f32 v86, v254, v86\n"
    "v_mul_f32 v87, v254, v87\n"
    "v_cvt_pk_bf16_f32 v80, v80, v81\n"
    "v_cvt_pk_bf16_f32 v81, v82, v83\n"
    "v_cvt_pk_bf16_f32 v82, v84, v85\n"
    "v_cvt_pk_bf16_f32 v83, v86, v87\n"
    "v_mfma_f32_16x16x32_bf16 v[116:119], a[116:119], v[230:233], v[116:119]\n"
    "ds_write_b128 v240, v[80:83] offset:36864\n"
    "v_mul_f32 v88, v254, v88\n"
    "v_mul_f32 v89, v254, v89\n"
    "v_mul_f32 v90, v254, v90\n"
    "v_mul_f32 v91, v254, v91\n"
    "v_mfma_f32_16x16x32_bf16 v[120:123], a[120:123], v[230:233], v[120:123]\n"
    "v_mul_f32 v92, v254, v92\n"
    "v_mul_f32 v93, v254, v93\n"
    "v_mul_f32 v94, v254, v94\n"
    "v_mul_f32 v95, v254, v95\n"
    "v_cvt_pk_bf16_f32 v88, v88, v89\n"
    "v_cvt_pk_bf16_f32 v89, v90, v91\n"
    "v_cvt_pk_bf16_f32 v90, v92, v93\n"
    "v_cvt_pk_bf16_f32 v91, v94, v95\n"
    "v_mfma_f32_16x16x32_bf16 v[124:127], a[124:127], v[230:233], v[124:127]\n"
    "ds_write_b128 v240, v[88:91] offset:1024\n"
    "v_mfma_f32_16x16x32_bf16 a[144:147], a[148:151], v[176:179], a[144:147]\n"
    "v_accvgpr_read_b32 v254, a140\n"
    "v_rcp_f32 v254, v254\n"
    "s_nop 0\n"
    "v_mul_f32 v96, v254, v96\n"
    "v_mul_f32 v97, v254, v97\n"
    "v_mul_f32 v98, v254, v98\n"
    "v_mul_f32 v99, v254, v99\n"
    "v_mfma_f32_16x16x32_bf16 v[128:131], a[96:99], v[176:179], v[128:131]\n"
    "v_mul_f32 v100, v254, v100\n"
    "v_mul_f32 v101, v254, v101\n"
    "v_mul_f32 v102, v254, v102\n"
    "v_mul_f32 v103, v254, v103\n"
    "v_cvt_pk_bf16_f32 v96, v96, v97\n"
    "v_cvt_pk_bf16_f32 v97, v98, v99\n"
    "v_cvt_pk_bf16_f32 v98, v100, v101\n"
    "v_cvt_pk_bf16_f32 v99, v102, v103\n"
    "v_mfma_f32_16x16x32_bf16 v[132:135], a[100:103], v[176:179], v[132:135]\n"
    "ds_write_b128 v240, v[96:99] offset:27648\n"
    "v_mul_f32 v104, v254, v104\n"
    "v_mul_f32 v105, v254, v105\n"
    "v_mul_f32 v106, v254, v106\n"
    "v_mul_f32 v107, v254, v107\n"
    "v_mfma_f32_16x16x32_bf16 v[136:139], a[104:107], v[176:179], v[136:139]\n"
    "v_mul_f32 v108, v254, v108\n"
    "v_mul_f32 v109, v254, v109\n"
    "v_mul_f32 v110, v254, v110\n"
    "v_mul_f32 v111, v254, v111\n"
    "v_cvt_pk_bf16_f32 v104, v104, v105\n"
    "v_cvt_pk_bf16_f32 v105, v106, v107\n"
    "v_cvt_pk_bf16_f32 v106, v108, v109\n"
    "v_cvt_pk_bf16_f32 v107, v110, v111\n"
    "v_mfma_f32_16x16x32_bf16 v[140:143], a[108:111], v[176:179], v[140:143]\n"
    "ds_write_b128 v240, v[104:107] offset:32768\n"
    "v_mul_f32 v112, v254, v112\n"
    "v_mul_f32 v113, v254, v113\n"
    "v_mul_f32 v114, v254, v114\n"
    "v_mul_f32 v115, v254, v115\n"
    "v_mfma_f32_16x16x32_bf16 v[144:147], a[112:115], v[176:179], v[144:147]\n"
    "v_mul_f32 v116, v254, v116\n"
    "v_mul_f32 v117, v254, v117\n"
    "v_mul_f32 v118, v254, v118\n"
    "v_mul_f32 v119, v254, v119\n"
    "v_cvt_pk_bf16_f32 v112, v112, v113\n"
    "v_cvt_pk_bf16_f32 v113, v114, v115\n"
    "v_cvt_pk_bf16_f32 v114, v116, v117\n"
    "v_cvt_pk_bf16_f32 v115, v118, v119\n"
    "v_mfma_f32_16x16x32_bf16 v[148:151], a[116:119], v[176:179], v[148:151]\n"
    "ds_write_b128 v240, v[112:115] offset:37888\n"
    "v_mul_f32 v120, v254, v120\n"
    "v_mul_f32 v121, v254, v121\n"
    "v_mul_f32 v122, v254, v122\n"
    "v_mul_f32 v123, v254, v123\n"
    "v_mfma_f32_16x16x32_bf16 v[152:155], a[120:123], v[176:179], v[152:155]\n"
    "v_mul_f32 v124, v254, v124\n"
    "v_mul_f32 v125, v254, v125\n"
    "v_mul_f32 v126, v254, v126\n"
    "v_mul_f32 v127, v254, v127\n"
    "v_cvt_pk_bf16_f32 v120, v120, v121\n"
    "v_cvt_pk_bf16_f32 v121, v122, v123\n"
    "v_cvt_pk_bf16_f32 v122, v124, v125\n"
    "v_cvt_pk_bf16_f32 v123, v126, v127\n"
    "v_mfma_f32_16x16x32_bf16 v[156:159], a[124:127], v[176:179], v[156:159]\n"
    "ds_write_b128 v240, v[120:123] offset:2048\n"
    "s_nop 1\n"
    "v_accvgpr_read_b32 v254, a144\n"
    "v_rcp_f32 v254, v254\n"
    "s_nop 0\n"
    "v_mul_f32 v128, v254, v128\n"
    "v_mul_f32 v129, v254, v129\n"
    "v_mul_f32 v130, v254, v130\n"
    "v_mul_f32 v131, v254, v131\n"
    "v_mul_f32 v132, v254, v132\n"
    "v_mul_f32 v133, v254, v133\n"
    "v_mul_f32 v134, v254, v134\n"
    "v_mul_f32 v135, v254, v135\n"
    "v_cvt_pk_bf16_f32 v128, v128, v129\n"
    "v_cvt_pk_bf16_f32 v129, v130, v131\n"
    "v_cvt_pk_bf16_f32 v130, v132, v133\n"
    "v_cvt_pk_bf16_f32 v131, v134, v135\n"
    "ds_write_b128 v240, v[128:131] offset:28672\n"
    "v_mul_f32 v136, v254, v136\n"
    "v_mul_f32 v137, v254, v137\n"
    "v_mul_f32 v138, v254, v138\n"
    "v_mul_f32 v139, v254, v139\n"
    "v_mul_f32 v140, v254, v140\n"
    "v_mul_f32 v141, v254, v141\n"
    "v_mul_f32 v142, v254, v142\n"
    "v_mul_f32 v143, v254, v143\n"
    "v_cvt_pk_bf16_f32 v136, v136, v137\n"
    "v_cvt_pk_bf16_f32 v137, v138, v139\n"
    "v_cvt_pk_bf16_f32 v138, v140, v141\n"
    "v_cvt_pk_bf16_f32 v139, v142, v143\n"
    "ds_write_b128 v240, v[136:139] offset:33792\n"
    "v_mul_f32 v144, v254, v144\n"
    "v_mul_f32 v145, v254, v145\n"
    "v_mul_f32 v146, v254, v146\n"
    "v_mul_f32 v147, v254, v147\n"
    "v_mul_f32 v148, v254, v148\n"
    "v_mul_f32 v149, v254, v149\n"
    "v_mul_f32 v150, v254, v150\n"
    "v_mul_f32 v151, v254, v151\n"
    "v_cvt_pk_bf16_f32 v144, v144, v145\n"
    "v_cvt_pk_bf16_f32 v145, v146, v147\n"
    "v_cvt_pk_bf16_f32 v146, v148, v149\n"
    "v_cvt_pk_bf16_f32 v147, v150, v151\n"
    "ds_write_b128 v240, v[144:147] offset:38912\n"
    "v_mul_f32 v152, v254, v152\n"
    "v_mul_f32 v153, v254, v153\n"
    "v_mul_f32 v154, v254, v154\n"
    "v_mul_f32 v155, v254, v155\n"
    "v_mul_f32 v156, v254, v156\n"
    "v_mul_f32 v157, v254, v157\n"
    "v_mul_f32 v158, v254, v158\n"
    "v_mul_f32 v159, v254, v159\n"
    "v_cvt_pk_bf16_f32 v152, v152, v153\n"
    "v_cvt_pk_bf16_f32 v153, v154, v155\n"
    "v_cvt_pk_bf16_f32 v154, v156, v157\n"
    "v_cvt_pk_bf16_f32 v155, v158, v159\n"
    "ds_write_b128 v240, v[152:155] offset:3072\n"
    "s_add_u32 s85, s87, s86\n"
    "s_mov_b32 s80, 1\n"
    "s_mov_b32 s84, 0x2000\n"
    "s_mov_b32 s88, 0x3000\n"
    "s_add_u32 s89, s35, 1\n"
    "s_mul_i32 s89, s89, 0x14000\n"
    "s_mov_b32 s81, 0\n"
    "v_add_u32 v230, 0x2000, v244\n"
    "v_add_u32 v231, 0x2000, v245\n"
    "v_add_u32 v232, 0x2000, v246\n"
    "v_add_u32 v233, 0x2000, v247\n"
    "s_waitcnt vmcnt(0) lgkmcnt(0)\n"
    "s_barrier\n"
    "ds_read_b64_tr_b16 v[84:85], v230 offset:0\n"
    "ds_read_b64_tr_b16 v[86:87], v230 offset:2048\n"
    "ds_read_b64_tr_b16 v[92:93], v231 offset:0\n"
    "ds_read_b64_tr_b16 v[94:95], v231 offset:2048\n"
    "ds_read_b128 a[0:3], v241 offset:24576\n"
    "ds_read_b128 a[4:7], v241 offset:25600\n"
    "ds_read_b128 a[8:11], v241 offset:26624\n"
    "ds_read_b128 a[12:15], v241 offset:27648\n"
    "ds_read_b128 a[16:19], v241 offset:28672\n"
    "s_waitcnt lgkmcnt(4)\n"
    "v_mfma_f32_16x16x32_bf16 v[4:7], v[84:87], v[0:3], 0\n"
    "ds_read_b64_tr_b16 v[100:101], v232 offset:0\n"
    "ds_read_b64_tr_b16 v[102:103], v232 offset:2048\n"
    "v_mfma_f32_16x16x32_bf16 v[4:7], v[92:95], a[0:3], v[4:7]\n"
    "s_waitcnt lgkmcnt(5)\n"
    "ds_read_b128 a[20:23], v242 offset:24576\n"
    "ds_read_b128 a[40:43], v243 offset:24576\n"
    "v_mfma_f32_16x16x32_bf16 v[12:15], v[92:95], a[4:7], 0\n"
    "ds_read_b64_tr_b16 v[108:109], v233 offset:0\n"
    "ds_read_b64_tr_b16 v[110:111], v233 offset:2048\n"
    "v_add_u32 v230, s88, v244\n"
    "v_mfma_f32_16x16x32_bf16 v[12:15], v[84:87], v[32:35], v[12:15]\n"
    "s_waitcnt lgkmcnt(8)\n"
    "ds_read_b128 a[24:27], v242 offset:25600\n"
    "ds_read_b128 a[44:47], v243 offset:25600\n"
    "v_mfma_f32_16x16x32_bf16 v[20:23], v[84:87], v[64:67], 0\n"
    "ds_read_b128 a[28:31], v242 offset:26624\n"
    "ds_read_b128 a[48:51], v243 offset:26624\n"
    "v_add_u32 v231, s88, v245\n"
    "v_mfma_f32_16x16x32_bf16 v[20:23], v[92:95], a[8:11], v[20:23]\n"
    "s_waitcnt lgkmcnt(11)\n"
    "ds_read_b128 a[32:35], v242 offset:27648\n"
    "ds_read_b128 a[52:55], v243 offset:27648\n"
    "v_mfma_f32_16x16x32_bf16 v[28:31], v[92:95], a[12:15], 0\n"
    "ds_read_b128 a[36:39], v242 offset:28672\n"
    "ds_read_b128 a[56:59], v243 offset:28672\n"
    "v_mfma_f32_16x16x32_bf16 v[28:31], v[84:87], v[96:99], v[28:31]\n"
    "s_waitcnt lgkmcnt(14)\n"
    "ds_read_b128 a[60:63], v241 offset:29696\n"
    "ds_read_b128 a[64:67], v241 offset:30720\n"
    "v_mfma_f32_16x16x32_bf16 v[36:39], v[84:87], v[128:131], 0\n"
    "ds_read_b128 a[68:71], v241 offset:31744\n"
    "ds_read_b128 a[72:75], v241 offset:32768\n"
    "v_mfma_f32_16x16x32_bf16 v[36:39], v[92:95], a[16:19], v[36:39]\n"
    "s_waitcnt lgkmcnt(14)\n"
    "s_waitcnt vmcnt(8) lgkmcnt(12)\n"
    "s_barrier\n"
    "v_add_u32 v233, s88, v247\n"
    "v_mfma_f32_16x16x32_bf16 v[4:7], v[100:103], a[20:23], v[4:7]\n"
    "ds_read_b64_tr_b16 v[84:85], v230 offset:0\n"
    "ds_read_b64_tr_b16 v[86:87], v230 offset:2048\n"
    "v_mfma_f32_16x16x32_bf16 v[4:7], v[108:111], a[40:43], v[4:7]\n"
    "s_waitcnt lgkmcnt(12)\n"
    "ds_read_b128 a[76:79], v241 offset:33792\n"
    "ds_read_b128 a[80:83], v242 offset:29696\n"
    "v_mfma_f32_16x16x32_bf16 v[12:15], v[108:111], a[44:47], v[12:15]\n"
    "ds_read_b64_tr_b16 v[92:93], v231 offset:0\n"
    "ds_read_b64_tr_b16 v[94:95], v231 offset:2048\n"
    "v_mfma_f32_16x16x32_bf16 v[12:15], v[100:103], a[24:27], v[12:15]\n"
    "s_waitcnt lgkmcnt(14)\n"
    "ds_read_b128 a[100:103], v243 offset:29696\n"
    "ds_read_b128 a[84:87], v242 offset:30720\n"
    "v_mfma_f32_16x16x32_bf16 v[20:23], v[100:103], a[28:31], v[20:23]\n"
    "ds_read_b128 a[104:107], v243 offset:30720\n"
    "v_add_u32 v232, s88, v246\n"
    "v_mfma_f32_16x16x32_bf16 v[20:23], v[108:111], a[48:51], v[20:23]\n"
    "s_waitcnt lgkmcnt(15)\n"
    "s_add_u32 s78, s85, 0x9000\n"
    "ds_read_b128 a[88:91], v242 offset:31744\n"
    "v_mfma_f32_16x16x32_bf16 v[28:31], v[108:111], a[52:55], v[28:31]\n"
    "s_add_u32 m0, s71, s84\n"
    "s_add_u32 s88, s88, 0x1000\n"
    "ds_read_b128 a[108:111], v243 offset:31744\n"
    "v_mfma_f32_16x16x32_bf16 v[28:31], v[100:103], a[32:35], v[28:31]\n"
    "s_waitcnt lgkmcnt(15)\n"
    "s_cmp_eq_u32 s88, 0x6000\n"
    "s_cselect_b32 s88, 0x1000, s88\n"
    "buffer_load_dwordx4 v234, s[52:55], s78 offen lds\n"
    "v_mfma_f32_16x16x32_bf16 v[36:39], v[100:103], a[36:39], v[36:39]\n"
    "ds_read_b128 a[92:95], v242 offset:32768\n"
    "ds_read_b128 a[112:115], v243 offset:32768\n"
    "v_mfma_f32_16x16x32_bf16 v[36:39], v[108:111], a[56:59], v[36:39]\n"
    "s_waitcnt lgkmcnt(15)\n"
    "s_waitcnt lgkmcnt(7)\n"
    "ds_read_b128 a[96:99], v242 offset:33792\n"
    "v_mfma_f32_16x16x32_bf16 v[4:7], v[84:87], v[8:11], v[4:7]\n"
    "s_add_u32 s84, s84, 0x1000\n"
    "ds_read_b64_tr_b16 v[100:101], v232 offset:0\n"
    "ds_read_b64_tr_b16 v[102:103], v232 offset:2048\n"
    "v_mfma_f32_16x16x32_bf16 v[4:7], v[92:95], a[60:63], v[4:7]\n"
    "s_waitcnt lgkmcnt(15)\n"
    "ds_read_b128 a[116:119], v243 offset:33792\n"
    "ds_read_b128 a[120:123], v241 offset:34816\n"
    "v_mfma_f32_16x16x32_bf16 v[12:15], v[92:95], a[64:67], v[12:15]\n"
    "ds_read_b64_tr_b16 v[108:109], v233 offset:0\n"
    "ds_read_b64_tr_b16 v[110:111], v233 offset:2048\n"
    "v_mfma_f32_16x16x32_bf16 v[12:15], v[84:87], v[40:43], v[12:15]\n"
    "s_waitcnt lgkmcnt(15)\n"
    "buffer_load_dwordx4 v235, s[52:55], s78 offen offset:1024 lds\n"
    "v_mfma_f32_16x16x32_bf16 v[20:23], v[84:87], v[72:75], v[20:23]\n"
    "ds_read_b128 a[124:127], v241 offset:35840\n"
    "v_add_u32 v230, s88, v244\n"
    "v_mfma_f32_16x16x32_bf16 v[20:23], v[92:95], a[68:71], v[20:23]\n"
    "s_waitcnt lgkmcnt(15)\n"
    "ds_read_b128 a[128:131], v241 offset:36864\n"
    "v_add_u32 v231, s88, v245\n"
    "v_mfma_f32_16x16x32_bf16 v[28:31], v[92:95], a[72:75], v[28:31]\n"
    "s_cmp_eq_u32 s84, 0x6000\n"
    "s_cselect_b32 s84, 0x1000, s84\n"
    "v_add_u32 v232, s88, v246\n"
    "v_mfma_f32_16x16x32_bf16 v[28:31], v[84:87], v[104:107], v[28:31]\n"
    "s_waitcnt lgkmcnt(15)\n"
    "buffer_load_dwordx4 v236, s[52:55], s78 offen offset:2048 lds\n"
    "v_mfma_f32_16x16x32_bf16 v[36:39], v[84:87], v[136:139], v[36:39]\n"
    "ds_read_b128 a[132:135], v241 offset:37888\n"
    "v_and_b32 v229, 15, v239\n"
    "v_mfma_f32_16x16x32_bf16 v[36:39], v[92:95], a[76:79], v[36:39]\n"
    "s_waitcnt lgkmcnt(15)\n"
    "s_waitcnt vmcnt(15) lgkmcnt(3)\n"
    "s_barrier\n"
    "v_mfma_f32_16x16x32_bf16 v[4:7], v[100:103], a[80:83], v[4:7]\n"
    "ds_read_b64_tr_b16 v[84:85], v230 offset:0\n"
    "ds_read_b64_tr_b16 v[86:87], v230 offset:2048\n"
    "v_mfma_f32_16x16x32_bf16 v[4:7], v[108:111], a[100:103], v[4:7]\n"
    "s_waitcnt lgkmcnt(15)\n"
    "buffer_load_dwordx4 v237, s[52:55], s78 offen offset:3072 lds\n"
    "v_add_u32 v233, s88, v247\n"
    "v_mfma_f32_16x16x32_bf16 v[12:15], v[108:111], a[104:107], v[12:15]\n"
    "ds_read_b64_tr_b16 v[92:93], v231 offset:0\n"
    "ds_read_b64_tr_b16 v[94:95], v231 offset:2048\n"
    "v_mfma_f32_16x16x32_bf16 v[12:15], v[100:103], a[84:87], v[12:15]\n"
    "s_waitcnt lgkmcnt(15)\n"
    "ds_read_b128 a[136:139], v241 offset:38912\n"
    "ds_read_b128 a[140:143], v242 offset:34816\n"
    "v_mfma_f32_16x16x32_bf16 v[20:23], v[100:103], a[88:91], v[20:23]\n"
    "ds_read_b128 a[160:163], v243 offset:34816\n"
    "ds_read_b128 a[144:147], v242 offset:35840\n"
    "v_mfma_f32_16x16x32_bf16 v[20:23], v[108:111], a[108:111], v[20:23]\n"
    "s_waitcnt lgkmcnt(15)\n"
    "s_add_u32 s78, s85, 0x12000\n"
    "ds_read_b128 a[164:167], v243 offset:35840\n"
    "v_mfma_f32_16x16x32_bf16 v[28:31], v[108:111], a[112:115], v[28:31]\n"
    "s_add_u32 m0, s71, s84\n"
    "ds_read_b128 a[148:151], v242 offset:36864\n"
    "ds_read_b128 a[168:171], v243 offset:36864\n"
    "v_mfma_f32_16x16x32_bf16 v[28:31], v[100:103], a[92:95], v[28:31]\n"
    "s_waitcnt lgkmcnt(15)\n"
    "s_add_u32 s88, s88, 0x1000\n"
    "buffer_load_dwordx4 v234, s[52:55], s78 offen lds\n"
    "v_mfma_f32_16x16x32_bf16 v[36:39], v[100:103], a[96:99], v[36:39]\n"
    "s_cmp_eq_u32 s88, 0x6000\n"
    "s_cselect_b32 s88, 0x1000, s88\n"
    "ds_read_b128 a[152:155], v242 offset:37888\n"
    "ds_read_b128 a[172:175], v243 offset:37888\n"
    "v_mfma_f32_16x16x32_bf16 v[36:39], v[108:111], a[116:119], v[36:39]\n"
    "s_waitcnt lgkmcnt(15)\n"
    "s_waitcnt lgkmcnt(9)\n"
    "ds_read_b128 a[156:159], v242 offset:38912\n"
    "v_mfma_f32_16x16x32_bf16 v[4:7], v[84:87], v[16:19], v[4:7]\n"
    "s_add_u32 s84, s84, 0x1000\n"
    "ds_read_b64_tr_b16 v[100:101], v232 offset:0\n"
    "ds_read_b64_tr_b16 v[102:103], v232 offset:2048\n"
    "v_mfma_f32_16x16x32_bf16 v[4:7], v[92:95], a[120:123], v[4:7]\n"
    "s_waitcnt lgkmcnt(15)\n"
    "ds_read_b128 a[176:179], v243 offset:38912\n"
    "ds_read_b128 a[180:183], v241 offset:39936\n"
    "v_mfma_f32_16x16x32_bf16 v[12:15], v[92:95], a[124:127], v[12:15]\n"
    "ds_read_b64_tr_b16 v[108:109], v233 offset:0\n"
    "ds_read_b64_tr_b16 v[110:111], v233 offset:2048\n"
    "v_mfma_f32_16x16x32_bf16 v[12:15], v[84:87], v[48:51], v[12:15]\n"
    "s_waitcnt lgkmcnt(15)\n"
    "buffer_load_dwordx4 v235, s[52:55], s78 offen offset:1024 lds\n"
    "v_mfma_f32_16x16x32_bf16 v[20:23], v[84:87], v[80:83], v[20:23]\n"
    "ds_read_b128 a[184:187], v241 offset:0\n"
    "v_add_u32 v230, s88, v244\n"
    "v_mfma_f32_16x16x32_bf16 v[20:23], v[92:95], a[128:131], v[20:23]\n"
    "s_waitcnt lgkmcnt(15)\n"
    "ds_read_b128 a[188:191], v241 offset:1024\n"
    "v_add_u32 v231, s88, v245\n"
    "v_mfma_f32_16x16x32_bf16 v[28:31], v[92:95], a[132:135], v[28:31]\n"
    "s_cmp_eq_u32 s84, 0x6000\n"
    "s_cselect_b32 s84, 0x1000, s84\n"
    "v_add_u32 v232, s88, v246\n"
    "v_mfma_f32_16x16x32_bf16 v[28:31], v[84:87], v[112:115], v[28:31]\n"
    "s_waitcnt lgkmcnt(15)\n"
    "buffer_load_dwordx4 v236, s[52:55], s78 offen offset:2048 lds\n"
    "v_mfma_f32_16x16x32_bf16 v[36:39], v[84:87], v[144:147], v[36:39]\n"
    "ds_read_b128 a[192:195], v241 offset:2048\n"
    "v_mul_u32_u24 v229, 0x90, v229\n"
    "v_mfma_f32_16x16x32_bf16 v[36:39], v[92:95], a[136:139], v[36:39]\n"
    "s_waitcnt lgkmcnt(15)\n"
    "s_waitcnt vmcnt(11) lgkmcnt(3)\n"
    "s_barrier\n"
    "v_lshrrev_b32 v228, 4, v239\n"
    "v_mfma_f32_16x16x32_bf16 v[4:7], v[100:103], a[140:143], v[4:7]\n"
    "ds_read_b64_tr_b16 v[84:85], v230 offset:0\n"
    "ds_read_b64_tr_b16 v[86:87], v230 offset:2048\n"
    "v_lshl_add_u32 v229, v228, 3, v229\n"
    "v_mfma_f32_16x16x32_bf16 v[4:7], v[108:111], a[160:163], v[4:7]\n"
    "s_waitcnt lgkmcnt(15)\n"
    "buffer_load_dwordx4 v237, s[52:55], s78 offen offset:3072 lds\n"
    "v_add_u32 v233, s88, v247\n"
    "v_mfma_f32_16x16x32_bf16 v[12:15], v[108:111], a[164:167], v[12:15]\n"
    "ds_read_b64_tr_b16 v[92:93], v231 offset:0\n"
    "ds_read_b64_tr_b16 v[94:95], v231 offset:2048\n"
    "v_lshl_add_u32 v229, s72, 5, v229\n"
    "v_mfma_f32_16x16x32_bf16 v[12:15], v[100:103], a[144:147], v[12:15]\n"
    "s_waitcnt lgkmcnt(15)\n"
    "ds_read_b128 a[196:199], v241 offset:3072\n"
    "ds_read_b128 a[200:203], v242 offset:39936\n"
    "v_mfma_f32_16x16x32_bf16 v[20:23], v[100:103], a[148:151], v[20:23]\n"
    "ds_read_b128 a[220:223], v243 offset:39936\n"
    "ds_read_b128 a[204:207], v242 offset:0\n"
    "v_lshrrev_b32 v228, 3, v239\n"
    "v_mfma_f32_16x16x32_bf16 v[20:23], v[108:111], a[168:171], v[20:23]\n"
    "s_waitcnt lgkmcnt(15)\n"
    "s_add_u32 s78, s85, 0x1b000\n"
    "ds_read_b128 a[224:227], v243 offset:0\n"
    "v_mul_u32_u24 v228, 0x880, v228\n"
    "v_mfma_f32_16x16x32_bf16 v[28:31], v[108:111], a[172:175], v[28:31]\n"
    "s_add_u32 m0, s71, s84\n"
    "ds_read_b128 a[208:211], v242 offset:1024\n"
    "ds_read_b128 a[228:231], v243 offset:1024\n"
    "v_mfma_f32_16x16x32_bf16 v[28:31], v[100:103], a[152:155], v[28:31]\n"
    "s_waitcnt lgkmcnt(15)\n"
    "s_add_u32 s88, s88, 0x1000\n"
    "buffer_load_dwordx4 v234, s[52:55], s78 offen lds\n"
    "v_mfma_f32_16x16x32_bf16 v[36:39], v[100:103], a[156:159], v[36:39]\n"
    "s_cmp_eq_u32 s88, 0x6000\n"
    "s_cselect_b32 s88, 0x1000, s88\n"
    "ds_read_b128 a[212:215], v242 offset:2048\n"
    "ds_read_b128 a[232:235], v243 offset:2048\n"
    "v_mfma_f32_16x16x32_bf16 v[36:39], v[108:111], a[176:179], v[36:39]\n"
    "s_waitcnt lgkmcnt(15)\n"
    "s_waitcnt lgkmcnt(9)\n"
    "ds_read_b128 a[216:219], v242 offset:3072\n"
    "v_add_u32 v228, v228, v238\n"
    "v_mfma_f32_16x16x32_bf16 v[4:7], v[84:87], v[24:27], v[4:7]\n"
    "s_add_u32 s84, s84, 0x1000\n"
    "ds_read_b64_tr_b16 v[100:101], v232 offset:0\n"
    "ds_read_b64_tr_b16 v[102:103], v232 offset:2048\n"
    "v_mfma_f32_16x16x32_bf16 v[4:7], v[92:95], a[180:183], v[4:7]\n"
    "s_waitcnt lgkmcnt(15)\n"
    "ds_read_b128 a[236:239], v243 offset:3072\n"
    "v_add_u32 v230, s88, v244\n"
    "v_mfma_f32_16x16x32_bf16 v[12:15], v[92:95], a[184:187], v[12:15]\n"
    "s_add_u32 s87, s87, 0x80\n"
    "ds_read_b64_tr_b16 v[108:109], v233 offset:0\n"
    "ds_read_b64_tr_b16 v[110:111], v233 offset:2048\n"
    "v_mfma_f32_16x16x32_bf16 v[12:15], v[84:87], v[56:59], v[12:15]\n"
    "s_waitcnt lgkmcnt(15)\n"
    "buffer_load_dwordx4 v235, s[52:55], s78 offen offset:1024 lds\n"
    "v_mfma_f32_16x16x32_bf16 v[20:23], v[84:87], v[88:91], v[20:23]\n"
    "v_add_u32 v231, s88, v245\n"
    "v_add_u32 v232, s88, v246\n"
    "v_mfma_f32_16x16x32_bf16 v[20:23], v[92:95], a[188:191], v[20:23]\n"
    "s_waitcnt lgkmcnt(15)\n"
    "s_cmp_eq_u32 s84, 0x6000\n"
    "s_cselect_b32 s84, 0x1000, s84\n"
    "v_mfma_f32_16x16x32_bf16 v[28:31], v[92:95], a[192:195], v[28:31]\n"
    "s_cmp_eq_u32 s87, 0x480\n"
    "s_cselect_b32 s87, 0, s87\n"
    "v_mfma_f32_16x16x32_bf16 v[28:31], v[84:87], v[120:123], v[28:31]\n"
    "s_waitcnt lgkmcnt(14)\n"
    "s_add_u32 s85, s87, s86\n"
    "buffer_load_dwordx4 v236, s[52:55], s78 offen offset:2048 lds\n"
    "v_mfma_f32_16x16x32_bf16 v[36:39], v[84:87], v[152:155], v[36:39]\n"
    "s_add_u32 s80, s80, 1\n"
    "v_mfma_f32_16x16x32_bf16 v[36:39], v[92:95], a[196:199], v[36:39]\n"
    "s_waitcnt lgkmcnt(12)\n"
    "s_waitcnt vmcnt(11) lgkmcnt(0)\n"
    "s_barrier\n"
    "v_mfma_f32_16x16x32_bf16 v[4:7], v[100:103], a[200:203], v[4:7]\n"
    "ds_read_b64_tr_b16 v[84:85], v230 offset:0\n"
    "ds_read_b64_tr_b16 v[86:87], v230 offset:2048\n"
    "v_mfma_f32_16x16x32_bf16 v[4:7], v[108:111], a[220:223], v[4:7]\n"
    "s_waitcnt lgkmcnt(12)\n"
    "buffer_load_dwordx4 v237, s[52:55], s78 offen offset:3072 lds\n"
    "v_mfma_f32_16x16x32_bf16 v[12:15], v[108:111], a[224:227], v[12:15]\n"
    "ds_read_b64_tr_b16 v[92:93], v231 offset:0\n"
    "ds_read_b64_tr_b16 v[94:95], v231 offset:2048\n"
    "v_mfma_f32_16x16x32_bf16 v[12:15], v[100:103], a[204:207], v[12:15]\n"
    "s_waitcnt lgkmcnt(12)\n"
    "v_add_u32 v233, s88, v247\n"
    "v_mov_b32 v241, 0x40000000\n"
    "v_mfma_f32_16x16x32_bf16 v[20:23], v[100:103], a[208:211], v[20:23]\n"
    "s_cmp_lt_u32 s80, 9\n"
    "s_cselect_b32 s85, s85, 0x40000000\n"
    "v_mov_b32 v253, 0x40000000\n"
    "v_mfma_f32_16x16x32_bf16 v[20:23], v[108:111], a[228:231], v[20:23]\n"
    "s_waitcnt lgkmcnt(10)\n"
    "s_add_u32 s78, s85, 0\n"
    "s_add_u32 s88, s88, 0x1000\n"
    "v_cndmask_b32 v242, v241, v238, s[28:29]\n"
    "v_mfma_f32_16x16x32_bf16 v[28:31], v[108:111], a[232:235], v[28:31]\n"
    "s_add_u32 m0, s71, s84\n"
    "s_cmp_eq_u32 s88, 0x6000\n"
    "s_cselect_b32 s88, 0x1000, s88\n"
    "v_lshrrev_b32 v243, 3, v239\n"
    "v_mfma_f32_16x16x32_bf16 v[28:31], v[100:103], a[212:215], v[28:31]\n"
    "s_waitcnt lgkmcnt(6)\n"
    "buffer_load_dwordx4 v234, s[52:55], s78 offen lds\n"
    "v_lshl_add_u32 v243, v243, 4, v238\n"
    "v_mfma_f32_16x16x32_bf16 v[36:39], v[100:103], a[216:219], v[36:39]\n"
    "v_mov_b32 v254, 0xd80\n"
    "v_mfma_f32_16x16x32_bf16 v[36:39], v[108:111], a[236:239], v[36:39]\n"
    "s_waitcnt lgkmcnt(0)\n"
    "v_mad_u32_u24 v243, s72, v254, v243\n"
    "A_OP_LOOP:\n"
    "v_mfma_f32_16x16x32_bf16 v[44:47], v[84:87], v[0:3], 0\n"
    "s_add_u32 s77, s89, s90\n"
    "ds_read_b64_tr_b16 v[100:101], v232 offset:0\n"
    "ds_read_b64_tr_b16 v[102:103], v232 offset:2048\n"
    "v_mfma_f32_16x16x32_bf16 v[44:47], v[92:95], a[0:3], v[44:47]\n"
    "s_add_u32 s84, s84, 0x1000\n"
    "v_add_u32 v230, s88, v244\n"
    "v_add_u32 v231, s88, v245\n"
    "v_mfma_f32_16x16x32_bf16 v[52:55], v[92:95], a[4:7], 0\n"
    "ds_read_b64_tr_b16 v[108:109], v233 offset:0\n"
    "ds_read_b64_tr_b16 v[110:111], v233 offset:2048\n"
    "v_mfma_f32_16x16x32_bf16 v[52:55], v[84:87], v[32:35], v[52:55]\n"
    "s_cmp_eq_u32 s84, 0x6000\n"
    "s_cselect_b32 s84, 0x1000, s84\n"
    "buffer_load_dwordx4 v235, s[52:55], s78 offen offset:1024 lds\n"
    "v_mfma_f32_16x16x32_bf16 v[60:63], v[84:87], v[64:67], 0\n"
    "v_cvt_pk_bf16_f32 v116, v4, v5\n"
    "v_cvt_pk_bf16_f32 v117, v6, v7\n"
    "v_mfma_f32_16x16x32_bf16 v[60:63], v[92:95], a[8:11], v[60:63]\n"
    "v_cvt_pk_bf16_f32 v118, v12, v13\n"
    "v_cvt_pk_bf16_f32 v119, v14, v15\n"
    "v_mfma_f32_16x16x32_bf16 v[68:71], v[92:95], a[12:15], 0\n"
    "v_cvt_pk_bf16_f32 v124, v20, v21\n"
    "v_cvt_pk_bf16_f32 v125, v22, v23\n"
    "v_mfma_f32_16x16x32_bf16 v[68:71], v[84:87], v[96:99], v[68:71]\n"
    "buffer_load_dwordx4 v236, s[52:55], s78 offen offset:2048 lds\n"
    "v_add_u32 v232, s88, v246\n"
    "v_mfma_f32_16x16x32_bf16 v[76:79], v[84:87], v[128:131], 0\n"
    "v_permlane32_swap_b32 v116, v118\n"
    "v_permlane32_swap_b32 v117, v119\n"
    "v_mfma_f32_16x16x32_bf16 v[76:79], v[92:95], a[16:19], v[76:79]\n"
    "s_waitcnt vmcnt(11) lgkmcnt(0)\n"
    "s_barrier\n"
    "v_cvt_pk_bf16_f32 v126, v28, v29\n"
    "v_mfma_f32_16x16x32_bf16 v[44:47], v[100:103], a[20:23], v[44:47]\n"
    "ds_read_b64_tr_b16 v[84:85], v230 offset:0\n"
    "ds_read_b64_tr_b16 v[86:87], v230 offset:2048\n"
    "v_mfma_f32_16x16x32_bf16 v[44:47], v[108:111], a[40:43], v[44:47]\n"
    "buffer_load_dwordx4 v237, s[52:55], s78 offen offset:3072 lds\n"
    "v_add_u32 v233, s88, v247\n"
    "v_mfma_f32_16x16x32_bf16 v[52:55], v[108:111], a[44:47], v[52:55]\n"
    "ds_read_b64_tr_b16 v[92:93], v231 offset:0\n"
    "ds_read_b64_tr_b16 v[94:95], v231 offset:2048\n"
    "v_mfma_f32_16x16x32_bf16 v[52:55], v[100:103], a[24:27], v[52:55]\n"
    "s_cmp_eq_u32 s81, 6\n"
    "s_cselect_b32 s79, 0, 0x40000000\n"
    "buffer_store_dword v252, v253, s[36:39], 0 offen sc1\n"
    "v_mfma_f32_16x16x32_bf16 v[60:63], v[100:103], a[28:31], v[60:63]\n"
    "v_cvt_pk_bf16_f32 v127, v30, v31\n"
    "v_cvt_pk_bf16_f32 v132, v36, v37\n"
    "v_mfma_f32_16x16x32_bf16 v[60:63], v[108:111], a[48:51], v[60:63]\n"
    "s_add_u32 s78, s85, 0x9000\n"
    "s_add_u32 s88, s88, 0x1000\n"
    "buffer_store_dwordx4 v[116:119], v238, s[48:51], s77 offen sc1\n"
    "v_mfma_f32_16x16x32_bf16 v[68:71], v[108:111], a[52:55], v[68:71]\n"
    "s_add_u32 m0, s71, s84\n"
    "v_permlane32_swap_b32 v124, v126\n"
    "v_permlane32_swap_b32 v125, v127\n"
    "v_mfma_f32_16x16x32_bf16 v[68:71], v[100:103], a[32:35], v[68:71]\n"
    "s_cmp_eq_u32 s88, 0x6000\n"
    "s_cselect_b32 s88, 0x1000, s88\n"
    "A_TT_FIRST1:\n"
    "A_TT_S1:\n"
    "s_cbranch_execz A_TT_RET\n"
    "buffer_load_dwordx4 v234, s[52:55], s78 offen lds\n"
    "v_mfma_f32_16x16x32_bf16 v[76:79], v[100:103], a[36:39], v[76:79]\n"
    "v_cvt_pk_bf16_f32 v133, v38, v39\n"
    "v_mfma_f32_16x16x32_bf16 v[76:79], v[108:111], a[56:59], v[76:79]\n"
    "s_waitcnt lgkmcnt(0)\n"
    "buffer_store_dwordx4 v[124:127], v238, s[48:51], s77 offen offset:1024 sc1\n"
    "v_mfma_f32_16x16x32_bf16 v[44:47], v[84:87], v[8:11], v[44:47]\n"
    "s_add_u32 s84, s84, 0x1000\n"
    "ds_read_b64_tr_b16 v[100:101], v232 offset:0\n"
    "ds_read_b64_tr_b16 v[102:103], v232 offset:2048\n"
    "v_mfma_f32_16x16x32_bf16 v[44:47], v[92:95], a[60:63], v[44:47]\n"
    "v_permlane32_swap_b32 v132, v134\n"
    "v_permlane32_swap_b32 v133, v135\n"
    "v_mfma_f32_16x16x32_bf16 v[52:55], v[92:95], a[64:67], v[52:55]\n"
    "s_lshl_b32 s28, 1, s81\n"
    "ds_read_b64_tr_b16 v[108:109], v233 offset:0\n"
    "ds_read_b64_tr_b16 v[110:111], v233 offset:2048\n"
    "v_mfma_f32_16x16x32_bf16 v[52:55], v[84:87], v[40:43], v[52:55]\n"
    "s_cmp_eq_u32 s84, 0x6000\n"
    "s_cselect_b32 s84, 0x1000, s84\n"
    "buffer_load_dwordx4 v235, s[52:55], s78 offen offset:1024 lds\n"
    "v_mfma_f32_16x16x32_bf16 v[60:63], v[84:87], v[72:75], v[60:63]\n"
    "v_add_u32 v230, s88, v244\n"
    "v_add_u32 v231, s88, v245\n"
    "v_mfma_f32_16x16x32_bf16 v[60:63], v[92:95], a[68:71], v[60:63]\n"
    "s_cmp_eq_u32 s81, s61\n"
    "s_cselect_b32 s28, 0xff00, s28\n"
    "buffer_store_dwordx4 v[132:135], v242, s[48:51], s77 offen offset:2048 sc1\n"
    "v_mfma_f32_16x16x32_bf16 v[68:71], v[92:95], a[72:75], v[68:71]\n"
    "v_add_u32 v232, s88, v246\n"
    "v_cndmask_b32 v253, v241, v250, s[28:29]\n"
    "v_mfma_f32_16x16x32_bf16 v[68:71], v[84:87], v[104:107], v[68:71]\n"
    "buffer_load_dwordx4 v236, s[52:55], s78 offen offset:2048 lds\n"
    "v_mfma_f32_16x16x32_bf16 v[76:79], v[84:87], v[136:139], v[76:79]\n"
    "v_mfma_f32_16x16x32_bf16 v[76:79], v[92:95], a[76:79], v[76:79]\n"
    "s_waitcnt vmcnt(15) lgkmcnt(0)\n"
    "s_barrier\n"
    "v_add_u32 v233, s88, v247\n"
    "v_mfma_f32_16x16x32_bf16 v[44:47], v[100:103], a[80:83], v[44:47]\n"
    "ds_read_b64_tr_b16 v[84:85], v230 offset:0\n"
    "ds_read_b64_tr_b16 v[86:87], v230 offset:2048\n"
    "v_mfma_f32_16x16x32_bf16 v[44:47], v[108:111], a[100:103], v[44:47]\n"
    "buffer_load_dwordx4 v237, s[52:55], s78 offen offset:3072 lds\n"
    "v_mfma_f32_16x16x32_bf16 v[52:55], v[108:111], a[104:107], v[52:55]\n"
    "ds_read_b64_tr_b16 v[92:93], v231 offset:0\n"
    "ds_read_b64_tr_b16 v[94:95], v231 offset:2048\n"
    "v_mfma_f32_16x16x32_bf16 v[52:55], v[100:103], a[84:87], v[52:55]\n"
    "v_mfma_f32_16x16x32_bf16 v[60:63], v[100:103], a[88:91], v[60:63]\n"
    "v_mfma_f32_16x16x32_bf16 v[60:63], v[108:111], a[108:111], v[60:63]\n"
    "s_add_u32 s78, s85, 0x12000\n"
    "s_add_u32 s88, s88, 0x1000\n"
    "buffer_load_dword v254, v249, s[36:39], s79 offen sc1\n"
    "v_mfma_f32_16x16x32_bf16 v[68:71], v[108:111], a[112:115], v[68:71]\n"
    "s_add_u32 m0, s71, s84\n"
    "s_cmp_eq_u32 s88, 0x6000\n"
    "s_cselect_b32 s88, 0x1000, s88\n"
    "v_mfma_f32_16x16x32_bf16 v[68:71], v[100:103], a[92:95], v[68:71]\n"
    "buffer_load_dwordx4 v234, s[52:55], s78 offen lds\n"
    "v_mfma_f32_16x16x32_bf16 v[76:79], v[100:103], a[96:99], v[76:79]\n"
    "v_mfma_f32_16x16x32_bf16 v[76:79], v[108:111], a[116:119], v[76:79]\n"
    "s_waitcnt lgkmcnt(0)\n"
    "v_mfma_f32_16x16x32_bf16 v[44:47], v[84:87], v[16:19], v[44:47]\n"
    "s_add_u32 s84, s84, 0x1000\n"
    "ds_read_b64_tr_b16 v[100:101], v232 offset:0\n"
    "ds_read_b64_tr_b16 v[102:103], v232 offset:2048\n"
    "v_mfma_f32_16x16x32_bf16 v[44:47], v[92:95], a[120:123], v[44:47]\n"
    "s_add_u32 s89, s89, 0x14000\n"
    "v_add_u32 v230, s88, v244\n"
    "v_add_u32 v231, s88, v245\n"
    "v_mfma_f32_16x16x32_bf16 v[52:55], v[92:95], a[124:127], v[52:55]\n"
    "ds_read_b64_tr_b16 v[108:109], v233 offset:0\n"
    "ds_read_b64_tr_b16 v[110:111], v233 offset:2048\n"
    "v_mfma_f32_16x16x32_bf16 v[52:55], v[84:87], v[48:51], v[52:55]\n"
    "s_cmp_eq_u32 s84, 0x6000\n"
    "s_cselect_b32 s84, 0x1000, s84\n"
    "buffer_load_dwordx4 v235, s[52:55], s78 offen offset:1024 lds\n"
    "v_mfma_f32_16x16x32_bf16 v[60:63], v[84:87], v[80:83], v[60:63]\n"
    "s_cmp_eq_u32 s89, 0xb4000\n"
    "s_cselect_b32 s89, 0, s89\n"
    "v_add_u32 v232, s88, v246\n"
    "v_mfma_f32_16x16x32_bf16 v[60:63], v[92:95], a[128:131], v[60:63]\n"
    "s_add_u32 s90, s90, 0x2800\n"
    "v_mfma_f32_16x16x32_bf16 v[68:71], v[92:95], a[132:135], v[68:71]\n"
    "s_add_u32 s81, s81, 1\n"
    "v_mfma_f32_16x16x32_bf16 v[68:71], v[84:87], v[112:115], v[68:71]\n"
    "buffer_load_dwordx4 v236, s[52:55], s78 offen offset:2048 lds\n"
    "v_mfma_f32_16x16x32_bf16 v[76:79], v[84:87], v[144:147], v[76:79]\n"
    "v_mfma_f32_16x16x32_bf16 v[76:79], v[92:95], a[136:139], v[76:79]\n"
    "s_waitcnt vmcnt(16) lgkmcnt(0)\n"
    "s_barrier\n"
    "v_add_u32 v233, s88, v247\n"
    "v_mfma_f32_16x16x32_bf16 v[44:47], v[100:103], a[140:143], v[44:47]\n"
    "ds_read_b64_tr_b16 v[84:85], v230 offset:0\n"
    "ds_read_b64_tr_b16 v[86:87], v230 offset:2048\n"
    "v_mfma_f32_16x16x32_bf16 v[44:47], v[108:111], a[160:163], v[44:47]\n"
    "buffer_load_dwordx4 v237, s[52:55], s78 offen offset:3072 lds\n"
    "v_mfma_f32_16x16x32_bf16 v[52:55], v[108:111], a[164:167], v[52:55]\n"
    "ds_read_b64_tr_b16 v[92:93], v231 offset:0\n"
    "ds_read_b64_tr_b16 v[94:95], v231 offset:2048\n"
    "v_mfma_f32_16x16x32_bf16 v[52:55], v[100:103], a[144:147], v[52:55]\n"
    "v_mfma_f32_16x16x32_bf16 v[60:63], v[100:103], a[148:151], v[60:63]\n"
    "v_mfma_f32_16x16x32_bf16 v[60:63], v[108:111], a[168:171], v[60:63]\n"
    "s_add_u32 s78, s85, 0x1b000\n"
    "s_add_u32 s88, s88, 0x1000\n"
    "v_mfma_f32_16x16x32_bf16 v[68:71], v[108:111], a[172:175], v[68:71]\n"
    "s_add_u32 m0, s71, s84\n"
    "s_cmp_eq_u32 s88, 0x6000\n"
    "s_cselect_b32 s88, 0x1000, s88\n"
    "v_mfma_f32_16x16x32_bf16 v[68:71], v[100:103], a[152:155], v[68:71]\n"
    "s_add_u32 s87, s87, 0x80\n"
    "buffer_load_dwordx4 v234, s[52:55], s78 offen lds\n"
    "v_mfma_f32_16x16x32_bf16 v[76:79], v[100:103], a[156:159], v[76:79]\n"
    "s_cmp_eq_u32 s87, 0x480\n"
    "s_cselect_b32 s87, 0, s87\n"
    "v_mfma_f32_16x16x32_bf16 v[76:79], v[108:111], a[176:179], v[76:79]\n"
    "s_waitcnt lgkmcnt(0)\n"
    "s_add_u32 s85, s87, s86\n"
    "v_mfma_f32_16x16x32_bf16 v[44:47], v[84:87], v[24:27], v[44:47]\n"
    "s_add_u32 s84, s84, 0x1000\n"
    "ds_read_b64_tr_b16 v[100:101], v232 offset:0\n"
    "ds_read_b64_tr_b16 v[102:103], v232 offset:2048\n"
    "v_mfma_f32_16x16x32_bf16 v[44:47], v[92:95], a[180:183], v[44:47]\n"
    "s_add_u32 s80, s80, 1\n"
    "v_add_u32 v230, s88, v244\n"
    "v_add_u32 v231, s88, v245\n"
    "v_mfma_f32_16x16x32_bf16 v[52:55], v[92:95], a[184:187], v[52:55]\n"
    "ds_read_b64_tr_b16 v[108:109], v233 offset:0\n"
    "ds_read_b64_tr_b16 v[110:111], v233 offset:2048\n"
    "v_mfma_f32_16x16x32_bf16 v[52:55], v[84:87], v[56:59], v[52:55]\n"
    "s_cmp_eq_u32 s84, 0x6000\n"
    "s_cselect_b32 s84, 0x1000, s84\n"
    "buffer_load_dwordx4 v235, s[52:55], s78 offen offset:1024 lds\n"
    "v_mfma_f32_16x16x32_bf16 v[60:63], v[84:87], v[88:91], v[60:63]\n"
    "s_cmp_lt_u32 s80, 9\n"
    "s_cselect_b32 s85, s85, 0x40000000\n"
    "v_add_u32 v232, s88, v246\n"
    "v_mfma_f32_16x16x32_bf16 v[60:63], v[92:95], a[188:191], v[60:63]\n"
    "v_mfma_f32_16x16x32_bf16 v[68:71], v[92:95], a[192:195], v[68:71]\n"
    "v_mfma_f32_16x16x32_bf16 v[68:71], v[84:87], v[120:123], v[68:71]\n"
    "buffer_load_dwordx4 v236, s[52:55], s78 offen offset:2048 lds\n"
    "v_mfma_f32_16x16x32_bf16 v[76:79], v[84:87], v[152:155], v[76:79]\n"
    "v_mfma_f32_16x16x32_bf16 v[76:79], v[92:95], a[196:199], v[76:79]\n"
    "s_waitcnt vmcnt(16) lgkmcnt(0)\n"
    "s_barrier\n"
    "v_add_u32 v233, s88, v247\n"
    "v_mfma_f32_16x16x32_bf16 v[44:47], v[100:103], a[200:203], v[44:47]\n"
    "ds_read_b64_tr_b16 v[84:85], v230 offset:0\n"
    "ds_read_b64_tr_b16 v[86:87], v230 offset:2048\n"
    "v_mfma_f32_16x16x32_bf16 v[44:47], v[108:111], a[220:223], v[44:47]\n"
    "buffer_load_dwordx4 v237, s[52:55], s78 offen offset:3072 lds\n"
    "v_mfma_f32_16x16x32_bf16 v[52:55], v[108:111], a[224:227], v[52:55]\n"
    "ds_read_b64_tr_b16 v[92:93], v231 offset:0\n"
    "ds_read_b64_tr_b16 v[94:95], v231 offset:2048\n"
    "v_mfma_f32_16x16x32_bf16 v[52:55], v[100:103], a[204:207], v[52:55]\n"
    "v_mfma_f32_16x16x32_bf16 v[60:63], v[100:103], a[208:211], v[60:63]\n"
    "v_mfma_f32_16x16x32_bf16 v[60:63], v[108:111], a[228:231], v[60:63]\n"
    "s_add_u32 s78, s85, 0\n"
    "s_add_u32 s88, s88, 0x1000\n"
    "v_mfma_f32_16x16x32_bf16 v[68:71], v[108:111], a[232:235], v[68:71]\n"
    "s_add_u32 m0, s71, s84\n"
    "s_cmp_eq_u32 s88, 0x6000\n"
    "s_cselect_b32 s88, 0x1000, s88\n"
    "v_mfma_f32_16x16x32_bf16 v[68:71], v[100:103], a[212:215], v[68:71]\n"
    "buffer_load_dwordx4 v234, s[52:55], s78 offen lds\n"
    "v_mfma_f32_16x16x32_bf16 v[76:79], v[100:103], a[216:219], v[76:79]\n"
    "v_mfma_f32_16x16x32_bf16 v[76:79], v[108:111], a[236:239], v[76:79]\n"
    "s_waitcnt lgkmcnt(0)\n"
    "s_cmp_eq_u32 s81, 7\n"
    "s_cbranch_scc1 A_OP_LAST\n"
    "v_mfma_f32_16x16x32_bf16 v[4:7], v[84:87], v[0:3], 0\n"
    "s_add_u32 s77, s89, s90\n"
    "ds_read_b64_tr_b16 v[100:101], v232 offset:0\n"
    "ds_read_b64_tr_b16 v[102:103], v232 offset:2048\n"
    "v_mfma_f32_16x16x32_bf16 v[4:7], v[92:95], a[0:3], v[4:7]\n"
    "s_add_u32 s84, s84, 0x1000\n"
    "v_add_u32 v230, s88, v244\n"
    "v_add_u32 v231, s88, v245\n"
    "v_mfma_f32_16x16x32_bf16 v[12:15], v[92:95], a[4:7], 0\n"
    "ds_read_b64_tr_b16 v[108:109], v233 offset:0\n"
    "ds_read_b64_tr_b16 v[110:111], v233 offset:2048\n"
    "v_mfma_f32_16x16x32_bf16 v[12:15], v[84:87], v[32:35], v[12:15]\n"
    "s_cmp_eq_u32 s84, 0x6000\n"
    "s_cselect_b32 s84, 0x1000, s84\n"
    "buffer_load_dwordx4 v235, s[52:55], s78 offen offset:1024 lds\n"
    "v_mfma_f32_16x16x32_bf16 v[20:23], v[84:87], v[64:67], 0\n"
    "v_cvt_pk_bf16_f32 v116, v44, v45\n"
    "v_cvt_pk_bf16_f32 v117, v46, v47\n"
    "v_mfma_f32_16x16x32_bf16 v[20:23], v[92:95], a[8:11], v[20:23]\n"
    "v_cvt_pk_bf16_f32 v118, v52, v53\n"
    "v_cvt_pk_bf16_f32 v119, v54, v55\n"
    "v_mfma_f32_16x16x32_bf16 v[28:31], v[92:95], a[12:15], 0\n"
    "v_cvt_pk_bf16_f32 v124, v60, v61\n"
    "v_cvt_pk_bf16_f32 v125, v62, v63\n"
    "v_mfma_f32_16x16x32_bf16 v[28:31], v[84:87], v[96:99], v[28:31]\n"
    "buffer_load_dwordx4 v236, s[52:55], s78 offen offset:2048 lds\n"
    "v_add_u32 v232, s88, v246\n"
    "v_mfma_f32_16x16x32_bf16 v[36:39], v[84:87], v[128:131], 0\n"
    "v_permlane32_swap_b32 v116, v118\n"
    "v_permlane32_swap_b32 v117, v119\n"
    "v_mfma_f32_16x16x32_bf16 v[36:39], v[92:95], a[16:19], v[36:39]\n"
    "s_waitcnt vmcnt(12) lgkmcnt(0)\n"
    "s_barrier\n"
    "v_cvt_pk_bf16_f32 v126, v68, v69\n"
    "v_mfma_f32_16x16x32_bf16 v[4:7], v[100:103], a[20:23], v[4:7]\n"
    "ds_read_b64_tr_b16 v[84:85], v230 offset:0\n"
    "ds_read_b64_tr_b16 v[86:87], v230 offset:2048\n"
    "v_mfma_f32_16x16x32_bf16 v[4:7], v[108:111], a[40:43], v[4:7]\n"
    "buffer_load_dwordx4 v237, s[52:55], s78 offen offset:3072 lds\n"
    "v_add_u32 v233, s88, v247\n"
    "v_mfma_f32_16x16x32_bf16 v[12:15], v[108:111], a[44:47], v[12:15]\n"
    "ds_read_b64_tr_b16 v[92:93], v231 offset:0\n"
    "ds_read_b64_tr_b16 v[94:95], v231 offset:2048\n"
    "v_mfma_f32_16x16x32_bf16 v[12:15], v[100:103], a[24:27], v[12:15]\n"
    "buffer_store_dword v252, v253, s[36:39], 0 offen sc1\n"
    "v_mfma_f32_16x16x32_bf16 v[20:23], v[100:103], a[28:31], v[20:23]\n"
    "v_cvt_pk_bf16_f32 v127, v70, v71\n"
    "v_cvt_pk_bf16_f32 v132, v76, v77\n"
    "v_mfma_f32_16x16x32_bf16 v[20:23], v[108:111], a[48:51], v[20:23]\n"
    "s_add_u32 s78, s85, 0x9000\n"
    "s_add_u32 s88, s88, 0x1000\n"
    "buffer_store_dwordx4 v[116:119], v238, s[48:51], s77 offen sc1\n"
    "v_mfma_f32_16x16x32_bf16 v[28:31], v[108:111], a[52:55], v[28:31]\n"
    "s_add_u32 m0, s71, s84\n"
    "v_permlane32_swap_b32 v124, v126\n"
    "v_permlane32_swap_b32 v125, v127\n"
    "v_mfma_f32_16x16x32_bf16 v[28:31], v[100:103], a[32:35], v[28:31]\n"
    "s_cmp_eq_u32 s88, 0x6000\n"
    "s_cselect_b32 s88, 0x1000, s88\n"
    "buffer_load_dwordx4 v234, s[52:55], s78 offen lds\n"
    "v_mfma_f32_16x16x32_bf16 v[36:39], v[100:103], a[36:39], v[36:39]\n"
    "v_cvt_pk_bf16_f32 v133, v78, v79\n"
    "v_mfma_f32_16x16x32_bf16 v[36:39], v[108:111], a[56:59], v[36:39]\n"
    "s_waitcnt lgkmcnt(0)\n"
    "buffer_store_dwordx4 v[124:127], v238, s[48:51], s77 offen offset:1024 sc1\n"
    "v_mfma_f32_16x16x32_bf16 v[4:7], v[84:87], v[8:11], v[4:7]\n"
    "s_add_u32 s84, s84, 0x1000\n"
    "ds_read_b64_tr_b16 v[100:101], v232 offset:0\n"
    "ds_read_b64_tr_b16 v[102:103], v232 offset:2048\n"
    "v_mfma_f32_16x16x32_bf16 v[4:7], v[92:95], a[60:63], v[4:7]\n"
    "v_permlane32_swap_b32 v132, v134\n"
    "v_permlane32_swap_b32 v133, v135\n"
    "v_mfma_f32_16x16x32_bf16 v[12:15], v[92:95], a[64:67], v[12:15]\n"
    "s_lshl_b32 s28, 1, s81\n"
    "ds_read_b64_tr_b16 v[108:109], v233 offset:0\n"
    "ds_read_b64_tr_b16 v[110:111], v233 offset:2048\n"
    "v_mfma_f32_16x16x32_bf16 v[12:15], v[84:87], v[40:43], v[12:15]\n"
    "s_cmp_eq_u32 s84, 0x6000\n"
    "s_cselect_b32 s84, 0x1000, s84\n"
    "buffer_load_dwordx4 v235, s[52:55], s78 offen offset:1024 lds\n"
    "v_mfma_f32_16x16x32_bf16 v[20:23], v[84:87], v[72:75], v[20:23]\n"
    "v_add_u32 v230, s88, v244\n"
    "v_add_u32 v231, s88, v245\n"
    "v_mfma_f32_16x16x32_bf16 v[20:23], v[92:95], a[68:71], v[20:23]\n"
    "s_cmp_eq_u32 s81, s61\n"
    "s_cselect_b32 s28, 0xff00, s28\n"
    "buffer_store_dwordx4 v[132:135], v242, s[48:51], s77 offen offset:2048 sc1\n"
    "v_mfma_f32_16x16x32_bf16 v[28:31], v[92:95], a[72:75], v[28:31]\n"
    "v_add_u32 v232, s88, v246\n"
    "v_cndmask_b32 v253, v241, v250, s[28:29]\n"
    "v_mfma_f32_16x16x32_bf16 v[28:31], v[84:87], v[104:107], v[28:31]\n"
    "buffer_load_dwordx4 v236, s[52:55], s78 offen offset:2048 lds\n"
    "v_mfma_f32_16x16x32_bf16 v[36:39], v[84:87], v[136:139], v[36:39]\n"
    "v_mfma_f32_16x16x32_bf16 v[36:39], v[92:95], a[76:79], v[36:39]\n"
    "s_waitcnt vmcnt(15) lgkmcnt(0)\n"
    "s_barrier\n"
    "v_add_u32 v233, s88, v247\n"
    "v_mfma_f32_16x16x32_bf16 v[4:7], v[100:103], a[80:83], v[4:7]\n"
    "ds_read_b64_tr_b16 v[84:85], v230 offset:0\n"
    "ds_read_b64_tr_b16 v[86:87], v230 offset:2048\n"
    "v_mfma_f32_16x16x32_bf16 v[4:7], v[108:111], a[100:103], v[4:7]\n"
    "buffer_load_dwordx4 v237, s[52:55], s78 offen offset:3072 lds\n"
    "v_mfma_f32_16x16x32_bf16 v[12:15], v[108:111], a[104:107], v[12:15]\n"
    "ds_read_b64_tr_b16 v[92:93], v231 offset:0\n"
    "ds_read_b64_tr_b16 v[94:95], v231 offset:2048\n"
    "v_mfma_f32_16x16x32_bf16 v[12:15], v[100:103], a[84:87], v[12:15]\n"
    "v_mfma_f32_16x16x32_bf16 v[20:23], v[100:103], a[88:91], v[20:23]\n"
    "v_mfma_f32_16x16x32_bf16 v[20:23], v[108:111], a[108:111], v[20:23]\n"
    "s_add_u32 s78, s85, 0x12000\n"
    "s_add_u32 s88, s88, 0x1000\n"
    "v_mfma_f32_16x16x32_bf16 v[28:31], v[108:111], a[112:115], v[28:31]\n"
    "s_add_u32 m0, s71, s84\n"
    "s_cmp_eq_u32 s88, 0x6000\n"
    "s_cselect_b32 s88, 0x1000, s88\n"
    "v_mfma_f32_16x16x32_bf16 v[28:31], v[100:103], a[92:95], v[28:31]\n"
    "buffer_load_dwordx4 v234, s[52:55], s78 offen lds\n"
    "v_mfma_f32_16x16x32_bf16 v[36:39], v[100:103], a[96:99], v[36:39]\n"
    "v_mfma_f32_16x16x32_bf16 v[36:39], v[108:111], a[116:119], v[36:39]\n"
    "s_waitcnt lgkmcnt(0)\n"
    "v_mfma_f32_16x16x32_bf16 v[4:7], v[84:87], v[16:19], v[4:7]\n"
    "s_add_u32 s84, s84, 0x1000\n"
    "ds_read_b64_tr_b16 v[100:101], v232 offset:0\n"
    "ds_read_b64_tr_b16 v[102:103], v232 offset:2048\n"
    "v_mfma_f32_16x16x32_bf16 v[4:7], v[92:95], a[120:123], v[4:7]\n"
    "s_add_u32 s89, s89, 0x14000\n"
    "v_add_u32 v230, s88, v244\n"
    "v_add_u32 v231, s88, v245\n"
    "v_mfma_f32_16x16x32_bf16 v[12:15], v[92:95], a[124:127], v[12:15]\n"
    "ds_read_b64_tr_b16 v[108:109], v233 offset:0\n"
    "ds_read_b64_tr_b16 v[110:111], v233 offset:2048\n"
    "v_mfma_f32_16x16x32_bf16 v[12:15], v[84:87], v[48:51], v[12:15]\n"
    "s_cmp_eq_u32 s84, 0x6000\n"
    "s_cselect_b32 s84, 0x1000, s84\n"
    "buffer_load_dwordx4 v235, s[52:55], s78 offen offset:1024 lds\n"
    "v_mfma_f32_16x16x32_bf16 v[20:23], v[84:87], v[80:83], v[20:23]\n"
    "s_cmp_eq_u32 s89, 0xb4000\n"
    "s_cselect_b32 s89, 0, s89\n"
    "v_add_u32 v232, s88, v246\n"
    "v_mfma_f32_16x16x32_bf16 v[20:23], v[92:95], a[128:131], v[20:23]\n"
    "s_add_u32 s90, s90, 0x2800\n"
    "v_mfma_f32_16x16x32_bf16 v[28:31], v[92:95], a[132:135], v[28:31]\n"
    "s_add_u32 s81, s81, 1\n"
    "v_mfma_f32_16x16x32_bf16 v[28:31], v[84:87], v[112:115], v[28:31]\n"
    "buffer_load_dwordx4 v236, s[52:55], s78 offen offset:2048 lds\n"
    "v_mfma_f32_16x16x32_bf16 v[36:39], v[84:87], v[144:147], v[36:39]\n"
    "v_mfma_f32_16x16x32_bf16 v[36:39], v[92:95], a[136:139], v[36:39]\n"
    "s_waitcnt vmcnt(15) lgkmcnt(0)\n"
    "s_barrier\n"
    "v_add_u32 v233, s88, v247\n"
    "v_mfma_f32_16x16x32_bf16 v[4:7], v[100:103], a[140:143], v[4:7]\n"
    "ds_read_b64_tr_b16 v[84:85], v230 offset:0\n"
    "ds_read_b64_tr_b16 v[86:87], v230 offset:2048\n"
    "v_mfma_f32_16x16x32_bf16 v[4:7], v[108:111], a[160:163], v[4:7]\n"
    "buffer_load_dwordx4 v237, s[52:55], s78 offen offset:3072 lds\n"
    "v_mfma_f32_16x16x32_bf16 v[12:15], v[108:111], a[164:167], v[12:15]\n"
    "ds_read_b64_tr_b16 v[92:93], v231 offset:0\n"
    "ds_read_b64_tr_b16 v[94:95], v231 offset:2048\n"
    "v_mfma_f32_16x16x32_bf16 v[12:15], v[100:103], a[144:147], v[12:15]\n"
    "v_mfma_f32_16x16x32_bf16 v[20:23], v[100:103], a[148:151], v[20:23]\n"
    "v_mfma_f32_16x16x32_bf16 v[20:23], v[108:111], a[168:171], v[20:23]\n"
    "s_add_u32 s78, s85, 0x1b000\n"
    "s_add_u32 s88, s88, 0x1000\n"
    "v_mfma_f32_16x16x32_bf16 v[28:31], v[108:111], a[172:175], v[28:31]\n"
    "s_add_u32 m0, s71, s84\n"
    "s_cmp_eq_u32 s88, 0x6000\n"
    "s_cselect_b32 s88, 0x1000, s88\n"
    "v_mfma_f32_16x16x32_bf16 v[28:31], v[100:103], a[152:155], v[28:31]\n"
    "s_add_u32 s87, s87, 0x80\n"
    "buffer_load_dwordx4 v234, s[52:55], s78 offen lds\n"
    "v_mfma_f32_16x16x32_bf16 v[36:39], v[100:103], a[156:159], v[36:39]\n"
    "s_cmp_eq_u32 s87, 0x480\n"
    "s_cselect_b32 s87, 0, s87\n"
    "v_mfma_f32_16x16x32_bf16 v[36:39], v[108:111], a[176:179], v[36:39]\n"
    "s_waitcnt lgkmcnt(0)\n"
    "s_add_u32 s85, s87, s86\n"
    "v_mfma_f32_16x16x32_bf16 v[4:7], v[84:87], v[24:27], v[4:7]\n"
    "s_add_u32 s84, s84, 0x1000\n"
    "ds_read_b64_tr_b16 v[100:101], v232 offset:0\n"
    "ds_read_b64_tr_b16 v[102:103], v232 offset:2048\n"
    "v_mfma_f32_16x16x32_bf16 v[4:7], v[92:95], a[180:183], v[4:7]\n"
    "s_add_u32 s80, s80, 1\n"
    "v_add_u32 v230, s88, v244\n"
    "v_add_u32 v231, s88, v245\n"
    "v_mfma_f32_16x16x32_bf16 v[12:15], v[92:95], a[184:187], v[12:15]\n"
    "ds_read_b64_tr_b16 v[108:109], v233 offset:0\n"
    "ds_read_b64_tr_b16 v[110:111], v233 offset:2048\n"
    "v_mfma_f32_16x16x32_bf16 v[12:15], v[84:87], v[56:59], v[12:15]\n"
    "s_cmp_eq_u32 s84, 0x6000\n"
    "s_cselect_b32 s84, 0x1000, s84\n"
    "buffer_load_dwordx4 v235, s[52:55], s78 offen offset:1024 lds\n"
    "v_mfma_f32_16x16x32_bf16 v[20:23], v[84:87], v[88:91], v[20:23]\n"
    "s_cmp_lt_u32 s80, 9\n"
    "s_cselect_b32 s85, s85, 0x40000000\n"
    "v_add_u32 v232, s88, v246\n"
    "v_mfma_f32_16x16x32_bf16 v[20:23], v[92:95], a[188:191], v[20:23]\n"
    "v_mfma_f32_16x16x32_bf16 v[28:31], v[92:95], a[192:195], v[28:31]\n"
    "v_mfma_f32_16x16x32_bf16 v[28:31], v[84:87], v[120:123], v[28:31]\n"
    "buffer_load_dwordx4 v236, s[52:55], s78 offen offset:2048 lds\n"
    "v_mfma_f32_16x16x32_bf16 v[36:39], v[84:87], v[152:155], v[36:39]\n"
    "v_mfma_f32_16x16x32_bf16 v[36:39], v[92:95], a[196:199], v[36:39]\n"
    "s_waitcnt vmcnt(15) lgkmcnt(0)\n"
    "s_barrier\n"
    "v_add_u32 v233, s88, v247\n"
    "v_mfma_f32_16x16x32_bf16 v[4:7], v[100:103], a[200:203], v[4:7]\n"
    "ds_read_b64_tr_b16 v[84:85], v230 offset:0\n"
    "ds_read_b64_tr_b16 v[86:87], v230 offset:2048\n"
    "v_mfma_f32_16x16x32_bf16 v[4:7], v[108:111], a[220:223], v[4:7]\n"
    "buffer_load_dwordx4 v237, s[52:55], s78 offen offset:3072 lds\n"
    "v_mfma_f32_16x16x32_bf16 v[12:15], v[108:111], a[224:227], v[12:15]\n"
    "ds_read_b64_tr_b16 v[92:93], v231 offset:0\n"
    "ds_read_b64_tr_b16 v[94:95], v231 offset:2048\n"
    "v_mfma_f32_16x16x32_bf16 v[12:15], v[100:103], a[204:207], v[12:15]\n"
    "v_mfma_f32_16x16x32_bf16 v[20:23], v[100:103], a[208:211], v[20:23]\n"
    "v_mfma_f32_16x16x32_bf16 v[20:23], v[108:111], a[228:231], v[20:23]\n"
    "s_add_u32 s78, s85, 0\n"
    "s_add_u32 s88, s88, 0x1000\n"
    "v_mfma_f32_16x16x32_bf16 v[28:31], v[108:111], a[232:235], v[28:31]\n"
    "s_add_u32 m0, s71, s84\n"
    "s_cmp_eq_u32 s88, 0x6000\n"
    "s_cselect_b32 s88, 0x1000, s88\n"
    "v_mfma_f32_16x16x32_bf16 v[28:31], v[100:103], a[212:215], v[28:31]\n"
    "buffer_load_dwordx4 v234, s[52:55], s78 offen lds\n"
    "v_mfma_f32_16x16x32_bf16 v[36:39], v[100:103], a[216:219], v[36:39]\n"
    "v_mfma_f32_16x16x32_bf16 v[36:39], v[108:111], a[236:239], v[36:39]\n"
    "s_waitcnt lgkmcnt(0)\n"
    "s_branch A_OP_LOOP\n"
    "A_OP_LAST:\n"
    "s_waitcnt vmcnt(9)\n"
    "s_mov_b32 s65, 0\n"
    "A_C1_CHK:\n"
    "v_cmp_eq_u32 vcc, v254, v251\n"
    "s_or_b32 s63, vcc_lo, s60\n"
    "s_andn2_b32 s76, 0xf0f, s63\n"
    "s_cbranch_scc1 A_C1_SLOW\n"
    "A_C1_OK:\n"
    "v_mfma_f32_16x16x32_bf16 v[4:7], v[84:87], v[0:3], 0\n"
    "s_add_u32 s77, s89, s90\n"
    "ds_read_b64_tr_b16 v[100:101], v232 offset:0\n"
    "ds_read_b64_tr_b16 v[102:103], v232 offset:2048\n"
    "v_mfma_f32_16x16x32_bf16 v[4:7], v[92:95], a[0:3], v[4:7]\n"
    "v_add_u32 v230, s88, v244\n"
    "v_add_u32 v231, s88, v245\n"
    "v_mfma_f32_16x16x32_bf16 v[12:15], v[92:95], a[4:7], 0\n"
    "ds_read_b64_tr_b16 v[108:109], v233 offset:0\n"
    "ds_read_b64_tr_b16 v[110:111], v233 offset:2048\n"
    "v_mfma_f32_16x16x32_bf16 v[12:15], v[84:87], v[32:35], v[12:15]\n"
    "v_cvt_pk_bf16_f32 v116, v44, v45\n"
    "v_cvt_pk_bf16_f32 v117, v46, v47\n"
    "v_mfma_f32_16x16x32_bf16 v[20:23], v[84:87], v[64:67], 0\n"
    "v_cvt_pk_bf16_f32 v118, v52, v53\n"
    "v_cvt_pk_bf16_f32 v119, v54, v55\n"
    "v_mfma_f32_16x16x32_bf16 v[20:23], v[92:95], a[8:11], v[20:23]\n"
    "v_cvt_pk_bf16_f32 v124, v60, v61\n"
    "v_cvt_pk_bf16_f32 v125, v62, v63\n"
    "v_mfma_f32_16x16x32_bf16 v[28:31], v[92:95], a[12:15], 0\n"
    "v_permlane32_swap_b32 v116, v118\n"
    "v_permlane32_swap_b32 v117, v119\n"
    "v_mfma_f32_16x16x32_bf16 v[28:31], v[84:87], v[96:99], v[28:31]\n"
    "buffer_store_dwordx4 v[116:119], v238, s[48:51], s77 offen sc1\n"
    "v_add_u32 v232, s88, v246\n"
    "v_mfma_f32_16x16x32_bf16 v[36:39], v[84:87], v[128:131], 0\n"
    "v_cvt_pk_bf16_f32 v126, v68, v69\n"
    "v_cvt_pk_bf16_f32 v127, v70, v71\n"
    "v_mfma_f32_16x16x32_bf16 v[36:39], v[92:95], a[16:19], v[36:39]\n"
    "s_waitcnt vmcnt(11) lgkmcnt(0)\n"
    "s_barrier\n"
    "v_cvt_pk_bf16_f32 v132, v76, v77\n"
    "v_mfma_f32_16x16x32_bf16 v[4:7], v[100:103], a[20:23], v[4:7]\n"
    "ds_read_b64_tr_b16 v[84:85], v230 offset:0\n"
    "ds_read_b64_tr_b16 v[86:87], v230 offset:2048\n"
    "v_permlane32_swap_b32 v124, v126\n"
    "v_permlane32_swap_b32 v125, v127\n"
    "v_mfma_f32_16x16x32_bf16 v[4:7], v[108:111], a[40:43], v[4:7]\n"
    "buffer_store_dwordx4 v[124:127], v238, s[48:51], s77 offen offset:1024 sc1\n"
    "v_cvt_pk_bf16_f32 v133, v78, v79\n"
    "v_mfma_f32_16x16x32_bf16 v[12:15], v[108:111], a[44:47], v[12:15]\n"
    "ds_read_b64_tr_b16 v[92:93], v231 offset:0\n"
    "ds_read_b64_tr_b16 v[94:95], v231 offset:2048\n"
    "v_mfma_f32_16x16x32_bf16 v[12:15], v[100:103], a[24:27], v[12:15]\n"
    "buffer_store_dword v252, v253, s[36:39], 0 offen sc1\n"
    "v_add_u32 v233, s88, v247\n"
    "v_mfma_f32_16x16x32_bf16 v[20:23], v[100:103], a[28:31], v[20:23]\n"
    "v_permlane32_swap_b32 v132, v134\n"
    "v_permlane32_swap_b32 v133, v135\n"
    "v_mfma_f32_16x16x32_bf16 v[20:23], v[108:111], a[48:51], v[20:23]\n"
    "s_add_u32 s88, s88, 0x1000\n"
    "buffer_store_dwordx4 v[132:135], v242, s[48:51], s77 offen offset:2048 sc1\n"
    "v_mfma_f32_16x16x32_bf16 v[28:31], v[108:111], a[52:55], v[28:31]\n"
    "s_cmp_eq_u32 s88, 0x6000\n"
    "s_cselect_b32 s88, 0x1000, s88\n"
    "v_mfma_f32_16x16x32_bf16 v[28:31], v[100:103], a[32:35], v[28:31]\n"
    "s_lshl_b32 s28, 1, s81\n"
    "v_mfma_f32_16x16x32_bf16 v[36:39], v[100:103], a[36:39], v[36:39]\n"
    "s_cmp_eq_u32 s81, s61\n"
    "s_cselect_b32 s28, 0xff00, s28\n"
    "v_mfma_f32_16x16x32_bf16 v[36:39], v[108:111], a[56:59], v[36:39]\n"
    "s_waitcnt lgkmcnt(0)\n"
    "v_mfma_f32_16x16x32_bf16 v[4:7], v[84:87], v[8:11], v[4:7]\n"
    "ds_read_b64_tr_b16 v[100:101], v232 offset:0\n"
    "ds_read_b64_tr_b16 v[102:103], v232 offset:2048\n"
    "v_mfma_f32_16x16x32_bf16 v[4:7], v[92:95], a[60:63], v[4:7]\n"
    "v_add_u32 v230, s88, v244\n"
    "v_add_u32 v231, s88, v245\n"
    "v_mfma_f32_16x16x32_bf16 v[12:15], v[92:95], a[64:67], v[12:15]\n"
    "ds_read_b64_tr_b16 v[108:109], v233 offset:0\n"
    "ds_read_b64_tr_b16 v[110:111], v233 offset:2048\n"
    "v_mfma_f32_16x16x32_bf16 v[12:15], v[84:87], v[40:43], v[12:15]\n"
    "v_cndmask_b32 v253, v241, v250, s[28:29]\n"
    "v_mfma_f32_16x16x32_bf16 v[20:23], v[84:87], v[72:75], v[20:23]\n"
    "v_add_u32 v232, s88, v246\n"
    "v_mfma_f32_16x16x32_bf16 v[20:23], v[92:95], a[68:71], v[20:23]\n"
    "s_add_u32 s78, s62, 0\n"
    "s_cmp_eq_u32 s35, 0\n"
    "s_cselect_b32 s78, 0x40000000, s78\n"
    "v_mfma_f32_16x16x32_bf16 v[28:31], v[92:95], a[72:75], v[28:31]\n"
    "s_add_u32 s79, s66, 0\n"
    "buffer_load_dwordx4 v[168:171], v238, s[48:51], s78 offen sc1\n"
    "v_mfma_f32_16x16x32_bf16 v[28:31], v[84:87], v[104:107], v[28:31]\n"
    "buffer_load_dwordx4 v[172:175], v238, s[48:51], s78 offen offset:1024 sc1\n"
    "v_mfma_f32_16x16x32_bf16 v[36:39], v[84:87], v[136:139], v[36:39]\n"
    "buffer_load_dwordx2 v[184:185], v248, s[48:51], s78 offen offset:2048 sc1\n"
    "v_mfma_f32_16x16x32_bf16 v[36:39], v[92:95], a[76:79], v[36:39]\n"
    "s_waitcnt vmcnt(12) lgkmcnt(0)\n"
    "s_barrier\n"
    "v_add_u32 v233, s88, v247\n"
    "v_mfma_f32_16x16x32_bf16 v[4:7], v[100:103], a[80:83], v[4:7]\n"
    "ds_read_b64_tr_b16 v[84:85], v230 offset:0\n"
    "ds_read_b64_tr_b16 v[86:87], v230 offset:2048\n"
    "v_mfma_f32_16x16x32_bf16 v[4:7], v[108:111], a[100:103], v[4:7]\n"
    "buffer_load_dwordx2 v[148:149], v248, s[48:51], s79 offen sc1\n"
    "v_mfma_f32_16x16x32_bf16 v[12:15], v[108:111], a[104:107], v[12:15]\n"
    "ds_read_b64_tr_b16 v[92:93], v231 offset:0\n"
    "ds_read_b64_tr_b16 v[94:95], v231 offset:2048\n"
    "v_mfma_f32_16x16x32_bf16 v[12:15], v[100:103], a[84:87], v[12:15]\n"
    "s_add_u32 s85, s62, 0x2800\n"
    "s_cmp_eq_u32 s35, 1\n"
    "s_cselect_b32 s85, 0x40000000, s85\n"
    "v_mfma_f32_16x16x32_bf16 v[20:23], v[100:103], a[88:91], v[20:23]\n"
    "s_add_u32 s87, s66, 0x2800\n"
    "buffer_load_dwordx4 v[176:179], v238, s[48:51], s85 offen sc1\n"
    "v_mfma_f32_16x16x32_bf16 v[20:23], v[108:111], a[108:111], v[20:23]\n"
    "s_add_u32 s88, s88, 0x1000\n"
    "buffer_load_dwordx4 v[180:183], v238, s[48:51], s85 offen offset:1024 sc1\n"
    "v_mfma_f32_16x16x32_bf16 v[28:31], v[108:111], a[112:115], v[28:31]\n"
    "s_cmp_eq_u32 s88, 0x6000\n"
    "s_cselect_b32 s88, 0x1000, s88\n"
    "buffer_load_dwordx2 v[186:187], v248, s[48:51], s85 offen offset:2048 sc1\n"
    "v_mfma_f32_16x16x32_bf16 v[28:31], v[100:103], a[92:95], v[28:31]\n"
    "buffer_load_dwordx2 v[150:151], v248, s[48:51], s87 offen sc1\n"
    "v_mfma_f32_16x16x32_bf16 v[36:39], v[100:103], a[96:99], v[36:39]\n"
    "s_add_u32 s78, s62, 0x5000\n"
    "s_cmp_eq_u32 s35, 2\n"
    "s_cselect_b32 s78, 0x40000000, s78\n"
    "v_mfma_f32_16x16x32_bf16 v[36:39], v[108:111], a[116:119], v[36:39]\n"
    "s_waitcnt lgkmcnt(0)\n"
    "s_add_u32 s79, s66, 0x5000\n"
    "buffer_load_dwordx4 v[188:191], v238, s[48:51], s78 offen sc1\n"
    "v_mfma_f32_16x16x32_bf16 v[4:7], v[84:87], v[16:19], v[4:7]\n"
    "ds_read_b64_tr_b16 v[100:101], v232 offset:0\n"
    "ds_read_b64_tr_b16 v[102:103], v232 offset:2048\n"
    "v_mfma_f32_16x16x32_bf16 v[4:7], v[92:95], a[120:123], v[4:7]\n"
    "v_add_u32 v230, s88, v244\n"
    "v_add_u32 v231, s88, v245\n"
    "v_mfma_f32_16x16x32_bf16 v[12:15], v[92:95], a[124:127], v[12:15]\n"
    "ds_read_b64_tr_b16 v[108:109], v233 offset:0\n"
    "ds_read_b64_tr_b16 v[110:111], v233 offset:2048\n"
    "v_mfma_f32_16x16x32_bf16 v[12:15], v[84:87], v[48:51], v[12:15]\n"
    "buffer_load_dwordx4 v[192:195], v238, s[48:51], s78 offen offset:1024 sc1\n"
    "v_mfma_f32_16x16x32_bf16 v[20:23], v[84:87], v[80:83], v[20:23]\n"
    "v_add_u32 v232, s88, v246\n"
    "v_mfma_f32_16x16x32_bf16 v[20:23], v[92:95], a[128:131], v[20:23]\n"
    "buffer_load_dwordx2 v[204:205], v248, s[48:51], s78 offen offset:2048 sc1\n"
    "v_mfma_f32_16x16x32_bf16 v[28:31], v[92:95], a[132:135], v[28:31]\n"
    "buffer_load_dwordx2 v[156:157], v248, s[48:51], s79 offen sc1\n"
    "v_mfma_f32_16x16x32_bf16 v[28:31], v[84:87], v[112:115], v[28:31]\n"
    "v_mfma_f32_16x16x32_bf16 v[36:39], v[84:87], v[144:147], v[36:39]\n"
    "v_mfma_f32_16x16x32_bf16 v[36:39], v[92:95], a[136:139], v[36:39]\n"
    "s_waitcnt vmcnt(17) lgkmcnt(0)\n"
    "s_barrier\n"
    "v_add_u32 v233, s88, v247\n"
    "v_mfma_f32_16x16x32_bf16 v[4:7], v[100:103], a[140:143], v[4:7]\n"
    "ds_read_b64_tr_b16 v[84:85], v230 offset:0\n"
    "ds_read_b64_tr_b16 v[86:87], v230 offset:2048\n"
    "v_mfma_f32_16x16x32_bf16 v[4:7], v[108:111], a[160:163], v[4:7]\n"
    "v_mfma_f32_16x16x32_bf16 v[12:15], v[108:111], a[164:167], v[12:15]\n"
    "ds_read_b64_tr_b16 v[92:93], v231 offset:0\n"
    "ds_read_b64_tr_b16 v[94:95], v231 offset:2048\n"
    "v_mfma_f32_16x16x32_bf16 v[12:15], v[100:103], a[144:147], v[12:15]\n"
    "v_mfma_f32_16x16x32_bf16 v[20:23], v[100:103], a[148:151], v[20:23]\n"
    "v_mfma_f32_16x16x32_bf16 v[20:23], v[108:111], a[168:171], v[20:23]\n"
    "s_add_u32 s88, s88, 0x1000\n"
    "v_mfma_f32_16x16x32_bf16 v[28:31], v[108:111], a[172:175], v[28:31]\n"
    "s_cmp_eq_u32 s88, 0x6000\n"
    "s_cselect_b32 s88, 0x1000, s88\n"
    "v_mfma_f32_16x16x32_bf16 v[28:31], v[100:103], a[152:155], v[28:31]\n"
    "v_mfma_f32_16x16x32_bf16 v[36:39], v[100:103], a[156:159], v[36:39]\n"
    "v_mfma_f32_16x16x32_bf16 v[36:39], v[108:111], a[176:179], v[36:39]\n"
    "s_waitcnt lgkmcnt(0)\n"
    "v_mfma_f32_16x16x32_bf16 v[4:7], v[84:87], v[24:27], v[4:7]\n"
    "ds_read_b64_tr_b16 v[100:101], v232 offset:0\n"
    "ds_read_b64_tr_b16 v[102:103], v232 offset:2048\n"
    "v_mfma_f32_16x16x32_bf16 v[4:7], v[92:95], a[180:183], v[4:7]\n"
    "v_mfma_f32_16x16x32_bf16 v[12:15], v[92:95], a[184:187], v[12:15]\n"
    "ds_read_b64_tr_b16 v[108:109], v233 offset:0\n"
    "ds_read_b64_tr_b16 v[110:111], v233 offset:2048\n"
    "v_mfma_f32_16x16x32_bf16 v[12:15], v[84:87], v[56:59], v[12:15]\n"
    "v_mfma_f32_16x16x32_bf16 v[20:23], v[84:87], v[88:91], v[20:23]\n"
    "v_mfma_f32_16x16x32_bf16 v[20:23], v[92:95], a[188:191], v[20:23]\n"
    "v_mfma_f32_16x16x32_bf16 v[28:31], v[92:95], a[192:195], v[28:31]\n"
    "v_mfma_f32_16x16x32_bf16 v[28:31], v[84:87], v[120:123], v[28:31]\n"
    "s_waitcnt vmcnt(12)\n"
    "buffer_store_dword v252, v253, s[36:39], 0 offen sc1\n"
    "A_TT_FIRST2:\n"
    "A_TT_S2:\n"
    "s_cbranch_execz A_TT_RET\n"
    "v_mfma_f32_16x16x32_bf16 v[36:39], v[84:87], v[152:155], v[36:39]\n"
    "s_add_u32 s85, s62, 0x7800\n"
    "s_cmp_eq_u32 s35, 3\n"
    "s_cselect_b32 s85, 0x40000000, s85\n"
    "v_mfma_f32_16x16x32_bf16 v[36:39], v[92:95], a[196:199], v[36:39]\n"
    "s_waitcnt lgkmcnt(0)\n"
    "s_add_u32 s87, s66, 0x7800\n"
    "buffer_load_dwordx4 v[196:199], v238, s[48:51], s85 offen sc1\n"
    "v_mfma_f32_16x16x32_bf16 v[4:7], v[100:103], a[200:203], v[4:7]\n"
    "buffer_load_dwordx4 v[200:203], v238, s[48:51], s85 offen offset:1024 sc1\n"
    "v_mfma_f32_16x16x32_bf16 v[4:7], v[108:111], a[220:223], v[4:7]\n"
    "buffer_load_dwordx2 v[206:207], v248, s[48:51], s85 offen offset:2048 sc1\n"
    "v_mfma_f32_16x16x32_bf16 v[12:15], v[108:111], a[224:227], v[12:15]\n"
    "buffer_load_dwordx2 v[158:159], v248, s[48:51], s87 offen sc1\n"
    "v_mfma_f32_16x16x32_bf16 v[12:15], v[100:103], a[204:207], v[12:15]\n"
    "v_mfma_f32_16x16x32_bf16 v[20:23], v[100:103], a[208:211], v[20:23]\n"
    "v_mfma_f32_16x16x32_bf16 v[20:23], v[108:111], a[228:231], v[20:23]\n"
    "v_mfma_f32_16x16x32_bf16 v[28:31], v[108:111], a[232:235], v[28:31]\n"
    "v_mfma_f32_16x16x32_bf16 v[28:31], v[100:103], a[212:215], v[28:31]\n"
    "v_mfma_f32_16x16x32_bf16 v[36:39], v[100:103], a[216:219], v[36:39]\n"
    "v_mfma_f32_16x16x32_bf16 v[36:39], v[108:111], a[236:239], v[36:39]\n"
    "s_andn2_b32 s64, 0xf0f0, s63\n"
    "s_cbranch_scc1 A_C2_LATE\n"
    "s_add_u32 s78, s62, 0xa000\n"
    "s_cmp_eq_u32 s35, 4\n"
    "s_cselect_b32 s78, 0x40000000, s78\n"
    "buffer_load_dwordx4 v[208:211], v238, s[48:51], s78 offen sc1\n"
    "s_add_u32 s79, s66, 0xa000\n"
    "buffer_load_dwordx4 v[212:215], v238, s[48:51], s78 offen offset:1024 sc1\n"
    "buffer_load_dwordx2 v[224:225], v248, s[48:51], s78 offen offset:2048 sc1\n"
    "buffer_load_dwordx2 v[160:161], v248, s[48:51], s79 offen sc1\n"
    "s_add_u32 s85, s62, 0xc800\n"
    "s_cmp_eq_u32 s35, 5\n"
    "s_cselect_b32 s85, 0x40000000, s85\n"
    "buffer_load_dwordx4 v[216:219], v238, s[48:51], s85 offen sc1\n"
    "s_add_u32 s87, s66, 0xc800\n"
    "buffer_load_dwordx4 v[220:223], v238, s[48:51], s85 offen offset:1024 sc1\n"
    "buffer_load_dwordx2 v[226:227], v248, s[48:51], s85 offen offset:2048 sc1\n"
    "buffer_load_dwordx2 v[162:163], v248, s[48:51], s87 offen sc1\n"
    "s_add_u32 s78, s62, 0xf000\n"
    "s_cmp_eq_u32 s35, 6\n"
    "s_cselect_b32 s78, 0x40000000, s78\n"
    "buffer_load_dwordx4 v[44:47], v238, s[48:51], s78 offen sc1\n"
    "s_add_u32 s79, s66, 0xf000\n"
    "buffer_load_dwordx4 v[52:55], v238, s[48:51], s78 offen offset:1024 sc1\n"
    "buffer_load_dwordx2 v[76:77], v248, s[48:51], s78 offen offset:2048 sc1\n"
    "buffer_load_dwordx2 v[164:165], v248, s[48:51], s79 offen sc1\n"
    "s_add_u32 s85, s62, 0x11800\n"
    "s_cmp_eq_u32 s35, 7\n"
    "s_cselect_b32 s85, 0x40000000, s85\n"
    "buffer_load_dwordx4 v[60:63], v238, s[48:51], s85 offen sc1\n"
    "s_add_u32 s87, s66, 0x11800\n"
    "buffer_load_dwordx4 v[68:71], v238, s[48:51], s85 offen offset:1024 sc1\n"
    "buffer_load_dwordx2 v[78:79], v248, s[48:51], s85 offen offset:2048 sc1\n"
    "buffer_load_dwordx2 v[166:167], v248, s[48:51], s87 offen sc1\n"
    "s_waitcnt vmcnt(29)\n"
    "v_mfma_f32_16x16x32_bf16 v[4:7], a[244:247], v[168:171], v[4:7]\n"
    "v_mfma_f32_16x16x32_bf16 v[12:15], a[248:251], v[168:171], v[12:15]\n"
    "v_mfma_f32_16x16x32_bf16 v[20:23], a[244:247], v[172:175], v[20:23]\n"
    "v_mfma_f32_16x16x32_bf16 v[28:31], a[248:251], v[172:175], v[28:31]\n"
    "s_waitcnt vmcnt(25)\n"
    "v_mfma_f32_16x16x32_bf16 v[4:7], a[244:247], v[176:179], v[4:7]\n"
    "v_mfma_f32_16x16x32_bf16 v[12:15], a[248:251], v[176:179], v[12:15]\n"
    "v_mfma_f32_16x16x32_bf16 v[20:23], a[244:247], v[180:183], v[20:23]\n"
    "v_mfma_f32_16x16x32_bf16 v[28:31], a[248:251], v[180:183], v[28:31]\n"
    "v_mfma_f32_16x16x32_bf16 v[36:39], a[240:243], v[184:187], v[36:39]\n"
    "v_mfma_f32_16x16x32_bf16 v[140:143], a[240:243], v[148:151], 0\n"
    "s_waitcnt vmcnt(21)\n"
    "v_mfma_f32_16x16x32_bf16 v[4:7], a[244:247], v[188:191], v[4:7]\n"
    "v_mfma_f32_16x16x32_bf16 v[12:15], a[248:251], v[188:191], v[12:15]\n"
    "v_mfma_f32_16x16x32_bf16 v[20:23], a[244:247], v[192:195], v[20:23]\n"
    "v_mfma_f32_16x16x32_bf16 v[28:31], a[248:251], v[192:195], v[28:31]\n"
    "s_waitcnt vmcnt(16)\n"
    "v_mfma_f32_16x16x32_bf16 v[4:7], a[244:247], v[196:199], v[4:7]\n"
    "v_mfma_f32_16x16x32_bf16 v[12:15], a[248:251], v[196:199], v[12:15]\n"
    "v_mfma_f32_16x16x32_bf16 v[20:23], a[244:247], v[200:203], v[20:23]\n"
    "v_mfma_f32_16x16x32_bf16 v[28:31], a[248:251], v[200:203], v[28:31]\n"
    "v_mfma_f32_16x16x32_bf16 v[36:39], a[240:243], v[204:207], v[36:39]\n"
    "v_mfma_f32_16x16x32_bf16 v[140:143], a[240:243], v[156:159], v[140:143]\n"
    "A_C2_BACK:\n"
    "s_waitcnt vmcnt(12)\n"
    "v_mfma_f32_16x16x32_bf16 v[4:7], a[244:247], v[208:211], v[4:7]\n"
    "v_mfma_f32_16x16x32_bf16 v[12:15], a[248:251], v[208:211], v[12:15]\n"
    "v_mfma_f32_16x16x32_bf16 v[20:23], a[244:247], v[212:215], v[20:23]\n"
    "v_mfma_f32_16x16x32_bf16 v[28:31], a[248:251], v[212:215], v[28:31]\n"
    "s_waitcnt vmcnt(8)\n"
    "v_mfma_f32_16x16x32_bf16 v[4:7], a[244:247], v[216:219], v[4:7]\n"
    "v_mfma_f32_16x16x32_bf16 v[12:15], a[248:251], v[216:219], v[12:15]\n"
    "v_mfma_f32_16x16x32_bf16 v[20:23], a[244:247], v[220:223], v[20:23]\n"
    "v_mfma_f32_16x16x32_bf16 v[28:31], a[248:251], v[220:223], v[28:31]\n"
    "v_mfma_f32_16x16x32_bf16 v[36:39], a[240:243], v[224:227], v[36:39]\n"
    "v_mfma_f32_16x16x32_bf16 v[140:143], a[240:243], v[160:163], v[140:143]\n"
    "s_waitcnt vmcnt(4)\n"
    "v_mfma_f32_16x16x32_bf16 v[4:7], a[244:247], v[44:47], v[4:7]\n"
    "v_mfma_f32_16x16x32_bf16 v[12:15], a[248:251], v[44:47], v[12:15]\n"
    "v_mfma_f32_16x16x32_bf16 v[20:23], a[244:247], v[52:55], v[20:23]\n"
    "v_mfma_f32_16x16x32_bf16 v[28:31], a[248:251], v[52:55], v[28:31]\n"
    "s_waitcnt vmcnt(0)\n"
    "v_mfma_f32_16x16x32_bf16 v[4:7], a[244:247], v[60:63], v[4:7]\n"
    "v_mfma_f32_16x16x32_bf16 v[12:15], a[248:251], v[60:63], v[12:15]\n"
    "v_mfma_f32_16x16x32_bf16 v[20:23], a[244:247], v[68:71], v[20:23]\n"
    "v_mfma_f32_16x16x32_bf16 v[28:31], a[248:251], v[68:71], v[28:31]\n"
    "v_mfma_f32_16x16x32_bf16 v[36:39], a[240:243], v[76:79], v[36:39]\n"
    "v_mfma_f32_16x16x32_bf16 v[140:143], a[240:243], v[164:167], v[140:143]\n"
    "s_mul_i32 s76, s72, 0xd800\n"
    "s_add_u32 s60, s30, s76\n"
    "s_add_u32 s61, s60, 0x4800\n"
    "s_add_u32 s62, s60, 0x9000\n"
    "s_add_u32 s76, s31, 0x4800\n"
    "s_cmp_eq_u32 s72, 3\n"
    "s_cselect_b32 s61, s31, s61\n"
    "s_cselect_b32 s62, s76, s62\n"
    "v_cvt_pk_bf16_f32 v4, v4, v5\n"
    "v_cvt_pk_bf16_f32 v5, v6, v7\n"
    "ds_write_b64 v229, v[4:5] offset:24576\n"
    "v_cvt_pk_bf16_f32 v12, v12, v13\n"
    "v_cvt_pk_bf16_f32 v13, v14, v15\n"
    "ds_write_b64 v229, v[12:13] offset:26880\n"
    "v_cvt_pk_bf16_f32 v20, v20, v21\n"
    "v_cvt_pk_bf16_f32 v21, v22, v23\n"
    "ds_write_b64 v229, v[20:21] offset:29184\n"
    "v_cvt_pk_bf16_f32 v28, v28, v29\n"
    "v_cvt_pk_bf16_f32 v29, v30, v31\n"
    "ds_write_b64 v229, v[28:29] offset:31488\n"
    "v_cvt_pk_bf16_f32 v36, v36, v37\n"
    "v_cvt_pk_bf16_f32 v37, v38, v39\n"
    "ds_write_b64 v229, v[36:37] offset:33792\n"
    "v_cvt_pk_bf16_f32 v140, v140, v141\n"
    "v_cvt_pk_bf16_f32 v141, v142, v143\n"
    "ds_write_b64 v229, v[140:141] offset:36096\n"
    "s_waitcnt lgkmcnt(0)\n"
    "s_barrier\n"
    "ds_read_b128 v[84:87], v243 offset:24576\n"
    "ds_read_b128 v[92:95], v243 offset:25728\n"
    "ds_read_b128 v[100:103], v243 offset:26880\n"
    "s_waitcnt lgkmcnt(2)\n"
    "buffer_store_dwordx4 v[84:87], v228, s[92:95], s60 offen nt\n"
    "s_waitcnt lgkmcnt(1)\n"
    "buffer_store_dwordx4 v[92:95], v228, s[92:95], s61 offen nt\n"
    "s_waitcnt lgkmcnt(0)\n"
    "buffer_store_dwordx4 v[100:103], v228, s[92:95], s62 offen nt\n"
    "s_endpgm\n"
    "A_HS_SLOW:\n"
    "s_add_u32 s65, s65, 1\n"
    "s_cmp_gt_u32 s65, 0x80000\n"
    "s_cbranch_scc1 A_HS_OK\n"
    "s_sleep 1\n"
    "buffer_load_dword v253, v250, s[36:39], s67 offen sc1\n"
    "s_waitcnt vmcnt(0)\n"
    "s_branch A_HS_CHK\n"
    "A_C1_SLOW:\n"
    "s_add_u32 s65, s65, 1\n"
    "s_cmp_gt_u32 s65, 0x80000\n"
    "s_cbranch_scc1 A_C1_OK\n"
    "s_sleep 1\n"
    "buffer_load_dword v254, v249, s[36:39], 0 offen sc1\n"
    "s_waitcnt vmcnt(0)\n"
    "s_branch A_C1_CHK\n"
    "A_C2_LATE:\n"
    "buffer_load_dword v254, v249, s[36:39], 0 offen sc1\n"
    "s_waitcnt vmcnt(13)\n"
    "v_mfma_f32_16x16x32_bf16 v[4:7], a[244:247], v[168:171], v[4:7]\n"
    "v_mfma_f32_16x16x32_bf16 v[12:15], a[248:251], v[168:171], v[12:15]\n"
    "v_mfma_f32_16x16x32_bf16 v[20:23], a[244:247], v[172:175], v[20:23]\n"
    "v_mfma_f32_16x16x32_bf16 v[28:31], a[248:251], v[172:175], v[28:31]\n"
    "s_waitcnt vmcnt(9)\n"
    "v_mfma_f32_16x16x32_bf16 v[4:7], a[244:247], v[176:179], v[4:7]\n"
    "v_mfma_f32_16x16x32_bf16 v[12:15], a[248:251], v[176:179], v[12:15]\n"
    "v_mfma_f32_16x16x32_bf16 v[20:23], a[244:247], v[180:183], v[20:23]\n"
    "v_mfma_f32_16x16x32_bf16 v[28:31], a[248:251], v[180:183], v[28:31]\n"
    "v_mfma_f32_16x16x32_bf16 v[36:39], a[240:243], v[184:187], v[36:39]\n"
    "v_mfma_f32_16x16x32_bf16 v[140:143], a[240:243], v[148:151], 0\n"
    "s_waitcnt vmcnt(5)\n"
    "v_mfma_f32_16x16x32_bf16 v[4:7], a[244:247], v[188:191], v[4:7]\n"
    "v_mfma_f32_16x16x32_bf16 v[12:15], a[248:251], v[188:191], v[12:15]\n"
    "v_mfma_f32_16x16x32_bf16 v[20:23], a[244:247], v[192:195], v[20:23]\n"
    "v_mfma_f32_16x16x32_bf16 v[28:31], a[248:251], v[192:195], v[28:31]\n"
    "s_waitcnt vmcnt(1)\n"
    "v_mfma_f32_16x16x32_bf16 v[4:7], a[244:247], v[196:199], v[4:7]\n"
    "v_mfma_f32_16x16x32_bf16 v[12:15], a[248:251], v[196:199], v[12:15]\n"
    "v_mfma_f32_16x16x32_bf16 v[20:23], a[244:247], v[200:203], v[20:23]\n"
    "v_mfma_f32_16x16x32_bf16 v[28:31], a[248:251], v[200:203], v[28:31]\n"
    "v_mfma_f32_16x16x32_bf16 v[36:39], a[240:243], v[204:207], v[36:39]\n"
    "v_mfma_f32_16x16x32_bf16 v[140:143], a[240:243], v[156:159], v[140:143]\n"
    "s_waitcnt vmcnt(0)\n"
    "s_mov_b32 s65, 0\n"
    "A_C3_CHK4:\n"
    "v_cmp_eq_u32 vcc, v254, v251\n"
    "s_or_b32 s63, vcc_lo, s60\n"
    "s_andn2_b32 s76, 0x1010, s63\n"
    "s_cbranch_scc1 A_C3_SLOW4\n"
    "A_C3_OK4:\n"
    "s_add_u32 s78, s62, 0xa000\n"
    "s_cmp_eq_u32 s35, 4\n"
    "s_cselect_b32 s78, 0x40000000, s78\n"
    "buffer_load_dwordx4 v[208:211], v238, s[48:51], s78 offen sc1\n"
    "s_add_u32 s79, s66, 0xa000\n"
    "buffer_load_dwordx4 v[212:215], v238, s[48:51], s78 offen offset:1024 sc1\n"
    "buffer_load_dwordx2 v[224:225], v248, s[48:51], s78 offen offset:2048 sc1\n"
    "buffer_load_dwordx2 v[160:161], v248, s[48:51], s79 offen sc1\n"
    "A_C3_CHK5:\n"
    "v_cmp_eq_u32 vcc, v254, v251\n"
    "s_or_b32 s63, vcc_lo, s60\n"
    "s_andn2_b32 s76, 0x2020, s63\n"
    "s_cbranch_scc1 A_C3_SLOW5\n"
    "A_C3_OK5:\n"
    "s_add_u32 s85, s62, 0xc800\n"
    "s_cmp_eq_u32 s35, 5\n"
    "s_cselect_b32 s85, 0x40000000, s85\n"
    "buffer_load_dwordx4 v[216:219], v238, s[48:51], s85 offen sc1\n"
    "s_add_u32 s87, s66, 0xc800\n"
    "buffer_load_dwordx4 v[220:223], v238, s[48:51], s85 offen offset:1024 sc1\n"
    "buffer_load_dwordx2 v[226:227], v248, s[48:51], s85 offen offset:2048 sc1\n"
    "buffer_load_dwordx2 v[162:163], v248, s[48:51], s87 offen sc1\n"
    "A_C3_CHK6:\n"
    "v_cmp_eq_u32 vcc, v254, v251\n"
    "s_or_b32 s63, vcc_lo, s60\n"
    "s_andn2_b32 s76, 0x4040, s63\n"
    "s_cbranch_scc1 A_C3_SLOW6\n"
    "A_C3_OK6:\n"
    "s_add_u32 s78, s62, 0xf000\n"
    "s_cmp_eq_u32 s35, 6\n"
    "s_cselect_b32 s78, 0x40000000, s78\n"
    "buffer_load_dwordx4 v[44:47], v238, s[48:51], s78 offen sc1\n"
    "s_add_u32 s79, s66, 0xf000\n"
    "buffer_load_dwordx4 v[52:55], v238, s[48:51], s78 offen offset:1024 sc1\n"
    "buffer_load_dwordx2 v[76:77], v248, s[48:51], s78 offen offset:2048 sc1\n"
    "buffer_load_dwordx2 v[164:165], v248, s[48:51], s79 offen sc1\n"
    "A_C3_CHK7:\n"
    "v_cmp_eq_u32 vcc, v254, v251\n"
    "s_or_b32 s63, vcc_lo, s60\n"
    "s_andn2_b32 s76, 0x8080, s63\n"
    "s_cbranch_scc1 A_C3_SLOW7\n"
    "A_C3_OK7:\n"
    "s_add_u32 s85, s62, 0x11800\n"
    "s_cmp_eq_u32 s35, 7\n"
    "s_cselect_b32 s85, 0x40000000, s85\n"
    "buffer_load_dwordx4 v[60:63], v238, s[48:51], s85 offen sc1\n"
    "s_add_u32 s87, s66, 0x11800\n"
    "buffer_load_dwordx4 v[68:71], v238, s[48:51], s85 offen offset:1024 sc1\n"
    "buffer_load_dwordx2 v[78:79], v248, s[48:51], s85 offen offset:2048 sc1\n"
    "buffer_load_dwordx2 v[166:167], v248, s[48:51], s87 offen sc1\n"
    "s_branch A_C2_BACK\n"
    "A_C3_SLOW4:\n"
    "s_add_u32 s65, s65, 1\n"
    "s_cmp_gt_u32 s65, 0x80000\n"
    "s_cbranch_scc1 A_C3_OK4\n"
    "s_nop 0\n"
    "buffer_load_dword v254, v249, s[36:39], 0 offen sc1\n"
    "s_waitcnt vmcnt(0)\n"
    "s_branch A_C3_CHK4\n"
    "A_C3_SLOW5:\n"
    "s_add_u32 s65, s65, 1\n"
    "s_cmp_gt_u32 s65, 0x80000\n"
    "s_cbranch_scc1 A_C3_OK5\n"
    "s_nop 0\n"
    "buffer_load_dword v254, v249, s[36:39], 0 offen sc1\n"
    "s_waitcnt vmcnt(0)\n"
    "s_branch A_C3_CHK5\n"
    "A_C3_SLOW6:\n"
    "s_add_u32 s65, s65, 1\n"
    "s_cmp_gt_u32 s65, 0x80000\n"
    "s_cbranch_scc1 A_C3_OK6\n"
    "s_nop 0\n"
    "buffer_load_dword v254, v249, s[36:39], 0 offen sc1\n"
    "s_waitcnt vmcnt(0)\n"
    "s_branch A_C3_CHK6\n"
    "A_C3_SLOW7:\n"
    "s_add_u32 s65, s65, 1\n"
    "s_cmp_gt_u32 s65, 0x80000\n"
    "s_cbranch_scc1 A_C3_OK7\n"
    "s_nop 0\n"
    "buffer_load_dword v254, v249, s[36:39], 0 offen sc1\n"
    "s_waitcnt vmcnt(0)\n"
    "s_branch A_C3_CHK7\n"
    "A_TT_END:\n"
    "A_RARE_EP_0:\n"
    "v_mov_b32 v197, v196\n"
    "s_nop 1\n"
    "v_permlane16_swap_b32 v196, v197\n"
    "s_nop 0\n"
    "v_max_f32 v196, v196, v197\n"
    "v_mov_b32 v197, v196\n"
    "s_nop 1\n"
    "v_permlane32_swap_b32 v196, v197\n"
    "s_nop 0\n"
    "v_max3_f32 v196, v196, v197, v184\n"
    "v_sub_f32 v197, v184, v196\n"
    "v_mul_f32 v197, s69, v197\n"
    "v_exp_f32 v197, v197\n"
    "v_mov_b32 v184, v196\n"
    "v_add_f32 v189, 0x423c35a6, v196\n"
    "v_mul_f32 v200, s70, v196\n"
    "s_cmp_eq_u32 s80, 0\n"
    "s_cbranch_scc1 A_BACK_EP_0\n"
    "v_mul_f32 v0, v197, v0\n"
    "v_mul_f32 v1, v197, v1\n"
    "v_mul_f32 v2, v197, v2\n"
    "v_mul_f32 v3, v197, v3\n"
    "v_mul_f32 v4, v197, v4\n"
    "v_mul_f32 v5, v197, v5\n"
    "v_mul_f32 v6, v197, v6\n"
    "v_mul_f32 v7, v197, v7\n"
    "v_mul_f32 v8, v197, v8\n"
    "v_mul_f32 v9, v197, v9\n"
    "v_mul_f32 v10, v197, v10\n"
    "v_mul_f32 v11, v197, v11\n"
    "v_mul_f32 v12, v197, v12\n"
    "v_mul_f32 v13, v197, v13\n"
    "v_mul_f32 v14, v197, v14\n"
    "v_mul_f32 v15, v197, v15\n"
    "v_mul_f32 v16, v197, v16\n"
    "v_mul_f32 v17, v197, v17\n"
    "v_mul_f32 v18, v197, v18\n"
    "v_mul_f32 v19, v197, v19\n"
    "v_mul_f32 v20, v197, v20\n"
    "v_mul_f32 v21, v197, v21\n"
    "v_mul_f32 v22, v197, v22\n"
    "v_mul_f32 v23, v197, v23\n"
    "v_mul_f32 v24, v197, v24\n"
    "v_mul_f32 v25, v197, v25\n"
    "v_mul_f32 v26, v197, v26\n"
    "v_mul_f32 v27, v197, v27\n"
    "v_mul_f32 v28, v197, v28\n"
    "v_mul_f32 v29, v197, v29\n"
    "v_mul_f32 v30, v197, v30\n"
    "v_mul_f32 v31, v197, v31\n"
    "v_accvgpr_read_b32 v198, a128\n"
    "v_mul_f32 v198, v197, v198\n"
    "v_accvgpr_write_b32 a128, v198\n"
    "v_accvgpr_read_b32 v198, a129\n"
    "v_mul_f32 v198, v197, v198\n"
    "v_accvgpr_write_b32 a129, v198\n"
    "v_accvgpr_read_b32 v198, a130\n"
    "v_mul_f32 v198, v197, v198\n"
    "v_accvgpr_write_b32 a130, v198\n"
    "v_accvgpr_read_b32 v198, a131\n"
    "v_mul_f32 v198, v197, v198\n"
    "v_accvgpr_write_b32 a131, v198\n"
    "s_nop 1\n"
    "s_branch A_BACK_EP_0\n"
    "A_RARE_EP_1:\n"
    "v_mov_b32 v197, v196\n"
    "s_nop 1\n"
    "v_permlane16_swap_b32 v196, v197\n"
    "s_nop 0\n"
    "v_max_f32 v196, v196, v197\n"
    "v_mov_b32 v197, v196\n"
    "s_nop 1\n"
    "v_permlane32_swap_b32 v196, v197\n"
    "s_nop 0\n"
    "v_max3_f32 v196, v196, v197, v185\n"
    "v_sub_f32 v197, v185, v196\n"
    "v_mul_f32 v197, s69, v197\n"
    "v_exp_f32 v197, v197\n"
    "v_mov_b32 v185, v196\n"
    "v_add_f32 v190, 0x423c35a6, v196\n"
    "v_mul_f32 v201, s70, v196\n"
    "s_cmp_eq_u32 s80, 0\n"
    "s_cbranch_scc1 A_BACK_EP_1\n"
    "v_mul_f32 v32, v197, v32\n"
    "v_mul_f32 v33, v197, v33\n"
    "v_mul_f32 v34, v197, v34\n"
    "v_mul_f32 v35, v197, v35\n"
    "v_mul_f32 v36, v197, v36\n"
    "v_mul_f32 v37, v197, v37\n"
    "v_mul_f32 v38, v197, v38\n"
    "v_mul_f32 v39, v197, v39\n"
    "v_mul_f32 v40, v197, v40\n"
    "v_mul_f32 v41, v197, v41\n"
    "v_mul_f32 v42, v197, v42\n"
    "v_mul_f32 v43, v197, v43\n"
    "v_mul_f32 v44, v197, v44\n"
    "v_mul_f32 v45, v197, v45\n"
    "v_mul_f32 v46, v197, v46\n"
    "v_mul_f32 v47, v197, v47\n"
    "v_mul_f32 v48, v197, v48\n"
    "v_mul_f32 v49, v197, v49\n"
    "v_mul_f32 v50, v197, v50\n"
    "v_mul_f32 v51, v197, v51\n"
    "v_mul_f32 v52, v197, v52\n"
    "v_mul_f32 v53, v197, v53\n"
    "v_mul_f32 v54, v197, v54\n"
    "v_mul_f32 v55, v197, v55\n"
    "v_mul_f32 v56, v197, v56\n"
    "v_mul_f32 v57, v197, v57\n"
    "v_mul_f32 v58, v197, v58\n"
    "v_mul_f32 v59, v197, v59\n"
    "v_mul_f32 v60, v197, v60\n"
    "v_mul_f32 v61, v197, v61\n"
    "v_mul_f32 v62, v197, v62\n"
    "v_mul_f32 v63, v197, v63\n"
    "v_accvgpr_read_b32 v198, a132\n"
    "v_mul_f32 v198, v197, v198\n"
    "v_accvgpr_write_b32 a132, v198\n"
    "v_accvgpr_read_b32 v198, a133\n"
    "v_mul_f32 v198, v197, v198\n"
    "v_accvgpr_write_b32 a133, v198\n"
    "v_accvgpr_read_b32 v198, a134\n"
    "v_mul_f32 v198, v197, v198\n"
    "v_accvgpr_write_b32 a134, v198\n"
    "v_accvgpr_read_b32 v198, a135\n"
    "v_mul_f32 v198, v197, v198\n"
    "v_accvgpr_write_b32 a135, v198\n"
    "s_nop 1\n"
    "s_branch A_BACK_EP_1\n"
    "A_RARE_EP_2:\n"
    "v_mov_b32 v197, v196\n"
    "s_nop 1\n"
    "v_permlane16_swap_b32 v196, v197\n"
    "s_nop 0\n"
    "v_max_f32 v196, v196, v197\n"
    "v_mov_b32 v197, v196\n"
    "s_nop 1\n"
    "v_permlane32_swap_b32 v196, v197\n"
    "s_nop 0\n"
    "v_max3_f32 v196, v196, v197, v186\n"
    "v_sub_f32 v197, v186, v196\n"
    "v_mul_f32 v197, s69, v197\n"
    "v_exp_f32 v197, v197\n"
    "v_mov_b32 v186, v196\n"
    "v_add_f32 v191, 0x423c35a6, v196\n"
    "v_mul_f32 v202, s70, v196\n"
    "s_cmp_eq_u32 s80, 0\n"
    "s_cbranch_scc1 A_BACK_EP_2\n"
    "v_mul_f32 v64, v197, v64\n"
    "v_mul_f32 v65, v197, v65\n"
    "v_mul_f32 v66, v197, v66\n"
    "v_mul_f32 v67, v197, v67\n"
    "v_mul_f32 v68, v197, v68\n"
    "v_mul_f32 v69, v197, v69\n"
    "v_mul_f32 v70, v197, v70\n"
    "v_mul_f32 v71, v197, v71\n"
    "v_mul_f32 v72, v197, v72\n"
    "v_mul_f32 v73, v197, v73\n"
    "v_mul_f32 v74, v197, v74\n"
    "v_mul_f32 v75, v197, v75\n"
    "v_mul_f32 v76, v197, v76\n"
    "v_mul_f32 v77, v197, v77\n"
    "v_mul_f32 v78, v197, v78\n"
    "v_mul_f32 v79, v197, v79\n"
    "v_mul_f32 v80, v197, v80\n"
    "v_mul_f32 v81, v197, v81\n"
    "v_mul_f32 v82, v197, v82\n"
    "v_mul_f32 v83, v197, v83\n"
    "v_mul_f32 v84, v197, v84\n"
    "v_mul_f32 v85, v197, v85\n"
    "v_mul_f32 v86, v197, v86\n"
    "v_mul_f32 v87, v197, v87\n"
    "v_mul_f32 v88, v197, v88\n"
    "v_mul_f32 v89, v197, v89\n"
    "v_mul_f32 v90, v197, v90\n"
    "v_mul_f32 v91, v197, v91\n"
    "v_mul_f32 v92, v197, v92\n"
    "v_mul_f32 v93, v197, v93\n"
    "v_mul_f32 v94, v197, v94\n"
    "v_mul_f32 v95, v197, v95\n"
    "v_accvgpr_read_b32 v198, a136\n"
    "v_mul_f32 v198, v197, v198\n"
    "v_accvgpr_write_b32 a136, v198\n"
    "v_accvgpr_read_b32 v198, a137\n"
    "v_mul_f32 v198, v197, v198\n"
    "v_accvgpr_write_b32 a137, v198\n"
    "v_accvgpr_read_b32 v198, a138\n"
    "v_mul_f32 v198, v197, v198\n"
    "v_accvgpr_write_b32 a138, v198\n"
    "v_accvgpr_read_b32 v198, a139\n"
    "v_mul_f32 v198, v197, v198\n"
    "v_accvgpr_write_b32 a139, v198\n"
    "s_nop 1\n"
    "s_branch A_BACK_EP_2\n"
    "A_RARE_EP_3:\n"
    "v_mov_b32 v197, v196\n"
    "s_nop 1\n"
    "v_permlane16_swap_b32 v196, v197\n"
    "s_nop 0\n"
    "v_max_f32 v196, v196, v197\n"
    "v_mov_b32 v197, v196\n"
    "s_nop 1\n"
    "v_permlane32_swap_b32 v196, v197\n"
    "s_nop 0\n"
    "v_max3_f32 v196, v196, v197, v187\n"
    "v_sub_f32 v197, v187, v196\n"
    "v_mul_f32 v197, s69, v197\n"
    "v_exp_f32 v197, v197\n"
    "v_mov_b32 v187, v196\n"
    "v_add_f32 v192, 0x423c35a6, v196\n"
    "v_mul_f32 v203, s70, v196\n"
    "s_cmp_eq_u32 s80, 0\n"
    "s_cbranch_scc1 A_BACK_EP_3\n"
    "v_mul_f32 v96, v197, v96\n"
    "v_mul_f32 v97, v197, v97\n"
    "v_mul_f32 v98, v197, v98\n"
    "v_mul_f32 v99, v197, v99\n"
    "v_mul_f32 v100, v197, v100\n"
    "v_mul_f32 v101, v197, v101\n"
    "v_mul_f32 v102, v197, v102\n"
    "v_mul_f32 v103, v197, v103\n"
    "v_mul_f32 v104, v197, v104\n"
    "v_mul_f32 v105, v197, v105\n"
    "v_mul_f32 v106, v197, v106\n"
    "v_mul_f32 v107, v197, v107\n"
    "v_mul_f32 v108, v197, v108\n"
    "v_mul_f32 v109, v197, v109\n"
    "v_mul_f32 v110, v197, v110\n"
    "v_mul_f32 v111, v197, v111\n"
    "v_mul_f32 v112, v197, v112\n"
    "v_mul_f32 v113, v197, v113\n"
    "v_mul_f32 v114, v197, v114\n"
    "v_mul_f32 v115, v197, v115\n"
    "v_mul_f32 v116, v197, v116\n"
    "v_mul_f32 v117, v197, v117\n"
    "v_mul_f32 v118, v197, v118\n"
    "v_mul_f32 v119, v197, v119\n"
    "v_mul_f32 v120, v197, v120\n"
    "v_mul_f32 v121, v197, v121\n"
    "v_mul_f32 v122, v197, v122\n"
    "v_mul_f32 v123, v197, v123\n"
    "v_mul_f32 v124, v197, v124\n"
    "v_mul_f32 v125, v197, v125\n"
    "v_mul_f32 v126, v197, v126\n"
    "v_mul_f32 v127, v197, v127\n"
    "v_accvgpr_read_b32 v198, a140\n"
    "v_mul_f32 v198, v197, v198\n"
    "v_accvgpr_write_b32 a140, v198\n"
    "v_accvgpr_read_b32 v198, a141\n"
    "v_mul_f32 v198, v197, v198\n"
    "v_accvgpr_write_b32 a141, v198\n"
    "v_accvgpr_read_b32 v198, a142\n"
    "v_mul_f32 v198, v197, v198\n"
    "v_accvgpr_write_b32 a142, v198\n"
    "v_accvgpr_read_b32 v198, a143\n"
    "v_mul_f32 v198, v197, v198\n"
    "v_accvgpr_write_b32 a143, v198\n"
    "s_nop 1\n"
    "s_branch A_BACK_EP_3\n"
    "A_RARE_EP_4:\n"
    "v_mov_b32 v197, v196\n"
    "s_nop 1\n"
    "v_permlane16_swap_b32 v196, v197\n"
    "s_nop 0\n"
    "v_max_f32 v196, v196, v197\n"
    "v_mov_b32 v197, v196\n"
    "s_nop 1\n"
    "v_permlane32_swap_b32 v196, v197\n"
    "s_nop 0\n"
    "v_max3_f32 v196, v196, v197, v188\n"
    "v_sub_f32 v197, v188, v196\n"
    "v_mul_f32 v197, s69, v197\n"
    "v_exp_f32 v197, v197\n"
    "v_mov_b32 v188, v196\n"
    "v_add_f32 v193, 0x423c35a6, v196\n"
    "v_mul_f32 v204, s70, v196\n"
    "s_cmp_eq_u32 s80, 0\n"
    "s_cbranch_scc1 A_BACK_EP_4\n"
    "v_mul_f32 v128, v197, v128\n"
    "v_mul_f32 v129, v197, v129\n"
    "v_mul_f32 v130, v197, v130\n"
    "v_mul_f32 v131, v197, v131\n"
    "v_mul_f32 v132, v197, v132\n"
    "v_mul_f32 v133, v197, v133\n"
    "v_mul_f32 v134, v197, v134\n"
    "v_mul_f32 v135, v197, v135\n"
    "v_mul_f32 v136, v197, v136\n"
    "v_mul_f32 v137, v197, v137\n"
    "v_mul_f32 v138, v197, v138\n"
    "v_mul_f32 v139, v197, v139\n"
    "v_mul_f32 v140, v197, v140\n"
    "v_mul_f32 v141, v197, v141\n"
    "v_mul_f32 v142, v197, v142\n"
    "v_mul_f32 v143, v197, v143\n"
    "v_mul_f32 v144, v197, v144\n"
    "v_mul_f32 v145, v197, v145\n"
    "v_mul_f32 v146, v197, v146\n"
    "v_mul_f32 v147, v197, v147\n"
    "v_mul_f32 v148, v197, v148\n"
    "v_mul_f32 v149, v197, v149\n"
    "v_mul_f32 v150, v197, v150\n"
    "v_mul_f32 v151, v197, v151\n"
    "v_mul_f32 v152, v197, v152\n"
    "v_mul_f32 v153, v197, v153\n"
    "v_mul_f32 v154, v197, v154\n"
    "v_mul_f32 v155, v197, v155\n"
    "v_mul_f32 v156, v197, v156\n"
    "v_mul_f32 v157, v197, v157\n"
    "v_mul_f32 v158, v197, v158\n"
    "v_mul_f32 v159, v197, v159\n"
    "v_accvgpr_read_b32 v198, a144\n"
    "v_mul_f32 v198, v197, v198\n"
    "v_accvgpr_write_b32 a144, v198\n"
    "v_accvgpr_read_b32 v198, a145\n"
    "v_mul_f32 v198, v197, v198\n"
    "v_accvgpr_write_b32 a145, v198\n"
    "v_accvgpr_read_b32 v198, a146\n"
    "v_mul_f32 v198, v197, v198\n"
    "v_accvgpr_write_b32 a146, v198\n"
    "v_accvgpr_read_b32 v198, a147\n"
    "v_mul_f32 v198, v197, v198\n"
    "v_accvgpr_write_b32 a147, v198\n"
    "s_nop 1\n"
    "s_branch A_BACK_EP_4\n"
    "A_RARE_F_0:\n"
    "v_mov_b32 v197, v196\n"
    "s_nop 1\n"
    "v_permlane16_swap_b32 v196, v197\n"
    "s_nop 0\n"
    "v_max_f32 v196, v196, v197\n"
    "v_mov_b32 v197, v196\n"
    "s_nop 1\n"
    "v_permlane32_swap_b32 v196, v197\n"
    "s_nop 0\n"
    "v_max3_f32 v196, v196, v197, v184\n"
    "v_sub_f32 v197, v184, v196\n"
    "v_mul_f32 v197, s69, v197\n"
    "v_exp_f32 v197, v197\n"
    "v_mov_b32 v184, v196\n"
    "v_add_f32 v189, 0x423c35a6, v196\n"
    "v_mul_f32 v200, s70, v196\n"
    "v_mul_f32 v0, v197, v0\n"
    "v_mul_f32 v1, v197, v1\n"
    "v_mul_f32 v2, v197, v2\n"
    "v_mul_f32 v3, v197, v3\n"
    "v_mul_f32 v4, v197, v4\n"
    "v_mul_f32 v5, v197, v5\n"
    "v_mul_f32 v6, v197, v6\n"
    "v_mul_f32 v7, v197, v7\n"
    "v_mul_f32 v8, v197, v8\n"
    "v_mul_f32 v9, v197, v9\n"
    "v_mul_f32 v10, v197, v10\n"
    "v_mul_f32 v11, v197, v11\n"
    "v_mul_f32 v12, v197, v12\n"
    "v_mul_f32 v13, v197, v13\n"
    "v_mul_f32 v14, v197, v14\n"
    "v_mul_f32 v15, v197, v15\n"
    "v_mul_f32 v16, v197, v16\n"
    "v_mul_f32 v17, v197, v17\n"
    "v_mul_f32 v18, v197, v18\n"
    "v_mul_f32 v19, v197, v19\n"
    "v_mul_f32 v20, v197, v20\n"
    "v_mul_f32 v21, v197, v21\n"
    "v_mul_f32 v22, v197, v22\n"
    "v_mul_f32 v23, v197, v23\n"
    "v_mul_f32 v24, v197, v24\n"
    "v_mul_f32 v25, v197, v25\n"
    "v_mul_f32 v26, v197, v26\n"
    "v_mul_f32 v27, v197, v27\n"
    "v_mul_f32 v28, v197, v28\n"
    "v_mul_f32 v29, v197, v29\n"
    "v_mul_f32 v30, v197, v30\n"
    "v_mul_f32 v31, v197, v31\n"
    "v_accvgpr_read_b32 v198, a128\n"
    "v_mul_f32 v198, v197, v198\n"
    "v_accvgpr_write_b32 a128, v198\n"
    "v_accvgpr_read_b32 v198, a129\n"
    "v_mul_f32 v198, v197, v198\n"
    "v_accvgpr_write_b32 a129, v198\n"
    "v_accvgpr_read_b32 v198, a130\n"
    "v_mul_f32 v198, v197, v198\n"
    "v_accvgpr_write_b32 a130, v198\n"
    "v_accvgpr_read_b32 v198, a131\n"
    "v_mul_f32 v198, v197, v198\n"
    "v_accvgpr_write_b32 a131, v198\n"
    "s_nop 1\n"
    "s_branch A_BACK_F_0\n"
    "A_RARE_F_1:\n"
    "v_mov_b32 v197, v196\n"
    "s_nop 1\n"
    "v_permlane16_swap_b32 v196, v197\n"
    "s_nop 0\n"
    "v_max_f32 v196, v196, v197\n"
    "v_mov_b32 v197, v196\n"
    "s_nop 1\n"
    "v_permlane32_swap_b32 v196, v197\n"
    "s_nop 0\n"
    "v_max3_f32 v196, v196, v197, v185\n"
    "v_sub_f32 v197, v185, v196\n"
    "v_mul_f32 v197, s69, v197\n"
    "v_exp_f32 v197, v197\n"
    "v_mov_b32 v185, v196\n"
    "v_add_f32 v190, 0x423c35a6, v196\n"
    "v_mul_f32 v201, s70, v196\n"
    "v_mul_f32 v32, v197, v32\n"
    "v_mul_f32 v33, v197, v33\n"
    "v_mul_f32 v34, v197, v34\n"
    "v_mul_f32 v35, v197, v35\n"
    "v_mul_f32 v36, v197, v36\n"
    "v_mul_f32 v37, v197, v37\n"
    "v_mul_f32 v38, v197, v38\n"
    "v_mul_f32 v39, v197, v39\n"
    "v_mul_f32 v40, v197, v40\n"
    "v_mul_f32 v41, v197, v41\n"
    "v_mul_f32 v42, v197, v42\n"
    "v_mul_f32 v43, v197, v43\n"
    "v_mul_f32 v44, v197, v44\n"
    "v_mul_f32 v45, v197, v45\n"
    "v_mul_f32 v46, v197, v46\n"
    "v_mul_f32 v47, v197, v47\n"
    "v_mul_f32 v48, v197, v48\n"
    "v_mul_f32 v49, v197, v49\n"
    "v_mul_f32 v50, v197, v50\n"
    "v_mul_f32 v51, v197, v51\n"
    "v_mul_f32 v52, v197, v52\n"
    "v_mul_f32 v53, v197, v53\n"
    "v_mul_f32 v54, v197, v54\n"
    "v_mul_f32 v55, v197, v55\n"
    "v_mul_f32 v56, v197, v56\n"
    "v_mul_f32 v57, v197, v57\n"
    "v_mul_f32 v58, v197, v58\n"
    "v_mul_f32 v59, v197, v59\n"
    "v_mul_f32 v60, v197, v60\n"
    "v_mul_f32 v61, v197, v61\n"
    "v_mul_f32 v62, v197, v62\n"
    "v_mul_f32 v63, v197, v63\n"
    "v_accvgpr_read_b32 v198, a132\n"
    "v_mul_f32 v198, v197, v198\n"
    "v_accvgpr_write_b32 a132, v198\n"
    "v_accvgpr_read_b32 v198, a133\n"
    "v_mul_f32 v198, v197, v198\n"
    "v_accvgpr_write_b32 a133, v198\n"
    "v_accvgpr_read_b32 v198, a134\n"
    "v_mul_f32 v198, v197, v198\n"
    "v_accvgpr_write_b32 a134, v198\n"
    "v_accvgpr_read_b32 v198, a135\n"
    "v_mul_f32 v198, v197, v198\n"
    "v_accvgpr_write_b32 a135, v198\n"
    "s_nop 1\n"
    "s_branch A_BACK_F_1\n"
    "A_RARE_F_2:\n"
    "v_mov_b32 v197, v196\n"
    "s_nop 1\n"
    "v_permlane16_swap_b32 v196, v197\n"
    "s_nop 0\n"
    "v_max_f32 v196, v196, v197\n"
    "v_mov_b32 v197, v196\n"
    "s_nop 1\n"
    "v_permlane32_swap_b32 v196, v197\n"
    "s_nop 0\n"
    "v_max3_f32 v196, v196, v197, v186\n"
    "v_sub_f32 v197, v186, v196\n"
    "v_mul_f32 v197, s69, v197\n"
    "v_exp_f32 v197, v197\n"
    "v_mov_b32 v186, v196\n"
    "v_add_f32 v191, 0x423c35a6, v196\n"
    "v_mul_f32 v202, s70, v196\n"
    "v_mul_f32 v64, v197, v64\n"
    "v_mul_f32 v65, v197, v65\n"
    "v_mul_f32 v66, v197, v66\n"
    "v_mul_f32 v67, v197, v67\n"
    "v_mul_f32 v68, v197, v68\n"
    "v_mul_f32 v69, v197, v69\n"
    "v_mul_f32 v70, v197, v70\n"
    "v_mul_f32 v71, v197, v71\n"
    "v_mul_f32 v72, v197, v72\n"
    "v_mul_f32 v73, v197, v73\n"
    "v_mul_f32 v74, v197, v74\n"
    "v_mul_f32 v75, v197, v75\n"
    "v_mul_f32 v76, v197, v76\n"
    "v_mul_f32 v77, v197, v77\n"
    "v_mul_f32 v78, v197, v78\n"
    "v_mul_f32 v79, v197, v79\n"
    "v_mul_f32 v80, v197, v80\n"
    "v_mul_f32 v81, v197, v81\n"
    "v_mul_f32 v82, v197, v82\n"
    "v_mul_f32 v83, v197, v83\n"
    "v_mul_f32 v84, v197, v84\n"
    "v_mul_f32 v85, v197, v85\n"
    "v_mul_f32 v86, v197, v86\n"
    "v_mul_f32 v87, v197, v87\n"
    "v_mul_f32 v88, v197, v88\n"
    "v_mul_f32 v89, v197, v89\n"
    "v_mul_f32 v90, v197, v90\n"
    "v_mul_f32 v91, v197, v91\n"
    "v_mul_f32 v92, v197, v92\n"
    "v_mul_f32 v93, v197, v93\n"
    "v_mul_f32 v94, v197, v94\n"
    "v_mul_f32 v95, v197, v95\n"
    "v_accvgpr_read_b32 v198, a136\n"
    "v_mul_f32 v198, v197, v198\n"
    "v_accvgpr_write_b32 a136, v198\n"
    "v_accvgpr_read_b32 v198, a137\n"
    "v_mul_f32 v198, v197, v198\n"
    "v_accvgpr_write_b32 a137, v198\n"
    "v_accvgpr_read_b32 v198, a138\n"
    "v_mul_f32 v198, v197, v198\n"
    "v_accvgpr_write_b32 a138, v198\n"
    "v_accvgpr_read_b32 v198, a139\n"
    "v_mul_f32 v198, v197, v198\n"
    "v_accvgpr_write_b32 a139, v198\n"
    "s_nop 1\n"
    "s_branch A_BACK_F_2\n"
    "A_RARE_F_3:\n"
    "v_mov_b32 v197, v196\n"
    "s_nop 1\n"
    "v_permlane16_swap_b32 v196, v197\n"
    "s_nop 0\n"
    "v_max_f32 v196, v196, v197\n"
    "v_mov_b32 v197, v196\n"
    "s_nop 1\n"
    "v_permlane32_swap_b32 v196, v197\n"
    "s_nop 0\n"
    "v_max3_f32 v196, v196, v197, v187\n"
    "v_sub_f32 v197, v187, v196\n"
    "v_mul_f32 v197, s69, v197\n"
    "v_exp_f32 v197, v197\n"
    "v_mov_b32 v187, v196\n"
    "v_add_f32 v192, 0x423c35a6, v196\n"
    "v_mul_f32 v203, s70, v196\n"
    "v_mul_f32 v96, v197, v96\n"
    "v_mul_f32 v97, v197, v97\n"
    "v_mul_f32 v98, v197, v98\n"
    "v_mul_f32 v99, v197, v99\n"
    "v_mul_f32 v100, v197, v100\n"
    "v_mul_f32 v101, v197, v101\n"
    "v_mul_f32 v102, v197, v102\n"
    "v_mul_f32 v103, v197, v103\n"
    "v_mul_f32 v104, v197, v104\n"
    "v_mul_f32 v105, v197, v105\n"
    "v_mul_f32 v106, v197, v106\n"
    "v_mul_f32 v107, v197, v107\n"
    "v_mul_f32 v108, v197, v108\n"
    "v_mul_f32 v109, v197, v109\n"
    "v_mul_f32 v110, v197, v110\n"
    "v_mul_f32 v111, v197, v111\n"
    "v_mul_f32 v112, v197, v112\n"
    "v_mul_f32 v113, v197, v113\n"
    "v_mul_f32 v114, v197, v114\n"
    "v_mul_f32 v115, v197, v115\n"
    "v_mul_f32 v116, v197, v116\n"
    "v_mul_f32 v117, v197, v117\n"
    "v_mul_f32 v118, v197, v118\n"
    "v_mul_f32 v119, v197, v119\n"
    "v_mul_f32 v120, v197, v120\n"
    "v_mul_f32 v121, v197, v121\n"
    "v_mul_f32 v122, v197, v122\n"
    "v_mul_f32 v123, v197, v123\n"
    "v_mul_f32 v124, v197, v124\n"
    "v_mul_f32 v125, v197, v125\n"
    "v_mul_f32 v126, v197, v126\n"
    "v_mul_f32 v127, v197, v127\n"
    "v_accvgpr_read_b32 v198, a140\n"
    "v_mul_f32 v198, v197, v198\n"
    "v_accvgpr_write_b32 a140, v198\n"
    "v_accvgpr_read_b32 v198, a141\n"
    "v_mul_f32 v198, v197, v198\n"
    "v_accvgpr_write_b32 a141, v198\n"
    "v_accvgpr_read_b32 v198, a142\n"
    "v_mul_f32 v198, v197, v198\n"
    "v_accvgpr_write_b32 a142, v198\n"
    "v_accvgpr_read_b32 v198, a143\n"
    "v_mul_f32 v198, v197, v198\n"
    "v_accvgpr_write_b32 a143, v198\n"
    "s_nop 1\n"
    "s_branch A_BACK_F_3\n"
    "A_RARE_F_4:\n"
    "v_mov_b32 v197, v196\n"
    "s_nop 1\n"
    "v_permlane16_swap_b32 v196, v197\n"
    "s_nop 0\n"
    "v_max_f32 v196, v196, v197\n"
    "v_mov_b32 v197, v196\n"
    "s_nop 1\n"
    "v_permlane32_swap_b32 v196, v197\n"
    "s_nop 0\n"
    "v_max3_f32 v196, v196, v197, v188\n"
    "v_sub_f32 v197, v188, v196\n"
    "v_mul_f32 v197, s69, v197\n"
    "v_exp_f32 v197, v197\n"
    "v_mov_b32 v188, v196\n"
    "v_add_f32 v193, 0x423c35a6, v196\n"
    "v_mul_f32 v204, s70, v196\n"
    "v_mul_f32 v128, v197, v128\n"
    "v_mul_f32 v129, v197, v129\n"
    "v_mul_f32 v130, v197, v130\n"
    "v_mul_f32 v131, v197, v131\n"
    "v_mul_f32 v132, v197, v132\n"
    "v_mul_f32 v133, v197, v133\n"
    "v_mul_f32 v134, v197, v134\n"
    "v_mul_f32 v135, v197, v135\n"
    "v_mul_f32 v136, v197, v136\n"
    "v_mul_f32 v137, v197, v137\n"
    "v_mul_f32 v138, v197, v138\n"
    "v_mul_f32 v139, v197, v139\n"
    "v_mul_f32 v140, v197, v140\n"
    "v_mul_f32 v141, v197, v141\n"
    "v_mul_f32 v142, v197, v142\n"
    "v_mul_f32 v143, v197, v143\n"
    "v_mul_f32 v144, v197, v144\n"
    "v_mul_f32 v145, v197, v145\n"
    "v_mul_f32 v146, v197, v146\n"
    "v_mul_f32 v147, v197, v147\n"
    "v_mul_f32 v148, v197, v148\n"
    "v_mul_f32 v149, v197, v149\n"
    "v_mul_f32 v150, v197, v150\n"
    "v_mul_f32 v151, v197, v151\n"
    "v_mul_f32 v152, v197, v152\n"
    "v_mul_f32 v153, v197, v153\n"
    "v_mul_f32 v154, v197, v154\n"
    "v_mul_f32 v155, v197, v155\n"
    "v_mul_f32 v156, v197, v156\n"
    "v_mul_f32 v157, v197, v157\n"
    "v_mul_f32 v158, v197, v158\n"
    "v_mul_f32 v159, v197, v159\n"
    "v_accvgpr_read_b32 v198, a144\n"
    "v_mul_f32 v198, v197, v198\n"
    "v_accvgpr_write_b32 a144, v198\n"
    "v_accvgpr_read_b32 v198, a145\n"
    "v_mul_f32 v198, v197, v198\n"
    "v_accvgpr_write_b32 a145, v198\n"
    "v_accvgpr_read_b32 v198, a146\n"
    "v_mul_f32 v198, v197, v198\n"
    "v_accvgpr_write_b32 a146, v198\n"
    "v_accvgpr_read_b32 v198, a147\n"
    "v_mul_f32 v198, v197, v198\n"
    "v_accvgpr_write_b32 a147, v198\n"
    "s_nop 1\n"
    "s_branch A_BACK_F_4\n"
    "A_RARE_G_0:\n"
    "v_mov_b32 v197, v196\n"
    "s_nop 1\n"
    "v_permlane16_swap_b32 v196, v197\n"
    "s_nop 0\n"
    "v_max_f32 v196, v196, v197\n"
    "v_mov_b32 v197, v196\n"
    "s_nop 1\n"
    "v_permlane32_swap_b32 v196, v197\n"
    "s_nop 0\n"
    "v_max3_f32 v196, v196, v197, v184\n"
    "v_sub_f32 v197, v184, v196\n"
    "v_mul_f32 v197, s69, v197\n"
    "v_exp_f32 v197, v197\n"
    "v_mov_b32 v184, v196\n"
    "v_add_f32 v189, 0x423c35a6, v196\n"
    "v_mul_f32 v200, s70, v196\n"
    "v_mul_f32 v0, v197, v0\n"
    "v_mul_f32 v1, v197, v1\n"
    "v_mul_f32 v2, v197, v2\n"
    "v_mul_f32 v3, v197, v3\n"
    "v_mul_f32 v4, v197, v4\n"
    "v_mul_f32 v5, v197, v5\n"
    "v_mul_f32 v6, v197, v6\n"
    "v_mul_f32 v7, v197, v7\n"
    "v_mul_f32 v8, v197, v8\n"
    "v_mul_f32 v9, v197, v9\n"
    "v_mul_f32 v10, v197, v10\n"
    "v_mul_f32 v11, v197, v11\n"
    "v_mul_f32 v12, v197, v12\n"
    "v_mul_f32 v13, v197, v13\n"
    "v_mul_f32 v14, v197, v14\n"
    "v_mul_f32 v15, v197, v15\n"
    "v_mul_f32 v16, v197, v16\n"
    "v_mul_f32 v17, v197, v17\n"
    "v_mul_f32 v18, v197, v18\n"
    "v_mul_f32 v19, v197, v19\n"
    "v_mul_f32 v20, v197, v20\n"
    "v_mul_f32 v21, v197, v21\n"
    "v_mul_f32 v22, v197, v22\n"
    "v_mul_f32 v23, v197, v23\n"
    "v_mul_f32 v24, v197, v24\n"
    "v_mul_f32 v25, v197, v25\n"
    "v_mul_f32 v26, v197, v26\n"
    "v_mul_f32 v27, v197, v27\n"
    "v_mul_f32 v28, v197, v28\n"
    "v_mul_f32 v29, v197, v29\n"
    "v_mul_f32 v30, v197, v30\n"
    "v_mul_f32 v31, v197, v31\n"
    "v_accvgpr_read_b32 v198, a128\n"
    "v_mul_f32 v198, v197, v198\n"
    "v_accvgpr_write_b32 a128, v198\n"
    "v_accvgpr_read_b32 v198, a129\n"
    "v_mul_f32 v198, v197, v198\n"
    "v_accvgpr_write_b32 a129, v198\n"
    "v_accvgpr_read_b32 v198, a130\n"
    "v_mul_f32 v198, v197, v198\n"
    "v_accvgpr_write_b32 a130, v198\n"
    "v_accvgpr_read_b32 v198, a131\n"
    "v_mul_f32 v198, v197, v198\n"
    "v_accvgpr_write_b32 a131, v198\n"
    "s_nop 1\n"
    "s_branch A_BACK_G_0\n"
    "A_RARE_G_1:\n"
    "v_mov_b32 v197, v196\n"
    "s_nop 1\n"
    "v_permlane16_swap_b32 v196, v197\n"
    "s_nop 0\n"
    "v_max_f32 v196, v196, v197\n"
    "v_mov_b32 v197, v196\n"
    "s_nop 1\n"
    "v_permlane32_swap_b32 v196, v197\n"
    "s_nop 0\n"
    "v_max3_f32 v196, v196, v197, v185\n"
    "v_sub_f32 v197, v185, v196\n"
    "v_mul_f32 v197, s69, v197\n"
    "v_exp_f32 v197, v197\n"
    "v_mov_b32 v185, v196\n"
    "v_add_f32 v190, 0x423c35a6, v196\n"
    "v_mul_f32 v201, s70, v196\n"
    "v_mul_f32 v32, v197, v32\n"
    "v_mul_f32 v33, v197, v33\n"
    "v_mul_f32 v34, v197, v34\n"
    "v_mul_f32 v35, v197, v35\n"
    "v_mul_f32 v36, v197, v36\n"
    "v_mul_f32 v37, v197, v37\n"
    "v_mul_f32 v38, v197, v38\n"
    "v_mul_f32 v39, v197, v39\n"
    "v_mul_f32 v40, v197, v40\n"
    "v_mul_f32 v41, v197, v41\n"
    "v_mul_f32 v42, v197, v42\n"
    "v_mul_f32 v43, v197, v43\n"
    "v_mul_f32 v44, v197, v44\n"
    "v_mul_f32 v45, v197, v45\n"
    "v_mul_f32 v46, v197, v46\n"
    "v_mul_f32 v47, v197, v47\n"
    "v_mul_f32 v48, v197, v48\n"
    "v_mul_f32 v49, v197, v49\n"
    "v_mul_f32 v50, v197, v50\n"
    "v_mul_f32 v51, v197, v51\n"
    "v_mul_f32 v52, v197, v52\n"
    "v_mul_f32 v53, v197, v53\n"
    "v_mul_f32 v54, v197, v54\n"
    "v_mul_f32 v55, v197, v55\n"
    "v_mul_f32 v56, v197, v56\n"
    "v_mul_f32 v57, v197, v57\n"
    "v_mul_f32 v58, v197, v58\n"
    "v_mul_f32 v59, v197, v59\n"
    "v_mul_f32 v60, v197, v60\n"
    "v_mul_f32 v61, v197, v61\n"
    "v_mul_f32 v62, v197, v62\n"
    "v_mul_f32 v63, v197, v63\n"
    "v_accvgpr_read_b32 v198, a132\n"
    "v_mul_f32 v198, v197, v198\n"
    "v_accvgpr_write_b32 a132, v198\n"
    "v_accvgpr_read_b32 v198, a133\n"
    "v_mul_f32 v198, v197, v198\n"
    "v_accvgpr_write_b32 a133, v198\n"
    "v_accvgpr_read_b32 v198, a134\n"
    "v_mul_f32 v198, v197, v198\n"
    "v_accvgpr_write_b32 a134, v198\n"
    "v_accvgpr_read_b32 v198, a135\n"
    "v_mul_f32 v198, v197, v198\n"
    "v_accvgpr_write_b32 a135, v198\n"
    "s_nop 1\n"
    "s_branch A_BACK_G_1\n"
    "A_RARE_G_2:\n"
    "v_mov_b32 v197, v196\n"
    "s_nop 1\n"
    "v_permlane16_swap_b32 v196, v197\n"
    "s_nop 0\n"
    "v_max_f32 v196, v196, v197\n"
    "v_mov_b32 v197, v196\n"
    "s_nop 1\n"
    "v_permlane32_swap_b32 v196, v197\n"
    "s_nop 0\n"
    "v_max3_f32 v196, v196, v197, v186\n"
    "v_sub_f32 v197, v186, v196\n"
    "v_mul_f32 v197, s69, v197\n"
    "v_exp_f32 v197, v197\n"
    "v_mov_b32 v186, v196\n"
    "v_add_f32 v191, 0x423c35a6, v196\n"
    "v_mul_f32 v202, s70, v196\n"
    "v_mul_f32 v64, v197, v64\n"
    "v_mul_f32 v65, v197, v65\n"
    "v_mul_f32 v66, v197, v66\n"
    "v_mul_f32 v67, v197, v67\n"
    "v_mul_f32 v68, v197, v68\n"
    "v_mul_f32 v69, v197, v69\n"
    "v_mul_f32 v70, v197, v70\n"
    "v_mul_f32 v71, v197, v71\n"
    "v_mul_f32 v72, v197, v72\n"
    "v_mul_f32 v73, v197, v73\n"
    "v_mul_f32 v74, v197, v74\n"
    "v_mul_f32 v75, v197, v75\n"
    "v_mul_f32 v76, v197, v76\n"
    "v_mul_f32 v77, v197, v77\n"
    "v_mul_f32 v78, v197, v78\n"
    "v_mul_f32 v79, v197, v79\n"
    "v_mul_f32 v80, v197, v80\n"
    "v_mul_f32 v81, v197, v81\n"
    "v_mul_f32 v82, v197, v82\n"
    "v_mul_f32 v83, v197, v83\n"
    "v_mul_f32 v84, v197, v84\n"
    "v_mul_f32 v85, v197, v85\n"
    "v_mul_f32 v86, v197, v86\n"
    "v_mul_f32 v87, v197, v87\n"
    "v_mul_f32 v88, v197, v88\n"
    "v_mul_f32 v89, v197, v89\n"
    "v_mul_f32 v90, v197, v90\n"
    "v_mul_f32 v91, v197, v91\n"
    "v_mul_f32 v92, v197, v92\n"
    "v_mul_f32 v93, v197, v93\n"
    "v_mul_f32 v94, v197, v94\n"
    "v_mul_f32 v95, v197, v95\n"
    "v_accvgpr_read_b32 v198, a136\n"
    "v_mul_f32 v198, v197, v198\n"
    "v_accvgpr_write_b32 a136, v198\n"
    "v_accvgpr_read_b32 v198, a137\n"
    "v_mul_f32 v198, v197, v198\n"
    "v_accvgpr_write_b32 a137, v198\n"
    "v_accvgpr_read_b32 v198, a138\n"
    "v_mul_f32 v198, v197, v198\n"
    "v_accvgpr_write_b32 a138, v198\n"
    "v_accvgpr_read_b32 v198, a139\n"
    "v_mul_f32 v198, v197, v198\n"
    "v_accvgpr_write_b32 a139, v198\n"
    "s_nop 1\n"
    "s_branch A_BACK_G_2\n"
    "A_RARE_G_3:\n"
    "v_mov_b32 v197, v196\n"
    "s_nop 1\n"
    "v_permlane16_swap_b32 v196, v197\n"
    "s_nop 0\n"
    "v_max_f32 v196, v196, v197\n"
    "v_mov_b32 v197, v196\n"
    "s_nop 1\n"
    "v_permlane32_swap_b32 v196, v197\n"
    "s_nop 0\n"
    "v_max3_f32 v196, v196, v197, v187\n"
    "v_sub_f32 v197, v187, v196\n"
    "v_mul_f32 v197, s69, v197\n"
    "v_exp_f32 v197, v197\n"
    "v_mov_b32 v187, v196\n"
    "v_add_f32 v192, 0x423c35a6, v196\n"
    "v_mul_f32 v203, s70, v196\n"
    "v_mul_f32 v96, v197, v96\n"
    "v_mul_f32 v97, v197, v97\n"
    "v_mul_f32 v98, v197, v98\n"
    "v_mul_f32 v99, v197, v99\n"
    "v_mul_f32 v100, v197, v100\n"
    "v_mul_f32 v101, v197, v101\n"
    "v_mul_f32 v102, v197, v102\n"
    "v_mul_f32 v103, v197, v103\n"
    "v_mul_f32 v104, v197, v104\n"
    "v_mul_f32 v105, v197, v105\n"
    "v_mul_f32 v106, v197, v106\n"
    "v_mul_f32 v107, v197, v107\n"
    "v_mul_f32 v108, v197, v108\n"
    "v_mul_f32 v109, v197, v109\n"
    "v_mul_f32 v110, v197, v110\n"
    "v_mul_f32 v111, v197, v111\n"
    "v_mul_f32 v112, v197, v112\n"
    "v_mul_f32 v113, v197, v113\n"
    "v_mul_f32 v114, v197, v114\n"
    "v_mul_f32 v115, v197, v115\n"
    "v_mul_f32 v116, v197, v116\n"
    "v_mul_f32 v117, v197, v117\n"
    "v_mul_f32 v118, v197, v118\n"
    "v_mul_f32 v119, v197, v119\n"
    "v_mul_f32 v120, v197, v120\n"
    "v_mul_f32 v121, v197, v121\n"
    "v_mul_f32 v122, v197, v122\n"
    "v_mul_f32 v123, v197, v123\n"
    "v_mul_f32 v124, v197, v124\n"
    "v_mul_f32 v125, v197, v125\n"
    "v_mul_f32 v126, v197, v126\n"
    "v_mul_f32 v127, v197, v127\n"
    "v_accvgpr_read_b32 v198, a140\n"
    "v_mul_f32 v198, v197, v198\n"
    "v_accvgpr_write_b32 a140, v198\n"
    "v_accvgpr_read_b32 v198, a141\n"
    "v_mul_f32 v198, v197, v198\n"
    "v_accvgpr_write_b32 a141, v198\n"
    "v_accvgpr_read_b32 v198, a142\n"
    "v_mul_f32 v198, v197, v198\n"
    "v_accvgpr_write_b32 a142, v198\n"
    "v_accvgpr_read_b32 v198, a143\n"
    "v_mul_f32 v198, v197, v198\n"
    "v_accvgpr_write_b32 a143, v198\n"
    "s_nop 1\n"
    "s_branch A_BACK_G_3\n"
    "A_RARE_G_4:\n"
    "v_mov_b32 v197, v196\n"
    "s_nop 1\n"
    "v_permlane16_swap_b32 v196, v197\n"
    "s_nop 0\n"
    "v_max_f32 v196, v196, v197\n"
    "v_mov_b32 v197, v196\n"
    "s_nop 1\n"
    "v_permlane32_swap_b32 v196, v197\n"
    "s_nop 0\n"
    "v_max3_f32 v196, v196, v197, v188\n"
    "v_sub_f32 v197, v188, v196\n"
    "v_mul_f32 v197, s69, v197\n"
    "v_exp_f32 v197, v197\n"
    "v_mov_b32 v188, v196\n"
    "v_add_f32 v193, 0x423c35a6, v196\n"
    "v_mul_f32 v204, s70, v196\n"
    "v_mul_f32 v128, v197, v128\n"
    "v_mul_f32 v129, v197, v129\n"
    "v_mul_f32 v130, v197, v130\n"
    "v_mul_f32 v131, v197, v131\n"
    "v_mul_f32 v132, v197, v132\n"
    "v_mul_f32 v133, v197, v133\n"
    "v_mul_f32 v134, v197, v134\n"
    "v_mul_f32 v135, v197, v135\n"
    "v_mul_f32 v136, v197, v136\n"
    "v_mul_f32 v137, v197, v137\n"
    "v_mul_f32 v138, v197, v138\n"
    "v_mul_f32 v139, v197, v139\n"
    "v_mul_f32 v140, v197, v140\n"
    "v_mul_f32 v141, v197, v141\n"
    "v_mul_f32 v142, v197, v142\n"
    "v_mul_f32 v143, v197, v143\n"
    "v_mul_f32 v144, v197, v144\n"
    "v_mul_f32 v145, v197, v145\n"
    "v_mul_f32 v146, v197, v146\n"
    "v_mul_f32 v147, v197, v147\n"
    "v_mul_f32 v148, v197, v148\n"
    "v_mul_f32 v149, v197, v149\n"
    "v_mul_f32 v150, v197, v150\n"
    "v_mul_f32 v151, v197, v151\n"
    "v_mul_f32 v152, v197, v152\n"
    "v_mul_f32 v153, v197, v153\n"
    "v_mul_f32 v154, v197, v154\n"
    "v_mul_f32 v155, v197, v155\n"
    "v_mul_f32 v156, v197, v156\n"
    "v_mul_f32 v157, v197, v157\n"
    "v_mul_f32 v158, v197, v158\n"
    "v_mul_f32 v159, v197, v159\n"
    "v_accvgpr_read_b32 v198, a144\n"
    "v_mul_f32 v198, v197, v198\n"
    "v_accvgpr_write_b32 a144, v198\n"
    "v_accvgpr_read_b32 v198, a145\n"
    "v_mul_f32 v198, v197, v198\n"
    "v_accvgpr_write_b32 a145, v198\n"
    "v_accvgpr_read_b32 v198, a146\n"
    "v_mul_f32 v198, v197, v198\n"
    "v_accvgpr_write_b32 a146, v198\n"
    "v_accvgpr_read_b32 v198, a147\n"
    "v_mul_f32 v198, v197, v198\n"
    "v_accvgpr_write_b32 a147, v198\n"
    "s_nop 1\n"
    "s_branch A_BACK_G_4\n"
    "A_RARE_DP_0:\n"
    "v_mov_b32 v197, v196\n"
    "s_nop 1\n"
    "v_permlane16_swap_b32 v196, v197\n"
    "s_nop 0\n"
    "v_max_f32 v196, v196, v197\n"
    "v_mov_b32 v197, v196\n"
    "s_nop 1\n"
    "v_permlane32_swap_b32 v196, v197\n"
    "s_nop 0\n"
    "v_max3_f32 v196, v196, v197, v184\n"
    "v_sub_f32 v197, v184, v196\n"
    "v_mul_f32 v197, s69, v197\n"
    "v_exp_f32 v197, v197\n"
    "v_mov_b32 v184, v196\n"
    "v_add_f32 v189, 0x423c35a6, v196\n"
    "v_mul_f32 v200, s70, v196\n"
    "v_mul_f32 v0, v197, v0\n"
    "v_mul_f32 v1, v197, v1\n"
    "v_mul_f32 v2, v197, v2\n"
    "v_mul_f32 v3, v197, v3\n"
    "v_mul_f32 v4, v197, v4\n"
    "v_mul_f32 v5, v197, v5\n"
    "v_mul_f32 v6, v197, v6\n"
    "v_mul_f32 v7, v197, v7\n"
    "v_mul_f32 v8, v197, v8\n"
    "v_mul_f32 v9, v197, v9\n"
    "v_mul_f32 v10, v197, v10\n"
    "v_mul_f32 v11, v197, v11\n"
    "v_mul_f32 v12, v197, v12\n"
    "v_mul_f32 v13, v197, v13\n"
    "v_mul_f32 v14, v197, v14\n"
    "v_mul_f32 v15, v197, v15\n"
    "v_mul_f32 v16, v197, v16\n"
    "v_mul_f32 v17, v197, v17\n"
    "v_mul_f32 v18, v197, v18\n"
    "v_mul_f32 v19, v197, v19\n"
    "v_mul_f32 v20, v197, v20\n"
    "v_mul_f32 v21, v197, v21\n"
    "v_mul_f32 v22, v197, v22\n"
    "v_mul_f32 v23, v197, v23\n"
    "v_mul_f32 v24, v197, v24\n"
    "v_mul_f32 v25, v197, v25\n"
    "v_mul_f32 v26, v197, v26\n"
    "v_mul_f32 v27, v197, v27\n"
    "v_mul_f32 v28, v197, v28\n"
    "v_mul_f32 v29, v197, v29\n"
    "v_mul_f32 v30, v197, v30\n"
    "v_mul_f32 v31, v197, v31\n"
    "v_accvgpr_read_b32 v198, a128\n"
    "v_mul_f32 v198, v197, v198\n"
    "v_accvgpr_write_b32 a128, v198\n"
    "v_accvgpr_read_b32 v198, a129\n"
    "v_mul_f32 v198, v197, v198\n"
    "v_accvgpr_write_b32 a129, v198\n"
    "v_accvgpr_read_b32 v198, a130\n"
    "v_mul_f32 v198, v197, v198\n"
    "v_accvgpr_write_b32 a130, v198\n"
    "v_accvgpr_read_b32 v198, a131\n"
    "v_mul_f32 v198, v197, v198\n"
    "v_accvgpr_write_b32 a131, v198\n"
    "s_nop 1\n"
    "s_branch A_BACK_DP_0\n"
    "A_RARE_DP_1:\n"
    "v_mov_b32 v197, v196\n"
    "s_nop 1\n"
    "v_permlane16_swap_b32 v196, v197\n"
    "s_nop 0\n"
    "v_max_f32 v196, v196, v197\n"
    "v_mov_b32 v197, v196\n"
    "s_nop 1\n"
    "v_permlane32_swap_b32 v196, v197\n"
    "s_nop 0\n"
    "v_max3_f32 v196, v196, v197, v185\n"
    "v_sub_f32 v197, v185, v196\n"
    "v_mul_f32 v197, s69, v197\n"
    "v_exp_f32 v197, v197\n"
    "v_mov_b32 v185, v196\n"
    "v_add_f32 v190, 0x423c35a6, v196\n"
    "v_mul_f32 v201, s70, v196\n"
    "v_mul_f32 v32, v197, v32\n"
    "v_mul_f32 v33, v197, v33\n"
    "v_mul_f32 v34, v197, v34\n"
    "v_mul_f32 v35, v197, v35\n"
    "v_mul_f32 v36, v197, v36\n"
    "v_mul_f32 v37, v197, v37\n"
    "v_mul_f32 v38, v197, v38\n"
    "v_mul_f32 v39, v197, v39\n"
    "v_mul_f32 v40, v197, v40\n"
    "v_mul_f32 v41, v197, v41\n"
    "v_mul_f32 v42, v197, v42\n"
    "v_mul_f32 v43, v197, v43\n"
    "v_mul_f32 v44, v197, v44\n"
    "v_mul_f32 v45, v197, v45\n"
    "v_mul_f32 v46, v197, v46\n"
    "v_mul_f32 v47, v197, v47\n"
    "v_mul_f32 v48, v197, v48\n"
    "v_mul_f32 v49, v197, v49\n"
    "v_mul_f32 v50, v197, v50\n"
    "v_mul_f32 v51, v197, v51\n"
    "v_mul_f32 v52, v197, v52\n"
    "v_mul_f32 v53, v197, v53\n"
    "v_mul_f32 v54, v197, v54\n"
    "v_mul_f32 v55, v197, v55\n"
    "v_mul_f32 v56, v197, v56\n"
    "v_mul_f32 v57, v197, v57\n"
    "v_mul_f32 v58, v197, v58\n"
    "v_mul_f32 v59, v197, v59\n"
    "v_mul_f32 v60, v197, v60\n"
    "v_mul_f32 v61, v197, v61\n"
    "v_mul_f32 v62, v197, v62\n"
    "v_mul_f32 v63, v197, v63\n"
    "v_accvgpr_read_b32 v198, a132\n"
    "v_mul_f32 v198, v197, v198\n"
    "v_accvgpr_write_b32 a132, v198\n"
    "v_accvgpr_read_b32 v198, a133\n"
    "v_mul_f32 v198, v197, v198\n"
    "v_accvgpr_write_b32 a133, v198\n"
    "v_accvgpr_read_b32 v198, a134\n"
    "v_mul_f32 v198, v197, v198\n"
    "v_accvgpr_write_b32 a134, v198\n"
    "v_accvgpr_read_b32 v198, a135\n"
    "v_mul_f32 v198, v197, v198\n"
    "v_accvgpr_write_b32 a135, v198\n"
    "s_nop 1\n"
    "s_branch A_BACK_DP_1\n"
    "A_RARE_DP_2:\n"
    "v_mov_b32 v197, v196\n"
    "s_nop 1\n"
    "v_permlane16_swap_b32 v196, v197\n"
    "s_nop 0\n"
    "v_max_f32 v196, v196, v197\n"
    "v_mov_b32 v197, v196\n"
    "s_nop 1\n"
    "v_permlane32_swap_b32 v196, v197\n"
    "s_nop 0\n"
    "v_max3_f32 v196, v196, v197, v186\n"
    "v_sub_f32 v197, v186, v196\n"
    "v_mul_f32 v197, s69, v197\n"
    "v_exp_f32 v197, v197\n"
    "v_mov_b32 v186, v196\n"
    "v_add_f32 v191, 0x423c35a6, v196\n"
    "v_mul_f32 v202, s70, v196\n"
    "v_mul_f32 v64, v197, v64\n"
    "v_mul_f32 v65, v197, v65\n"
    "v_mul_f32 v66, v197, v66\n"
    "v_mul_f32 v67, v197, v67\n"
    "v_mul_f32 v68, v197, v68\n"
    "v_mul_f32 v69, v197, v69\n"
    "v_mul_f32 v70, v197, v70\n"
    "v_mul_f32 v71, v197, v71\n"
    "v_mul_f32 v72, v197, v72\n"
    "v_mul_f32 v73, v197, v73\n"
    "v_mul_f32 v74, v197, v74\n"
    "v_mul_f32 v75, v197, v75\n"
    "v_mul_f32 v76, v197, v76\n"
    "v_mul_f32 v77, v197, v77\n"
    "v_mul_f32 v78, v197, v78\n"
    "v_mul_f32 v79, v197, v79\n"
    "v_mul_f32 v80, v197, v80\n"
    "v_mul_f32 v81, v197, v81\n"
    "v_mul_f32 v82, v197, v82\n"
    "v_mul_f32 v83, v197, v83\n"
    "v_mul_f32 v84, v197, v84\n"
    "v_mul_f32 v85, v197, v85\n"
    "v_mul_f32 v86, v197, v86\n"
    "v_mul_f32 v87, v197, v87\n"
    "v_mul_f32 v88, v197, v88\n"
    "v_mul_f32 v89, v197, v89\n"
    "v_mul_f32 v90, v197, v90\n"
    "v_mul_f32 v91, v197, v91\n"
    "v_mul_f32 v92, v197, v92\n"
    "v_mul_f32 v93, v197, v93\n"
    "v_mul_f32 v94, v197, v94\n"
    "v_mul_f32 v95, v197, v95\n"
    "v_accvgpr_read_b32 v198, a136\n"
    "v_mul_f32 v198, v197, v198\n"
    "v_accvgpr_write_b32 a136, v198\n"
    "v_accvgpr_read_b32 v198, a137\n"
    "v_mul_f32 v198, v197, v198\n"
    "v_accvgpr_write_b32 a137, v198\n"
    "v_accvgpr_read_b32 v198, a138\n"
    "v_mul_f32 v198, v197, v198\n"
    "v_accvgpr_write_b32 a138, v198\n"
    "v_accvgpr_read_b32 v198, a139\n"
    "v_mul_f32 v198, v197, v198\n"
    "v_accvgpr_write_b32 a139, v198\n"
    "s_nop 1\n"
    "s_branch A_BACK_DP_2\n"
    "A_RARE_DP_3:\n"
    "v_mov_b32 v197, v196\n"
    "s_nop 1\n"
    "v_permlane16_swap_b32 v196, v197\n"
    "s_nop 0\n"
    "v_max_f32 v196, v196, v197\n"
    "v_mov_b32 v197, v196\n"
    "s_nop 1\n"
    "v_permlane32_swap_b32 v196, v197\n"
    "s_nop 0\n"
    "v_max3_f32 v196, v196, v197, v187\n"
    "v_sub_f32 v197, v187, v196\n"
    "v_mul_f32 v197, s69, v197\n"
    "v_exp_f32 v197, v197\n"
    "v_mov_b32 v187, v196\n"
    "v_add_f32 v192, 0x423c35a6, v196\n"
    "v_mul_f32 v203, s70, v196\n"
    "v_mul_f32 v96, v197, v96\n"
    "v_mul_f32 v97, v197, v97\n"
    "v_mul_f32 v98, v197, v98\n"
    "v_mul_f32 v99, v197, v99\n"
    "v_mul_f32 v100, v197, v100\n"
    "v_mul_f32 v101, v197, v101\n"
    "v_mul_f32 v102, v197, v102\n"
    "v_mul_f32 v103, v197, v103\n"
    "v_mul_f32 v104, v197, v104\n"
    "v_mul_f32 v105, v197, v105\n"
    "v_mul_f32 v106, v197, v106\n"
    "v_mul_f32 v107, v197, v107\n"
    "v_mul_f32 v108, v197, v108\n"
    "v_mul_f32 v109, v197, v109\n"
    "v_mul_f32 v110, v197, v110\n"
    "v_mul_f32 v111, v197, v111\n"
    "v_mul_f32 v112, v197, v112\n"
    "v_mul_f32 v113, v197, v113\n"
    "v_mul_f32 v114, v197, v114\n"
    "v_mul_f32 v115, v197, v115\n"
    "v_mul_f32 v116, v197, v116\n"
    "v_mul_f32 v117, v197, v117\n"
    "v_mul_f32 v118, v197, v118\n"
    "v_mul_f32 v119, v197, v119\n"
    "v_mul_f32 v120, v197, v120\n"
    "v_mul_f32 v121, v197, v121\n"
    "v_mul_f32 v122, v197, v122\n"
    "v_mul_f32 v123, v197, v123\n"
    "v_mul_f32 v124, v197, v124\n"
    "v_mul_f32 v125, v197, v125\n"
    "v_mul_f32 v126, v197, v126\n"
    "v_mul_f32 v127, v197, v127\n"
    "v_accvgpr_read_b32 v198, a140\n"
    "v_mul_f32 v198, v197, v198\n"
    "v_accvgpr_write_b32 a140, v198\n"
    "v_accvgpr_read_b32 v198, a141\n"
    "v_mul_f32 v198, v197, v198\n"
    "v_accvgpr_write_b32 a141, v198\n"
    "v_accvgpr_read_b32 v198, a142\n"
    "v_mul_f32 v198, v197, v198\n"
    "v_accvgpr_write_b32 a142, v198\n"
    "v_accvgpr_read_b32 v198, a143\n"
    "v_mul_f32 v198, v197, v198\n"
    "v_accvgpr_write_b32 a143, v198\n"
    "s_nop 1\n"
    "s_branch A_BACK_DP_3\n"
    "A_RARE_DP_4:\n"
    "v_mov_b32 v197, v196\n"
    "s_nop 1\n"
    "v_permlane16_swap_b32 v196, v197\n"
    "s_nop 0\n"
    "v_max_f32 v196, v196, v197\n"
    "v_mov_b32 v197, v196\n"
    "s_nop 1\n"
    "v_permlane32_swap_b32 v196, v197\n"
    "s_nop 0\n"
    "v_max3_f32 v196, v196, v197, v188\n"
    "v_sub_f32 v197, v188, v196\n"
    "v_mul_f32 v197, s69, v197\n"
    "v_exp_f32 v197, v197\n"
    "v_mov_b32 v188, v196\n"
    "v_add_f32 v193, 0x423c35a6, v196\n"
    "v_mul_f32 v204, s70, v196\n"
    "v_mul_f32 v128, v197, v128\n"
    "v_mul_f32 v129, v197, v129\n"
    "v_mul_f32 v130, v197, v130\n"
    "v_mul_f32 v131, v197, v131\n"
    "v_mul_f32 v132, v197, v132\n"
    "v_mul_f32 v133, v197, v133\n"
    "v_mul_f32 v134, v197, v134\n"
    "v_mul_f32 v135, v197, v135\n"
    "v_mul_f32 v136, v197, v136\n"
    "v_mul_f32 v137, v197, v137\n"
    "v_mul_f32 v138, v197, v138\n"
    "v_mul_f32 v139, v197, v139\n"
    "v_mul_f32 v140, v197, v140\n"
    "v_mul_f32 v141, v197, v141\n"
    "v_mul_f32 v142, v197, v142\n"
    "v_mul_f32 v143, v197, v143\n"
    "v_mul_f32 v144, v197, v144\n"
    "v_mul_f32 v145, v197, v145\n"
    "v_mul_f32 v146, v197, v146\n"
    "v_mul_f32 v147, v197, v147\n"
    "v_mul_f32 v148, v197, v148\n"
    "v_mul_f32 v149, v197, v149\n"
    "v_mul_f32 v150, v197, v150\n"
    "v_mul_f32 v151, v197, v151\n"
    "v_mul_f32 v152, v197, v152\n"
    "v_mul_f32 v153, v197, v153\n"
    "v_mul_f32 v154, v197, v154\n"
    "v_mul_f32 v155, v197, v155\n"
    "v_mul_f32 v156, v197, v156\n"
    "v_mul_f32 v157, v197, v157\n"
    "v_mul_f32 v158, v197, v158\n"
    "v_mul_f32 v159, v197, v159\n"
    "v_accvgpr_read_b32 v198, a144\n"
    "v_mul_f32 v198, v197, v198\n"
    "v_accvgpr_write_b32 a144, v198\n"
    "v_accvgpr_read_b32 v198, a145\n"
    "v_mul_f32 v198, v197, v198\n"
    "v_accvgpr_write_b32 a145, v198\n"
    "v_accvgpr_read_b32 v198, a146\n"
    "v_mul_f32 v198, v197, v198\n"
    "v_accvgpr_write_b32 a146, v198\n"
    "v_accvgpr_read_b32 v198, a147\n"
    "v_mul_f32 v198, v197, v198\n"
    "v_accvgpr_write_b32 a147, v198\n"
    "s_nop 1\n"
    "s_branch A_BACK_DP_4\n"
    "A_END:\n"
    :
    : "s"(__builtin_amdgcn_kernarg_segment_ptr()), "s"(blockIdx.x), "s"(w)
    : "memory","scc","vcc","s4","s5","s6","s7","s8","s9","s10","s11","s12","s13","s14","s15","s16","s17","s18","s19","s20","s21","s22","s23","s24","s25","s26","s27","s28","s29","s30","s31","s34","s35","s36","s37","s38","s39","s40","s41","s42","s43","s44","s45","s46","s47","s48","s49","s50","s51","s52","s53","s54","s55","s56","s57","s58","s59","s60","s61","s62","s63","s64","s65","s66","s67","s68","s69","s70","s71","s72","s73","s74","s75","s76","s77","s78","s79","s80","s81","s82","s83","s84","s85","s86","s87","s88","s89","s90","s91","s92","s93","s94","s95","s96","s97","s98","s99","v0","v1","v2","v3","v4","v5","v6","v7","v8","v9","v10","v11","v12","v13","v14","v15","v16","v17","v18","v19","v20","v21","v22","v23","v24","v25","v26","v27","v28","v29","v30","v31","v32","v33","v34","v35","v36","v37","v38","v39","v40","v41","v42","v43","v44","v45","v46","v47","v48","v49","v50","v51","v52","v53","v54","v55","v56","v57","v58","v59","v60","v61","v62","v63","v64","v65","v66","v67","v68","v69","v70","v71","v72","v73","v74","v75","v76","v77","v78","v79","v80","v81","v82","v83","v84","v85","v86","v87","v88","v89","v90","v91","v92","v93","v94","v95","v96","v97","v98","v99","v100","v101","v102","v103","v104","v105","v106","v107","v108","v109","v110","v111","v112","v113","v114","v115","v116","v117","v118","v119","v120","v121","v122","v123","v124","v125","v126","v127","v128","v129","v130","v131","v132","v133","v134","v135","v136","v137","v138","v139","v140","v141","v142","v143","v144","v145","v146","v147","v148","v149","v150","v151","v152","v153","v154","v155","v156","v157","v158","v159","v160","v161","v162","v163","v164","v165","v166","v167","v168","v169","v170","v171","v172","v173","v174","v175","v176","v177","v178","v179","v180","v181","v182","v183","v184","v185","v186","v187","v188","v189","v190","v191","v192","v193","v194","v195","v196","v197","v198","v199","v200","v201","v202","v203","v204","v205","v206","v207","v208","v209","v210","v211","v212","v213","v214","v215","v216","v217","v218","v219","v220","v221","v222","v223","v224","v225","v226","v227","v228","v229","v230","v231","v232","v233","v234","v235","v236","v237","v238","v239","v240","v241","v242","v243","v244","v245","v246","v247","v248","v249","v250","v251","v252","v253","v254","v255","a0","a1","a2","a3","a4","a5","a6","a7","a8","a9","a10","a11","a12","a13","a14","a15","a16","a17","a18","a19","a20","a21","a22","a23","a24","a25","a26","a27","a28","a29","a30","a31","a32","a33","a34","a35","a36","a37","a38","a39","a40","a41","a42","a43","a44","a45","a46","a47","a48","a49","a50","a51","a52","a53","a54","a55","a56","a57","a58","a59","a60","a61","a62","a63","a64","a65","a66","a67","a68","a69","a70","a71","a72","a73","a74","a75","a76","a77","a78","a79","a80","a81","a82","a83","a84","a85","a86","a87","a88","a89","a90","a91","a92","a93","a94","a95","a96","a97","a98","a99","a100","a101","a102","a103","a104","a105","a106","a107","a108","a109","a110","a111","a112","a113","a114","a115","a116","a117","a118","a119","a120","a121","a122","a123","a124","a125","a126","a127","a128","a129","a130","a131","a132","a133","a134","a135","a136","a137","a138","a139","a140","a141","a142","a143","a144","a145","a146","a147","a148","a149","a150","a151","a152","a153","a154","a155","a156","a157","a158","a159","a160","a161","a162","a163","a164","a165","a166","a167","a168","a169","a170","a171","a172","a173","a174","a175","a176","a177","a178","a179","a180","a181","a182","a183","a184","a185","a186","a187","a188","a189","a190","a191","a192","a193","a194","a195","a196","a197","a198","a199","a200","a201","a202","a203","a204","a205","a206","a207","a208","a209","a210","a211","a212","a213","a214","a215","a216","a217","a218","a219","a220","a221","a222","a223","a224","a225","a226","a227","a228","a229","a230","a231","a232","a233","a234","a235","a236","a237","a238","a239","a240","a241","a242","a243","a244","a245","a246","a247","a248","a249","a250","a251","a252","a253","a254","a255");
}

extern "C" void kernel_launch(void* const* d_in, const int* in_sizes, int n_in, void* d_out, int out_size, void* d_ws, size_t ws_size,
                              hipStream_t stream) {
  k1_attn<<<256, 256, 0, stream>>>(d_in[0], d_in[1], d_in[2], d_in[3], d_in[4], d_in[5], d_in[6], d_in[7], d_in[8], d_in[9], d_ws, d_out);
}
```
